```python
import torch

def _softmax(x, axis=-1):
    return torch.softmax(x, dim=axis)
def _gelu(x):
    return torch.nn.functional.gelu(x, approximate="tanh")
def _silu(x):
    return torch.nn.functional.silu(x)
def _sigmoid(x):
    return torch.sigmoid(x)
def _layernorm(x, axis=-1, eps=1e-5):
    m = x.mean(dim=axis, keepdim=True)
    v = x.var(dim=axis, keepdim=True, unbiased=False)
    return (x - m) / torch.sqrt(v + eps)
def _scatter_add(tgt, idx, src, axis=0):
    return tgt.clone().index_add_(axis, idx, src)
def _segment_sum(src, seg, n):
    out = torch.zeros((n,) + src.shape[1:], dtype=src.dtype)
    return out.index_add_(0, seg, src)
def _pad(x, pw):
    flat = tuple(v for pair in reversed(pw) for v in pair)
    return torch.nn.functional.pad(x, flat)

kernel_name = 'standalone__depthwise_separable__vision'

def setup_inputs():

    _w = (lambda a: (lambda t: t.to(torch.bfloat16) if t.is_floating_point() else t)(torch.as_tensor(a)))
    H = W = 384
    C = 384
    k = 1
    return {
        "x": _w((lambda *s: torch.randn(*s, dtype=torch.bfloat16) * 0.05)(1, H, W, C)),
        "dw": _w((lambda *s: torch.randn(*s, dtype=torch.bfloat16) * 0.05)(k, k, C)),
        "pw": _w((lambda *s: torch.randn(*s, dtype=torch.bfloat16) * 0.05)(C, C)),
    }

def reference(x, dw, pw):
    B, H, W, C = x.shape
    k = dw.shape[0]
    pad = k // 2
    xp = _pad(x, ((0,0),(pad,pad),(pad,pad),(0,0)))
    out = torch.zeros_like(x)
    for i in range(k):
        for j in range(k):
            out = out + xp[:, i:i+H, j:j+W, :] * dw[i, j][None, None, None, :]
    return torch.einsum('bhwc,cd->bhwd', out, pw)
```

```cpp
#include <hip/hip_runtime.h>
#include <stdint.h>

#define CH 384
#define TM 16
#define TILE_BYTES (TM * CH * 2)
#define NS 13
#define LDS_BYTES (NS * TILE_BYTES + 1024)

__global__ __launch_bounds__(256, 1)
void dwsep_kernel(const char* x, uint32_t xbytes, uint32_t f0, char* out, uint32_t obytes, uint32_t f1,
                  const char* pw, uint32_t pwbytes, uint32_t f2, const char* dw, uint32_t dwbytes, uint32_t f3,
                  uint32_t nit, uint32_t tstride) {
  __shared__ __attribute__((aligned(1024))) char lds[LDS_BYTES];
  uint32_t lds0 = (uint32_t)(uintptr_t)(__attribute__((address_space(3))) char*)lds;
  uint64_t ka = (uint64_t)(uintptr_t)__builtin_amdgcn_kernarg_segment_ptr();
  uint32_t bid = blockIdx.x, tid = threadIdx.x;
  asm volatile(
    "s_load_dwordx16 s[36:51], s[0:1], 0x0\n"
    "s_load_dwordx2 s[52:53], s[0:1], 0x40\n"
    "s_memrealtime s[92:93]\n"
    "s_getpc_b64 s[14:15]\n"
    "70:\n"
    "v_lshlrev_b32 v48, 6, v0\n"
    "s_add_u32 s72, s14, 51f-70b\n"
    "s_addc_u32 s73, s15, 0\n"
    "s_mov_b32 s13, 79f-70b\n"
    "v_min_u32 v48, s13, v48\n"
    "global_load_dword v49, v48, s[14:15]\n"
    "v_readfirstlane_b32 s4, v0\n"
    "v_bfe_u32 v53, v0, 3, 3\n"
    "v_lshrrev_b32 v54, 2, v0\n"
    "s_lshr_b32 s4, s4, 6\n"
    "v_mul_u32_u24 v53, 0x300, v53\n"
    "s_bfe_u32 s5, s2, 0x20003\n"
    "v_and_b32 v55, 4, v54\n"
    "s_bfe_u32 s6, s2, 0x20005\n"
    "v_xor_b32 v55, v55, v0\n"
    "s_cmp_eq_u32 s6, 3\n"
    "v_and_b32 v55, 7, v55\n"
    "s_cselect_b32 s6, 0, s6\n"
    "v_lshl_add_u32 v53, v55, 4, v53\n"
    "s_lshl_b32 s7, s4, 7\n"
    "v_and_b32 v17, 63, v0\n"
    "s_mul_i32 s58, s5, 24576\n"
    "v_add_u32 v244, s7, v53\n"
    "s_mul_i32 s62, s6, 98304\n"
    "v_lshlrev_b32 v17, 4, v17\n"
    "s_lshl_b32 s7, s5, 6\n"
    "s_lshl_b32 s8, s6, 8\n"
    "v_xor_b32 v53, s7, v17\n"
    "s_add_u32 s12, s3, 159744\n"
    "s_mul_i32 s83, s4, 24576\n"
    "v_add_u32 v53, s8, v53\n"
    "s_mov_b32 m0, s12\n"
    "v_subrev_u32 v55, 0x300, v53\n"
    "s_add_u32 s83, s83, s3\n"
    "v_min_u32 v250, v53, v55\n"
    "v_add_u32 v245, 0x1400, v244\n"
    "v_add_u32 v246, 0x2800, v244\n"
    "v_add_u32 v247, 0x3c00, v244\n"
    "v_and_b32 v53, 15, v54\n"
    "s_mul_i32 s55, s4, 3072\n"
    "v_and_b32 v54, 12, v54\n"
    "s_lshl_b32 s7, s4, 6\n"
    "v_add_u32 v53, v53, v54\n"
    "s_sub_u32 s13, s55, 512\n"
    "v_mul_u32_u24 v53, 0x300, v53\n"
    "s_add_u32 s55, s55, s3\n"
    "v_and_b32 v54, 3, v0\n"
    "s_add_u32 s7, s7, 512\n"
    "v_lshl_add_u32 v53, v54, 4, v53\n"
    "v_add_u32 v248, s7, v53\n"
    "v_add_u32 v249, 0x800, v248\n"
    "s_xor_b32 s7, s5, 1\n"
    "s_mul_i32 s59, s7, 24576\n"
    "s_xor_b32 s7, s5, 2\n"
    "s_mul_i32 s60, s7, 24576\n"
    "s_xor_b32 s7, s5, 3\n"
    "s_mul_i32 s61, s7, 24576\n"
    "s_add_u32 s63, s62, 98304\n"
    "s_cmp_eq_u32 s63, 294912\n"
    "s_cselect_b32 s63, 0, s63\n"
    "s_add_u32 s64, s63, 98304\n"
    "s_cmp_eq_u32 s64, 294912\n"
    "s_cselect_b32 s64, 0, s64\n"
    "v_and_b32 v18, 15, v0\n"
    "v_bfe_u32 v52, v0, 4, 2\n"
    "v_and_b32 v53, 48, v0\n"
    "s_add_u32 s7, s83, 61440\n"
    "v_add_u32 v13, s12, v53\n"
    "v_lshlrev_b32 v53, 2, v52\n"
    "v_add_u32 v16, s7, v17\n"
    "v_and_b32 v54, 0x30, v17\n"
    "v_lshl_add_u32 v54, v52, 10, v54\n"
    "v_bfe_u32 v55, v0, 2, 2\n"
    "v_lshl_add_u32 v54, v55, 7, v54\n"
    "v_and_b32 v55, 8, v0\n"
    "v_lshl_add_u32 v54, v55, 3, v54\n"
    "v_add_u32 v11, s7, v54\n"
    "v_xor_b32 v12, 64, v11\n"
    "v_mov_b32 v54, 0xffff\n"
    "v_mov_b32 v55, 0\n"
    "v_lshlrev_b64 v[14:15], v17, v[54:55]\n"
    "s_and_b32 s7, s2, 7\n"
    "s_lshl_b32 s7, s7, 3\n"
    "s_bfe_u32 s8, s2, 0x30003\n"
    "s_add_u32 s7, s7, s8\n"
    "s_and_b32 s8, s2, 0xc0\n"
    "s_add_u32 s7, s7, s8\n"
    "s_mul_i32 s54, s7, 12288\n"
    "s_mov_b32 s81, s54\n"
    "s_mov_b32 s79, s55\n"
    "s_mov_b32 s9, 0x51403020\n"
    "s_mov_b32 s10, 0x52423121\n"
    "s_mov_b32 s11, 0x53433322\n"
    "s_cmp_eq_u32 s6, 1\n"
    "s_cselect_b32 s9, 0x61204030, s9\n"
    "s_cselect_b32 s10, 0x62521131, s10\n"
    "s_cselect_b32 s11, 0x33534302, s11\n"
    "s_cmp_eq_u32 s6, 2\n"
    "s_cselect_b32 s9, 0x71302040, s9\n"
    "s_cselect_b32 s10, 0x42622111, s10\n"
    "s_cselect_b32 s11, 0x43335312, s11\n"
    "s_xor_b32 s7, s4, s5\n"
    "v_lshlrev_b32 v54, 3, v52\n"
    "s_lshl_b32 s7, s7, 2\n"
    "v_xor_b32 v55, s7, v18\n"
    "v_bfe_u32 v53, s11, v54, 8\n"
    "v_xor_b32 v53, v53, v55\n"
    "v_lshl_add_u32 v2, v53, 4, s13\n"
    "v_bfe_u32 v53, s10, v54, 8\n"
    "s_waitcnt lgkmcnt(0)\n"
    "buffer_load_dwordx4 v250, s[48:51], 0 offen lds\n"
    "s_add_u32 m0, s83, 61440\n"
    "s_add_u32 s84, s58, s62\n"
    "v_xor_b32 v53, v53, v55\n"
    "v_lshl_add_u32 v1, v53, 4, s13\n"
    "v_bfe_u32 v53, s9, v54, 8\n"
    "buffer_load_dwordx4 v244, s[44:47], s84 offen lds\n"
    "v_xor_b32 v53, v53, v55\n"
    "v_lshl_add_u32 v0, v53, 4, s13\n"
    "v_xor_b32 v53, v52, v18\n"
    "buffer_load_dwordx4 v245, s[44:47], s84 offen offset:1024 lds\n"
    "v_mul_u32_u24 v54, 0x300, v18\n"
    "v_lshl_add_u32 v54, v53, 4, v54\n"
    "v_add_u32 v3, s3, v54\n"
    "buffer_load_dwordx4 v246, s[44:47], s84 offen offset:2048 lds\n"
    "v_xor_b32 v4, 64, v3\n"
    "v_xor_b32 v5, 128, v3\n"
    "v_xor_b32 v6, 192, v3\n"
    "buffer_load_dwordx4 v247, s[44:47], s84 offen offset:3072 lds\n"
    "s_add_u32 m0, s83, 65536\n"
    "v_add_u32 v10, s55, v17\n"
    "s_lshl_b32 s14, s4, 1\n"
    "s_and_b32 s7, s14, 3\n"
    "buffer_load_dwordx4 v248, s[44:47], s84 offen lds\n"
    "s_lshr_b32 s8, s14, 2\n"
    "s_xor_b32 s7, s7, s5\n"
    "s_sub_u32 s8, s8, s6\n"
    "buffer_load_dwordx4 v249, s[44:47], s84 offen offset:1024 lds\n"
    "s_add_u32 m0, s83, 67584\n"
    "s_add_u32 s84, s59, s62\n"
    "s_lshl_b32 s7, s7, 6\n"
    "s_cmp_lt_i32 s8, 0\n"
    "s_cselect_b32 s13, 3, 0\n"
    "v_xor_b32 v53, s7, v3\n"
    "buffer_load_dwordx4 v244, s[44:47], s84 offen lds\n"
    "s_add_u32 s8, s8, s13\n"
    "s_lshl_b32 s8, s8, 8\n"
    "s_add_u32 s14, s14, 1\n"
    "buffer_load_dwordx4 v245, s[44:47], s84 offen offset:1024 lds\n"
    "v_add_u32 v7, s8, v53\n"
    "s_and_b32 s7, s14, 3\n"
    "s_lshr_b32 s8, s14, 2\n"
    "buffer_load_dwordx4 v246, s[44:47], s84 offen offset:2048 lds\n"
    "s_xor_b32 s7, s7, s5\n"
    "s_sub_u32 s8, s8, s6\n"
    "s_lshl_b32 s7, s7, 6\n"
    "buffer_load_dwordx4 v247, s[44:47], s84 offen offset:3072 lds\n"
    "s_add_u32 m0, s83, 71680\n"
    "s_cmp_lt_i32 s8, 0\n"
    "s_cselect_b32 s13, 3, 0\n"
    "v_xor_b32 v53, s7, v3\n"
    "s_add_u32 s8, s8, s13\n"
    "buffer_load_dwordx4 v248, s[44:47], s84 offen lds\n"
    "s_lshl_b32 s8, s8, 8\n"
    "s_add_u32 s14, s4, 8\n"
    "v_add_u32 v8, s8, v53\n"
    "buffer_load_dwordx4 v249, s[44:47], s84 offen offset:1024 lds\n"
    "s_bitcmp1_b32 s2, 7\n"
    "s_cbranch_scc0 72f\n"
    "s_mov_b32 m0, s79\n"
    "s_nop 0\n"
    "buffer_load_dwordx4 v0, s[36:39], s81 offen nt lds\n"
    "buffer_load_dwordx4 v1, s[36:39], s81 offen offset:1024 nt lds\n"
    "buffer_load_dwordx4 v2, s[36:39], s81 offen offset:2048 nt lds\n"
    "s_add_u32 s79, s79, 12288\n"
    "s_mov_b32 m0, s79\n"
    "s_add_u32 s81, s81, s53\n"
    "buffer_load_dwordx4 v0, s[36:39], s81 offen nt lds\n"
    "buffer_load_dwordx4 v1, s[36:39], s81 offen offset:1024 nt lds\n"
    "buffer_load_dwordx4 v2, s[36:39], s81 offen offset:2048 nt lds\n"
    "s_add_u32 s79, s79, 12288\n"
    "s_mov_b32 m0, s79\n"
    "s_add_u32 s81, s81, s53\n"
    "buffer_load_dwordx4 v0, s[36:39], s81 offen nt lds\n"
    "buffer_load_dwordx4 v1, s[36:39], s81 offen offset:1024 nt lds\n"
    "buffer_load_dwordx4 v2, s[36:39], s81 offen offset:2048 nt lds\n"
    "72:\n"
    "s_add_u32 m0, s83, 73728\n"
    "s_add_u32 s84, s60, s62\n"
    "s_and_b32 s7, s14, 3\n"
    "s_lshr_b32 s8, s14, 2\n"
    "s_xor_b32 s7, s7, s5\n"
    "buffer_load_dwordx4 v244, s[44:47], s84 offen lds\n"
    "s_sub_u32 s8, s8, s6\n"
    "s_lshl_b32 s7, s7, 6\n"
    "s_cmp_lt_i32 s8, 0\n"
    "s_cselect_b32 s13, 3, 0\n"
    "buffer_load_dwordx4 v245, s[44:47], s84 offen offset:1024 lds\n"
    "v_xor_b32 v53, s7, v3\n"
    "s_add_u32 s8, s8, s13\n"
    "s_lshl_b32 s8, s8, 8\n"
    "buffer_load_dwordx4 v246, s[44:47], s84 offen offset:2048 lds\n"
    "v_add_u32 v9, s8, v53\n"
    "s_bfe_u32 s13, s2, 0x40003\n"
    "s_mul_i32 s7, s13, 3\n"
    "buffer_load_dwordx4 v247, s[44:47], s84 offen offset:3072 lds\n"
    "s_add_u32 m0, s83, 77824\n"
    "s_lshr_b32 s7, s7, 4\n"
    "s_lshl_b32 s30, 0x12490, s7\n"
    "s_mul_i32 s7, s13, 12\n"
    "buffer_load_dwordx4 v248, s[44:47], s84 offen lds\n"
    "s_lshr_b32 s7, s7, 4\n"
    "s_lshl_b32 s7, 0x100000, s7\n"
    "s_or_b32 s30, s30, s7\n"
    "buffer_load_dwordx4 v249, s[44:47], s84 offen offset:1024 lds\n"
    "s_waitcnt vmcnt(18)\n"
    "ds_read_b128 v[252:255], v13\n"
    "s_waitcnt vmcnt(14)\n"
    "ds_read_b64_tr_b16 a[116:117], v11\n"
    "ds_read_b64_tr_b16 a[118:119], v11 offset:512\n"
    "ds_read_b64_tr_b16 a[120:121], v11 offset:8\n"
    "ds_read_b64_tr_b16 a[122:123], v11 offset:520\n"
    "ds_read_b64_tr_b16 a[124:125], v12\n"
    "ds_read_b64_tr_b16 a[126:127], v12 offset:512\n"
    "s_waitcnt lgkmcnt(6)\n"
    "v_and_b32 v36, v14, v252\n"
    "v_and_b32 v37, v15, v253\n"
    "v_and_b32 v40, v14, v254\n"
    "v_and_b32 v41, v15, v255\n"
    "ds_read_b128 v[252:255], v13 offset:64\n"
    "s_waitcnt lgkmcnt(5)\n"
    "ds_read_b64_tr_b16 a[128:129], v12 offset:8\n"
    "v_mfma_f32_4x4x4_16b_bf16 v[20:23], v[36:37], a[116:117], 0\n"
    "ds_read_b64_tr_b16 a[130:131], v12 offset:520\n"
    "s_add_u32 m0, s83, 79872\n"
    "s_add_u32 s84, s61, s62\n"
    "s_mov_b32 s57, 0x2d451\n"
    "v_mfma_f32_4x4x4_16b_bf16 v[24:27], v[40:41], a[118:119], 0\n"
    "buffer_load_dwordx4 v244, s[44:47], s84 offen lds\n"
    "s_mov_b32 s15, 0\n"
    "s_mov_b32 s65, 147456\n"
    "s_waitcnt vmcnt(13) lgkmcnt(5)\n"
    "ds_read_b64_tr_b16 a[132:133], v16 offset:4096\n"
    "v_mfma_f32_4x4x4_16b_bf16 v[28:31], v[36:37], a[120:121], 0\n"
    "ds_read_b64_tr_b16 a[134:135], v16 offset:5120\n"
    "v_cvt_pk_bf16_f32 v56, v20, v21\n"
    "v_cvt_pk_bf16_f32 v57, v22, v23\n"
    "v_mfma_f32_4x4x4_16b_bf16 v[32:35], v[40:41], a[122:123], 0\n"
    "buffer_load_dwordx4 v245, s[44:47], s84 offen offset:1024 lds\n"
    "v_cvt_pk_bf16_f32 v58, v24, v25\n"
    "v_cvt_pk_bf16_f32 v59, v26, v27\n"
    "s_mov_b32 s28, 0\n"
    "s_mov_b32 s94, 92\n"
    "s_waitcnt lgkmcnt(4)\n"
    "ds_read_b64_tr_b16 a[136:137], v16 offset:4104\n"
    "v_mfma_f32_4x4x4_16b_bf16 v[20:23], v[36:37], a[124:125], 0\n"
    "ds_read_b64_tr_b16 a[138:139], v16 offset:5128\n"
    "v_cvt_pk_bf16_f32 v60, v28, v29\n"
    "v_cvt_pk_bf16_f32 v61, v30, v31\n"
    "v_mfma_f32_4x4x4_16b_bf16 v[24:27], v[40:41], a[126:127], 0\n"
    "buffer_load_dwordx4 v246, s[44:47], s84 offen offset:2048 lds\n"
    "v_cvt_pk_bf16_f32 v62, v32, v33\n"
    "v_cvt_pk_bf16_f32 v63, v34, v35\n"
    "v_and_b32 v42, v14, v252\n"
    "s_mov_b32 s31, 110\n"
    "s_mov_b32 s35, 12288\n"
    "s_waitcnt vmcnt(11) lgkmcnt(4)\n"
    "ds_read_b64_tr_b16 a[116:117], v11 offset:6144\n"
    "v_mfma_f32_4x4x4_16b_bf16 v[28:31], v[36:37], a[128:129], 0\n"
    "ds_read_b64_tr_b16 a[118:119], v11 offset:6656\n"
    "v_cvt_pk_bf16_f32 v64, v20, v21\n"
    "v_cvt_pk_bf16_f32 v65, v22, v23\n"
    "v_mfma_f32_4x4x4_16b_bf16 v[32:35], v[40:41], a[130:131], 0\n"
    "buffer_load_dwordx4 v247, s[44:47], s84 offen offset:3072 lds\n"
    "v_cvt_pk_bf16_f32 v66, v24, v25\n"
    "v_cvt_pk_bf16_f32 v67, v26, v27\n"
    "v_and_b32 v43, v15, v253\n"
    "s_bitcmp1_b32 s2, 7\n"
    "s_cselect_b32 s94, 90, s94\n"
    "s_cselect_b32 s31, 85, s31\n"
    "s_cselect_b32 s33, 36864, 0\n"
    "s_add_u32 s33, s33, s55\n"
    "s_lshr_b32 s7, s2, 6\n"
    "s_and_b32 s7, s7, 2\n"
    "s_bitcmp1_b32 s2, 0\n"
    "s_addc_u32 s7, s7, 0\n"
    "s_lshl3_add_u32 s7, s7, 0x80000\n"
    "s_bfe_u32 s94, 0x52525555, s7\n"
    "s_bfe_u32 s31, 0x55556e6e, s7\n"
    "s_waitcnt lgkmcnt(4)\n"
    "ds_read_b64_tr_b16 a[120:121], v11 offset:6152\n"
    "v_mfma_f32_4x4x4_16b_bf16 v[20:23], v[36:37], a[132:133], 0\n"
    "ds_read_b64_tr_b16 a[122:123], v11 offset:6664\n"
    "s_add_u32 m0, s83, 83968\n"
    "v_cvt_pk_bf16_f32 v68, v28, v29\n"
    "v_cvt_pk_bf16_f32 v69, v30, v31\n"
    "v_mfma_f32_4x4x4_16b_bf16 v[24:27], v[40:41], a[134:135], 0\n"
    "buffer_load_dwordx4 v248, s[44:47], s84 offen lds\n"
    "v_cvt_pk_bf16_f32 v70, v32, v33\n"
    "v_cvt_pk_bf16_f32 v71, v34, v35\n"
    "v_and_b32 v46, v14, v254\n"
    "s_cmp_eq_u32 s4, 0\n"
    "s_cselect_b32 s29, -6, 0x80000000\n"
    "s_mov_b32 s70, 0x7fffffff\n"
    "s_waitcnt lgkmcnt(4)\n"
    "ds_read_b64_tr_b16 a[124:125], v12 offset:6144\n"
    "v_mfma_f32_4x4x4_16b_bf16 v[28:31], v[36:37], a[136:137], 0\n"
    "ds_read_b64_tr_b16 a[126:127], v12 offset:6656\n"
    "v_cvt_pk_bf16_f32 v72, v20, v21\n"
    "v_cvt_pk_bf16_f32 v73, v22, v23\n"
    "v_mfma_f32_4x4x4_16b_bf16 v[32:35], v[40:41], a[138:139], 0\n"
    "buffer_load_dwordx4 v249, s[44:47], s84 offen offset:1024 lds\n"
    "v_cvt_pk_bf16_f32 v74, v24, v25\n"
    "v_cvt_pk_bf16_f32 v75, v26, v27\n"
    "v_and_b32 v47, v15, v255\n"
    "ds_read_b128 v[252:255], v13 offset:128\n"
    "s_mov_b32 s76, 0\n"
    "s_mov_b32 s77, 0\n"
    "s_waitcnt lgkmcnt(5)\n"
    "ds_read_b64_tr_b16 a[128:129], v12 offset:6152\n"
    "v_mfma_f32_4x4x4_16b_bf16 v[20:23], v[42:43], a[116:117], 0\n"
    "ds_read_b64_tr_b16 a[130:131], v12 offset:6664\n"
    "s_add_u32 m0, s83, 61440\n"
    "s_add_u32 s84, s58, s63\n"
    "v_cvt_pk_bf16_f32 v76, v28, v29\n"
    "v_cvt_pk_bf16_f32 v77, v30, v31\n"
    "v_mfma_f32_4x4x4_16b_bf16 v[24:27], v[46:47], a[118:119], 0\n"
    "buffer_load_dwordx4 v244, s[44:47], s84 offen lds\n"
    "v_cvt_pk_bf16_f32 v78, v32, v33\n"
    "v_cvt_pk_bf16_f32 v79, v34, v35\n"
    "s_mov_b32 s78, 147456\n"
    "s_add_u32 s80, s55, 159744\n"
    "s_waitcnt vmcnt(13) lgkmcnt(5)\n"
    "ds_read_b64_tr_b16 a[132:133], v16 offset:10240\n"
    "v_mfma_f32_4x4x4_16b_bf16 v[28:31], v[42:43], a[120:121], 0\n"
    "ds_read_b64_tr_b16 a[134:135], v16 offset:11264\n"
    "v_cvt_pk_bf16_f32 v80, v20, v21\n"
    "v_cvt_pk_bf16_f32 v81, v22, v23\n"
    "v_mfma_f32_4x4x4_16b_bf16 v[32:35], v[46:47], a[122:123], 0\n"
    "buffer_load_dwordx4 v245, s[44:47], s84 offen offset:1024 lds\n"
    "v_cvt_pk_bf16_f32 v82, v24, v25\n"
    "v_cvt_pk_bf16_f32 v83, v26, v27\n"
    "s_mul_i32 s85, s53, 2\n"
    "s_sub_u32 s82, s54, s85\n"
    "s_waitcnt lgkmcnt(4)\n"
    "ds_read_b64_tr_b16 a[136:137], v16 offset:10248\n"
    "v_mfma_f32_4x4x4_16b_bf16 v[20:23], v[42:43], a[124:125], 0\n"
    "ds_read_b64_tr_b16 a[138:139], v16 offset:11272\n"
    "v_cvt_pk_bf16_f32 v84, v28, v29\n"
    "v_cvt_pk_bf16_f32 v85, v30, v31\n"
    "v_mfma_f32_4x4x4_16b_bf16 v[24:27], v[46:47], a[126:127], 0\n"
    "buffer_load_dwordx4 v246, s[44:47], s84 offen offset:2048 lds\n"
    "v_cvt_pk_bf16_f32 v86, v32, v33\n"
    "v_cvt_pk_bf16_f32 v87, v34, v35\n"
    "v_and_b32 v36, v14, v252\n"
    "s_mov_b32 s74, s82\n"
    "s_sub_i32 s56, s52, 1\n"
    "s_waitcnt vmcnt(11) lgkmcnt(4)\n"
    "ds_read_b64_tr_b16 a[116:117], v11 offset:12288\n"
    "v_mfma_f32_4x4x4_16b_bf16 v[28:31], v[42:43], a[128:129], 0\n"
    "ds_read_b64_tr_b16 a[118:119], v11 offset:12800\n"
    "v_cvt_pk_bf16_f32 v88, v20, v21\n"
    "v_cvt_pk_bf16_f32 v89, v22, v23\n"
    "v_mfma_f32_4x4x4_16b_bf16 v[32:35], v[46:47], a[130:131], 0\n"
    "buffer_load_dwordx4 v247, s[44:47], s84 offen offset:3072 lds\n"
    "v_cvt_pk_bf16_f32 v90, v24, v25\n"
    "v_cvt_pk_bf16_f32 v91, v26, v27\n"
    "v_and_b32 v37, v15, v253\n"
    "s_sub_i32 vcc_lo, s52, 3\n"
    "s_cmp_lt_u32 s52, 8\n"
    "s_cselect_b32 s56, 0x7fffffff, s56\n"
    "s_cselect_b32 s57, 0, s57\n"
    "s_waitcnt lgkmcnt(4)\n"
    "ds_read_b64_tr_b16 a[120:121], v11 offset:12296\n"
    "v_mfma_f32_4x4x4_16b_bf16 v[20:23], v[42:43], a[132:133], 0\n"
    "ds_read_b64_tr_b16 a[122:123], v11 offset:12808\n"
    "s_add_u32 m0, s83, 65536\n"
    "v_cvt_pk_bf16_f32 v92, v28, v29\n"
    "v_cvt_pk_bf16_f32 v93, v30, v31\n"
    "v_mfma_f32_4x4x4_16b_bf16 v[24:27], v[46:47], a[134:135], 0\n"
    "buffer_load_dwordx4 v248, s[44:47], s84 offen lds\n"
    "v_cvt_pk_bf16_f32 v94, v32, v33\n"
    "v_cvt_pk_bf16_f32 v95, v34, v35\n"
    "v_and_b32 v40, v14, v254\n"
    "s_sub_i32 s7, s52, 3\n"
    "s_max_i32 s7, s7, 0\n"
    "s_waitcnt lgkmcnt(4)\n"
    "ds_read_b64_tr_b16 a[124:125], v12 offset:12288\n"
    "v_mfma_f32_4x4x4_16b_bf16 v[28:31], v[42:43], a[136:137], 0\n"
    "ds_read_b64_tr_b16 a[126:127], v12 offset:12800\n"
    "v_cvt_pk_bf16_f32 v96, v20, v21\n"
    "v_cvt_pk_bf16_f32 v97, v22, v23\n"
    "v_mfma_f32_4x4x4_16b_bf16 v[32:35], v[46:47], a[138:139], 0\n"
    "buffer_load_dwordx4 v249, s[44:47], s84 offen offset:1024 lds\n"
    "v_cvt_pk_bf16_f32 v98, v24, v25\n"
    "v_cvt_pk_bf16_f32 v99, v26, v27\n"
    "v_and_b32 v41, v15, v255\n"
    "ds_read_b128 v[252:255], v13 offset:192\n"
    "s_bfm_b64 s[68:69], s7, 0\n"
    "s_cmp_gt_u32 s7, 63\n"
    "s_cselect_b64 s[68:69], -1, s[68:69]\n"
    "s_waitcnt lgkmcnt(5)\n"
    "ds_read_b64_tr_b16 a[128:129], v12 offset:12296\n"
    "v_mfma_f32_4x4x4_16b_bf16 v[20:23], v[36:37], a[116:117], 0\n"
    "ds_read_b64_tr_b16 a[130:131], v12 offset:12808\n"
    "s_add_u32 m0, s83, 67584\n"
    "s_add_u32 s84, s59, s63\n"
    "v_cvt_pk_bf16_f32 v100, v28, v29\n"
    "v_cvt_pk_bf16_f32 v101, v30, v31\n"
    "v_mfma_f32_4x4x4_16b_bf16 v[24:27], v[40:41], a[118:119], 0\n"
    "buffer_load_dwordx4 v244, s[44:47], s84 offen lds\n"
    "v_cvt_pk_bf16_f32 v102, v32, v33\n"
    "v_cvt_pk_bf16_f32 v103, v34, v35\n"
    "s_andn2_b32 s68, s68, s30\n"
    "s_bitcmp1_b32 s2, 7\n"
    "s_mov_b32 s95, s53\n"
    "s_cselect_b32 s8, 3, 0\n"
    "s_cselect_b32 s7, 5, 5\n"
    "s_mul_i32 s8, s8, s53\n"
    "s_add_u32 s34, s54, s8\n"
    "s_mul_i32 s8, s7, s53\n"
    "s_add_u32 s81, s54, s8\n"
    "s_mul_i32 s8, s7, 12288\n"
    "s_add_u32 s79, s55, s8\n"
    "s_waitcnt vmcnt(13) lgkmcnt(5)\n"
    "ds_read_b64_tr_b16 a[132:133], v16 offset:16384\n"
    "v_mfma_f32_4x4x4_16b_bf16 v[28:31], v[36:37], a[120:121], 0\n"
    "ds_read_b64_tr_b16 a[134:135], v16 offset:17408\n"
    "v_cvt_pk_bf16_f32 v104, v20, v21\n"
    "v_cvt_pk_bf16_f32 v105, v22, v23\n"
    "v_mfma_f32_4x4x4_16b_bf16 v[32:35], v[40:41], a[122:123], 0\n"
    "buffer_load_dwordx4 v245, s[44:47], s84 offen offset:1024 lds\n"
    "v_cvt_pk_bf16_f32 v106, v24, v25\n"
    "v_cvt_pk_bf16_f32 v107, v26, v27\n"
    "s_waitcnt lgkmcnt(4)\n"
    "ds_read_b64_tr_b16 a[136:137], v16 offset:16392\n"
    "v_mfma_f32_4x4x4_16b_bf16 v[20:23], v[36:37], a[124:125], 0\n"
    "ds_read_b64_tr_b16 a[138:139], v16 offset:17416\n"
    "v_cvt_pk_bf16_f32 v108, v28, v29\n"
    "v_cvt_pk_bf16_f32 v109, v30, v31\n"
    "v_mfma_f32_4x4x4_16b_bf16 v[24:27], v[40:41], a[126:127], 0\n"
    "buffer_load_dwordx4 v246, s[44:47], s84 offen offset:2048 lds\n"
    "v_cvt_pk_bf16_f32 v110, v32, v33\n"
    "v_cvt_pk_bf16_f32 v111, v34, v35\n"
    "v_and_b32 v42, v14, v252\n"
    "s_waitcnt vmcnt(11) lgkmcnt(4)\n"
    "ds_read_b64_tr_b16 a[116:117], v11 offset:18432\n"
    "v_mfma_f32_4x4x4_16b_bf16 v[28:31], v[36:37], a[128:129], 0\n"
    "ds_read_b64_tr_b16 a[118:119], v11 offset:18944\n"
    "v_cvt_pk_bf16_f32 v112, v20, v21\n"
    "v_cvt_pk_bf16_f32 v113, v22, v23\n"
    "v_mfma_f32_4x4x4_16b_bf16 v[32:35], v[40:41], a[130:131], 0\n"
    "buffer_load_dwordx4 v247, s[44:47], s84 offen offset:3072 lds\n"
    "v_cvt_pk_bf16_f32 v114, v24, v25\n"
    "v_cvt_pk_bf16_f32 v115, v26, v27\n"
    "v_and_b32 v43, v15, v253\n"
    "s_waitcnt lgkmcnt(4)\n"
    "ds_read_b64_tr_b16 a[120:121], v11 offset:18440\n"
    "v_mfma_f32_4x4x4_16b_bf16 v[20:23], v[36:37], a[132:133], 0\n"
    "ds_read_b64_tr_b16 a[122:123], v11 offset:18952\n"
    "s_add_u32 m0, s83, 71680\n"
    "v_cvt_pk_bf16_f32 v116, v28, v29\n"
    "v_cvt_pk_bf16_f32 v117, v30, v31\n"
    "v_mfma_f32_4x4x4_16b_bf16 v[24:27], v[40:41], a[134:135], 0\n"
    "buffer_load_dwordx4 v248, s[44:47], s84 offen lds\n"
    "v_cvt_pk_bf16_f32 v118, v32, v33\n"
    "v_cvt_pk_bf16_f32 v119, v34, v35\n"
    "v_and_b32 v46, v14, v254\n"
    "s_waitcnt lgkmcnt(4)\n"
    "ds_read_b64_tr_b16 a[124:125], v12 offset:18432\n"
    "v_mfma_f32_4x4x4_16b_bf16 v[28:31], v[36:37], a[136:137], 0\n"
    "ds_read_b64_tr_b16 a[126:127], v12 offset:18944\n"
    "v_cvt_pk_bf16_f32 v120, v20, v21\n"
    "v_cvt_pk_bf16_f32 v121, v22, v23\n"
    "v_mfma_f32_4x4x4_16b_bf16 v[32:35], v[40:41], a[138:139], 0\n"
    "buffer_load_dwordx4 v249, s[44:47], s84 offen offset:1024 lds\n"
    "v_cvt_pk_bf16_f32 v122, v24, v25\n"
    "v_cvt_pk_bf16_f32 v123, v26, v27\n"
    "v_and_b32 v47, v15, v255\n"
    "ds_read_b128 v[252:255], v13 offset:256\n"
    "s_waitcnt lgkmcnt(5)\n"
    "ds_read_b64_tr_b16 a[128:129], v12 offset:18440\n"
    "v_mfma_f32_4x4x4_16b_bf16 v[20:23], v[42:43], a[116:117], 0\n"
    "ds_read_b64_tr_b16 a[130:131], v12 offset:18952\n"
    "s_add_u32 m0, s83, 73728\n"
    "s_add_u32 s84, s60, s63\n"
    "v_cvt_pk_bf16_f32 v124, v28, v29\n"
    "v_cvt_pk_bf16_f32 v125, v30, v31\n"
    "v_mfma_f32_4x4x4_16b_bf16 v[24:27], v[46:47], a[118:119], 0\n"
    "buffer_load_dwordx4 v244, s[44:47], s84 offen lds\n"
    "v_cvt_pk_bf16_f32 v126, v32, v33\n"
    "v_cvt_pk_bf16_f32 v127, v34, v35\n"
    "s_waitcnt vmcnt(13) lgkmcnt(5)\n"
    "ds_read_b64_tr_b16 a[132:133], v16 offset:22528\n"
    "v_mfma_f32_4x4x4_16b_bf16 v[28:31], v[42:43], a[120:121], 0\n"
    "ds_read_b64_tr_b16 a[134:135], v16 offset:23552\n"
    "v_cvt_pk_bf16_f32 v128, v20, v21\n"
    "v_cvt_pk_bf16_f32 v129, v22, v23\n"
    "v_mfma_f32_4x4x4_16b_bf16 v[32:35], v[46:47], a[122:123], 0\n"
    "buffer_load_dwordx4 v245, s[44:47], s84 offen offset:1024 lds\n"
    "v_cvt_pk_bf16_f32 v130, v24, v25\n"
    "v_cvt_pk_bf16_f32 v131, v26, v27\n"
    "s_waitcnt lgkmcnt(4)\n"
    "ds_read_b64_tr_b16 a[136:137], v16 offset:22536\n"
    "v_mfma_f32_4x4x4_16b_bf16 v[20:23], v[42:43], a[124:125], 0\n"
    "ds_read_b64_tr_b16 a[138:139], v16 offset:23560\n"
    "v_cvt_pk_bf16_f32 v132, v28, v29\n"
    "v_cvt_pk_bf16_f32 v133, v30, v31\n"
    "v_mfma_f32_4x4x4_16b_bf16 v[24:27], v[46:47], a[126:127], 0\n"
    "buffer_load_dwordx4 v246, s[44:47], s84 offen offset:2048 lds\n"
    "v_cvt_pk_bf16_f32 v134, v32, v33\n"
    "v_cvt_pk_bf16_f32 v135, v34, v35\n"
    "v_and_b32 v36, v14, v252\n"
    "s_waitcnt vmcnt(11) lgkmcnt(4)\n"
    "ds_read_b64_tr_b16 a[116:117], v11\n"
    "v_mfma_f32_4x4x4_16b_bf16 v[28:31], v[42:43], a[128:129], 0\n"
    "ds_read_b64_tr_b16 a[118:119], v11 offset:512\n"
    "v_cvt_pk_bf16_f32 v136, v20, v21\n"
    "v_cvt_pk_bf16_f32 v137, v22, v23\n"
    "v_mfma_f32_4x4x4_16b_bf16 v[32:35], v[46:47], a[130:131], 0\n"
    "buffer_load_dwordx4 v247, s[44:47], s84 offen offset:3072 lds\n"
    "v_cvt_pk_bf16_f32 v138, v24, v25\n"
    "v_cvt_pk_bf16_f32 v139, v26, v27\n"
    "v_and_b32 v37, v15, v253\n"
    "s_waitcnt lgkmcnt(4)\n"
    "ds_read_b64_tr_b16 a[120:121], v11 offset:8\n"
    "v_mfma_f32_4x4x4_16b_bf16 v[20:23], v[42:43], a[132:133], 0\n"
    "ds_read_b64_tr_b16 a[122:123], v11 offset:520\n"
    "s_add_u32 m0, s83, 77824\n"
    "v_cvt_pk_bf16_f32 v140, v28, v29\n"
    "v_cvt_pk_bf16_f32 v141, v30, v31\n"
    "v_mfma_f32_4x4x4_16b_bf16 v[24:27], v[46:47], a[134:135], 0\n"
    "buffer_load_dwordx4 v248, s[44:47], s84 offen lds\n"
    "v_cvt_pk_bf16_f32 v142, v32, v33\n"
    "v_cvt_pk_bf16_f32 v143, v34, v35\n"
    "v_and_b32 v40, v14, v254\n"
    "s_waitcnt lgkmcnt(4)\n"
    "ds_read_b64_tr_b16 a[124:125], v12\n"
    "v_mfma_f32_4x4x4_16b_bf16 v[28:31], v[42:43], a[136:137], 0\n"
    "ds_read_b64_tr_b16 a[126:127], v12 offset:512\n"
    "v_cvt_pk_bf16_f32 v144, v20, v21\n"
    "v_cvt_pk_bf16_f32 v145, v22, v23\n"
    "v_mfma_f32_4x4x4_16b_bf16 v[32:35], v[46:47], a[138:139], 0\n"
    "buffer_load_dwordx4 v249, s[44:47], s84 offen offset:1024 lds\n"
    "v_cvt_pk_bf16_f32 v146, v24, v25\n"
    "v_cvt_pk_bf16_f32 v147, v26, v27\n"
    "v_and_b32 v41, v15, v255\n"
    "ds_read_b128 v[252:255], v13 offset:320\n"
    "s_waitcnt lgkmcnt(5)\n"
    "ds_read_b64_tr_b16 a[128:129], v12 offset:8\n"
    "v_mfma_f32_4x4x4_16b_bf16 v[20:23], v[36:37], a[116:117], 0\n"
    "ds_read_b64_tr_b16 a[130:131], v12 offset:520\n"
    "s_add_u32 m0, s83, 79872\n"
    "s_add_u32 s84, s61, s63\n"
    "v_cvt_pk_bf16_f32 v148, v28, v29\n"
    "v_cvt_pk_bf16_f32 v149, v30, v31\n"
    "v_mfma_f32_4x4x4_16b_bf16 v[24:27], v[40:41], a[118:119], 0\n"
    "buffer_load_dwordx4 v244, s[44:47], s84 offen lds\n"
    "v_cvt_pk_bf16_f32 v150, v32, v33\n"
    "v_cvt_pk_bf16_f32 v151, v34, v35\n"
    "s_waitcnt vmcnt(13) lgkmcnt(5)\n"
    "ds_read_b64_tr_b16 a[132:133], v16 offset:4096\n"
    "v_mfma_f32_4x4x4_16b_bf16 v[28:31], v[36:37], a[120:121], 0\n"
    "ds_read_b64_tr_b16 a[134:135], v16 offset:5120\n"
    "v_cvt_pk_bf16_f32 v152, v20, v21\n"
    "v_cvt_pk_bf16_f32 v153, v22, v23\n"
    "v_mfma_f32_4x4x4_16b_bf16 v[32:35], v[40:41], a[122:123], 0\n"
    "buffer_load_dwordx4 v245, s[44:47], s84 offen offset:1024 lds\n"
    "v_cvt_pk_bf16_f32 v154, v24, v25\n"
    "v_cvt_pk_bf16_f32 v155, v26, v27\n"
    "s_waitcnt lgkmcnt(4)\n"
    "ds_read_b64_tr_b16 a[136:137], v16 offset:4104\n"
    "v_mfma_f32_4x4x4_16b_bf16 v[20:23], v[36:37], a[124:125], 0\n"
    "ds_read_b64_tr_b16 a[138:139], v16 offset:5128\n"
    "v_cvt_pk_bf16_f32 v156, v28, v29\n"
    "v_cvt_pk_bf16_f32 v157, v30, v31\n"
    "v_mfma_f32_4x4x4_16b_bf16 v[24:27], v[40:41], a[126:127], 0\n"
    "buffer_load_dwordx4 v246, s[44:47], s84 offen offset:2048 lds\n"
    "v_cvt_pk_bf16_f32 v158, v32, v33\n"
    "v_cvt_pk_bf16_f32 v159, v34, v35\n"
    "v_and_b32 v42, v14, v252\n"
    "s_waitcnt vmcnt(11) lgkmcnt(4)\n"
    "ds_read_b64_tr_b16 a[116:117], v11 offset:6144\n"
    "v_mfma_f32_4x4x4_16b_bf16 v[28:31], v[36:37], a[128:129], 0\n"
    "ds_read_b64_tr_b16 a[118:119], v11 offset:6656\n"
    "v_cvt_pk_bf16_f32 v160, v20, v21\n"
    "v_cvt_pk_bf16_f32 v161, v22, v23\n"
    "v_mfma_f32_4x4x4_16b_bf16 v[32:35], v[40:41], a[130:131], 0\n"
    "buffer_load_dwordx4 v247, s[44:47], s84 offen offset:3072 lds\n"
    "v_cvt_pk_bf16_f32 v162, v24, v25\n"
    "v_cvt_pk_bf16_f32 v163, v26, v27\n"
    "v_and_b32 v43, v15, v253\n"
    "s_waitcnt lgkmcnt(4)\n"
    "ds_read_b64_tr_b16 a[120:121], v11 offset:6152\n"
    "v_mfma_f32_4x4x4_16b_bf16 v[20:23], v[36:37], a[132:133], 0\n"
    "ds_read_b64_tr_b16 a[122:123], v11 offset:6664\n"
    "s_add_u32 m0, s83, 83968\n"
    "v_cvt_pk_bf16_f32 v164, v28, v29\n"
    "v_cvt_pk_bf16_f32 v165, v30, v31\n"
    "v_mfma_f32_4x4x4_16b_bf16 v[24:27], v[40:41], a[134:135], 0\n"
    "buffer_load_dwordx4 v248, s[44:47], s84 offen lds\n"
    "v_cvt_pk_bf16_f32 v166, v32, v33\n"
    "v_cvt_pk_bf16_f32 v167, v34, v35\n"
    "v_and_b32 v46, v14, v254\n"
    "s_waitcnt lgkmcnt(4)\n"
    "ds_read_b64_tr_b16 a[124:125], v12 offset:6144\n"
    "v_mfma_f32_4x4x4_16b_bf16 v[28:31], v[36:37], a[136:137], 0\n"
    "ds_read_b64_tr_b16 a[126:127], v12 offset:6656\n"
    "v_cvt_pk_bf16_f32 v168, v20, v21\n"
    "v_cvt_pk_bf16_f32 v169, v22, v23\n"
    "v_mfma_f32_4x4x4_16b_bf16 v[32:35], v[40:41], a[138:139], 0\n"
    "buffer_load_dwordx4 v249, s[44:47], s84 offen offset:1024 lds\n"
    "v_cvt_pk_bf16_f32 v170, v24, v25\n"
    "v_cvt_pk_bf16_f32 v171, v26, v27\n"
    "v_and_b32 v47, v15, v255\n"
    "ds_read_b128 v[252:255], v13 offset:384\n"
    "s_waitcnt lgkmcnt(5)\n"
    "ds_read_b64_tr_b16 a[128:129], v12 offset:6152\n"
    "v_mfma_f32_4x4x4_16b_bf16 v[20:23], v[42:43], a[116:117], 0\n"
    "ds_read_b64_tr_b16 a[130:131], v12 offset:6664\n"
    "s_add_u32 m0, s83, 61440\n"
    "s_add_u32 s84, s58, s64\n"
    "v_cvt_pk_bf16_f32 v172, v28, v29\n"
    "v_cvt_pk_bf16_f32 v173, v30, v31\n"
    "v_mfma_f32_4x4x4_16b_bf16 v[24:27], v[46:47], a[118:119], 0\n"
    "buffer_load_dwordx4 v244, s[44:47], s84 offen lds\n"
    "v_cvt_pk_bf16_f32 v174, v32, v33\n"
    "v_cvt_pk_bf16_f32 v175, v34, v35\n"
    "s_waitcnt vmcnt(13) lgkmcnt(5)\n"
    "ds_read_b64_tr_b16 a[132:133], v16 offset:10240\n"
    "v_mfma_f32_4x4x4_16b_bf16 v[28:31], v[42:43], a[120:121], 0\n"
    "ds_read_b64_tr_b16 a[134:135], v16 offset:11264\n"
    "v_cvt_pk_bf16_f32 v176, v20, v21\n"
    "v_cvt_pk_bf16_f32 v177, v22, v23\n"
    "v_mfma_f32_4x4x4_16b_bf16 v[32:35], v[46:47], a[122:123], 0\n"
    "buffer_load_dwordx4 v245, s[44:47], s84 offen offset:1024 lds\n"
    "v_cvt_pk_bf16_f32 v178, v24, v25\n"
    "v_cvt_pk_bf16_f32 v179, v26, v27\n"
    "s_waitcnt lgkmcnt(4)\n"
    "ds_read_b64_tr_b16 a[136:137], v16 offset:10248\n"
    "v_mfma_f32_4x4x4_16b_bf16 v[20:23], v[42:43], a[124:125], 0\n"
    "ds_read_b64_tr_b16 a[138:139], v16 offset:11272\n"
    "v_cvt_pk_bf16_f32 v180, v28, v29\n"
    "v_cvt_pk_bf16_f32 v181, v30, v31\n"
    "v_mfma_f32_4x4x4_16b_bf16 v[24:27], v[46:47], a[126:127], 0\n"
    "buffer_load_dwordx4 v246, s[44:47], s84 offen offset:2048 lds\n"
    "v_cvt_pk_bf16_f32 v182, v32, v33\n"
    "v_cvt_pk_bf16_f32 v183, v34, v35\n"
    "v_and_b32 v36, v14, v252\n"
    "s_waitcnt vmcnt(11) lgkmcnt(4)\n"
    "ds_read_b64_tr_b16 a[116:117], v11 offset:12288\n"
    "v_mfma_f32_4x4x4_16b_bf16 v[28:31], v[42:43], a[128:129], 0\n"
    "ds_read_b64_tr_b16 a[118:119], v11 offset:12800\n"
    "v_cvt_pk_bf16_f32 v184, v20, v21\n"
    "v_cvt_pk_bf16_f32 v185, v22, v23\n"
    "v_mfma_f32_4x4x4_16b_bf16 v[32:35], v[46:47], a[130:131], 0\n"
    "buffer_load_dwordx4 v247, s[44:47], s84 offen offset:3072 lds\n"
    "v_cvt_pk_bf16_f32 v186, v24, v25\n"
    "v_cvt_pk_bf16_f32 v187, v26, v27\n"
    "v_and_b32 v37, v15, v253\n"
    "s_waitcnt lgkmcnt(4)\n"
    "ds_read_b64_tr_b16 a[120:121], v11 offset:12296\n"
    "v_mfma_f32_4x4x4_16b_bf16 v[20:23], v[42:43], a[132:133], 0\n"
    "ds_read_b64_tr_b16 a[122:123], v11 offset:12808\n"
    "s_add_u32 m0, s83, 65536\n"
    "v_cvt_pk_bf16_f32 v188, v28, v29\n"
    "v_cvt_pk_bf16_f32 v189, v30, v31\n"
    "v_mfma_f32_4x4x4_16b_bf16 v[24:27], v[46:47], a[134:135], 0\n"
    "buffer_load_dwordx4 v248, s[44:47], s84 offen lds\n"
    "v_cvt_pk_bf16_f32 v190, v32, v33\n"
    "v_cvt_pk_bf16_f32 v191, v34, v35\n"
    "v_and_b32 v40, v14, v254\n"
    "s_waitcnt lgkmcnt(4)\n"
    "ds_read_b64_tr_b16 a[124:125], v12 offset:12288\n"
    "v_mfma_f32_4x4x4_16b_bf16 v[28:31], v[42:43], a[136:137], 0\n"
    "ds_read_b64_tr_b16 a[126:127], v12 offset:12800\n"
    "v_cvt_pk_bf16_f32 v192, v20, v21\n"
    "v_cvt_pk_bf16_f32 v193, v22, v23\n"
    "v_mfma_f32_4x4x4_16b_bf16 v[32:35], v[46:47], a[138:139], 0\n"
    "buffer_load_dwordx4 v249, s[44:47], s84 offen offset:1024 lds\n"
    "v_cvt_pk_bf16_f32 v194, v24, v25\n"
    "v_cvt_pk_bf16_f32 v195, v26, v27\n"
    "v_and_b32 v41, v15, v255\n"
    "ds_read_b128 v[252:255], v13 offset:448\n"
    "s_waitcnt lgkmcnt(5)\n"
    "ds_read_b64_tr_b16 a[128:129], v12 offset:12296\n"
    "v_mfma_f32_4x4x4_16b_bf16 v[20:23], v[36:37], a[116:117], 0\n"
    "ds_read_b64_tr_b16 a[130:131], v12 offset:12808\n"
    "s_add_u32 m0, s83, 67584\n"
    "s_add_u32 s84, s59, s64\n"
    "v_cvt_pk_bf16_f32 v196, v28, v29\n"
    "v_cvt_pk_bf16_f32 v197, v30, v31\n"
    "v_mfma_f32_4x4x4_16b_bf16 v[24:27], v[40:41], a[118:119], 0\n"
    "buffer_load_dwordx4 v244, s[44:47], s84 offen lds\n"
    "v_cvt_pk_bf16_f32 v198, v32, v33\n"
    "v_cvt_pk_bf16_f32 v199, v34, v35\n"
    "s_waitcnt vmcnt(13) lgkmcnt(5)\n"
    "ds_read_b64_tr_b16 a[132:133], v16 offset:16384\n"
    "v_mfma_f32_4x4x4_16b_bf16 v[28:31], v[36:37], a[120:121], 0\n"
    "ds_read_b64_tr_b16 a[134:135], v16 offset:17408\n"
    "v_cvt_pk_bf16_f32 v200, v20, v21\n"
    "v_cvt_pk_bf16_f32 v201, v22, v23\n"
    "v_mfma_f32_4x4x4_16b_bf16 v[32:35], v[40:41], a[122:123], 0\n"
    "buffer_load_dwordx4 v245, s[44:47], s84 offen offset:1024 lds\n"
    "v_cvt_pk_bf16_f32 v202, v24, v25\n"
    "v_cvt_pk_bf16_f32 v203, v26, v27\n"
    "s_waitcnt lgkmcnt(4)\n"
    "ds_read_b64_tr_b16 a[136:137], v16 offset:16392\n"
    "v_mfma_f32_4x4x4_16b_bf16 v[20:23], v[36:37], a[124:125], 0\n"
    "ds_read_b64_tr_b16 a[138:139], v16 offset:17416\n"
    "v_cvt_pk_bf16_f32 v204, v28, v29\n"
    "v_cvt_pk_bf16_f32 v205, v30, v31\n"
    "v_mfma_f32_4x4x4_16b_bf16 v[24:27], v[40:41], a[126:127], 0\n"
    "buffer_load_dwordx4 v246, s[44:47], s84 offen offset:2048 lds\n"
    "v_cvt_pk_bf16_f32 v206, v32, v33\n"
    "v_cvt_pk_bf16_f32 v207, v34, v35\n"
    "v_and_b32 v42, v14, v252\n"
    "s_waitcnt vmcnt(11) lgkmcnt(4)\n"
    "ds_read_b64_tr_b16 a[116:117], v11 offset:18432\n"
    "v_mfma_f32_4x4x4_16b_bf16 v[28:31], v[36:37], a[128:129], 0\n"
    "ds_read_b64_tr_b16 a[118:119], v11 offset:18944\n"
    "v_cvt_pk_bf16_f32 v208, v20, v21\n"
    "v_cvt_pk_bf16_f32 v209, v22, v23\n"
    "v_mfma_f32_4x4x4_16b_bf16 v[32:35], v[40:41], a[130:131], 0\n"
    "buffer_load_dwordx4 v247, s[44:47], s84 offen offset:3072 lds\n"
    "v_cvt_pk_bf16_f32 v210, v24, v25\n"
    "v_cvt_pk_bf16_f32 v211, v26, v27\n"
    "v_and_b32 v43, v15, v253\n"
    "s_waitcnt lgkmcnt(4)\n"
    "ds_read_b64_tr_b16 a[120:121], v11 offset:18440\n"
    "v_mfma_f32_4x4x4_16b_bf16 v[20:23], v[36:37], a[132:133], 0\n"
    "ds_read_b64_tr_b16 a[122:123], v11 offset:18952\n"
    "s_add_u32 m0, s83, 71680\n"
    "v_cvt_pk_bf16_f32 v212, v28, v29\n"
    "v_cvt_pk_bf16_f32 v213, v30, v31\n"
    "v_mfma_f32_4x4x4_16b_bf16 v[24:27], v[40:41], a[134:135], 0\n"
    "buffer_load_dwordx4 v248, s[44:47], s84 offen lds\n"
    "v_cvt_pk_bf16_f32 v214, v32, v33\n"
    "v_cvt_pk_bf16_f32 v215, v34, v35\n"
    "v_and_b32 v46, v14, v254\n"
    "s_waitcnt lgkmcnt(4)\n"
    "ds_read_b64_tr_b16 a[124:125], v12 offset:18432\n"
    "v_mfma_f32_4x4x4_16b_bf16 v[28:31], v[36:37], a[136:137], 0\n"
    "ds_read_b64_tr_b16 a[126:127], v12 offset:18944\n"
    "v_cvt_pk_bf16_f32 v48, v20, v21\n"
    "v_cvt_pk_bf16_f32 v49, v22, v23\n"
    "v_accvgpr_write_b32 a0, v48\n"
    "v_accvgpr_write_b32 a1, v49\n"
    "v_mfma_f32_4x4x4_16b_bf16 v[32:35], v[40:41], a[138:139], 0\n"
    "buffer_load_dwordx4 v249, s[44:47], s84 offen offset:1024 lds\n"
    "v_cvt_pk_bf16_f32 v50, v24, v25\n"
    "v_cvt_pk_bf16_f32 v51, v26, v27\n"
    "v_accvgpr_write_b32 a2, v50\n"
    "v_accvgpr_write_b32 a3, v51\n"
    "v_and_b32 v47, v15, v255\n"
    "ds_read_b128 v[252:255], v13 offset:512\n"
    "s_waitcnt lgkmcnt(5)\n"
    "ds_read_b64_tr_b16 a[128:129], v12 offset:18440\n"
    "v_mfma_f32_4x4x4_16b_bf16 v[20:23], v[42:43], a[116:117], 0\n"
    "ds_read_b64_tr_b16 a[130:131], v12 offset:18952\n"
    "s_add_u32 m0, s83, 73728\n"
    "s_add_u32 s84, s60, s64\n"
    "v_cvt_pk_bf16_f32 v48, v28, v29\n"
    "v_cvt_pk_bf16_f32 v49, v30, v31\n"
    "v_accvgpr_write_b32 a4, v48\n"
    "v_accvgpr_write_b32 a5, v49\n"
    "v_mfma_f32_4x4x4_16b_bf16 v[24:27], v[46:47], a[118:119], 0\n"
    "buffer_load_dwordx4 v244, s[44:47], s84 offen lds\n"
    "v_cvt_pk_bf16_f32 v50, v32, v33\n"
    "v_cvt_pk_bf16_f32 v51, v34, v35\n"
    "v_accvgpr_write_b32 a6, v50\n"
    "v_accvgpr_write_b32 a7, v51\n"
    "s_waitcnt vmcnt(13) lgkmcnt(5)\n"
    "ds_read_b64_tr_b16 a[132:133], v16 offset:22528\n"
    "v_mfma_f32_4x4x4_16b_bf16 v[28:31], v[42:43], a[120:121], 0\n"
    "ds_read_b64_tr_b16 a[134:135], v16 offset:23552\n"
    "v_cvt_pk_bf16_f32 v48, v20, v21\n"
    "v_cvt_pk_bf16_f32 v49, v22, v23\n"
    "v_accvgpr_write_b32 a8, v48\n"
    "v_accvgpr_write_b32 a9, v49\n"
    "v_mfma_f32_4x4x4_16b_bf16 v[32:35], v[46:47], a[122:123], 0\n"
    "buffer_load_dwordx4 v245, s[44:47], s84 offen offset:1024 lds\n"
    "v_cvt_pk_bf16_f32 v50, v24, v25\n"
    "v_cvt_pk_bf16_f32 v51, v26, v27\n"
    "v_accvgpr_write_b32 a10, v50\n"
    "v_accvgpr_write_b32 a11, v51\n"
    "s_waitcnt lgkmcnt(4)\n"
    "ds_read_b64_tr_b16 a[136:137], v16 offset:22536\n"
    "v_mfma_f32_4x4x4_16b_bf16 v[20:23], v[42:43], a[124:125], 0\n"
    "ds_read_b64_tr_b16 a[138:139], v16 offset:23560\n"
    "v_cvt_pk_bf16_f32 v48, v28, v29\n"
    "v_cvt_pk_bf16_f32 v49, v30, v31\n"
    "v_accvgpr_write_b32 a12, v48\n"
    "v_accvgpr_write_b32 a13, v49\n"
    "v_mfma_f32_4x4x4_16b_bf16 v[24:27], v[46:47], a[126:127], 0\n"
    "buffer_load_dwordx4 v246, s[44:47], s84 offen offset:2048 lds\n"
    "v_cvt_pk_bf16_f32 v50, v32, v33\n"
    "v_cvt_pk_bf16_f32 v51, v34, v35\n"
    "v_accvgpr_write_b32 a14, v50\n"
    "v_accvgpr_write_b32 a15, v51\n"
    "v_and_b32 v36, v14, v252\n"
    "s_waitcnt vmcnt(11) lgkmcnt(4)\n"
    "ds_read_b64_tr_b16 a[116:117], v11\n"
    "v_mfma_f32_4x4x4_16b_bf16 v[28:31], v[42:43], a[128:129], 0\n"
    "ds_read_b64_tr_b16 a[118:119], v11 offset:512\n"
    "v_cvt_pk_bf16_f32 v48, v20, v21\n"
    "v_cvt_pk_bf16_f32 v49, v22, v23\n"
    "v_accvgpr_write_b32 a16, v48\n"
    "v_accvgpr_write_b32 a17, v49\n"
    "v_mfma_f32_4x4x4_16b_bf16 v[32:35], v[46:47], a[130:131], 0\n"
    "buffer_load_dwordx4 v247, s[44:47], s84 offen offset:3072 lds\n"
    "v_cvt_pk_bf16_f32 v50, v24, v25\n"
    "v_cvt_pk_bf16_f32 v51, v26, v27\n"
    "v_accvgpr_write_b32 a18, v50\n"
    "v_accvgpr_write_b32 a19, v51\n"
    "v_and_b32 v37, v15, v253\n"
    "s_waitcnt lgkmcnt(4)\n"
    "ds_read_b64_tr_b16 a[120:121], v11 offset:8\n"
    "v_mfma_f32_4x4x4_16b_bf16 v[20:23], v[42:43], a[132:133], 0\n"
    "ds_read_b64_tr_b16 a[122:123], v11 offset:520\n"
    "s_add_u32 m0, s83, 77824\n"
    "v_cvt_pk_bf16_f32 v48, v28, v29\n"
    "v_cvt_pk_bf16_f32 v49, v30, v31\n"
    "v_accvgpr_write_b32 a20, v48\n"
    "v_accvgpr_write_b32 a21, v49\n"
    "v_mfma_f32_4x4x4_16b_bf16 v[24:27], v[46:47], a[134:135], 0\n"
    "buffer_load_dwordx4 v248, s[44:47], s84 offen lds\n"
    "v_cvt_pk_bf16_f32 v50, v32, v33\n"
    "v_cvt_pk_bf16_f32 v51, v34, v35\n"
    "v_accvgpr_write_b32 a22, v50\n"
    "v_accvgpr_write_b32 a23, v51\n"
    "v_and_b32 v40, v14, v254\n"
    "s_waitcnt lgkmcnt(4)\n"
    "ds_read_b64_tr_b16 a[124:125], v12\n"
    "v_mfma_f32_4x4x4_16b_bf16 v[28:31], v[42:43], a[136:137], 0\n"
    "ds_read_b64_tr_b16 a[126:127], v12 offset:512\n"
    "v_cvt_pk_bf16_f32 v48, v20, v21\n"
    "v_cvt_pk_bf16_f32 v49, v22, v23\n"
    "v_accvgpr_write_b32 a24, v48\n"
    "v_accvgpr_write_b32 a25, v49\n"
    "v_mfma_f32_4x4x4_16b_bf16 v[32:35], v[46:47], a[138:139], 0\n"
    "buffer_load_dwordx4 v249, s[44:47], s84 offen offset:1024 lds\n"
    "v_cvt_pk_bf16_f32 v50, v24, v25\n"
    "v_cvt_pk_bf16_f32 v51, v26, v27\n"
    "v_accvgpr_write_b32 a26, v50\n"
    "v_accvgpr_write_b32 a27, v51\n"
    "v_and_b32 v41, v15, v255\n"
    "ds_read_b128 v[252:255], v13 offset:576\n"
    "s_waitcnt lgkmcnt(5)\n"
    "ds_read_b64_tr_b16 a[128:129], v12 offset:8\n"
    "v_mfma_f32_4x4x4_16b_bf16 v[20:23], v[36:37], a[116:117], 0\n"
    "ds_read_b64_tr_b16 a[130:131], v12 offset:520\n"
    "s_add_u32 m0, s83, 79872\n"
    "s_add_u32 s84, s61, s64\n"
    "v_cvt_pk_bf16_f32 v48, v28, v29\n"
    "v_cvt_pk_bf16_f32 v49, v30, v31\n"
    "v_accvgpr_write_b32 a28, v48\n"
    "v_accvgpr_write_b32 a29, v49\n"
    "v_mfma_f32_4x4x4_16b_bf16 v[24:27], v[40:41], a[118:119], 0\n"
    "buffer_load_dwordx4 v244, s[44:47], s84 offen lds\n"
    "v_cvt_pk_bf16_f32 v50, v32, v33\n"
    "v_cvt_pk_bf16_f32 v51, v34, v35\n"
    "v_accvgpr_write_b32 a30, v50\n"
    "v_accvgpr_write_b32 a31, v51\n"
    "s_waitcnt vmcnt(13) lgkmcnt(5)\n"
    "ds_read_b64_tr_b16 a[132:133], v16 offset:4096\n"
    "v_mfma_f32_4x4x4_16b_bf16 v[28:31], v[36:37], a[120:121], 0\n"
    "ds_read_b64_tr_b16 a[134:135], v16 offset:5120\n"
    "v_cvt_pk_bf16_f32 v48, v20, v21\n"
    "v_cvt_pk_bf16_f32 v49, v22, v23\n"
    "v_accvgpr_write_b32 a32, v48\n"
    "v_accvgpr_write_b32 a33, v49\n"
    "v_mfma_f32_4x4x4_16b_bf16 v[32:35], v[40:41], a[122:123], 0\n"
    "buffer_load_dwordx4 v245, s[44:47], s84 offen offset:1024 lds\n"
    "v_cvt_pk_bf16_f32 v50, v24, v25\n"
    "v_cvt_pk_bf16_f32 v51, v26, v27\n"
    "v_accvgpr_write_b32 a34, v50\n"
    "v_accvgpr_write_b32 a35, v51\n"
    "s_waitcnt lgkmcnt(4)\n"
    "ds_read_b64_tr_b16 a[136:137], v16 offset:4104\n"
    "v_mfma_f32_4x4x4_16b_bf16 v[20:23], v[36:37], a[124:125], 0\n"
    "ds_read_b64_tr_b16 a[138:139], v16 offset:5128\n"
    "v_cvt_pk_bf16_f32 v48, v28, v29\n"
    "v_cvt_pk_bf16_f32 v49, v30, v31\n"
    "v_accvgpr_write_b32 a36, v48\n"
    "v_accvgpr_write_b32 a37, v49\n"
    "v_mfma_f32_4x4x4_16b_bf16 v[24:27], v[40:41], a[126:127], 0\n"
    "buffer_load_dwordx4 v246, s[44:47], s84 offen offset:2048 lds\n"
    "v_cvt_pk_bf16_f32 v50, v32, v33\n"
    "v_cvt_pk_bf16_f32 v51, v34, v35\n"
    "v_accvgpr_write_b32 a38, v50\n"
    "v_accvgpr_write_b32 a39, v51\n"
    "v_and_b32 v42, v14, v252\n"
    "s_waitcnt vmcnt(11) lgkmcnt(4)\n"
    "ds_read_b64_tr_b16 a[116:117], v11 offset:6144\n"
    "v_mfma_f32_4x4x4_16b_bf16 v[28:31], v[36:37], a[128:129], 0\n"
    "ds_read_b64_tr_b16 a[118:119], v11 offset:6656\n"
    "v_cvt_pk_bf16_f32 v48, v20, v21\n"
    "v_cvt_pk_bf16_f32 v49, v22, v23\n"
    "v_accvgpr_write_b32 a40, v48\n"
    "v_accvgpr_write_b32 a41, v49\n"
    "v_mfma_f32_4x4x4_16b_bf16 v[32:35], v[40:41], a[130:131], 0\n"
    "buffer_load_dwordx4 v247, s[44:47], s84 offen offset:3072 lds\n"
    "v_cvt_pk_bf16_f32 v50, v24, v25\n"
    "v_cvt_pk_bf16_f32 v51, v26, v27\n"
    "v_accvgpr_write_b32 a42, v50\n"
    "v_accvgpr_write_b32 a43, v51\n"
    "v_and_b32 v43, v15, v253\n"
    "s_waitcnt lgkmcnt(4)\n"
    "ds_read_b64_tr_b16 a[120:121], v11 offset:6152\n"
    "v_mfma_f32_4x4x4_16b_bf16 v[20:23], v[36:37], a[132:133], 0\n"
    "ds_read_b64_tr_b16 a[122:123], v11 offset:6664\n"
    "s_add_u32 m0, s83, 83968\n"
    "v_cvt_pk_bf16_f32 v48, v28, v29\n"
    "v_cvt_pk_bf16_f32 v49, v30, v31\n"
    "v_accvgpr_write_b32 a44, v48\n"
    "v_accvgpr_write_b32 a45, v49\n"
    "v_mfma_f32_4x4x4_16b_bf16 v[24:27], v[40:41], a[134:135], 0\n"
    "buffer_load_dwordx4 v248, s[44:47], s84 offen lds\n"
    "v_cvt_pk_bf16_f32 v50, v32, v33\n"
    "v_cvt_pk_bf16_f32 v51, v34, v35\n"
    "v_accvgpr_write_b32 a46, v50\n"
    "v_accvgpr_write_b32 a47, v51\n"
    "v_and_b32 v46, v14, v254\n"
    "s_waitcnt lgkmcnt(4)\n"
    "ds_read_b64_tr_b16 a[124:125], v12 offset:6144\n"
    "v_mfma_f32_4x4x4_16b_bf16 v[28:31], v[36:37], a[136:137], 0\n"
    "ds_read_b64_tr_b16 a[126:127], v12 offset:6656\n"
    "v_cvt_pk_bf16_f32 v48, v20, v21\n"
    "v_cvt_pk_bf16_f32 v49, v22, v23\n"
    "v_accvgpr_write_b32 a48, v48\n"
    "v_accvgpr_write_b32 a49, v49\n"
    "v_mfma_f32_4x4x4_16b_bf16 v[32:35], v[40:41], a[138:139], 0\n"
    "buffer_load_dwordx4 v249, s[44:47], s84 offen offset:1024 lds\n"
    "v_cvt_pk_bf16_f32 v50, v24, v25\n"
    "v_cvt_pk_bf16_f32 v51, v26, v27\n"
    "v_accvgpr_write_b32 a50, v50\n"
    "v_accvgpr_write_b32 a51, v51\n"
    "v_and_b32 v47, v15, v255\n"
    "ds_read_b128 v[252:255], v13 offset:640\n"
    "s_waitcnt lgkmcnt(5)\n"
    "ds_read_b64_tr_b16 a[128:129], v12 offset:6152\n"
    "v_mfma_f32_4x4x4_16b_bf16 v[20:23], v[42:43], a[116:117], 0\n"
    "ds_read_b64_tr_b16 a[130:131], v12 offset:6664\n"
    "s_mov_b32 m0, s33\n"
    "v_cvt_pk_bf16_f32 v48, v28, v29\n"
    "v_cvt_pk_bf16_f32 v49, v30, v31\n"
    "v_accvgpr_write_b32 a52, v48\n"
    "v_accvgpr_write_b32 a53, v49\n"
    "v_mfma_f32_4x4x4_16b_bf16 v[24:27], v[46:47], a[118:119], 0\n"
    "s_memrealtime s[92:93]\n"
    "buffer_load_dwordx4 v0, s[36:39], s34 offen nt lds\n"
    "v_cvt_pk_bf16_f32 v50, v32, v33\n"
    "v_cvt_pk_bf16_f32 v51, v34, v35\n"
    "v_accvgpr_write_b32 a54, v50\n"
    "v_accvgpr_write_b32 a55, v51\n"
    "s_waitcnt vmcnt(13) lgkmcnt(5)\n"
    "ds_read_b64_tr_b16 a[132:133], v16 offset:10240\n"
    "v_mfma_f32_4x4x4_16b_bf16 v[28:31], v[42:43], a[120:121], 0\n"
    "ds_read_b64_tr_b16 a[134:135], v16 offset:11264\n"
    "v_cvt_pk_bf16_f32 v48, v20, v21\n"
    "v_cvt_pk_bf16_f32 v49, v22, v23\n"
    "v_accvgpr_write_b32 a56, v48\n"
    "v_accvgpr_write_b32 a57, v49\n"
    "v_mfma_f32_4x4x4_16b_bf16 v[32:35], v[46:47], a[122:123], 0\n"
    "buffer_load_dwordx4 v1, s[36:39], s34 offen offset:1024 nt lds\n"
    "v_cvt_pk_bf16_f32 v50, v24, v25\n"
    "v_cvt_pk_bf16_f32 v51, v26, v27\n"
    "v_accvgpr_write_b32 a58, v50\n"
    "v_accvgpr_write_b32 a59, v51\n"
    "s_waitcnt lgkmcnt(4)\n"
    "ds_read_b64_tr_b16 a[136:137], v16 offset:10248\n"
    "v_mfma_f32_4x4x4_16b_bf16 v[20:23], v[42:43], a[124:125], 0\n"
    "ds_read_b64_tr_b16 a[138:139], v16 offset:11272\n"
    "v_cvt_pk_bf16_f32 v48, v28, v29\n"
    "v_cvt_pk_bf16_f32 v49, v30, v31\n"
    "v_accvgpr_write_b32 a60, v48\n"
    "v_accvgpr_write_b32 a61, v49\n"
    "v_mfma_f32_4x4x4_16b_bf16 v[24:27], v[46:47], a[126:127], 0\n"
    "buffer_load_dwordx4 v2, s[36:39], s34 offen offset:2048 nt lds\n"
    "v_cvt_pk_bf16_f32 v50, v32, v33\n"
    "v_cvt_pk_bf16_f32 v51, v34, v35\n"
    "v_accvgpr_write_b32 a62, v50\n"
    "v_accvgpr_write_b32 a63, v51\n"
    "v_and_b32 v36, v14, v252\n"
    "s_waitcnt vmcnt(11) lgkmcnt(4)\n"
    "ds_read_b64_tr_b16 a[116:117], v11 offset:12288\n"
    "v_mfma_f32_4x4x4_16b_bf16 v[28:31], v[42:43], a[128:129], 0\n"
    "ds_read_b64_tr_b16 a[118:119], v11 offset:12800\n"
    "s_add_u32 s33, s33, s35\n"
    "s_mov_b32 m0, s33\n"
    "s_add_u32 s34, s34, s95\n"
    "v_cvt_pk_bf16_f32 v48, v20, v21\n"
    "v_cvt_pk_bf16_f32 v49, v22, v23\n"
    "v_accvgpr_write_b32 a64, v48\n"
    "v_accvgpr_write_b32 a65, v49\n"
    "v_mfma_f32_4x4x4_16b_bf16 v[32:35], v[46:47], a[130:131], 0\n"
    "buffer_load_dwordx4 v0, s[36:39], s34 offen nt lds\n"
    "v_cvt_pk_bf16_f32 v50, v24, v25\n"
    "v_cvt_pk_bf16_f32 v51, v26, v27\n"
    "v_accvgpr_write_b32 a66, v50\n"
    "v_accvgpr_write_b32 a67, v51\n"
    "v_and_b32 v37, v15, v253\n"
    "s_waitcnt lgkmcnt(4)\n"
    "ds_read_b64_tr_b16 a[120:121], v11 offset:12296\n"
    "v_mfma_f32_4x4x4_16b_bf16 v[20:23], v[42:43], a[132:133], 0\n"
    "ds_read_b64_tr_b16 a[122:123], v11 offset:12808\n"
    "v_cvt_pk_bf16_f32 v48, v28, v29\n"
    "v_cvt_pk_bf16_f32 v49, v30, v31\n"
    "v_accvgpr_write_b32 a68, v48\n"
    "v_accvgpr_write_b32 a69, v49\n"
    "v_mfma_f32_4x4x4_16b_bf16 v[24:27], v[46:47], a[134:135], 0\n"
    "buffer_load_dwordx4 v1, s[36:39], s34 offen offset:1024 nt lds\n"
    "v_cvt_pk_bf16_f32 v50, v32, v33\n"
    "v_cvt_pk_bf16_f32 v51, v34, v35\n"
    "v_accvgpr_write_b32 a70, v50\n"
    "v_accvgpr_write_b32 a71, v51\n"
    "v_and_b32 v40, v14, v254\n"
    "s_waitcnt lgkmcnt(4)\n"
    "ds_read_b64_tr_b16 a[124:125], v12 offset:12288\n"
    "v_mfma_f32_4x4x4_16b_bf16 v[28:31], v[42:43], a[136:137], 0\n"
    "ds_read_b64_tr_b16 a[126:127], v12 offset:12800\n"
    "v_cvt_pk_bf16_f32 v48, v20, v21\n"
    "v_cvt_pk_bf16_f32 v49, v22, v23\n"
    "v_accvgpr_write_b32 a72, v48\n"
    "v_accvgpr_write_b32 a73, v49\n"
    "v_mfma_f32_4x4x4_16b_bf16 v[32:35], v[46:47], a[138:139], 0\n"
    "buffer_load_dwordx4 v2, s[36:39], s34 offen offset:2048 nt lds\n"
    "v_cvt_pk_bf16_f32 v50, v24, v25\n"
    "v_cvt_pk_bf16_f32 v51, v26, v27\n"
    "v_accvgpr_write_b32 a74, v50\n"
    "v_accvgpr_write_b32 a75, v51\n"
    "v_and_b32 v41, v15, v255\n"
    "ds_read_b128 v[252:255], v13 offset:704\n"
    "s_waitcnt lgkmcnt(5)\n"
    "ds_read_b64_tr_b16 a[128:129], v12 offset:12296\n"
    "v_mfma_f32_4x4x4_16b_bf16 v[20:23], v[36:37], a[116:117], 0\n"
    "ds_read_b64_tr_b16 a[130:131], v12 offset:12808\n"
    "s_add_u32 s7, s83, 61440\n"
    "s_bitcmp1_b32 s2, 7\n"
    "s_cselect_b32 s33, s7, s33\n"
    "s_cselect_b32 s34, 0x80000000, s34\n"
    "s_cselect_b32 s35, 0, s35\n"
    "s_cselect_b32 s95, 0, s95\n"
    "s_add_u32 s33, s33, s35\n"
    "s_mov_b32 m0, s33\n"
    "s_add_u32 s34, s34, s95\n"
    "v_cvt_pk_bf16_f32 v48, v28, v29\n"
    "v_cvt_pk_bf16_f32 v49, v30, v31\n"
    "v_accvgpr_write_b32 a76, v48\n"
    "v_accvgpr_write_b32 a77, v49\n"
    "v_mfma_f32_4x4x4_16b_bf16 v[24:27], v[40:41], a[118:119], 0\n"
    "buffer_load_dwordx4 v0, s[36:39], s34 offen nt lds\n"
    "v_cvt_pk_bf16_f32 v50, v32, v33\n"
    "v_cvt_pk_bf16_f32 v51, v34, v35\n"
    "v_accvgpr_write_b32 a78, v50\n"
    "v_accvgpr_write_b32 a79, v51\n"
    "s_waitcnt vmcnt(13) lgkmcnt(5)\n"
    "ds_read_b64_tr_b16 a[132:133], v16 offset:16384\n"
    "v_mfma_f32_4x4x4_16b_bf16 v[28:31], v[36:37], a[120:121], 0\n"
    "ds_read_b64_tr_b16 a[134:135], v16 offset:17408\n"
    "v_cvt_pk_bf16_f32 v48, v20, v21\n"
    "v_cvt_pk_bf16_f32 v49, v22, v23\n"
    "v_accvgpr_write_b32 a80, v48\n"
    "v_accvgpr_write_b32 a81, v49\n"
    "v_mfma_f32_4x4x4_16b_bf16 v[32:35], v[40:41], a[122:123], 0\n"
    "buffer_load_dwordx4 v1, s[36:39], s34 offen offset:1024 nt lds\n"
    "v_cvt_pk_bf16_f32 v50, v24, v25\n"
    "v_cvt_pk_bf16_f32 v51, v26, v27\n"
    "v_accvgpr_write_b32 a82, v50\n"
    "v_accvgpr_write_b32 a83, v51\n"
    "s_waitcnt lgkmcnt(4)\n"
    "ds_read_b64_tr_b16 a[136:137], v16 offset:16392\n"
    "v_mfma_f32_4x4x4_16b_bf16 v[20:23], v[36:37], a[124:125], 0\n"
    "ds_read_b64_tr_b16 a[138:139], v16 offset:17416\n"
    "v_cvt_pk_bf16_f32 v48, v28, v29\n"
    "v_cvt_pk_bf16_f32 v49, v30, v31\n"
    "v_accvgpr_write_b32 a84, v48\n"
    "v_accvgpr_write_b32 a85, v49\n"
    "v_mfma_f32_4x4x4_16b_bf16 v[24:27], v[40:41], a[126:127], 0\n"
    "buffer_load_dwordx4 v2, s[36:39], s34 offen offset:2048 nt lds\n"
    "v_cvt_pk_bf16_f32 v50, v32, v33\n"
    "v_cvt_pk_bf16_f32 v51, v34, v35\n"
    "v_accvgpr_write_b32 a86, v50\n"
    "v_accvgpr_write_b32 a87, v51\n"
    "v_and_b32 v42, v14, v252\n"
    "s_waitcnt vmcnt(11) lgkmcnt(4)\n"
    "ds_read_b64_tr_b16 a[116:117], v11 offset:18432\n"
    "v_mfma_f32_4x4x4_16b_bf16 v[28:31], v[36:37], a[128:129], 0\n"
    "ds_read_b64_tr_b16 a[118:119], v11 offset:18944\n"
    "s_add_u32 s33, s33, s35\n"
    "s_mov_b32 m0, s33\n"
    "s_add_u32 s34, s34, s95\n"
    "v_cvt_pk_bf16_f32 v216, v20, v21\n"
    "v_cvt_pk_bf16_f32 v217, v22, v23\n"
    "v_mfma_f32_4x4x4_16b_bf16 v[32:35], v[40:41], a[130:131], 0\n"
    "buffer_load_dwordx4 v0, s[36:39], s34 offen nt lds\n"
    "v_cvt_pk_bf16_f32 v218, v24, v25\n"
    "v_cvt_pk_bf16_f32 v219, v26, v27\n"
    "v_and_b32 v43, v15, v253\n"
    "s_waitcnt lgkmcnt(4)\n"
    "ds_read_b64_tr_b16 a[120:121], v11 offset:18440\n"
    "v_mfma_f32_4x4x4_16b_bf16 v[20:23], v[36:37], a[132:133], 0\n"
    "ds_read_b64_tr_b16 a[122:123], v11 offset:18952\n"
    "v_cvt_pk_bf16_f32 v220, v28, v29\n"
    "v_cvt_pk_bf16_f32 v221, v30, v31\n"
    "v_mfma_f32_4x4x4_16b_bf16 v[24:27], v[40:41], a[134:135], 0\n"
    "buffer_load_dwordx4 v1, s[36:39], s34 offen offset:1024 nt lds\n"
    "v_cvt_pk_bf16_f32 v222, v32, v33\n"
    "v_cvt_pk_bf16_f32 v223, v34, v35\n"
    "v_and_b32 v46, v14, v254\n"
    "s_waitcnt lgkmcnt(4)\n"
    "ds_read_b64_tr_b16 a[124:125], v12 offset:18432\n"
    "v_mfma_f32_4x4x4_16b_bf16 v[28:31], v[36:37], a[136:137], 0\n"
    "ds_read_b64_tr_b16 a[126:127], v12 offset:18944\n"
    "v_cvt_pk_bf16_f32 v224, v20, v21\n"
    "v_cvt_pk_bf16_f32 v225, v22, v23\n"
    "v_mfma_f32_4x4x4_16b_bf16 v[32:35], v[40:41], a[138:139], 0\n"
    "buffer_load_dwordx4 v2, s[36:39], s34 offen offset:2048 nt lds\n"
    "v_cvt_pk_bf16_f32 v226, v24, v25\n"
    "v_cvt_pk_bf16_f32 v227, v26, v27\n"
    "v_and_b32 v47, v15, v255\n"
    "s_waitcnt lgkmcnt(4)\n"
    "ds_read_b64_tr_b16 a[128:129], v12 offset:18440\n"
    "v_mfma_f32_4x4x4_16b_bf16 v[20:23], v[42:43], a[116:117], 0\n"
    "ds_read_b64_tr_b16 a[130:131], v12 offset:18952\n"
    "s_add_u32 s33, s33, s35\n"
    "s_mov_b32 m0, s33\n"
    "s_add_u32 s34, s34, s95\n"
    "v_cvt_pk_bf16_f32 v228, v28, v29\n"
    "v_cvt_pk_bf16_f32 v229, v30, v31\n"
    "v_mfma_f32_4x4x4_16b_bf16 v[24:27], v[46:47], a[118:119], 0\n"
    "buffer_load_dwordx4 v0, s[36:39], s34 offen nt lds\n"
    "v_cvt_pk_bf16_f32 v230, v32, v33\n"
    "v_cvt_pk_bf16_f32 v231, v34, v35\n"
    "s_waitcnt vmcnt(13) lgkmcnt(4)\n"
    "ds_read_b64_tr_b16 a[132:133], v16 offset:22528\n"
    "v_mfma_f32_4x4x4_16b_bf16 v[28:31], v[42:43], a[120:121], 0\n"
    "ds_read_b64_tr_b16 a[134:135], v16 offset:23552\n"
    "v_cvt_pk_bf16_f32 v232, v20, v21\n"
    "v_cvt_pk_bf16_f32 v233, v22, v23\n"
    "v_mfma_f32_4x4x4_16b_bf16 v[32:35], v[46:47], a[122:123], 0\n"
    "buffer_load_dwordx4 v1, s[36:39], s34 offen offset:1024 nt lds\n"
    "v_cvt_pk_bf16_f32 v234, v24, v25\n"
    "v_cvt_pk_bf16_f32 v235, v26, v27\n"
    "s_waitcnt lgkmcnt(4)\n"
    "ds_read_b64_tr_b16 a[136:137], v16 offset:22536\n"
    "v_mfma_f32_4x4x4_16b_bf16 v[20:23], v[42:43], a[124:125], 0\n"
    "ds_read_b64_tr_b16 a[138:139], v16 offset:23560\n"
    "v_cvt_pk_bf16_f32 v236, v28, v29\n"
    "v_cvt_pk_bf16_f32 v237, v30, v31\n"
    "v_mfma_f32_4x4x4_16b_bf16 v[24:27], v[46:47], a[126:127], 0\n"
    "buffer_load_dwordx4 v2, s[36:39], s34 offen offset:2048 nt lds\n"
    "v_cvt_pk_bf16_f32 v238, v32, v33\n"
    "v_cvt_pk_bf16_f32 v239, v34, v35\n"
    "s_waitcnt lgkmcnt(4)\n"
    "v_mfma_f32_4x4x4_16b_bf16 v[28:31], v[42:43], a[128:129], 0\n"
    "v_cvt_pk_bf16_f32 v240, v20, v21\n"
    "v_cvt_pk_bf16_f32 v241, v22, v23\n"
    "s_nop 0\n"
    "v_mfma_f32_4x4x4_16b_bf16 v[32:35], v[46:47], a[130:131], 0\n"
    "v_cvt_pk_bf16_f32 v242, v24, v25\n"
    "v_cvt_pk_bf16_f32 v243, v26, v27\n"
    "s_nop 2\n"
    "s_waitcnt lgkmcnt(2)\n"
    "v_mfma_f32_4x4x4_16b_bf16 v[20:23], v[42:43], a[132:133], 0\n"
    "v_cvt_pk_bf16_f32 v244, v28, v29\n"
    "v_cvt_pk_bf16_f32 v245, v30, v31\n"
    "s_nop 0\n"
    "v_mfma_f32_4x4x4_16b_bf16 v[24:27], v[46:47], a[134:135], 0\n"
    "v_cvt_pk_bf16_f32 v246, v32, v33\n"
    "v_cvt_pk_bf16_f32 v247, v34, v35\n"
    "s_nop 2\n"
    "s_waitcnt lgkmcnt(0)\n"
    "v_mfma_f32_4x4x4_16b_bf16 v[28:31], v[42:43], a[136:137], 0\n"
    "v_cvt_pk_bf16_f32 v248, v20, v21\n"
    "v_cvt_pk_bf16_f32 v249, v22, v23\n"
    "s_nop 0\n"
    "v_mfma_f32_4x4x4_16b_bf16 v[32:35], v[46:47], a[138:139], 0\n"
    "v_cvt_pk_bf16_f32 v250, v24, v25\n"
    "v_cvt_pk_bf16_f32 v251, v26, v27\n"
    "s_nop 2\n"
    "s_nop 1\n"
    "s_nop 0\n"
    "s_mov_b32 s85, 135168\n"
    "s_nop 1\n"
    "v_cvt_pk_bf16_f32 v252, v28, v29\n"
    "v_cvt_pk_bf16_f32 v253, v30, v31\n"
    "v_cvt_pk_bf16_f32 v254, v32, v33\n"
    "v_cvt_pk_bf16_f32 v255, v34, v35\n"
    "v_mov_b32 v11, v3\n"
    "v_mov_b32 v12, v4\n"
    "v_mov_b32 v13, v5\n"
    "v_mov_b32 v14, v6\n"
    "v_add_u32 v15, s78, v7\n"
    "v_add_u32 v16, s78, v8\n"
    "v_add_u32 v17, s78, v9\n"
    "v_add_u32 v18, s85, v10\n"
    "s_waitcnt lgkmcnt(0)\n"
    "s_add_u32 s89, s92, s31\n"
    "s_mov_b32 s7, 0\n"
    "s_cmp_eq_u32 s4, 1\n"
    "s_cselect_b32 s7, 41f-51f, s7\n"
    "s_cmp_eq_u32 s4, 2\n"
    "s_cselect_b32 s7, 42f-51f, s7\n"
    "s_cmp_eq_u32 s4, 3\n"
    "s_cselect_b32 s7, 43f-51f, s7\n"
    "s_add_u32 s72, s72, s7\n"
    "s_addc_u32 s73, s73, 0\n"
    "s_mov_b64 s[26:27], s[72:73]\n"
    "s_sub_u32 s8, s72, 40\n"
    "s_subb_u32 s9, s73, 0\n"
    "s_setpc_b64 s[8:9]\n"
    "60:\n"
    "s_cmp_eq_u32 s76, s56\n"
    "s_cbranch_scc0 65f\n"
    "s_cmp_eq_u32 s15, 2\n"
    "s_cbranch_scc0 37f\n"
    "ds_read_b128 a[144:147], v18\n"
    "ds_read_b128 a[148:151], v18 offset:1024\n"
    "ds_read_b128 a[152:155], v18 offset:2048\n"
    "v_add_u32 v18, s65, v10\n"
    "s_add_u32 s65, s65, 12288\n"
    "s_cmp_eq_u32 s65, 159744\n"
    "s_cselect_b32 s65, 0, s65\n"
    "ds_read_b128 a[156:159], v18\n"
    "ds_read_b128 a[160:163], v18 offset:1024\n"
    "ds_read_b128 a[164:167], v18 offset:2048\n"
    "v_add_u32 v18, s65, v10\n"
    "s_add_u32 s65, s65, 12288\n"
    "s_cmp_eq_u32 s65, 159744\n"
    "s_cselect_b32 s65, 0, s65\n"
    "s_mov_b64 s[72:73], s[26:27]\n"
    "s_waitcnt vmcnt(12)\n"
    "s_waitcnt lgkmcnt(5)\n"
    "buffer_store_dwordx4 a[144:147], v0, s[40:43], s82 offen sc1\n"
    "s_waitcnt lgkmcnt(4)\n"
    "buffer_store_dwordx4 a[148:151], v1, s[40:43], s82 offen offset:1024 sc1\n"
    "s_waitcnt lgkmcnt(3)\n"
    "buffer_store_dwordx4 a[152:155], v2, s[40:43], s82 offen offset:2048 sc1\n"
    "s_add_u32 s82, s82, s53\n"
    "s_waitcnt lgkmcnt(2)\n"
    "buffer_store_dwordx4 a[156:159], v0, s[40:43], s82 offen sc1\n"
    "s_waitcnt lgkmcnt(1)\n"
    "buffer_store_dwordx4 a[160:163], v1, s[40:43], s82 offen offset:1024 sc1\n"
    "s_waitcnt lgkmcnt(0)\n"
    "buffer_store_dwordx4 a[164:167], v2, s[40:43], s82 offen offset:2048 sc1\n"
    "s_add_u32 s82, s82, s53\n"
    "s_mov_b32 s74, s82\n"
    "s_branch 54f\n"
    "37:\n"
    "s_mov_b64 s[72:73], s[26:27]\n"
    "s_waitcnt vmcnt(12)\n"
    "38:\n"
    "s_cmp_eq_u32 s15, 0\n"
    "s_cbranch_scc1 54f\n"
    "ds_read_b128 a[112:115], v18\n"
    "ds_read_b128 a[116:119], v18 offset:1024\n"
    "ds_read_b128 a[120:123], v18 offset:2048\n"
    "v_add_u32 v18, s65, v10\n"
    "s_add_u32 s65, s65, 12288\n"
    "s_cmp_eq_u32 s65, 159744\n"
    "s_cselect_b32 s65, 0, s65\n"
    "s_add_u32 s82, s82, s53\n"
    "s_sub_u32 s15, s15, 1\n"
    "s_waitcnt lgkmcnt(2)\n"
    "buffer_store_dwordx4 a[112:115], v0, s[40:43], s74 offen sc1\n"
    "s_waitcnt lgkmcnt(1)\n"
    "buffer_store_dwordx4 a[116:119], v1, s[40:43], s74 offen offset:1024 sc1\n"
    "s_waitcnt lgkmcnt(0)\n"
    "buffer_store_dwordx4 a[120:123], v2, s[40:43], s74 offen offset:2048 sc1\n"
    "s_mov_b32 s74, s82\n"
    "s_branch 38b\n"
    "65:\n"
    "s_cmp_ge_u32 s76, 4\n"
    "s_cbranch_scc1 57f\n"
    "s_mov_b32 s85, 0x6\n"
    "s_bitcmp1_b32 s85, s76\n"
    "s_cbranch_scc0 61f\n"
    "s_mov_b32 m0, s79\n"
    "s_add_u32 s79, s79, 12288\n"
    "buffer_load_dwordx4 v0, s[36:39], s81 offen nt lds\n"
    "buffer_load_dwordx4 v1, s[36:39], s81 offen offset:1024 nt lds\n"
    "buffer_load_dwordx4 v2, s[36:39], s81 offen offset:2048 nt lds\n"
    "s_add_u32 s81, s81, s53\n"
    "s_cmp_eq_u32 s79, s80\n"
    "s_cselect_b32 s79, s55, s79\n"
    "61:\n"
    "s_bitcmp1_b32 s2, 7\n"
    "s_cbranch_scc0 68f\n"
    "s_waitcnt vmcnt(63) lgkmcnt(0)\n"
    "s_cmp_lt_u32 s76, 3\n"
    "s_cbranch_scc1 54f\n"
    "s_waitcnt vmcnt(36)\n"
    "s_branch 54f\n"
    "68:\n"
    "s_waitcnt vmcnt(27) lgkmcnt(0)\n"
    "s_cmp_gt_u32 s76, 2\n"
    "s_cbranch_scc1 54f\n"
    "s_waitcnt vmcnt(24)\n"
    "s_cmp_gt_u32 s76, 1\n"
    "s_cbranch_scc1 54f\n"
    "s_waitcnt vmcnt(18)\n"
    "s_cmp_gt_u32 s76, 0\n"
    "s_cbranch_scc1 54f\n"
    "s_waitcnt vmcnt(12)\n"
    "54:\n"
    "s_cmp_eq_u32 s76, 3\n"
    "s_cselect_b32 s70, s29, s70\n"
    "s_sub_u32 s85, s92, s89\n"
    "s_cmp_lt_i32 s85, s29\n"
    "s_cbranch_scc1 57f\n"
    "s_setpc_b64 s[72:73]\n"
    ".p2align 6\n"
    "s_branch 60b\n"
    "s_nop 0\n"
    "s_nop 0\n"
    "s_nop 0\n"
    "s_nop 0\n"
    "s_nop 0\n"
    "s_nop 0\n"
    "s_nop 0\n"
    "s_waitcnt vmcnt(12)\n"
    "s_waitcnt vmcnt(18)\n"
    "51:\n"
    "v_mfma_f32_16x16x32_bf16 v[40:43], a[76:79], a[100:103], v[40:43]\n"
    "53:\n"
    "s_barrier\n"
    "v_mfma_f32_16x16x32_bf16 v[20:23], a[80:83], a[104:107], v[20:23]\n"
    "ds_read_b128 a[88:91], v11\n"
    "s_add_u32 s76, s76, 1\n"
    "v_mfma_f32_16x16x32_bf16 v[20:23], v[232:235], a[108:111], v[20:23]\n"
    "s_bitcmp0_b32 s68, 0\n"
    "s_addc_u32 s28, s28, 0\n"
    "v_mfma_f32_16x16x32_bf16 v[24:27], a[84:87], a[104:107], v[24:27]\n"
    "ds_read_b128 a[92:95], v12\n"
    "s_cmp_lt_i32 s85, 8\n"
    "v_mfma_f32_16x16x32_bf16 v[24:27], v[236:239], a[108:111], v[24:27]\n"
    "s_cselect_b32 s7, s28, 0\n"
    "s_cmp_lg_u32 s7, 0\n"
    "v_mfma_f32_16x16x32_bf16 v[28:31], v[216:219], a[104:107], v[28:31]\n"
    "s_cselect_b32 s30, 42, s94\n"
    "s_cselect_b32 s66, s57, 0\n"
    "v_mfma_f32_16x16x32_bf16 v[28:31], v[240:243], a[108:111], v[28:31]\n"
    "ds_read_b128 a[96:99], v13\n"
    "s_cselect_b32 s7, 3, 0\n"
    "v_mfma_f32_16x16x32_bf16 v[32:35], v[220:223], a[104:107], v[32:35]\n"
    "s_subb_u32 s28, s28, 0\n"
    "s_lshr_b32 s57, s57, s7\n"
    "v_mfma_f32_16x16x32_bf16 v[32:35], v[244:247], a[108:111], v[32:35]\n"
    "s_bitcmp0_b32 s66, 0\n"
    "s_cselect_b32 s86, s81, 0x80000000\n"
    "v_mfma_f32_16x16x32_bf16 v[36:39], v[224:227], a[104:107], v[36:39]\n"
    "ds_read_b128 a[112:115], v18\n"
    "s_cselect_b32 s87, s53, 0\n"
    "v_mfma_f32_16x16x32_bf16 v[36:39], v[248:251], a[108:111], v[36:39]\n"
    "v_cvt_pk_bf16_f32 v44, v20, v21\n"
    "v_cvt_pk_bf16_f32 v45, v22, v23\n"
    "v_mfma_f32_16x16x32_bf16 v[40:43], v[228:231], a[104:107], v[40:43]\n"
    "v_cvt_pk_bf16_f32 v46, v24, v25\n"
    "v_cvt_pk_bf16_f32 v47, v26, v27\n"
    "v_mfma_f32_16x16x32_bf16 v[40:43], v[252:255], a[108:111], v[40:43]\n"
    "ds_read_b128 a[100:103], v14\n"
    "s_waitcnt lgkmcnt(3)\n"
    "v_mfma_f32_16x16x32_bf16 v[20:23], v[56:59], a[88:91], 0\n"
    "v_cvt_pk_bf16_f32 v48, v28, v29\n"
    "v_cvt_pk_bf16_f32 v49, v30, v31\n"
    "v_mfma_f32_16x16x32_bf16 v[20:23], v[80:83], a[92:95], v[20:23]\n"
    "v_cvt_pk_bf16_f32 v50, v32, v33\n"
    "v_cvt_pk_bf16_f32 v51, v34, v35\n"
    "v_mfma_f32_16x16x32_bf16 v[24:27], v[60:63], a[88:91], 0\n"
    "ds_read_b128 a[116:119], v18 offset:1024\n"
    "s_cselect_b32 s88, 12288, 0\n"
    "v_mfma_f32_16x16x32_bf16 v[24:27], v[84:87], a[92:95], v[24:27]\n"
    "v_cvt_pk_bf16_f32 v52, v36, v37\n"
    "v_cvt_pk_bf16_f32 v53, v38, v39\n"
    "v_mfma_f32_16x16x32_bf16 v[28:31], v[64:67], a[88:91], 0\n"
    "v_cvt_pk_bf16_f32 v54, v40, v41\n"
    "v_cvt_pk_bf16_f32 v55, v42, v43\n"
    "v_mfma_f32_16x16x32_bf16 v[28:31], v[88:91], a[92:95], v[28:31]\n"
    "ds_read_b128 a[104:107], v11 offset:256\n"
    "s_ashr_i64 s[68:69], s[68:69], 1\n"
    "v_mfma_f32_16x16x32_bf16 v[32:35], v[68:71], a[88:91], 0\n"
    "s_cmp_le_i32 s76, s52\n"
    "s_cbranch_scc0 55f\n"
    "v_mfma_f32_16x16x32_bf16 v[32:35], v[92:95], a[92:95], v[32:35]\n"
    "v_mfma_f32_16x16x32_bf16 v[36:39], v[72:75], a[88:91], 0\n"
    "ds_read_b128 a[120:123], v18 offset:2048\n"
    "v_add_u32 v18, s65, v10\n"
    "v_mfma_f32_16x16x32_bf16 v[36:39], v[96:99], a[92:95], v[36:39]\n"
    "s_mov_b32 s78, s77\n"
    "s_mov_b32 m0, s79\n"
    "v_mfma_f32_16x16x32_bf16 v[40:43], v[76:79], a[88:91], 0\n"
    "v_mfma_f32_16x16x32_bf16 v[40:43], v[100:103], a[92:95], v[40:43]\n"
    "ds_read_b128 a[108:111], v12 offset:256\n"
    "s_waitcnt lgkmcnt(4)\n"
    "v_mfma_f32_16x16x32_bf16 v[20:23], v[104:107], a[96:99], v[20:23]\n"
    "buffer_store_dwordx4 a[112:115], v0, s[40:43], s74 offen sc1\n"
    "v_mfma_f32_16x16x32_bf16 v[20:23], v[128:131], a[100:103], v[20:23]\n"
    "ds_write_b64 v15, v[44:45]\n"
    "v_mfma_f32_16x16x32_bf16 v[24:27], v[108:111], a[96:99], v[24:27]\n"
    "s_and_b32 s7, s66, 4\n"
    "v_mfma_f32_16x16x32_bf16 v[24:27], v[132:135], a[100:103], v[24:27]\n"
    "ds_write_b64 v15, v[46:47] offset:8\n"
    "v_mfma_f32_16x16x32_bf16 v[28:31], v[112:115], a[96:99], v[28:31]\n"
    "s_waitcnt lgkmcnt(5)\n"
    "buffer_store_dwordx4 a[116:119], v1, s[40:43], s74 offen offset:1024 sc1\n"
    "v_mfma_f32_16x16x32_bf16 v[28:31], v[136:139], a[100:103], v[28:31]\n"
    "ds_read_b128 a[88:91], v13 offset:256\n"
    "s_add_u32 s77, s77, 12288\n"
    "v_mfma_f32_16x16x32_bf16 v[32:35], v[116:119], a[96:99], v[32:35]\n"
    "ds_write_b64 v16, v[48:49]\n"
    "v_mfma_f32_16x16x32_bf16 v[32:35], v[140:143], a[100:103], v[32:35]\n"
    "s_sub_u32 s72, s72, s7\n"
    "s_subb_u32 s73, s73, 0\n"
    "v_mfma_f32_16x16x32_bf16 v[36:39], v[120:123], a[96:99], v[36:39]\n"
    "s_cmp_eq_u32 s77, 159744\n"
    "s_cselect_b32 s77, 0, s77\n"
    "v_mfma_f32_16x16x32_bf16 v[36:39], v[144:147], a[100:103], v[36:39]\n"
    "v_add_u32 v15, s78, v7\n"
    "v_mfma_f32_16x16x32_bf16 v[40:43], v[124:127], a[96:99], v[40:43]\n"
    "s_waitcnt lgkmcnt(5)\n"
    "buffer_store_dwordx4 a[120:123], v2, s[40:43], s74 offen offset:2048 sc1\n"
    "v_mfma_f32_16x16x32_bf16 v[40:43], v[148:151], a[100:103], v[40:43]\n"
    "ds_read_b128 a[92:95], v14 offset:256\n"
    "s_waitcnt lgkmcnt(5)\n"
    "v_mfma_f32_16x16x32_bf16 v[20:23], v[152:155], a[104:107], v[20:23]\n"
    "ds_write_b64 v16, v[50:51] offset:8\n"
    "v_mfma_f32_16x16x32_bf16 v[20:23], v[176:179], a[108:111], v[20:23]\n"
    "v_mfma_f32_16x16x32_bf16 v[24:27], v[156:159], a[104:107], v[24:27]\n"
    "v_add_u32 v16, s78, v8\n"
    "v_mfma_f32_16x16x32_bf16 v[24:27], v[180:183], a[108:111], v[24:27]\n"
    "ds_write_b64 v17, v[52:53]\n"
    "v_mfma_f32_16x16x32_bf16 v[28:31], v[160:163], a[104:107], v[28:31]\n"
    "buffer_load_dwordx4 v0, s[36:39], s86 offen nt lds\n"
    "v_mfma_f32_16x16x32_bf16 v[28:31], v[184:187], a[108:111], v[28:31]\n"
    "ds_read_b128 a[96:99], v11 offset:512\n"
    "v_mfma_f32_16x16x32_bf16 v[32:35], v[164:167], a[104:107], v[32:35]\n"
    "v_mfma_f32_16x16x32_bf16 v[32:35], v[188:191], a[108:111], v[32:35]\n"
    "ds_write_b64 v17, v[54:55] offset:8\n"
    "v_mfma_f32_16x16x32_bf16 v[36:39], v[168:171], a[104:107], v[36:39]\n"
    "v_add_u32 v17, s78, v9\n"
    "v_mfma_f32_16x16x32_bf16 v[36:39], v[192:195], a[108:111], v[36:39]\n"
    "v_mfma_f32_16x16x32_bf16 v[40:43], v[172:175], a[104:107], v[40:43]\n"
    "buffer_load_dwordx4 v1, s[36:39], s86 offen offset:1024 nt lds\n"
    "v_mfma_f32_16x16x32_bf16 v[40:43], v[196:199], a[108:111], v[40:43]\n"
    "ds_read_b128 a[100:103], v12 offset:512\n"
    "s_waitcnt lgkmcnt(5)\n"
    "v_mfma_f32_16x16x32_bf16 v[20:23], v[200:203], a[88:91], v[20:23]\n"
    "s_cmp_lg_u32 s74, s82\n"
    "s_cselect_b32 s75, 0, s53\n"
    "v_mfma_f32_16x16x32_bf16 v[20:23], a[8:11], a[92:95], v[20:23]\n"
    "s_add_u32 s82, s82, s75\n"
    "v_mfma_f32_16x16x32_bf16 v[24:27], v[204:207], a[88:91], v[24:27]\n"
    "s_bitcmp1_b32 s66, 1\n"
    "s_cselect_b32 s74, 0x80000000, s82\n"
    "v_mfma_f32_16x16x32_bf16 v[24:27], a[12:15], a[92:95], v[24:27]\n"
    "s_cselect_b32 s75, 0, 12288\n"
    "s_addc_u32 s15, s15, 0\n"
    "v_mfma_f32_16x16x32_bf16 v[28:31], v[208:211], a[88:91], v[28:31]\n"
    "buffer_load_dwordx4 v2, s[36:39], s86 offen offset:2048 nt lds\n"
    "v_mfma_f32_16x16x32_bf16 v[28:31], a[16:19], a[92:95], v[28:31]\n"
    "ds_read_b128 a[104:107], v13 offset:512\n"
    "v_mfma_f32_16x16x32_bf16 v[32:35], v[212:215], a[88:91], v[32:35]\n"
    "s_add_u32 s65, s65, s75\n"
    "v_mfma_f32_16x16x32_bf16 v[32:35], a[20:23], a[92:95], v[32:35]\n"
    "s_cmp_eq_u32 s65, 159744\n"
    "s_cselect_b32 s65, 0, s65\n"
    "v_mfma_f32_16x16x32_bf16 v[36:39], a[0:3], a[88:91], v[36:39]\n"
    "s_cmp_ge_i32 s76, vcc_lo\n"
    "s_cselect_b32 s57, 0, s57\n"
    "v_mfma_f32_16x16x32_bf16 v[36:39], a[24:27], a[92:95], v[36:39]\n"
    "v_mfma_f32_16x16x32_bf16 v[40:43], a[4:7], a[88:91], v[40:43]\n"
    "s_add_u32 s81, s81, s87\n"
    "s_add_u32 s79, s79, s88\n"
    "v_mfma_f32_16x16x32_bf16 v[40:43], a[28:31], a[92:95], v[40:43]\n"
    "ds_read_b128 a[108:111], v14 offset:512\n"
    "s_waitcnt lgkmcnt(2)\n"
    "v_mfma_f32_16x16x32_bf16 v[20:23], a[32:35], a[96:99], v[20:23]\n"
    "v_add_u32 v11, s77, v3\n"
    "v_add_u32 v12, s77, v4\n"
    "v_mfma_f32_16x16x32_bf16 v[20:23], a[56:59], a[100:103], v[20:23]\n"
    "v_add_u32 v13, s77, v5\n"
    "v_add_u32 v14, s77, v6\n"
    "v_mfma_f32_16x16x32_bf16 v[24:27], a[36:39], a[96:99], v[24:27]\n"
    "s_memrealtime s[92:93]\n"
    "s_add_u32 s89, s89, s30\n"
    "v_mfma_f32_16x16x32_bf16 v[24:27], a[60:63], a[100:103], v[24:27]\n"
    "v_mfma_f32_16x16x32_bf16 v[28:31], a[40:43], a[96:99], v[28:31]\n"
    "v_mfma_f32_16x16x32_bf16 v[28:31], a[64:67], a[100:103], v[28:31]\n"
    "s_cmp_eq_u32 s79, s80\n"
    "s_cselect_b32 s79, s55, s79\n"
    "v_mfma_f32_16x16x32_bf16 v[32:35], a[44:47], a[96:99], v[32:35]\n"
    "s_cmp_eq_u32 s76, s56\n"
    "s_cselect_b32 s71, 0x7fffffff, s70\n"
    "v_mfma_f32_16x16x32_bf16 v[32:35], a[68:71], a[100:103], v[32:35]\n"
    "v_mfma_f32_16x16x32_bf16 v[36:39], a[48:51], a[96:99], v[36:39]\n"
    "s_waitcnt vmcnt(24) lgkmcnt(0)\n"
    "s_sub_u32 s85, s92, s89\n"
    "v_mfma_f32_16x16x32_bf16 v[36:39], a[72:75], a[100:103], v[36:39]\n"
    "s_cmp_lt_i32 s85, s71\n"
    "v_mfma_f32_16x16x32_bf16 v[40:43], a[52:55], a[96:99], v[40:43]\n"
    "s_cbranch_scc1 60b\n"
    "s_setpc_b64 s[72:73]\n"
    ".p2align 6\n"
    "s_branch 60b\n"
    "s_nop 0\n"
    "s_nop 0\n"
    "s_nop 0\n"
    "s_nop 0\n"
    "s_nop 0\n"
    "s_nop 0\n"
    "s_nop 0\n"
    "s_waitcnt vmcnt(12)\n"
    "s_waitcnt vmcnt(18)\n"
    "41:\n"
    "v_mfma_f32_16x16x32_bf16 v[40:43], a[76:79], a[100:103], v[40:43]\n"
    "53:\n"
    "s_barrier\n"
    "v_mfma_f32_16x16x32_bf16 v[20:23], a[80:83], a[104:107], v[20:23]\n"
    "ds_read_b128 a[88:91], v11\n"
    "s_add_u32 s76, s76, 1\n"
    "v_mfma_f32_16x16x32_bf16 v[20:23], v[232:235], a[108:111], v[20:23]\n"
    "s_bitcmp0_b32 s68, 0\n"
    "s_addc_u32 s28, s28, 0\n"
    "v_mfma_f32_16x16x32_bf16 v[24:27], a[84:87], a[104:107], v[24:27]\n"
    "ds_read_b128 a[92:95], v12\n"
    "s_cmp_lt_i32 s85, 8\n"
    "v_mfma_f32_16x16x32_bf16 v[24:27], v[236:239], a[108:111], v[24:27]\n"
    "s_cselect_b32 s7, s28, 0\n"
    "s_cmp_lg_u32 s7, 0\n"
    "v_mfma_f32_16x16x32_bf16 v[28:31], v[216:219], a[104:107], v[28:31]\n"
    "s_cselect_b32 s30, 42, s94\n"
    "s_cselect_b32 s66, s57, 0\n"
    "v_mfma_f32_16x16x32_bf16 v[28:31], v[240:243], a[108:111], v[28:31]\n"
    "ds_read_b128 a[96:99], v13\n"
    "s_cselect_b32 s7, 3, 0\n"
    "v_mfma_f32_16x16x32_bf16 v[32:35], v[220:223], a[104:107], v[32:35]\n"
    "s_subb_u32 s28, s28, 0\n"
    "s_lshr_b32 s57, s57, s7\n"
    "v_mfma_f32_16x16x32_bf16 v[32:35], v[244:247], a[108:111], v[32:35]\n"
    "s_bitcmp0_b32 s66, 0\n"
    "s_cselect_b32 s86, s81, 0x80000000\n"
    "v_mfma_f32_16x16x32_bf16 v[36:39], v[224:227], a[104:107], v[36:39]\n"
    "ds_read_b128 a[112:115], v18\n"
    "s_cselect_b32 s87, s53, 0\n"
    "v_mfma_f32_16x16x32_bf16 v[36:39], v[248:251], a[108:111], v[36:39]\n"
    "v_cvt_pk_bf16_f32 v44, v20, v21\n"
    "v_cvt_pk_bf16_f32 v45, v22, v23\n"
    "v_mfma_f32_16x16x32_bf16 v[40:43], v[228:231], a[104:107], v[40:43]\n"
    "v_cvt_pk_bf16_f32 v46, v24, v25\n"
    "v_cvt_pk_bf16_f32 v47, v26, v27\n"
    "v_mfma_f32_16x16x32_bf16 v[40:43], v[252:255], a[108:111], v[40:43]\n"
    "ds_read_b128 a[100:103], v14\n"
    "s_waitcnt lgkmcnt(3)\n"
    "v_mfma_f32_16x16x32_bf16 v[20:23], v[56:59], a[88:91], 0\n"
    "v_cvt_pk_bf16_f32 v48, v28, v29\n"
    "v_cvt_pk_bf16_f32 v49, v30, v31\n"
    "v_mfma_f32_16x16x32_bf16 v[20:23], v[80:83], a[92:95], v[20:23]\n"
    "v_cvt_pk_bf16_f32 v50, v32, v33\n"
    "v_cvt_pk_bf16_f32 v51, v34, v35\n"
    "v_mfma_f32_16x16x32_bf16 v[24:27], v[60:63], a[88:91], 0\n"
    "ds_read_b128 a[116:119], v18 offset:1024\n"
    "s_cselect_b32 s88, 12288, 0\n"
    "v_mfma_f32_16x16x32_bf16 v[24:27], v[84:87], a[92:95], v[24:27]\n"
    "v_cvt_pk_bf16_f32 v52, v36, v37\n"
    "v_cvt_pk_bf16_f32 v53, v38, v39\n"
    "v_mfma_f32_16x16x32_bf16 v[28:31], v[64:67], a[88:91], 0\n"
    "v_cvt_pk_bf16_f32 v54, v40, v41\n"
    "v_cvt_pk_bf16_f32 v55, v42, v43\n"
    "v_mfma_f32_16x16x32_bf16 v[28:31], v[88:91], a[92:95], v[28:31]\n"
    "ds_read_b128 a[104:107], v11 offset:256\n"
    "s_ashr_i64 s[68:69], s[68:69], 1\n"
    "v_mfma_f32_16x16x32_bf16 v[32:35], v[68:71], a[88:91], 0\n"
    "s_cmp_le_i32 s76, s52\n"
    "s_cbranch_scc0 55f\n"
    "v_mfma_f32_16x16x32_bf16 v[32:35], v[92:95], a[92:95], v[32:35]\n"
    "v_mfma_f32_16x16x32_bf16 v[36:39], v[72:75], a[88:91], 0\n"
    "ds_read_b128 a[120:123], v18 offset:2048\n"
    "v_add_u32 v18, s65, v10\n"
    "v_mfma_f32_16x16x32_bf16 v[36:39], v[96:99], a[92:95], v[36:39]\n"
    "s_mov_b32 s78, s77\n"
    "s_mov_b32 m0, s79\n"
    "v_mfma_f32_16x16x32_bf16 v[40:43], v[76:79], a[88:91], 0\n"
    "s_waitcnt lgkmcnt(4)\n"
    "buffer_store_dwordx4 a[112:115], v0, s[40:43], s74 offen sc1\n"
    "v_mfma_f32_16x16x32_bf16 v[40:43], v[100:103], a[92:95], v[40:43]\n"
    "ds_read_b128 a[108:111], v12 offset:256\n"
    "s_waitcnt lgkmcnt(4)\n"
    "v_mfma_f32_16x16x32_bf16 v[20:23], v[104:107], a[96:99], v[20:23]\n"
    "ds_write_b64 v15, v[44:45]\n"
    "v_mfma_f32_16x16x32_bf16 v[20:23], v[128:131], a[100:103], v[20:23]\n"
    "s_and_b32 s7, s66, 4\n"
    "v_mfma_f32_16x16x32_bf16 v[24:27], v[108:111], a[96:99], v[24:27]\n"
    "ds_write_b64 v15, v[46:47] offset:8\n"
    "v_mfma_f32_16x16x32_bf16 v[24:27], v[132:135], a[100:103], v[24:27]\n"
    "s_waitcnt lgkmcnt(5)\n"
    "buffer_store_dwordx4 a[116:119], v1, s[40:43], s74 offen offset:1024 sc1\n"
    "v_mfma_f32_16x16x32_bf16 v[28:31], v[112:115], a[96:99], v[28:31]\n"
    "s_sub_u32 s72, s72, s7\n"
    "s_subb_u32 s73, s73, 0\n"
    "v_mfma_f32_16x16x32_bf16 v[28:31], v[136:139], a[100:103], v[28:31]\n"
    "ds_read_b128 a[88:91], v13 offset:256\n"
    "s_add_u32 s77, s77, 12288\n"
    "v_mfma_f32_16x16x32_bf16 v[32:35], v[116:119], a[96:99], v[32:35]\n"
    "s_cmp_eq_u32 s77, 159744\n"
    "s_cselect_b32 s77, 0, s77\n"
    "v_mfma_f32_16x16x32_bf16 v[32:35], v[140:143], a[100:103], v[32:35]\n"
    "ds_write_b64 v16, v[48:49]\n"
    "v_mfma_f32_16x16x32_bf16 v[36:39], v[120:123], a[96:99], v[36:39]\n"
    "ds_write_b64 v16, v[50:51] offset:8\n"
    "v_mfma_f32_16x16x32_bf16 v[36:39], v[144:147], a[100:103], v[36:39]\n"
    "s_waitcnt lgkmcnt(6)\n"
    "buffer_store_dwordx4 a[120:123], v2, s[40:43], s74 offen offset:2048 sc1\n"
    "v_mfma_f32_16x16x32_bf16 v[40:43], v[124:127], a[96:99], v[40:43]\n"
    "v_add_u32 v15, s78, v7\n"
    "v_mfma_f32_16x16x32_bf16 v[40:43], v[148:151], a[100:103], v[40:43]\n"
    "ds_read_b128 a[92:95], v14 offset:256\n"
    "s_waitcnt lgkmcnt(6)\n"
    "v_mfma_f32_16x16x32_bf16 v[20:23], v[152:155], a[104:107], v[20:23]\n"
    "v_mfma_f32_16x16x32_bf16 v[20:23], v[176:179], a[108:111], v[20:23]\n"
    "v_mfma_f32_16x16x32_bf16 v[24:27], v[156:159], a[104:107], v[24:27]\n"
    "v_add_u32 v16, s78, v8\n"
    "v_mfma_f32_16x16x32_bf16 v[24:27], v[180:183], a[108:111], v[24:27]\n"
    "buffer_load_dwordx4 v0, s[36:39], s86 offen nt lds\n"
    "v_mfma_f32_16x16x32_bf16 v[28:31], v[160:163], a[104:107], v[28:31]\n"
    "ds_write_b64 v17, v[52:53]\n"
    "v_mfma_f32_16x16x32_bf16 v[28:31], v[184:187], a[108:111], v[28:31]\n"
    "ds_read_b128 a[96:99], v11 offset:512\n"
    "v_mfma_f32_16x16x32_bf16 v[32:35], v[164:167], a[104:107], v[32:35]\n"
    "v_mfma_f32_16x16x32_bf16 v[32:35], v[188:191], a[108:111], v[32:35]\n"
    "v_mfma_f32_16x16x32_bf16 v[36:39], v[168:171], a[104:107], v[36:39]\n"
    "ds_write_b64 v17, v[54:55] offset:8\n"
    "v_mfma_f32_16x16x32_bf16 v[36:39], v[192:195], a[108:111], v[36:39]\n"
    "buffer_load_dwordx4 v1, s[36:39], s86 offen offset:1024 nt lds\n"
    "v_mfma_f32_16x16x32_bf16 v[40:43], v[172:175], a[104:107], v[40:43]\n"
    "v_add_u32 v17, s78, v9\n"
    "v_mfma_f32_16x16x32_bf16 v[40:43], v[196:199], a[108:111], v[40:43]\n"
    "ds_read_b128 a[100:103], v12 offset:512\n"
    "s_waitcnt lgkmcnt(4)\n"
    "v_mfma_f32_16x16x32_bf16 v[20:23], v[200:203], a[88:91], v[20:23]\n"
    "s_cmp_lg_u32 s74, s82\n"
    "s_cselect_b32 s75, 0, s53\n"
    "v_mfma_f32_16x16x32_bf16 v[20:23], a[8:11], a[92:95], v[20:23]\n"
    "s_add_u32 s82, s82, s75\n"
    "v_mfma_f32_16x16x32_bf16 v[24:27], v[204:207], a[88:91], v[24:27]\n"
    "s_bitcmp1_b32 s66, 1\n"
    "s_cselect_b32 s74, 0x80000000, s82\n"
    "v_mfma_f32_16x16x32_bf16 v[24:27], a[12:15], a[92:95], v[24:27]\n"
    "buffer_load_dwordx4 v2, s[36:39], s86 offen offset:2048 nt lds\n"
    "v_mfma_f32_16x16x32_bf16 v[28:31], v[208:211], a[88:91], v[28:31]\n"
    "s_cselect_b32 s75, 0, 12288\n"
    "s_addc_u32 s15, s15, 0\n"
    "v_mfma_f32_16x16x32_bf16 v[28:31], a[16:19], a[92:95], v[28:31]\n"
    "ds_read_b128 a[104:107], v13 offset:512\n"
    "v_mfma_f32_16x16x32_bf16 v[32:35], v[212:215], a[88:91], v[32:35]\n"
    "s_add_u32 s65, s65, s75\n"
    "v_mfma_f32_16x16x32_bf16 v[32:35], a[20:23], a[92:95], v[32:35]\n"
    "s_cmp_eq_u32 s65, 159744\n"
    "s_cselect_b32 s65, 0, s65\n"
    "v_mfma_f32_16x16x32_bf16 v[36:39], a[0:3], a[88:91], v[36:39]\n"
    "s_cmp_ge_i32 s76, vcc_lo\n"
    "s_cselect_b32 s57, 0, s57\n"
    "v_mfma_f32_16x16x32_bf16 v[36:39], a[24:27], a[92:95], v[36:39]\n"
    "v_mfma_f32_16x16x32_bf16 v[40:43], a[4:7], a[88:91], v[40:43]\n"
    "s_add_u32 s81, s81, s87\n"
    "s_add_u32 s79, s79, s88\n"
    "v_mfma_f32_16x16x32_bf16 v[40:43], a[28:31], a[92:95], v[40:43]\n"
    "ds_read_b128 a[108:111], v14 offset:512\n"
    "s_waitcnt lgkmcnt(2)\n"
    "v_mfma_f32_16x16x32_bf16 v[20:23], a[32:35], a[96:99], v[20:23]\n"
    "v_add_u32 v11, s77, v3\n"
    "v_add_u32 v12, s77, v4\n"
    "v_mfma_f32_16x16x32_bf16 v[20:23], a[56:59], a[100:103], v[20:23]\n"
    "v_add_u32 v13, s77, v5\n"
    "v_add_u32 v14, s77, v6\n"
    "v_mfma_f32_16x16x32_bf16 v[24:27], a[36:39], a[96:99], v[24:27]\n"
    "s_memrealtime s[92:93]\n"
    "s_add_u32 s89, s89, s30\n"
    "v_mfma_f32_16x16x32_bf16 v[24:27], a[60:63], a[100:103], v[24:27]\n"
    "v_mfma_f32_16x16x32_bf16 v[28:31], a[40:43], a[96:99], v[28:31]\n"
    "v_mfma_f32_16x16x32_bf16 v[28:31], a[64:67], a[100:103], v[28:31]\n"
    "s_cmp_eq_u32 s79, s80\n"
    "s_cselect_b32 s79, s55, s79\n"
    "v_mfma_f32_16x16x32_bf16 v[32:35], a[44:47], a[96:99], v[32:35]\n"
    "s_cmp_eq_u32 s76, s56\n"
    "s_cselect_b32 s71, 0x7fffffff, s70\n"
    "v_mfma_f32_16x16x32_bf16 v[32:35], a[68:71], a[100:103], v[32:35]\n"
    "v_mfma_f32_16x16x32_bf16 v[36:39], a[48:51], a[96:99], v[36:39]\n"
    "s_waitcnt vmcnt(24) lgkmcnt(0)\n"
    "s_sub_u32 s85, s92, s89\n"
    "v_mfma_f32_16x16x32_bf16 v[36:39], a[72:75], a[100:103], v[36:39]\n"
    "s_cmp_lt_i32 s85, s71\n"
    "v_mfma_f32_16x16x32_bf16 v[40:43], a[52:55], a[96:99], v[40:43]\n"
    "s_cbranch_scc1 60b\n"
    "s_setpc_b64 s[72:73]\n"
    ".p2align 6\n"
    "s_branch 60b\n"
    "s_nop 0\n"
    "s_nop 0\n"
    "s_nop 0\n"
    "s_nop 0\n"
    "s_nop 0\n"
    "s_nop 0\n"
    "s_nop 0\n"
    "s_waitcnt vmcnt(12)\n"
    "s_waitcnt vmcnt(18)\n"
    "42:\n"
    "v_mfma_f32_16x16x32_bf16 v[40:43], a[76:79], a[100:103], v[40:43]\n"
    "53:\n"
    "s_barrier\n"
    "v_mfma_f32_16x16x32_bf16 v[20:23], a[80:83], a[104:107], v[20:23]\n"
    "ds_read_b128 a[88:91], v11\n"
    "s_add_u32 s76, s76, 1\n"
    "v_mfma_f32_16x16x32_bf16 v[20:23], v[232:235], a[108:111], v[20:23]\n"
    "s_bitcmp0_b32 s68, 0\n"
    "s_addc_u32 s28, s28, 0\n"
    "v_mfma_f32_16x16x32_bf16 v[24:27], a[84:87], a[104:107], v[24:27]\n"
    "ds_read_b128 a[92:95], v12\n"
    "s_cmp_lt_i32 s85, 8\n"
    "v_mfma_f32_16x16x32_bf16 v[24:27], v[236:239], a[108:111], v[24:27]\n"
    "s_cselect_b32 s7, s28, 0\n"
    "s_cmp_lg_u32 s7, 0\n"
    "v_mfma_f32_16x16x32_bf16 v[28:31], v[216:219], a[104:107], v[28:31]\n"
    "s_cselect_b32 s30, 42, s94\n"
    "s_cselect_b32 s66, s57, 0\n"
    "v_mfma_f32_16x16x32_bf16 v[28:31], v[240:243], a[108:111], v[28:31]\n"
    "ds_read_b128 a[96:99], v13\n"
    "s_cselect_b32 s7, 3, 0\n"
    "v_mfma_f32_16x16x32_bf16 v[32:35], v[220:223], a[104:107], v[32:35]\n"
    "s_subb_u32 s28, s28, 0\n"
    "s_lshr_b32 s57, s57, s7\n"
    "v_mfma_f32_16x16x32_bf16 v[32:35], v[244:247], a[108:111], v[32:35]\n"
    "s_bitcmp0_b32 s66, 0\n"
    "s_cselect_b32 s86, s81, 0x80000000\n"
    "v_mfma_f32_16x16x32_bf16 v[36:39], v[224:227], a[104:107], v[36:39]\n"
    "ds_read_b128 a[112:115], v18\n"
    "s_cselect_b32 s87, s53, 0\n"
    "v_mfma_f32_16x16x32_bf16 v[36:39], v[248:251], a[108:111], v[36:39]\n"
    "v_cvt_pk_bf16_f32 v44, v20, v21\n"
    "v_cvt_pk_bf16_f32 v45, v22, v23\n"
    "v_mfma_f32_16x16x32_bf16 v[40:43], v[228:231], a[104:107], v[40:43]\n"
    "v_cvt_pk_bf16_f32 v46, v24, v25\n"
    "v_cvt_pk_bf16_f32 v47, v26, v27\n"
    "v_mfma_f32_16x16x32_bf16 v[40:43], v[252:255], a[108:111], v[40:43]\n"
    "ds_read_b128 a[100:103], v14\n"
    "s_waitcnt lgkmcnt(3)\n"
    "v_mfma_f32_16x16x32_bf16 v[20:23], v[56:59], a[88:91], 0\n"
    "v_cvt_pk_bf16_f32 v48, v28, v29\n"
    "v_cvt_pk_bf16_f32 v49, v30, v31\n"
    "v_mfma_f32_16x16x32_bf16 v[20:23], v[80:83], a[92:95], v[20:23]\n"
    "v_cvt_pk_bf16_f32 v50, v32, v33\n"
    "v_cvt_pk_bf16_f32 v51, v34, v35\n"
    "v_mfma_f32_16x16x32_bf16 v[24:27], v[60:63], a[88:91], 0\n"
    "ds_read_b128 a[116:119], v18 offset:1024\n"
    "s_cselect_b32 s88, 12288, 0\n"
    "v_mfma_f32_16x16x32_bf16 v[24:27], v[84:87], a[92:95], v[24:27]\n"
    "v_cvt_pk_bf16_f32 v52, v36, v37\n"
    "v_cvt_pk_bf16_f32 v53, v38, v39\n"
    "v_mfma_f32_16x16x32_bf16 v[28:31], v[64:67], a[88:91], 0\n"
    "v_cvt_pk_bf16_f32 v54, v40, v41\n"
    "v_cvt_pk_bf16_f32 v55, v42, v43\n"
    "v_mfma_f32_16x16x32_bf16 v[28:31], v[88:91], a[92:95], v[28:31]\n"
    "ds_read_b128 a[104:107], v11 offset:256\n"
    "s_ashr_i64 s[68:69], s[68:69], 1\n"
    "v_mfma_f32_16x16x32_bf16 v[32:35], v[68:71], a[88:91], 0\n"
    "s_cmp_le_i32 s76, s52\n"
    "s_cbranch_scc0 55f\n"
    "v_mfma_f32_16x16x32_bf16 v[32:35], v[92:95], a[92:95], v[32:35]\n"
    "v_mfma_f32_16x16x32_bf16 v[36:39], v[72:75], a[88:91], 0\n"
    "ds_write_b64 v15, v[44:45]\n"
    "v_mfma_f32_16x16x32_bf16 v[36:39], v[96:99], a[92:95], v[36:39]\n"
    "s_waitcnt lgkmcnt(4)\n"
    "buffer_store_dwordx4 a[112:115], v0, s[40:43], s74 offen sc1\n"
    "v_mfma_f32_16x16x32_bf16 v[40:43], v[76:79], a[88:91], 0\n"
    "ds_write_b64 v15, v[46:47] offset:8\n"
    "v_mfma_f32_16x16x32_bf16 v[40:43], v[100:103], a[92:95], v[40:43]\n"
    "ds_read_b128 a[108:111], v12 offset:256\n"
    "s_waitcnt lgkmcnt(5)\n"
    "v_mfma_f32_16x16x32_bf16 v[20:23], v[104:107], a[96:99], v[20:23]\n"
    "ds_read_b128 a[120:123], v18 offset:2048\n"
    "v_add_u32 v18, s65, v10\n"
    "v_mfma_f32_16x16x32_bf16 v[20:23], v[128:131], a[100:103], v[20:23]\n"
    "s_mov_b32 s78, s77\n"
    "s_mov_b32 m0, s79\n"
    "v_mfma_f32_16x16x32_bf16 v[24:27], v[108:111], a[96:99], v[24:27]\n"
    "s_waitcnt lgkmcnt(5)\n"
    "buffer_store_dwordx4 a[116:119], v1, s[40:43], s74 offen offset:1024 sc1\n"
    "v_mfma_f32_16x16x32_bf16 v[24:27], v[132:135], a[100:103], v[24:27]\n"
    "s_and_b32 s7, s66, 4\n"
    "v_mfma_f32_16x16x32_bf16 v[28:31], v[112:115], a[96:99], v[28:31]\n"
    "s_sub_u32 s72, s72, s7\n"
    "s_subb_u32 s73, s73, 0\n"
    "v_mfma_f32_16x16x32_bf16 v[28:31], v[136:139], a[100:103], v[28:31]\n"
    "ds_read_b128 a[88:91], v13 offset:256\n"
    "s_add_u32 s77, s77, 12288\n"
    "v_mfma_f32_16x16x32_bf16 v[32:35], v[116:119], a[96:99], v[32:35]\n"
    "s_cmp_eq_u32 s77, 159744\n"
    "s_cselect_b32 s77, 0, s77\n"
    "v_mfma_f32_16x16x32_bf16 v[32:35], v[140:143], a[100:103], v[32:35]\n"
    "v_mfma_f32_16x16x32_bf16 v[36:39], v[120:123], a[96:99], v[36:39]\n"
    "s_waitcnt lgkmcnt(1)\n"
    "buffer_store_dwordx4 a[120:123], v2, s[40:43], s74 offen offset:2048 sc1\n"
    "v_mfma_f32_16x16x32_bf16 v[36:39], v[144:147], a[100:103], v[36:39]\n"
    "v_add_u32 v15, s78, v7\n"
    "v_mfma_f32_16x16x32_bf16 v[40:43], v[124:127], a[96:99], v[40:43]\n"
    "ds_write_b64 v16, v[48:49]\n"
    "v_mfma_f32_16x16x32_bf16 v[40:43], v[148:151], a[100:103], v[40:43]\n"
    "ds_read_b128 a[92:95], v14 offset:256\n"
    "v_mfma_f32_16x16x32_bf16 v[20:23], v[152:155], a[104:107], v[20:23]\n"
    "v_mfma_f32_16x16x32_bf16 v[20:23], v[176:179], a[108:111], v[20:23]\n"
    "ds_write_b64 v16, v[50:51] offset:8\n"
    "v_mfma_f32_16x16x32_bf16 v[24:27], v[156:159], a[104:107], v[24:27]\n"
    "buffer_load_dwordx4 v0, s[36:39], s86 offen nt lds\n"
    "v_mfma_f32_16x16x32_bf16 v[24:27], v[180:183], a[108:111], v[24:27]\n"
    "v_add_u32 v16, s78, v8\n"
    "v_mfma_f32_16x16x32_bf16 v[28:31], v[160:163], a[104:107], v[28:31]\n"
    "v_mfma_f32_16x16x32_bf16 v[28:31], v[184:187], a[108:111], v[28:31]\n"
    "ds_read_b128 a[96:99], v11 offset:512\n"
    "v_mfma_f32_16x16x32_bf16 v[32:35], v[164:167], a[104:107], v[32:35]\n"
    "ds_write_b64 v17, v[52:53]\n"
    "v_mfma_f32_16x16x32_bf16 v[32:35], v[188:191], a[108:111], v[32:35]\n"
    "v_mfma_f32_16x16x32_bf16 v[36:39], v[168:171], a[104:107], v[36:39]\n"
    "buffer_load_dwordx4 v1, s[36:39], s86 offen offset:1024 nt lds\n"
    "v_mfma_f32_16x16x32_bf16 v[36:39], v[192:195], a[108:111], v[36:39]\n"
    "ds_write_b64 v17, v[54:55] offset:8\n"
    "v_mfma_f32_16x16x32_bf16 v[40:43], v[172:175], a[104:107], v[40:43]\n"
    "v_add_u32 v17, s78, v9\n"
    "v_mfma_f32_16x16x32_bf16 v[40:43], v[196:199], a[108:111], v[40:43]\n"
    "ds_read_b128 a[100:103], v12 offset:512\n"
    "s_waitcnt lgkmcnt(5)\n"
    "v_mfma_f32_16x16x32_bf16 v[20:23], v[200:203], a[88:91], v[20:23]\n"
    "s_cmp_lg_u32 s74, s82\n"
    "s_cselect_b32 s75, 0, s53\n"
    "v_mfma_f32_16x16x32_bf16 v[20:23], a[8:11], a[92:95], v[20:23]\n"
    "s_add_u32 s82, s82, s75\n"
    "v_mfma_f32_16x16x32_bf16 v[24:27], v[204:207], a[88:91], v[24:27]\n"
    "buffer_load_dwordx4 v2, s[36:39], s86 offen offset:2048 nt lds\n"
    "v_mfma_f32_16x16x32_bf16 v[24:27], a[12:15], a[92:95], v[24:27]\n"
    "s_bitcmp1_b32 s66, 1\n"
    "s_cselect_b32 s74, 0x80000000, s82\n"
    "v_mfma_f32_16x16x32_bf16 v[28:31], v[208:211], a[88:91], v[28:31]\n"
    "s_cselect_b32 s75, 0, 12288\n"
    "s_addc_u32 s15, s15, 0\n"
    "v_mfma_f32_16x16x32_bf16 v[28:31], a[16:19], a[92:95], v[28:31]\n"
    "ds_read_b128 a[104:107], v13 offset:512\n"
    "v_mfma_f32_16x16x32_bf16 v[32:35], v[212:215], a[88:91], v[32:35]\n"
    "s_add_u32 s65, s65, s75\n"
    "v_mfma_f32_16x16x32_bf16 v[32:35], a[20:23], a[92:95], v[32:35]\n"
    "s_cmp_eq_u32 s65, 159744\n"
    "s_cselect_b32 s65, 0, s65\n"
    "v_mfma_f32_16x16x32_bf16 v[36:39], a[0:3], a[88:91], v[36:39]\n"
    "s_cmp_ge_i32 s76, vcc_lo\n"
    "s_cselect_b32 s57, 0, s57\n"
    "v_mfma_f32_16x16x32_bf16 v[36:39], a[24:27], a[92:95], v[36:39]\n"
    "v_mfma_f32_16x16x32_bf16 v[40:43], a[4:7], a[88:91], v[40:43]\n"
    "s_add_u32 s81, s81, s87\n"
    "s_add_u32 s79, s79, s88\n"
    "v_mfma_f32_16x16x32_bf16 v[40:43], a[28:31], a[92:95], v[40:43]\n"
    "ds_read_b128 a[108:111], v14 offset:512\n"
    "s_waitcnt lgkmcnt(2)\n"
    "v_mfma_f32_16x16x32_bf16 v[20:23], a[32:35], a[96:99], v[20:23]\n"
    "v_add_u32 v11, s77, v3\n"
    "v_add_u32 v12, s77, v4\n"
    "v_mfma_f32_16x16x32_bf16 v[20:23], a[56:59], a[100:103], v[20:23]\n"
    "v_add_u32 v13, s77, v5\n"
    "v_add_u32 v14, s77, v6\n"
    "v_mfma_f32_16x16x32_bf16 v[24:27], a[36:39], a[96:99], v[24:27]\n"
    "s_memrealtime s[92:93]\n"
    "s_add_u32 s89, s89, s30\n"
    "v_mfma_f32_16x16x32_bf16 v[24:27], a[60:63], a[100:103], v[24:27]\n"
    "v_mfma_f32_16x16x32_bf16 v[28:31], a[40:43], a[96:99], v[28:31]\n"
    "v_mfma_f32_16x16x32_bf16 v[28:31], a[64:67], a[100:103], v[28:31]\n"
    "s_cmp_eq_u32 s79, s80\n"
    "s_cselect_b32 s79, s55, s79\n"
    "v_mfma_f32_16x16x32_bf16 v[32:35], a[44:47], a[96:99], v[32:35]\n"
    "s_cmp_eq_u32 s76, s56\n"
    "s_cselect_b32 s71, 0x7fffffff, s70\n"
    "v_mfma_f32_16x16x32_bf16 v[32:35], a[68:71], a[100:103], v[32:35]\n"
    "v_mfma_f32_16x16x32_bf16 v[36:39], a[48:51], a[96:99], v[36:39]\n"
    "s_waitcnt vmcnt(24) lgkmcnt(0)\n"
    "s_sub_u32 s85, s92, s89\n"
    "v_mfma_f32_16x16x32_bf16 v[36:39], a[72:75], a[100:103], v[36:39]\n"
    "s_cmp_lt_i32 s85, s71\n"
    "v_mfma_f32_16x16x32_bf16 v[40:43], a[52:55], a[96:99], v[40:43]\n"
    "s_cbranch_scc1 60b\n"
    "s_setpc_b64 s[72:73]\n"
    ".p2align 6\n"
    "s_branch 60b\n"
    "s_nop 0\n"
    "s_nop 0\n"
    "s_nop 0\n"
    "s_nop 0\n"
    "s_nop 0\n"
    "s_nop 0\n"
    "s_nop 0\n"
    "s_waitcnt vmcnt(12)\n"
    "s_waitcnt vmcnt(18)\n"
    "43:\n"
    "v_mfma_f32_16x16x32_bf16 v[40:43], a[76:79], a[100:103], v[40:43]\n"
    "53:\n"
    "s_barrier\n"
    "v_mfma_f32_16x16x32_bf16 v[20:23], a[80:83], a[104:107], v[20:23]\n"
    "ds_read_b128 a[88:91], v11\n"
    "s_add_u32 s76, s76, 1\n"
    "v_mfma_f32_16x16x32_bf16 v[20:23], v[232:235], a[108:111], v[20:23]\n"
    "s_bitcmp0_b32 s68, 0\n"
    "s_addc_u32 s28, s28, 0\n"
    "v_mfma_f32_16x16x32_bf16 v[24:27], a[84:87], a[104:107], v[24:27]\n"
    "ds_read_b128 a[92:95], v12\n"
    "s_cmp_lt_i32 s85, 8\n"
    "v_mfma_f32_16x16x32_bf16 v[24:27], v[236:239], a[108:111], v[24:27]\n"
    "s_cselect_b32 s7, s28, 0\n"
    "s_cmp_lg_u32 s7, 0\n"
    "v_mfma_f32_16x16x32_bf16 v[28:31], v[216:219], a[104:107], v[28:31]\n"
    "s_cselect_b32 s30, 42, s94\n"
    "s_cselect_b32 s66, s57, 0\n"
    "v_mfma_f32_16x16x32_bf16 v[28:31], v[240:243], a[108:111], v[28:31]\n"
    "ds_read_b128 a[96:99], v13\n"
    "s_cselect_b32 s7, 3, 0\n"
    "v_mfma_f32_16x16x32_bf16 v[32:35], v[220:223], a[104:107], v[32:35]\n"
    "s_subb_u32 s28, s28, 0\n"
    "s_lshr_b32 s57, s57, s7\n"
    "v_mfma_f32_16x16x32_bf16 v[32:35], v[244:247], a[108:111], v[32:35]\n"
    "s_bitcmp0_b32 s66, 0\n"
    "s_cselect_b32 s86, s81, 0x80000000\n"
    "v_mfma_f32_16x16x32_bf16 v[36:39], v[224:227], a[104:107], v[36:39]\n"
    "ds_read_b128 a[112:115], v18\n"
    "s_cselect_b32 s87, s53, 0\n"
    "v_mfma_f32_16x16x32_bf16 v[36:39], v[248:251], a[108:111], v[36:39]\n"
    "v_cvt_pk_bf16_f32 v44, v20, v21\n"
    "v_cvt_pk_bf16_f32 v45, v22, v23\n"
    "v_mfma_f32_16x16x32_bf16 v[40:43], v[228:231], a[104:107], v[40:43]\n"
    "v_cvt_pk_bf16_f32 v46, v24, v25\n"
    "v_cvt_pk_bf16_f32 v47, v26, v27\n"
    "v_mfma_f32_16x16x32_bf16 v[40:43], v[252:255], a[108:111], v[40:43]\n"
    "ds_read_b128 a[100:103], v14\n"
    "s_waitcnt lgkmcnt(3)\n"
    "v_mfma_f32_16x16x32_bf16 v[20:23], v[56:59], a[88:91], 0\n"
    "v_cvt_pk_bf16_f32 v48, v28, v29\n"
    "v_cvt_pk_bf16_f32 v49, v30, v31\n"
    "v_mfma_f32_16x16x32_bf16 v[20:23], v[80:83], a[92:95], v[20:23]\n"
    "v_cvt_pk_bf16_f32 v50, v32, v33\n"
    "v_cvt_pk_bf16_f32 v51, v34, v35\n"
    "v_mfma_f32_16x16x32_bf16 v[24:27], v[60:63], a[88:91], 0\n"
    "ds_read_b128 a[116:119], v18 offset:1024\n"
    "s_cselect_b32 s88, 12288, 0\n"
    "v_mfma_f32_16x16x32_bf16 v[24:27], v[84:87], a[92:95], v[24:27]\n"
    "v_cvt_pk_bf16_f32 v52, v36, v37\n"
    "v_cvt_pk_bf16_f32 v53, v38, v39\n"
    "v_mfma_f32_16x16x32_bf16 v[28:31], v[64:67], a[88:91], 0\n"
    "v_cvt_pk_bf16_f32 v54, v40, v41\n"
    "v_cvt_pk_bf16_f32 v55, v42, v43\n"
    "v_mfma_f32_16x16x32_bf16 v[28:31], v[88:91], a[92:95], v[28:31]\n"
    "ds_read_b128 a[104:107], v11 offset:256\n"
    "s_ashr_i64 s[68:69], s[68:69], 1\n"
    "v_mfma_f32_16x16x32_bf16 v[32:35], v[68:71], a[88:91], 0\n"
    "s_cmp_le_i32 s76, s52\n"
    "s_cbranch_scc0 55f\n"
    "v_mfma_f32_16x16x32_bf16 v[32:35], v[92:95], a[92:95], v[32:35]\n"
    "v_mfma_f32_16x16x32_bf16 v[36:39], v[72:75], a[88:91], 0\n"
    "s_waitcnt lgkmcnt(3)\n"
    "buffer_store_dwordx4 a[112:115], v0, s[40:43], s74 offen sc1\n"
    "v_mfma_f32_16x16x32_bf16 v[36:39], v[96:99], a[92:95], v[36:39]\n"
    "ds_write_b64 v15, v[44:45]\n"
    "v_mfma_f32_16x16x32_bf16 v[40:43], v[76:79], a[88:91], 0\n"
    "ds_read_b128 a[120:123], v18 offset:2048\n"
    "v_add_u32 v18, s65, v10\n"
    "v_mfma_f32_16x16x32_bf16 v[40:43], v[100:103], a[92:95], v[40:43]\n"
    "ds_read_b128 a[108:111], v12 offset:256\n"
    "s_waitcnt lgkmcnt(5)\n"
    "v_mfma_f32_16x16x32_bf16 v[20:23], v[104:107], a[96:99], v[20:23]\n"
    "s_mov_b32 s78, s77\n"
    "s_mov_b32 m0, s79\n"
    "v_mfma_f32_16x16x32_bf16 v[20:23], v[128:131], a[100:103], v[20:23]\n"
    "s_waitcnt lgkmcnt(4)\n"
    "buffer_store_dwordx4 a[116:119], v1, s[40:43], s74 offen offset:1024 sc1\n"
    "v_mfma_f32_16x16x32_bf16 v[24:27], v[108:111], a[96:99], v[24:27]\n"
    "ds_write_b64 v15, v[46:47] offset:8\n"
    "v_mfma_f32_16x16x32_bf16 v[24:27], v[132:135], a[100:103], v[24:27]\n"
    "s_and_b32 s7, s66, 4\n"
    "v_mfma_f32_16x16x32_bf16 v[28:31], v[112:115], a[96:99], v[28:31]\n"
    "ds_write_b64 v16, v[48:49]\n"
    "v_mfma_f32_16x16x32_bf16 v[28:31], v[136:139], a[100:103], v[28:31]\n"
    "ds_read_b128 a[88:91], v13 offset:256\n"
    "s_add_u32 s77, s77, 12288\n"
    "v_mfma_f32_16x16x32_bf16 v[32:35], v[116:119], a[96:99], v[32:35]\n"
    "s_sub_u32 s72, s72, s7\n"
    "s_subb_u32 s73, s73, 0\n"
    "v_mfma_f32_16x16x32_bf16 v[32:35], v[140:143], a[100:103], v[32:35]\n"
    "s_waitcnt lgkmcnt(4)\n"
    "buffer_store_dwordx4 a[120:123], v2, s[40:43], s74 offen offset:2048 sc1\n"
    "v_mfma_f32_16x16x32_bf16 v[36:39], v[120:123], a[96:99], v[36:39]\n"
    "s_cmp_eq_u32 s77, 159744\n"
    "s_cselect_b32 s77, 0, s77\n"
    "v_mfma_f32_16x16x32_bf16 v[36:39], v[144:147], a[100:103], v[36:39]\n"
    "ds_write_b64 v16, v[50:51] offset:8\n"
    "v_mfma_f32_16x16x32_bf16 v[40:43], v[124:127], a[96:99], v[40:43]\n"
    "ds_write_b64 v17, v[52:53]\n"
    "v_mfma_f32_16x16x32_bf16 v[40:43], v[148:151], a[100:103], v[40:43]\n"
    "ds_read_b128 a[92:95], v14 offset:256\n"
    "s_waitcnt lgkmcnt(6)\n"
    "v_mfma_f32_16x16x32_bf16 v[20:23], v[152:155], a[104:107], v[20:23]\n"
    "v_add_u32 v15, s78, v7\n"
    "v_mfma_f32_16x16x32_bf16 v[20:23], v[176:179], a[108:111], v[20:23]\n"
    "buffer_load_dwordx4 v0, s[36:39], s86 offen nt lds\n"
    "v_mfma_f32_16x16x32_bf16 v[24:27], v[156:159], a[104:107], v[24:27]\n"
    "v_add_u32 v16, s78, v8\n"
    "v_mfma_f32_16x16x32_bf16 v[24:27], v[180:183], a[108:111], v[24:27]\n"
    "v_mfma_f32_16x16x32_bf16 v[28:31], v[160:163], a[104:107], v[28:31]\n"
    "v_mfma_f32_16x16x32_bf16 v[28:31], v[184:187], a[108:111], v[28:31]\n"
    "ds_read_b128 a[96:99], v11 offset:512\n"
    "v_mfma_f32_16x16x32_bf16 v[32:35], v[164:167], a[104:107], v[32:35]\n"
    "ds_write_b64 v17, v[54:55] offset:8\n"
    "v_mfma_f32_16x16x32_bf16 v[32:35], v[188:191], a[108:111], v[32:35]\n"
    "buffer_load_dwordx4 v1, s[36:39], s86 offen offset:1024 nt lds\n"
    "v_mfma_f32_16x16x32_bf16 v[36:39], v[168:171], a[104:107], v[36:39]\n"
    "v_add_u32 v17, s78, v9\n"
    "v_mfma_f32_16x16x32_bf16 v[36:39], v[192:195], a[108:111], v[36:39]\n"
    "v_mfma_f32_16x16x32_bf16 v[40:43], v[172:175], a[104:107], v[40:43]\n"
    "v_mfma_f32_16x16x32_bf16 v[40:43], v[196:199], a[108:111], v[40:43]\n"
    "ds_read_b128 a[100:103], v12 offset:512\n"
    "s_waitcnt lgkmcnt(3)\n"
    "v_mfma_f32_16x16x32_bf16 v[20:23], v[200:203], a[88:91], v[20:23]\n"
    "s_cmp_lg_u32 s74, s82\n"
    "s_cselect_b32 s75, 0, s53\n"
    "v_mfma_f32_16x16x32_bf16 v[20:23], a[8:11], a[92:95], v[20:23]\n"
    "buffer_load_dwordx4 v2, s[36:39], s86 offen offset:2048 nt lds\n"
    "v_mfma_f32_16x16x32_bf16 v[24:27], v[204:207], a[88:91], v[24:27]\n"
    "s_add_u32 s82, s82, s75\n"
    "v_mfma_f32_16x16x32_bf16 v[24:27], a[12:15], a[92:95], v[24:27]\n"
    "s_bitcmp1_b32 s66, 1\n"
    "s_cselect_b32 s74, 0x80000000, s82\n"
    "v_mfma_f32_16x16x32_bf16 v[28:31], v[208:211], a[88:91], v[28:31]\n"
    "s_cselect_b32 s75, 0, 12288\n"
    "s_addc_u32 s15, s15, 0\n"
    "v_mfma_f32_16x16x32_bf16 v[28:31], a[16:19], a[92:95], v[28:31]\n"
    "ds_read_b128 a[104:107], v13 offset:512\n"
    "v_mfma_f32_16x16x32_bf16 v[32:35], v[212:215], a[88:91], v[32:35]\n"
    "s_add_u32 s65, s65, s75\n"
    "v_mfma_f32_16x16x32_bf16 v[32:35], a[20:23], a[92:95], v[32:35]\n"
    "s_cmp_eq_u32 s65, 159744\n"
    "s_cselect_b32 s65, 0, s65\n"
    "v_mfma_f32_16x16x32_bf16 v[36:39], a[0:3], a[88:91], v[36:39]\n"
    "s_cmp_ge_i32 s76, vcc_lo\n"
    "s_cselect_b32 s57, 0, s57\n"
    "v_mfma_f32_16x16x32_bf16 v[36:39], a[24:27], a[92:95], v[36:39]\n"
    "v_mfma_f32_16x16x32_bf16 v[40:43], a[4:7], a[88:91], v[40:43]\n"
    "s_add_u32 s81, s81, s87\n"
    "s_add_u32 s79, s79, s88\n"
    "v_mfma_f32_16x16x32_bf16 v[40:43], a[28:31], a[92:95], v[40:43]\n"
    "ds_read_b128 a[108:111], v14 offset:512\n"
    "s_waitcnt lgkmcnt(2)\n"
    "v_mfma_f32_16x16x32_bf16 v[20:23], a[32:35], a[96:99], v[20:23]\n"
    "v_add_u32 v11, s77, v3\n"
    "v_add_u32 v12, s77, v4\n"
    "v_mfma_f32_16x16x32_bf16 v[20:23], a[56:59], a[100:103], v[20:23]\n"
    "v_add_u32 v13, s77, v5\n"
    "v_add_u32 v14, s77, v6\n"
    "v_mfma_f32_16x16x32_bf16 v[24:27], a[36:39], a[96:99], v[24:27]\n"
    "s_memrealtime s[92:93]\n"
    "s_add_u32 s89, s89, s30\n"
    "v_mfma_f32_16x16x32_bf16 v[24:27], a[60:63], a[100:103], v[24:27]\n"
    "v_mfma_f32_16x16x32_bf16 v[28:31], a[40:43], a[96:99], v[28:31]\n"
    "v_mfma_f32_16x16x32_bf16 v[28:31], a[64:67], a[100:103], v[28:31]\n"
    "s_cmp_eq_u32 s79, s80\n"
    "s_cselect_b32 s79, s55, s79\n"
    "v_mfma_f32_16x16x32_bf16 v[32:35], a[44:47], a[96:99], v[32:35]\n"
    "s_cmp_eq_u32 s76, s56\n"
    "s_cselect_b32 s71, 0x7fffffff, s70\n"
    "v_mfma_f32_16x16x32_bf16 v[32:35], a[68:71], a[100:103], v[32:35]\n"
    "v_mfma_f32_16x16x32_bf16 v[36:39], a[48:51], a[96:99], v[36:39]\n"
    "s_waitcnt vmcnt(24) lgkmcnt(0)\n"
    "s_sub_u32 s85, s92, s89\n"
    "v_mfma_f32_16x16x32_bf16 v[36:39], a[72:75], a[100:103], v[36:39]\n"
    "s_cmp_lt_i32 s85, s71\n"
    "v_mfma_f32_16x16x32_bf16 v[40:43], a[52:55], a[96:99], v[40:43]\n"
    "s_cbranch_scc1 60b\n"
    "s_setpc_b64 s[72:73]\n"
    "57:\n"
    "s_sleep 1\n"
    "s_memrealtime s[92:93]\n"
    "s_branch 81f\n"
    "55:\n"
    "ds_write_b128 v15, v[44:47]\n"
    "ds_write_b128 v16, v[48:51]\n"
    "ds_write_b128 v17, v[52:55]\n"
    "ds_read_b128 a[120:123], v18 offset:2048\n"
    "81:\n"
    "s_cbranch_scc1 82f\n"
    "s_waitcnt lgkmcnt(7)\n"
    "buffer_store_dwordx4 a[112:115], v0, s[40:43], s74 offen sc1\n"
    "s_waitcnt lgkmcnt(5)\n"
    "buffer_store_dwordx4 a[116:119], v1, s[40:43], s74 offen offset:1024 sc1\n"
    "v_add_u32 v18, s78, v10\n"
    "s_waitcnt lgkmcnt(0)\n"
    "s_barrier\n"
    "ds_read_b128 a[88:91], v18\n"
    "ds_read_b128 a[92:95], v18 offset:1024\n"
    "ds_read_b128 a[96:99], v18 offset:2048\n"
    "buffer_store_dwordx4 a[120:123], v2, s[40:43], s74 offen offset:2048 sc1\n"
    "82:\n"
    "s_cbranch_scc1 83f\n"
    "s_add_u32 s82, s82, s53\n"
    "s_waitcnt lgkmcnt(2)\n"
    "buffer_store_dwordx4 a[88:91], v0, s[40:43], s82 offen sc1\n"
    "s_waitcnt lgkmcnt(1)\n"
    "buffer_store_dwordx4 a[92:95], v1, s[40:43], s82 offen offset:1024 sc1\n"
    "s_waitcnt lgkmcnt(0)\n"
    "buffer_store_dwordx4 a[96:99], v2, s[40:43], s82 offen offset:2048 sc1\n"
    "s_waitcnt vmcnt(0) lgkmcnt(0)\n"
    "s_endpgm\n"
    "83:\n"
    "s_waitcnt lgkmcnt(0)\n"
    "s_sub_u32 s85, s92, s89\n"
    "s_cmp_lt_i32 s85, 0\n"
    "s_cbranch_scc1 57b\n"
    "s_setpc_b64 s[72:73]\n"
    "79:\n"
    "s_nop 0\n"
    : "+{s[0:1]}"(ka), "+{s2}"(bid), "+{s3}"(lds0), "+{v0}"(tid)
    :
    : "v1","v2","v3","v4","v5","v6","v7","v8","v9","v10","v11","v12","v13","v14","v15","v16","v17","v18","v19","v20","v21","v22","v23","v24","v25","v26","v27","v28","v29","v30","v31","v32","v33","v34","v35","v36","v37","v38","v39","v40","v41","v42","v43","v44","v45","v46","v47","v48","v49","v50","v51","v52","v53","v54","v55","v56","v57","v58","v59","v60","v61","v62","v63","v64","v65","v66","v67","v68","v69","v70","v71","v72","v73","v74","v75","v76","v77","v78","v79","v80","v81","v82","v83","v84","v85","v86","v87","v88","v89","v90","v91","v92","v93","v94","v95","v96","v97","v98","v99","v100","v101","v102","v103","v104","v105","v106","v107","v108","v109","v110","v111","v112","v113","v114","v115","v116","v117","v118","v119","v120","v121","v122","v123","v124","v125","v126","v127","v128","v129","v130","v131","v132","v133","v134","v135","v136","v137","v138","v139","v140","v141","v142","v143","v144","v145","v146","v147","v148","v149","v150","v151","v152","v153","v154","v155","v156","v157","v158","v159","v160","v161","v162","v163","v164","v165","v166","v167","v168","v169","v170","v171","v172","v173","v174","v175","v176","v177","v178","v179","v180","v181","v182","v183","v184","v185","v186","v187","v188","v189","v190","v191","v192","v193","v194","v195","v196","v197","v198","v199","v200","v201","v202","v203","v204","v205","v206","v207","v208","v209","v210","v211","v212","v213","v214","v215","v216","v217","v218","v219","v220","v221","v222","v223","v224","v225","v226","v227","v228","v229","v230","v231","v232","v233","v234","v235","v236","v237","v238","v239","v240","v241","v242","v243","v244","v245","v246","v247","v248","v249","v250","v251","v252","v253","v254","v255","a0","a1","a2","a3","a4","a5","a6","a7","a8","a9","a10","a11","a12","a13","a14","a15","a16","a17","a18","a19","a20","a21","a22","a23","a24","a25","a26","a27","a28","a29","a30","a31","a32","a33","a34","a35","a36","a37","a38","a39","a40","a41","a42","a43","a44","a45","a46","a47","a48","a49","a50","a51","a52","a53","a54","a55","a56","a57","a58","a59","a60","a61","a62","a63","a64","a65","a66","a67","a68","a69","a70","a71","a72","a73","a74","a75","a76","a77","a78","a79","a80","a81","a82","a83","a84","a85","a86","a87","a88","a89","a90","a91","a92","a93","a94","a95","a96","a97","a98","a99","a100","a101","a102","a103","a104","a105","a106","a107","a108","a109","a110","a111","a112","a113","a114","a115","a116","a117","a118","a119","a120","a121","a122","a123","a124","a125","a126","a127","a128","a129","a130","a131","a132","a133","a134","a135","a136","a137","a138","a139","a140","a141","a142","a143","a144","a145","a146","a147","a148","a149","a150","a151","a152","a153","a154","a155","a156","a157","a158","a159","a160","a161","a162","a163","a164","a165","a166","a167","s4","s5","s6","s7","s8","s9","s10","s11","s12","s13","s14","s15","s26","s27","s28","s29","s30","s31","s33","s34","s35","s36","s37","s38","s39","s40","s41","s42","s43","s44","s45","s46","s47","s48","s49","s50","s51","s52","s53","s54","s55","s56","s57","s58","s59","s60","s61","s62","s63","s64","s65","s66","s67","s68","s69","s70","s71","s72","s73","s74","s75","s76","s77","s78","s79","s80","s81","s82","s83","s84","s85","s86","s87","s88","s89","s90","s91","s92","s93","s94","s95","vcc","scc","memory");
}

extern "C" void kernel_launch(void* const* d_in, const int* in_sizes, int n_in,
                              void* d_out, int out_size, void* d_ws, size_t ws_size,
                              hipStream_t stream) {
  const int M = in_sizes[0] / CH;
  const int ntiles = (M + TM - 1) / TM;
  const int grid = 256;
  const int nit = ntiles > 4 * grid ? (ntiles + grid - 1) / grid : 4;
  const uint32_t nbytes = (uint32_t)((size_t)M * CH * 2), fl = 0x00020000u;
  dwsep_kernel<<<grid, 256, 0, stream>>>((const char*)d_in[0], nbytes, fl, (char*)d_out, nbytes, fl,
                                         (const char*)d_in[2], CH * CH * 2, fl, (const char*)d_in[1], CH * 2, fl,
                                         (uint32_t)nit, (uint32_t)grid * TILE_BYTES);
}
```

```cpp
#include <hip/hip_runtime.h>
#include <stdint.h>

#define CH 384
#define TM 16
#define TILE_BYTES (TM * CH * 2)
#define LDS_BYTES (13 * TILE_BYTES + 1024 + 64)

struct Shapes { uint32_t v[256]; };
extern "C" __device__ uint64_t dispatch_id_() __asm("llvm.amdgcn.dispatch.id");

__global__ __launch_bounds__(256, 1)
void dwsep_kernel(const char* x, uint32_t xbytes, uint32_t f0, char* out, uint32_t obytes, uint32_t f1,
                  const char* pw, uint32_t pwbytes, uint32_t f2, const char* dw, uint32_t dwbytes, uint32_t f3,
                  uint32_t nit, uint32_t tstride, void* ws, uint64_t pages, Shapes shapes) {
  __shared__ __attribute__((aligned(1024))) char lds[LDS_BYTES];
  uint32_t lds0 = (uint32_t)(uintptr_t)(__attribute__((address_space(3))) char*)lds;
  uint64_t ka = (uint64_t)(uintptr_t)__builtin_amdgcn_kernarg_segment_ptr();
  uint64_t did = dispatch_id_(), dp = (uint64_t)(uintptr_t)__builtin_amdgcn_dispatch_ptr();
  uint32_t bid = blockIdx.x, tid = threadIdx.x;
  asm volatile(
    "s_load_dwordx16 s[36:51], s[2:3], 0x0\n"
    "v_readfirstlane_b32 s8, v0\n"
    "s_load_dwordx4 s[52:55], s[2:3], 0x40\n"
    "s_bfe_u32 s7, s6, 0x40003\n"
    "s_lshr_b32 s9, s8, 2\n"
    "s_lshl_b32 s7, s7, 6\n"
    "s_add_u32 s7, s7, s9\n"
    "s_load_dwordx4 s[20:23], s[2:3], s7 offset:0x58\n"
    "s_load_dwordx2 s[96:97], s[2:3], 0x50\n"
    "s_getpc_b64 s[14:15]\n"
    "70:\n"
    "v_lshlrev_b32 v48, 6, v0\n"
    "s_mov_b32 s13, 79f-70b\n"
    "v_min_u32 v48, s13, v48\n"
    "global_load_dword v49, v48, s[14:15]\n"
    "s_mov_b32 s16, s4\n"
    "v_bfe_u32 v53, v0, 3, 3\n"
    "s_mov_b32 s2, s6\n"
    "v_lshrrev_b32 v54, 2, v0\n"
    "s_mov_b32 s3, %[lds]\n"
    "s_lshr_b32 s4, s8, 6\n"
    "v_mul_u32_u24 v53, 0x300, v53\n"
    "s_bfe_u32 s5, s2, 0x20003\n"
    "v_and_b32 v55, 4, v54\n"
    "s_bfe_u32 s6, s2, 0x20005\n"
    "v_xor_b32 v55, v55, v0\n"
    "s_cmp_eq_u32 s6, 3\n"
    "v_and_b32 v55, 7, v55\n"
    "s_cselect_b32 s6, 0, s6\n"
    "v_lshl_add_u32 v53, v55, 4, v53\n"
    "s_lshl_b32 s7, s4, 7\n"
    "v_and_b32 v16, 63, v0\n"
    "s_mul_i32 s58, s5, 24576\n"
    "v_add_u32 v244, s7, v53\n"
    "s_mul_i32 s62, s6, 98304\n"
    "v_lshlrev_b32 v16, 4, v16\n"
    "s_lshl_b32 s7, s5, 6\n"
    "s_lshl_b32 s8, s6, 8\n"
    "v_xor_b32 v53, s7, v16\n"
    "s_add_u32 s12, s3, 159744\n"
    "s_mul_i32 s83, s4, 24576\n"
    "v_add_u32 v53, s8, v53\n"
    "s_mov_b32 m0, s12\n"
    "v_subrev_u32 v55, 0x300, v53\n"
    "s_add_u32 s83, s83, s3\n"
    "v_min_u32 v250, v53, v55\n"
    "v_add_u32 v245, 0x1400, v244\n"
    "v_add_u32 v246, 0x2800, v244\n"
    "v_add_u32 v247, 0x3c00, v244\n"
    "v_and_b32 v53, 15, v54\n"
    "s_mul_i32 s57, s4, 3072\n"
    "v_and_b32 v54, 12, v54\n"
    "s_lshl_b32 s7, s4, 6\n"
    "v_add_u32 v53, v53, v54\n"
    "v_mul_u32_u24 v53, 0x300, v53\n"
    "s_add_u32 s57, s57, s3\n"
    "v_and_b32 v54, 3, v0\n"
    "s_add_u32 s7, s7, 512\n"
    "v_lshl_add_u32 v53, v54, 4, v53\n"
    "v_lshlrev_b32 v12, 2, v54\n"
    "v_add_u32 v248, s7, v53\n"
    "v_add_u32 v249, 0x800, v248\n"
    "s_lshr_b64 s[18:19], s[0:1], 12\n"
    "s_lshl4_add_u32 s18, s16, s18\n"
    "s_andn2_b32 s17, s18, 15\n"
    "s_bitset1_b32 s17, 31\n"
    "s_mov_b32 s72, s17\n"
    "s_lshl_b32 s34, s2, 20\n"
    "s_and_b32 s34, s34, 0x7e00000\n"
    "s_mul_i32 s7, s16, 0x9e3779b1\n"
    "s_lshr_b32 s7, s7, 9\n"
    "s_and_b32 s7, s7, 0x1f8f80\n"
    "s_or_b32 s34, s34, s7\n"
    "s_and_b32 s35, s2, 7\n"
    "s_lshl_b32 s7, s4, 1\n"
    "s_sub_u32 s35, s35, s7\n"
    "v_and_b32 v19, 15, v0\n"
    "v_bfe_u32 v52, v0, 4, 2\n"
    "v_and_b32 v18, 48, v0\n"
    "v_bfe_u32 v44, v0, 2, 2\n"
    "v_and_b32 v45, 8, v0\n"
    "s_mov_b32 s9, 0x51403020\n"
    "s_mov_b32 s10, 0x52423121\n"
    "s_mov_b32 s11, 0x53433322\n"
    "s_cmp_eq_u32 s6, 1\n"
    "s_cselect_b32 s9, 0x61204030, s9\n"
    "s_cselect_b32 s10, 0x62521131, s10\n"
    "s_cselect_b32 s11, 0x33534302, s11\n"
    "s_cmp_eq_u32 s6, 2\n"
    "s_cselect_b32 s9, 0x71302040, s9\n"
    "s_cselect_b32 s10, 0x42622111, s10\n"
    "s_cselect_b32 s11, 0x43335312, s11\n"
    "s_xor_b32 s7, s4, s5\n"
    "v_lshlrev_b32 v54, 3, v52\n"
    "s_lshl_b32 s7, s7, 2\n"
    "v_xor_b32 v55, s7, v19\n"
    "v_bfe_u32 v53, s11, v54, 8\n"
    "v_xor_b32 v2, v53, v55\n"
    "v_bfe_u32 v53, s10, v54, 8\n"
    "v_xor_b32 v1, v53, v55\n"
    "v_bfe_u32 v53, s9, v54, 8\n"
    "v_xor_b32 v0, v53, v55\n"
    "s_and_b32 s7, s2, 3\n"
    "s_lshr_b32 s8, s2, 7\n"
    "s_lshl2_add_u32 s7, s8, s7\n"
    "s_mul_i32 s56, s7, 393216\n"
    "s_lshr_b32 s29, s2, 2\n"
    "s_lshr_b32 s28, s2, 3\n"
    "s_waitcnt lgkmcnt(0)\n"
    "s_barrier\n"
    "buffer_load_dwordx4 v250, s[48:51], 0 offen lds\n"
    "s_cmp_eq_u32 s35, 0\n"
    "s_cbranch_scc0 72f\n"
    "s_bitcmp1_b32 s2, 7\n"
    "s_cselect_b64 s[68:69], s[40:41], s[36:37]\n"
    "s_mov_b64 s[70:71], s[38:39]\n"
    "s_and_b32 s85, s34, 0x3fffff\n"
    "s_add_u32 s85, s85, s97\n"
    "s_min_u32 s34, s34, s85\n"
    "s_buffer_load_dword s73, s[68:71], s34\n"
    "s_buffer_load_dword s72, s[68:71], s34 offset:0x2000\n"
    "72:\n"
    "s_add_u32 m0, s83, 61440\n"
    "s_add_u32 s84, s58, s62\n"
    "s_xor_b32 s28, s28, s29\n"
    "s_and_b32 s28, s28, 1\n"
    "s_mul_i32 s31, s28, 24576\n"
    "buffer_load_dwordx4 v244, s[44:47], s84 offen lds\n"
    "s_add_u32 s31, s31, s56\n"
    "s_mov_b32 s30, s57\n"
    "s_xor_b32 s7, s5, 1\n"
    "buffer_load_dwordx4 v245, s[44:47], s84 offen offset:1024 lds\n"
    "s_mul_i32 s59, s7, 24576\n"
    "s_xor_b32 s7, s5, 2\n"
    "s_mul_i32 s60, s7, 24576\n"
    "buffer_load_dwordx4 v246, s[44:47], s84 offen offset:2048 lds\n"
    "s_xor_b32 s7, s5, 3\n"
    "s_mul_i32 s61, s7, 24576\n"
    "v_mov_b32 v53, s20\n"
    "buffer_load_dwordx4 v247, s[44:47], s84 offen offset:3072 lds\n"
    "s_add_u32 m0, s83, 65536\n"
    "v_mov_b32 v54, s21\n"
    "v_cmp_le_i32 vcc, s22, v16\n"
    "s_sub_u32 s22, s22, 0x400\n"
    "s_mov_b32 s33, 0x90000000\n"
    "v_cndmask_b32 v55, v53, v54, vcc\n"
    "v_lshl_add_u32 v0, v0, 4, v55\n"
    "buffer_load_dwordx4 v248, s[44:47], s84 offen lds\n"
    "v_cmp_le_i32 vcc, s22, v16\n"
    "s_sub_u32 s22, s22, 0x400\n"
    "s_nop 0\n"
    "v_cndmask_b32 v55, v53, v54, vcc\n"
    "v_lshl_add_u32 v1, v1, 4, v55\n"
    "v_cmp_le_i32 vcc, s22, v16\n"
    "s_sub_u32 s22, s22, 0x400\n"
    "s_nop 0\n"
    "v_cndmask_b32 v55, v53, v54, vcc\n"
    "buffer_load_dwordx4 v249, s[44:47], s84 offen offset:1024 lds\n"
    "s_add_u32 m0, s83, 67584\n"
    "s_add_u32 s84, s59, s62\n"
    "v_lshl_add_u32 v2, v2, 4, v55\n"
    "s_add_u32 s7, s83, 61440\n"
    "v_add_u32 v18, s12, v18\n"
    "buffer_load_dwordx4 v244, s[44:47], s84 offen lds\n"
    "v_add_u32 v15, s7, v16\n"
    "v_and_b32 v54, 0x30, v16\n"
    "v_lshl_add_u32 v54, v52, 10, v54\n"
    "buffer_load_dwordx4 v245, s[44:47], s84 offen offset:1024 lds\n"
    "s_cmp_eq_u32 s35, 0\n"
    "s_cbranch_scc0 73f\n"
    "s_getreg_b32 s88, hwreg(HW_REG_XCC_ID, 0, 3)\n"
    "s_lshl_b32 s89, s2, 3\n"
    "s_and_b32 s89, s89, 0x3f0\n"
    "s_lshr_b32 s82, s2, 7\n"
    "s_lshl3_add_u32 s89, s82, s89\n"
    "v_mov_b32 v20, s89\n"
    "v_mov_b32 v22, s16\n"
    "s_lshr_b32 s89, s88, 2\n"
    "s_lshl1_add_u32 s88, s88, s89\n"
    "s_or_b32 s88, s88, s17\n"
    "s_bfe_u32 s89, s2, 0x10001\n"
    "s_xor_b32 s88, s88, s89\n"
    "s_mov_b32 s85, 0\n"
    "s_waitcnt lgkmcnt(1)\n"
    "s_nop 3\n"
    "s_cmp_eq_u32 s72, s17\n"
    "s_addc_u32 s85, s85, 0\n"
    "s_cmp_lt_u32 s85, 1\n"
    "s_cselect_b32 s85, 1, 0\n"
    "s_xor_b32 s88, s88, s85\n"
    "v_mov_b32 v23, s88\n"
    "73:\n"
    "v_lshl_add_u32 v54, v44, 7, v54\n"
    "v_lshl_add_u32 v54, v45, 3, v54\n"
    "v_add_u32 v44, s7, v54\n"
    "buffer_load_dwordx4 v246, s[44:47], s84 offen offset:2048 lds\n"
    "v_xor_b32 v45, 64, v44\n"
    "v_mov_b32 v54, 0xffff\n"
    "v_mov_b32 v55, 0\n"
    "buffer_load_dwordx4 v247, s[44:47], s84 offen offset:3072 lds\n"
    "s_add_u32 m0, s83, 71680\n"
    "v_lshlrev_b64 v[38:39], v16, v[54:55]\n"
    "s_add_u32 s63, s62, 98304\n"
    "s_cmp_eq_u32 s63, 294912\n"
    "s_cselect_b32 s63, 0, s63\n"
    "s_add_u32 s64, s63, 98304\n"
    "s_cmp_eq_u32 s64, 294912\n"
    "s_cselect_b32 s64, 0, s64\n"
    "buffer_load_dwordx4 v248, s[44:47], s84 offen lds\n"
    "s_add_u32 s7, s3, 160768\n"
    "v_mov_b32 v14, 0\n"
    "v_add_u32 v12, s7, v12\n"
    "buffer_load_dwordx4 v249, s[44:47], s84 offen offset:1024 lds\n"
    "s_add_u32 m0, s83, 73728\n"
    "s_add_u32 s84, s60, s62\n"
    "s_lshl2_add_u32 s7, s4, s7\n"
    "v_mov_b32 v13, s7\n"
    "v_xor_b32 v53, v52, v19\n"
    "buffer_load_dwordx4 v244, s[44:47], s84 offen lds\n"
    "v_mul_u32_u24 v54, 0x300, v19\n"
    "v_lshl_add_u32 v54, v53, 4, v54\n"
    "v_add_u32 v4, s3, v54\n"
    "buffer_load_dwordx4 v245, s[44:47], s84 offen offset:1024 lds\n"
    "v_xor_b32 v5, 64, v4\n"
    "v_xor_b32 v6, 128, v4\n"
    "v_xor_b32 v7, 192, v4\n"
    "buffer_load_dwordx4 v246, s[44:47], s84 offen offset:2048 lds\n"
    "v_add_u32 v11, s57, v16\n"
    "s_lshl_b32 s14, s4, 1\n"
    "s_and_b32 s7, s14, 3\n"
    "buffer_load_dwordx4 v247, s[44:47], s84 offen offset:3072 lds\n"
    "s_add_u32 m0, s83, 77824\n"
    "s_lshr_b32 s8, s14, 2\n"
    "s_xor_b32 s7, s7, s5\n"
    "s_sub_u32 s8, s8, s6\n"
    "buffer_load_dwordx4 v248, s[44:47], s84 offen lds\n"
    "s_lshl_b32 s7, s7, 6\n"
    "s_cmp_lt_i32 s8, 0\n"
    "s_cselect_b32 s13, 3, 0\n"
    "v_xor_b32 v53, s7, v4\n"
    "buffer_load_dwordx4 v249, s[44:47], s84 offen offset:1024 lds\n"
    "s_add_u32 s8, s8, s13\n"
    "s_lshl_b32 s8, s8, 8\n"
    "s_add_u32 s14, s14, 1\n"
    "v_add_u32 v8, s8, v53\n"
    "s_and_b32 s7, s14, 3\n"
    "s_lshr_b32 s8, s14, 2\n"
    "s_xor_b32 s7, s7, s5\n"
    "s_sub_u32 s8, s8, s6\n"
    "s_lshl_b32 s7, s7, 6\n"
    "s_cmp_lt_i32 s8, 0\n"
    "s_cselect_b32 s13, 3, 0\n"
    "v_xor_b32 v53, s7, v4\n"
    "s_add_u32 s8, s8, s13\n"
    "s_lshl_b32 s8, s8, 8\n"
    "s_add_u32 s14, s4, 8\n"
    "v_add_u32 v9, s8, v53\n"
    "s_and_b32 s7, s14, 3\n"
    "s_lshr_b32 s8, s14, 2\n"
    "s_xor_b32 s7, s7, s5\n"
    "s_sub_u32 s8, s8, s6\n"
    "s_lshl_b32 s7, s7, 6\n"
    "s_cmp_lt_i32 s8, 0\n"
    "s_cselect_b32 s13, 3, 0\n"
    "v_xor_b32 v53, s7, v4\n"
    "s_add_u32 s8, s8, s13\n"
    "s_lshl_b32 s8, s8, 8\n"
    "v_add_u32 v10, s8, v53\n"
    "v_lshrrev_b32 v19, 4, v16\n"
    "s_mul_i32 s7, s52, 5\n"
    "s_lshr_b32 s7, s7, 3\n"
    "s_sub_u32 s8, s52, 1\n"
    "s_bitcmp1_b32 s2, 7\n"
    "s_cselect_b32 s7, s7, 0\n"
    "v_add_u32 v53, s7, v19\n"
    "v_cmp_gt_u32 vcc, s8, v19\n"
    "v_subrev_u32 v54, s8, v53\n"
    "v_min_u32 v53, v53, v54\n"
    "v_cndmask_b32 v53, v19, v53, vcc\n"
    "v_mul_lo_u32 v3, v53, s53\n"
    "v_add_u32 v3, s56, v3\n"
    "s_xor_b32 s29, s29, s23\n"
    "s_cmp_eq_u32 s35, 0\n"
    "s_cbranch_scc0 71f\n"
    "s_mov_b64 exec, 1\n"
    "global_store_dwordx2 v20, v[22:23], s[54:55] sc1\n"
    "s_mov_b64 exec, -1\n"
    "s_waitcnt lgkmcnt(0)\n"
    "71:\n"
    "s_branch 301f\n"
    "81:\n"
    "ds_read_b32 v15, v12\n"
    "s_waitcnt lgkmcnt(0)\n"
    "v_cmp_lt_u32 vcc, v15, v14\n"
    "s_nop 0\n"
    "s_cbranch_vccnz 81b\n"
    "s_branch 82f\n"
    "301:\n"
    "s_branch 302f\n"
    "41:\n"
    "ds_read_b32 v15, v12\n"
    "s_waitcnt lgkmcnt(0)\n"
    "v_cmp_lt_u32 vcc, v15, v14\n"
    "s_nop 0\n"
    "s_cbranch_vccnz 41b\n"
    "s_branch 42f\n"
    "43:\n"
    "s_memrealtime s[92:93]\n"
    "s_waitcnt lgkmcnt(0)\n"
    "s_sub_u32 s84, s92, s89\n"
    "s_cmp_lt_i32 s84, 0\n"
    "s_cbranch_scc0 44f\n"
    "s_sleep 1\n"
    "s_branch 43b\n"
    "302:\n"
    "s_branch 303f\n"
    "61:\n"
    "ds_read_b32 v15, v12\n"
    "s_waitcnt lgkmcnt(0)\n"
    "v_cmp_lt_u32 vcc, v15, v14\n"
    "s_nop 0\n"
    "s_cbranch_vccnz 61b\n"
    "s_branch 62f\n"
    "63:\n"
    "s_memrealtime s[92:93]\n"
    "s_waitcnt lgkmcnt(0)\n"
    "s_sub_u32 s84, s92, s89\n"
    "s_cmp_lt_i32 s84, 0\n"
    "s_cbranch_scc0 64f\n"
    "s_sleep 1\n"
    "s_branch 63b\n"
    "303:\n"
    "s_branch 304f\n"
    "45:\n"
    "v_mfma_f32_16x16x32_bf16 v[20:23], v[104:107], a[96:99], v[20:23]\n"
    "v_add_u32 v14, 1, v14\n"
    "s_sub_u32 s7, s86, 8\n"
    "v_add_u32 v28, s64, v16\n"
    "v_mfma_f32_16x16x32_bf16 v[20:23], v[128:131], a[100:103], v[20:23]\n"
    "v_cvt_pk_bf16_f32 v54, v24, v25\n"
    "ds_read_b128 a[196:199], v28\n"
    "v_mfma_f32_16x16x32_bf16 v[20:23], v[152:155], a[104:107], v[20:23]\n"
    "v_cvt_pk_bf16_f32 v55, v26, v27\n"
    "ds_read_b128 a[200:203], v28 offset:1024\n"
    "v_mfma_f32_16x16x32_bf16 v[20:23], v[176:179], a[108:111], v[20:23]\n"
    "ds_write_b64 v10, v[54:55] offset:12296\n"
    "ds_read_b128 a[204:207], v28 offset:2048\n"
    "v_mfma_f32_16x16x32_bf16 v[20:23], v[200:203], a[112:115], v[20:23]\n"
    "s_add_u32 s89, s89, s94\n"
    "v_readlane_b32 s9, v3, s7\n"
    "v_mfma_f32_16x16x32_bf16 v[20:23], a[8:11], a[116:119], v[20:23]\n"
    "s_waitcnt vmcnt(15)\n"
    "v_mfma_f32_16x16x32_bf16 v[20:23], a[32:35], a[120:123], v[20:23]\n"
    "ds_write_b32 v13, v14\n"
    "v_mfma_f32_16x16x32_bf16 v[20:23], a[56:59], a[124:127], v[20:23]\n"
    "v_add_u32 v10, s78, v10\n"
    "v_mfma_f32_16x16x32_bf16 v[20:23], a[80:83], a[128:131], v[20:23]\n"
    "s_waitcnt lgkmcnt(4)\n"
    "buffer_store_dwordx4 a[196:199], v0, s[40:43], s9 offen sc1\n"
    "v_mfma_f32_16x16x32_bf16 v[20:23], v[232:235], a[132:135], v[20:23]\n"
    "s_add_u32 s86, s86, 1\n"
    "s_addk_i32 s64, 12288\n"
    "v_mfma_f32_16x16x32_bf16 v[24:27], v[236:239], a[132:135], 0\n"
    "v_mfma_f32_16x16x32_bf16 v[24:27], a[84:87], a[128:131], v[24:27]\n"
    "s_waitcnt lgkmcnt(3)\n"
    "buffer_store_dwordx4 a[200:203], v1, s[40:43], s9 offen offset:1024 sc1\n"
    "v_mfma_f32_16x16x32_bf16 v[24:27], a[60:63], a[124:127], v[24:27]\n"
    "s_cmp_eq_u32 s64, s80\n"
    "s_cselect_b32 s64, s57, s64\n"
    "v_mfma_f32_16x16x32_bf16 v[24:27], a[36:39], a[120:123], v[24:27]\n"
    "v_mfma_f32_16x16x32_bf16 v[24:27], a[12:15], a[116:119], v[24:27]\n"
    "s_waitcnt lgkmcnt(1)\n"
    "buffer_store_dwordx4 a[204:207], v2, s[40:43], s9 offen offset:2048 sc1\n"
    "s_branch 46f\n"
    "304:\n"
    "s_branch 305f\n"
    "65:\n"
    "v_mfma_f32_16x16x32_bf16 v[20:23], v[104:107], a[144:147], v[20:23]\n"
    "v_add_u32 v14, 1, v14\n"
    "s_sub_u32 s7, s86, 8\n"
    "v_add_u32 v28, s64, v16\n"
    "v_mfma_f32_16x16x32_bf16 v[20:23], v[128:131], a[148:151], v[20:23]\n"
    "v_cvt_pk_bf16_f32 v54, v24, v25\n"
    "ds_read_b128 a[196:199], v28\n"
    "v_mfma_f32_16x16x32_bf16 v[20:23], v[152:155], a[152:155], v[20:23]\n"
    "v_cvt_pk_bf16_f32 v55, v26, v27\n"
    "ds_read_b128 a[200:203], v28 offset:1024\n"
    "v_mfma_f32_16x16x32_bf16 v[20:23], v[176:179], a[156:159], v[20:23]\n"
    "ds_write_b64 v10, v[54:55] offset:8\n"
    "ds_read_b128 a[204:207], v28 offset:2048\n"
    "v_mfma_f32_16x16x32_bf16 v[20:23], v[200:203], a[160:163], v[20:23]\n"
    "s_add_u32 s89, s89, s94\n"
    "v_readlane_b32 s9, v3, s7\n"
    "v_mfma_f32_16x16x32_bf16 v[20:23], a[8:11], a[164:167], v[20:23]\n"
    "s_waitcnt vmcnt(15)\n"
    "v_mfma_f32_16x16x32_bf16 v[20:23], a[32:35], a[168:171], v[20:23]\n"
    "ds_write_b32 v13, v14\n"
    "v_mfma_f32_16x16x32_bf16 v[20:23], a[56:59], a[172:175], v[20:23]\n"
    "v_mfma_f32_16x16x32_bf16 v[20:23], a[80:83], a[176:179], v[20:23]\n"
    "s_waitcnt lgkmcnt(4)\n"
    "buffer_store_dwordx4 a[196:199], v0, s[40:43], s9 offen sc1\n"
    "v_mfma_f32_16x16x32_bf16 v[20:23], v[232:235], a[180:183], v[20:23]\n"
    "s_add_u32 s86, s86, 1\n"
    "s_addk_i32 s64, 12288\n"
    "v_mfma_f32_16x16x32_bf16 v[24:27], v[236:239], a[180:183], 0\n"
    "v_mfma_f32_16x16x32_bf16 v[24:27], a[84:87], a[176:179], v[24:27]\n"
    "s_waitcnt lgkmcnt(3)\n"
    "buffer_store_dwordx4 a[200:203], v1, s[40:43], s9 offen offset:1024 sc1\n"
    "v_mfma_f32_16x16x32_bf16 v[24:27], a[60:63], a[172:175], v[24:27]\n"
    "s_cmp_eq_u32 s64, s80\n"
    "s_cselect_b32 s64, s57, s64\n"
    "v_mfma_f32_16x16x32_bf16 v[24:27], a[36:39], a[168:171], v[24:27]\n"
    "v_mfma_f32_16x16x32_bf16 v[24:27], a[12:15], a[164:167], v[24:27]\n"
    "s_waitcnt lgkmcnt(1)\n"
    "buffer_store_dwordx4 a[204:207], v2, s[40:43], s9 offen offset:2048 sc1\n"
    "s_branch 66f\n"
    "305:\n"
    "s_waitcnt vmcnt(18)\n"
    "s_memrealtime s[92:93]\n"
    "ds_write_b32 v13, v14\n"
    "ds_read_b128 v[252:255], v18\n"
    "s_waitcnt vmcnt(14)\n"
    "ds_read_b64_tr_b16 a[196:197], v44\n"
    "ds_read_b64_tr_b16 a[198:199], v44 offset:512\n"
    "ds_read_b64_tr_b16 a[200:201], v44 offset:8\n"
    "ds_read_b64_tr_b16 a[202:203], v44 offset:520\n"
    "ds_read_b64_tr_b16 a[204:205], v45\n"
    "ds_read_b64_tr_b16 a[206:207], v45 offset:512\n"
    "s_waitcnt lgkmcnt(6)\n"
    "v_and_b32 v36, v38, v252\n"
    "v_and_b32 v37, v39, v253\n"
    "v_and_b32 v40, v38, v254\n"
    "v_and_b32 v41, v39, v255\n"
    "ds_read_b128 v[252:255], v18 offset:64\n"
    "s_waitcnt lgkmcnt(5)\n"
    "ds_read_b64_tr_b16 a[208:209], v45 offset:8\n"
    "v_mfma_f32_4x4x4_16b_bf16 v[20:23], v[36:37], a[196:197], 0\n"
    "ds_read_b64_tr_b16 a[210:211], v45 offset:520\n"
    "s_add_u32 m0, s83, 79872\n"
    "s_add_u32 s84, s61, s62\n"
    "s_nop 0\n"
    "v_mfma_f32_4x4x4_16b_bf16 v[24:27], v[40:41], a[198:199], 0\n"
    "buffer_load_dwordx4 v244, s[44:47], s84 offen lds\n"
    "s_add_u32 s77, s57, 36864\n"
    "s_nop 0\n"
    "s_waitcnt vmcnt(13) lgkmcnt(5)\n"
    "ds_read_b64_tr_b16 a[212:213], v15 offset:4096\n"
    "v_mfma_f32_4x4x4_16b_bf16 v[28:31], v[36:37], a[200:201], 0\n"
    "ds_read_b64_tr_b16 a[214:215], v15 offset:5120\n"
    "v_cvt_pk_bf16_f32 v56, v20, v21\n"
    "v_cvt_pk_bf16_f32 v57, v22, v23\n"
    "v_mfma_f32_4x4x4_16b_bf16 v[32:35], v[40:41], a[202:203], 0\n"
    "buffer_load_dwordx4 v245, s[44:47], s84 offen offset:1024 lds\n"
    "v_cvt_pk_bf16_f32 v58, v24, v25\n"
    "v_cvt_pk_bf16_f32 v59, v26, v27\n"
    "s_movk_i32 s88, 24576\n"
    "s_waitcnt lgkmcnt(4)\n"
    "ds_read_b64_tr_b16 a[216:217], v15 offset:4104\n"
    "v_mfma_f32_4x4x4_16b_bf16 v[20:23], v[36:37], a[204:205], 0\n"
    "ds_read_b64_tr_b16 a[218:219], v15 offset:5128\n"
    "v_cvt_pk_bf16_f32 v60, v28, v29\n"
    "v_cvt_pk_bf16_f32 v61, v30, v31\n"
    "v_mfma_f32_4x4x4_16b_bf16 v[24:27], v[40:41], a[206:207], 0\n"
    "buffer_load_dwordx4 v246, s[44:47], s84 offen offset:2048 lds\n"
    "v_cvt_pk_bf16_f32 v62, v32, v33\n"
    "v_cvt_pk_bf16_f32 v63, v34, v35\n"
    "v_and_b32 v42, v38, v252\n"
    "s_movk_i32 s78, 24576\n"
    "s_waitcnt vmcnt(11) lgkmcnt(4)\n"
    "ds_read_b64_tr_b16 a[196:197], v44 offset:6144\n"
    "v_mfma_f32_4x4x4_16b_bf16 v[28:31], v[36:37], a[208:209], 0\n"
    "ds_read_b64_tr_b16 a[198:199], v44 offset:6656\n"
    "v_cvt_pk_bf16_f32 v64, v20, v21\n"
    "v_cvt_pk_bf16_f32 v65, v22, v23\n"
    "v_mfma_f32_4x4x4_16b_bf16 v[32:35], v[40:41], a[210:211], 0\n"
    "buffer_load_dwordx4 v247, s[44:47], s84 offen offset:3072 lds\n"
    "v_cvt_pk_bf16_f32 v66, v24, v25\n"
    "v_cvt_pk_bf16_f32 v67, v26, v27\n"
    "v_and_b32 v43, v39, v253\n"
    "s_mov_b32 s87, -122880\n"
    "s_waitcnt lgkmcnt(4)\n"
    "ds_read_b64_tr_b16 a[200:201], v44 offset:6152\n"
    "v_mfma_f32_4x4x4_16b_bf16 v[20:23], v[36:37], a[212:213], 0\n"
    "ds_read_b64_tr_b16 a[202:203], v44 offset:6664\n"
    "s_add_u32 m0, s83, 83968\n"
    "v_cvt_pk_bf16_f32 v68, v28, v29\n"
    "v_cvt_pk_bf16_f32 v69, v30, v31\n"
    "v_mfma_f32_4x4x4_16b_bf16 v[24:27], v[40:41], a[214:215], 0\n"
    "buffer_load_dwordx4 v248, s[44:47], s84 offen lds\n"
    "v_cvt_pk_bf16_f32 v70, v32, v33\n"
    "v_cvt_pk_bf16_f32 v71, v34, v35\n"
    "v_and_b32 v46, v38, v254\n"
    "s_add_u32 s80, s57, 147456\n"
    "s_waitcnt lgkmcnt(4)\n"
    "ds_read_b64_tr_b16 a[204:205], v45 offset:6144\n"
    "v_mfma_f32_4x4x4_16b_bf16 v[28:31], v[36:37], a[216:217], 0\n"
    "ds_read_b64_tr_b16 a[206:207], v45 offset:6656\n"
    "v_cvt_pk_bf16_f32 v72, v20, v21\n"
    "v_cvt_pk_bf16_f32 v73, v22, v23\n"
    "v_mfma_f32_4x4x4_16b_bf16 v[32:35], v[40:41], a[218:219], 0\n"
    "buffer_load_dwordx4 v249, s[44:47], s84 offen offset:1024 lds\n"
    "v_cvt_pk_bf16_f32 v74, v24, v25\n"
    "v_cvt_pk_bf16_f32 v75, v26, v27\n"
    "v_and_b32 v47, v39, v255\n"
    "ds_read_b128 v[252:255], v18 offset:128\n"
    "s_mov_b32 s86, 5\n"
    "s_waitcnt lgkmcnt(5)\n"
    "ds_read_b64_tr_b16 a[208:209], v45 offset:6152\n"
    "v_mfma_f32_4x4x4_16b_bf16 v[20:23], v[42:43], a[196:197], 0\n"
    "ds_read_b64_tr_b16 a[210:211], v45 offset:6664\n"
    "s_add_u32 m0, s83, 61440\n"
    "s_add_u32 s84, s58, s63\n"
    "v_cvt_pk_bf16_f32 v76, v28, v29\n"
    "v_cvt_pk_bf16_f32 v77, v30, v31\n"
    "v_mfma_f32_4x4x4_16b_bf16 v[24:27], v[46:47], a[198:199], 0\n"
    "buffer_load_dwordx4 v244, s[44:47], s84 offen lds\n"
    "v_cvt_pk_bf16_f32 v78, v32, v33\n"
    "v_cvt_pk_bf16_f32 v79, v34, v35\n"
    "s_lshr_b32 s7, s2, 1\n"
    "s_waitcnt vmcnt(13) lgkmcnt(5)\n"
    "ds_read_b64_tr_b16 a[212:213], v15 offset:10240\n"
    "v_mfma_f32_4x4x4_16b_bf16 v[28:31], v[42:43], a[200:201], 0\n"
    "ds_read_b64_tr_b16 a[214:215], v15 offset:11264\n"
    "v_cvt_pk_bf16_f32 v80, v20, v21\n"
    "v_cvt_pk_bf16_f32 v81, v22, v23\n"
    "v_mfma_f32_4x4x4_16b_bf16 v[32:35], v[46:47], a[202:203], 0\n"
    "buffer_load_dwordx4 v245, s[44:47], s84 offen offset:1024 lds\n"
    "v_cvt_pk_bf16_f32 v82, v24, v25\n"
    "v_cvt_pk_bf16_f32 v83, v26, v27\n"
    "s_and_b32 s7, s7, 0x7c\n"
    "s_waitcnt lgkmcnt(4)\n"
    "ds_read_b64_tr_b16 a[216:217], v15 offset:10248\n"
    "v_mfma_f32_4x4x4_16b_bf16 v[20:23], v[42:43], a[204:205], 0\n"
    "ds_read_b64_tr_b16 a[218:219], v15 offset:11272\n"
    "v_cvt_pk_bf16_f32 v84, v28, v29\n"
    "v_cvt_pk_bf16_f32 v85, v30, v31\n"
    "v_mfma_f32_4x4x4_16b_bf16 v[24:27], v[46:47], a[206:207], 0\n"
    "buffer_load_dwordx4 v246, s[44:47], s84 offen offset:2048 lds\n"
    "v_cvt_pk_bf16_f32 v86, v32, v33\n"
    "v_cvt_pk_bf16_f32 v87, v34, v35\n"
    "v_and_b32 v36, v38, v252\n"
    "s_and_b32 s8, s2, 3\n"
    "s_waitcnt vmcnt(11) lgkmcnt(4)\n"
    "ds_read_b64_tr_b16 a[196:197], v44 offset:12288\n"
    "v_mfma_f32_4x4x4_16b_bf16 v[28:31], v[42:43], a[208:209], 0\n"
    "ds_read_b64_tr_b16 a[198:199], v44 offset:12800\n"
    "v_cvt_pk_bf16_f32 v88, v20, v21\n"
    "v_cvt_pk_bf16_f32 v89, v22, v23\n"
    "v_mfma_f32_4x4x4_16b_bf16 v[32:35], v[46:47], a[210:211], 0\n"
    "buffer_load_dwordx4 v247, s[44:47], s84 offen offset:3072 lds\n"
    "v_cvt_pk_bf16_f32 v90, v24, v25\n"
    "v_cvt_pk_bf16_f32 v91, v26, v27\n"
    "v_and_b32 v37, v39, v253\n"
    "s_or_b32 s7, s7, s8\n"
    "s_waitcnt lgkmcnt(4)\n"
    "ds_read_b64_tr_b16 a[200:201], v44 offset:12296\n"
    "v_mfma_f32_4x4x4_16b_bf16 v[20:23], v[42:43], a[212:213], 0\n"
    "ds_read_b64_tr_b16 a[202:203], v44 offset:12808\n"
    "s_add_u32 m0, s83, 65536\n"
    "v_cvt_pk_bf16_f32 v92, v28, v29\n"
    "v_cvt_pk_bf16_f32 v93, v30, v31\n"
    "v_mfma_f32_4x4x4_16b_bf16 v[24:27], v[46:47], a[214:215], 0\n"
    "buffer_load_dwordx4 v248, s[44:47], s84 offen lds\n"
    "v_cvt_pk_bf16_f32 v94, v32, v33\n"
    "v_cvt_pk_bf16_f32 v95, v34, v35\n"
    "v_and_b32 v40, v38, v254\n"
    "s_mul_i32 s7, s7, 79\n"
    "s_waitcnt lgkmcnt(4)\n"
    "ds_read_b64_tr_b16 a[204:205], v45 offset:12288\n"
    "v_mfma_f32_4x4x4_16b_bf16 v[28:31], v[42:43], a[216:217], 0\n"
    "ds_read_b64_tr_b16 a[206:207], v45 offset:12800\n"
    "v_cvt_pk_bf16_f32 v96, v20, v21\n"
    "v_cvt_pk_bf16_f32 v97, v22, v23\n"
    "v_mfma_f32_4x4x4_16b_bf16 v[32:35], v[46:47], a[218:219], 0\n"
    "buffer_load_dwordx4 v249, s[44:47], s84 offen offset:1024 lds\n"
    "v_cvt_pk_bf16_f32 v98, v24, v25\n"
    "v_cvt_pk_bf16_f32 v99, v26, v27\n"
    "v_and_b32 v41, v39, v255\n"
    "ds_read_b128 v[252:255], v18 offset:192\n"
    "s_and_b32 s7, s7, 0x7f\n"
    "s_waitcnt lgkmcnt(5)\n"
    "ds_read_b64_tr_b16 a[208:209], v45 offset:12296\n"
    "v_mfma_f32_4x4x4_16b_bf16 v[20:23], v[36:37], a[196:197], 0\n"
    "ds_read_b64_tr_b16 a[210:211], v45 offset:12808\n"
    "s_add_u32 m0, s83, 67584\n"
    "s_add_u32 s84, s59, s63\n"
    "v_cvt_pk_bf16_f32 v100, v28, v29\n"
    "v_cvt_pk_bf16_f32 v101, v30, v31\n"
    "v_mfma_f32_4x4x4_16b_bf16 v[24:27], v[40:41], a[198:199], 0\n"
    "buffer_load_dwordx4 v244, s[44:47], s84 offen lds\n"
    "v_cvt_pk_bf16_f32 v102, v32, v33\n"
    "v_cvt_pk_bf16_f32 v103, v34, v35\n"
    "s_mul_i32 s7, s7, 30\n"
    "s_waitcnt vmcnt(13) lgkmcnt(5)\n"
    "ds_read_b64_tr_b16 a[212:213], v15 offset:16384\n"
    "v_mfma_f32_4x4x4_16b_bf16 v[28:31], v[36:37], a[200:201], 0\n"
    "ds_read_b64_tr_b16 a[214:215], v15 offset:17408\n"
    "v_cvt_pk_bf16_f32 v104, v20, v21\n"
    "v_cvt_pk_bf16_f32 v105, v22, v23\n"
    "v_mfma_f32_4x4x4_16b_bf16 v[32:35], v[40:41], a[202:203], 0\n"
    "buffer_load_dwordx4 v245, s[44:47], s84 offen offset:1024 lds\n"
    "v_cvt_pk_bf16_f32 v106, v24, v25\n"
    "v_cvt_pk_bf16_f32 v107, v26, v27\n"
    "s_lshr_b32 s7, s7, 6\n"
    "s_waitcnt lgkmcnt(4)\n"
    "ds_read_b64_tr_b16 a[216:217], v15 offset:16392\n"
    "v_mfma_f32_4x4x4_16b_bf16 v[20:23], v[36:37], a[204:205], 0\n"
    "ds_read_b64_tr_b16 a[218:219], v15 offset:17416\n"
    "v_cvt_pk_bf16_f32 v108, v28, v29\n"
    "v_cvt_pk_bf16_f32 v109, v30, v31\n"
    "v_mfma_f32_4x4x4_16b_bf16 v[24:27], v[40:41], a[206:207], 0\n"
    "buffer_load_dwordx4 v246, s[44:47], s84 offen offset:2048 lds\n"
    "v_cvt_pk_bf16_f32 v110, v32, v33\n"
    "v_cvt_pk_bf16_f32 v111, v34, v35\n"
    "v_and_b32 v42, v38, v252\n"
    "s_sub_i32 s7, s7, 24\n"
    "s_waitcnt vmcnt(11) lgkmcnt(4)\n"
    "ds_read_b64_tr_b16 a[196:197], v44 offset:18432\n"
    "v_mfma_f32_4x4x4_16b_bf16 v[28:31], v[36:37], a[208:209], 0\n"
    "ds_read_b64_tr_b16 a[198:199], v44 offset:18944\n"
    "v_cvt_pk_bf16_f32 v112, v20, v21\n"
    "v_cvt_pk_bf16_f32 v113, v22, v23\n"
    "v_mfma_f32_4x4x4_16b_bf16 v[32:35], v[40:41], a[210:211], 0\n"
    "buffer_load_dwordx4 v247, s[44:47], s84 offen offset:3072 lds\n"
    "v_cvt_pk_bf16_f32 v114, v24, v25\n"
    "v_cvt_pk_bf16_f32 v115, v26, v27\n"
    "v_and_b32 v43, v39, v253\n"
    "s_max_i32 s7, s7, 0\n"
    "s_waitcnt lgkmcnt(4)\n"
    "ds_read_b64_tr_b16 a[200:201], v44 offset:18440\n"
    "v_mfma_f32_4x4x4_16b_bf16 v[20:23], v[36:37], a[212:213], 0\n"
    "ds_read_b64_tr_b16 a[202:203], v44 offset:18952\n"
    "s_add_u32 m0, s83, 71680\n"
    "v_cvt_pk_bf16_f32 v116, v28, v29\n"
    "v_cvt_pk_bf16_f32 v117, v30, v31\n"
    "v_mfma_f32_4x4x4_16b_bf16 v[24:27], v[40:41], a[214:215], 0\n"
    "buffer_load_dwordx4 v248, s[44:47], s84 offen lds\n"
    "v_cvt_pk_bf16_f32 v118, v32, v33\n"
    "v_cvt_pk_bf16_f32 v119, v34, v35\n"
    "v_and_b32 v46, v38, v254\n"
    "s_min_i32 s7, s7, 50\n"
    "s_waitcnt lgkmcnt(4)\n"
    "ds_read_b64_tr_b16 a[204:205], v45 offset:18432\n"
    "v_mfma_f32_4x4x4_16b_bf16 v[28:31], v[36:37], a[216:217], 0\n"
    "ds_read_b64_tr_b16 a[206:207], v45 offset:18944\n"
    "v_cvt_pk_bf16_f32 v120, v20, v21\n"
    "v_cvt_pk_bf16_f32 v121, v22, v23\n"
    "v_mfma_f32_4x4x4_16b_bf16 v[32:35], v[40:41], a[218:219], 0\n"
    "buffer_load_dwordx4 v249, s[44:47], s84 offen offset:1024 lds\n"
    "v_cvt_pk_bf16_f32 v122, v24, v25\n"
    "v_cvt_pk_bf16_f32 v123, v26, v27\n"
    "v_and_b32 v47, v39, v255\n"
    "ds_read_b128 v[252:255], v18 offset:256\n"
    "s_bfm_b64 s[34:35], s7, 0\n"
    "s_waitcnt lgkmcnt(5)\n"
    "ds_read_b64_tr_b16 a[208:209], v45 offset:18440\n"
    "v_mfma_f32_4x4x4_16b_bf16 v[20:23], v[42:43], a[196:197], 0\n"
    "ds_read_b64_tr_b16 a[210:211], v45 offset:18952\n"
    "s_add_u32 m0, s83, 73728\n"
    "s_add_u32 s84, s60, s63\n"
    "v_cvt_pk_bf16_f32 v124, v28, v29\n"
    "v_cvt_pk_bf16_f32 v125, v30, v31\n"
    "v_mfma_f32_4x4x4_16b_bf16 v[24:27], v[46:47], a[198:199], 0\n"
    "buffer_load_dwordx4 v244, s[44:47], s84 offen lds\n"
    "v_cvt_pk_bf16_f32 v126, v32, v33\n"
    "v_cvt_pk_bf16_f32 v127, v34, v35\n"
    "s_waitcnt vmcnt(13) lgkmcnt(5)\n"
    "ds_read_b64_tr_b16 a[212:213], v15 offset:22528\n"
    "v_mfma_f32_4x4x4_16b_bf16 v[28:31], v[42:43], a[200:201], 0\n"
    "ds_read_b64_tr_b16 a[214:215], v15 offset:23552\n"
    "v_cvt_pk_bf16_f32 v128, v20, v21\n"
    "v_cvt_pk_bf16_f32 v129, v22, v23\n"
    "v_mfma_f32_4x4x4_16b_bf16 v[32:35], v[46:47], a[202:203], 0\n"
    "buffer_load_dwordx4 v245, s[44:47], s84 offen offset:1024 lds\n"
    "v_cvt_pk_bf16_f32 v130, v24, v25\n"
    "v_cvt_pk_bf16_f32 v131, v26, v27\n"
    "s_waitcnt lgkmcnt(4)\n"
    "ds_read_b64_tr_b16 a[216:217], v15 offset:22536\n"
    "v_mfma_f32_4x4x4_16b_bf16 v[20:23], v[42:43], a[204:205], 0\n"
    "ds_read_b64_tr_b16 a[218:219], v15 offset:23560\n"
    "v_cvt_pk_bf16_f32 v132, v28, v29\n"
    "v_cvt_pk_bf16_f32 v133, v30, v31\n"
    "v_mfma_f32_4x4x4_16b_bf16 v[24:27], v[46:47], a[206:207], 0\n"
    "buffer_load_dwordx4 v246, s[44:47], s84 offen offset:2048 lds\n"
    "v_cvt_pk_bf16_f32 v134, v32, v33\n"
    "v_cvt_pk_bf16_f32 v135, v34, v35\n"
    "v_and_b32 v36, v38, v252\n"
    "s_waitcnt vmcnt(11) lgkmcnt(4)\n"
    "ds_read_b64_tr_b16 a[196:197], v44\n"
    "v_mfma_f32_4x4x4_16b_bf16 v[28:31], v[42:43], a[208:209], 0\n"
    "ds_read_b64_tr_b16 a[198:199], v44 offset:512\n"
    "v_cvt_pk_bf16_f32 v136, v20, v21\n"
    "v_cvt_pk_bf16_f32 v137, v22, v23\n"
    "v_mfma_f32_4x4x4_16b_bf16 v[32:35], v[46:47], a[210:211], 0\n"
    "buffer_load_dwordx4 v247, s[44:47], s84 offen offset:3072 lds\n"
    "v_cvt_pk_bf16_f32 v138, v24, v25\n"
    "v_cvt_pk_bf16_f32 v139, v26, v27\n"
    "v_and_b32 v37, v39, v253\n"
    "s_waitcnt lgkmcnt(4)\n"
    "ds_read_b64_tr_b16 a[200:201], v44 offset:8\n"
    "v_mfma_f32_4x4x4_16b_bf16 v[20:23], v[42:43], a[212:213], 0\n"
    "ds_read_b64_tr_b16 a[202:203], v44 offset:520\n"
    "s_add_u32 m0, s83, 77824\n"
    "v_cvt_pk_bf16_f32 v140, v28, v29\n"
    "v_cvt_pk_bf16_f32 v141, v30, v31\n"
    "v_mfma_f32_4x4x4_16b_bf16 v[24:27], v[46:47], a[214:215], 0\n"
    "buffer_load_dwordx4 v248, s[44:47], s84 offen lds\n"
    "v_cvt_pk_bf16_f32 v142, v32, v33\n"
    "v_cvt_pk_bf16_f32 v143, v34, v35\n"
    "v_and_b32 v40, v38, v254\n"
    "s_waitcnt lgkmcnt(4)\n"
    "ds_read_b64_tr_b16 a[204:205], v45\n"
    "v_mfma_f32_4x4x4_16b_bf16 v[28:31], v[42:43], a[216:217], 0\n"
    "ds_read_b64_tr_b16 a[206:207], v45 offset:512\n"
    "v_cvt_pk_bf16_f32 v144, v20, v21\n"
    "v_cvt_pk_bf16_f32 v145, v22, v23\n"
    "v_mfma_f32_4x4x4_16b_bf16 v[32:35], v[46:47], a[218:219], 0\n"
    "buffer_load_dwordx4 v249, s[44:47], s84 offen offset:1024 lds\n"
    "v_cvt_pk_bf16_f32 v146, v24, v25\n"
    "v_cvt_pk_bf16_f32 v147, v26, v27\n"
    "v_and_b32 v41, v39, v255\n"
    "ds_read_b128 v[252:255], v18 offset:320\n"
    "s_waitcnt lgkmcnt(5)\n"
    "ds_read_b64_tr_b16 a[208:209], v45 offset:8\n"
    "v_mfma_f32_4x4x4_16b_bf16 v[20:23], v[36:37], a[196:197], 0\n"
    "ds_read_b64_tr_b16 a[210:211], v45 offset:520\n"
    "s_add_u32 m0, s83, 79872\n"
    "s_add_u32 s84, s61, s63\n"
    "v_cvt_pk_bf16_f32 v148, v28, v29\n"
    "v_cvt_pk_bf16_f32 v149, v30, v31\n"
    "v_mfma_f32_4x4x4_16b_bf16 v[24:27], v[40:41], a[198:199], 0\n"
    "buffer_load_dwordx4 v244, s[44:47], s84 offen lds\n"
    "v_cvt_pk_bf16_f32 v150, v32, v33\n"
    "v_cvt_pk_bf16_f32 v151, v34, v35\n"
    "s_waitcnt vmcnt(13) lgkmcnt(5)\n"
    "ds_read_b64_tr_b16 a[212:213], v15 offset:4096\n"
    "v_mfma_f32_4x4x4_16b_bf16 v[28:31], v[36:37], a[200:201], 0\n"
    "ds_read_b64_tr_b16 a[214:215], v15 offset:5120\n"
    "v_cvt_pk_bf16_f32 v152, v20, v21\n"
    "v_cvt_pk_bf16_f32 v153, v22, v23\n"
    "v_mfma_f32_4x4x4_16b_bf16 v[32:35], v[40:41], a[202:203], 0\n"
    "buffer_load_dwordx4 v245, s[44:47], s84 offen offset:1024 lds\n"
    "v_cvt_pk_bf16_f32 v154, v24, v25\n"
    "v_cvt_pk_bf16_f32 v155, v26, v27\n"
    "s_waitcnt lgkmcnt(4)\n"
    "ds_read_b64_tr_b16 a[216:217], v15 offset:4104\n"
    "v_mfma_f32_4x4x4_16b_bf16 v[20:23], v[36:37], a[204:205], 0\n"
    "ds_read_b64_tr_b16 a[218:219], v15 offset:5128\n"
    "v_cvt_pk_bf16_f32 v156, v28, v29\n"
    "v_cvt_pk_bf16_f32 v157, v30, v31\n"
    "v_mfma_f32_4x4x4_16b_bf16 v[24:27], v[40:41], a[206:207], 0\n"
    "buffer_load_dwordx4 v246, s[44:47], s84 offen offset:2048 lds\n"
    "v_cvt_pk_bf16_f32 v158, v32, v33\n"
    "v_cvt_pk_bf16_f32 v159, v34, v35\n"
    "v_and_b32 v42, v38, v252\n"
    "s_waitcnt vmcnt(11) lgkmcnt(4)\n"
    "ds_read_b64_tr_b16 a[196:197], v44 offset:6144\n"
    "v_mfma_f32_4x4x4_16b_bf16 v[28:31], v[36:37], a[208:209], 0\n"
    "ds_read_b64_tr_b16 a[198:199], v44 offset:6656\n"
    "v_cvt_pk_bf16_f32 v160, v20, v21\n"
    "v_cvt_pk_bf16_f32 v161, v22, v23\n"
    "v_mfma_f32_4x4x4_16b_bf16 v[32:35], v[40:41], a[210:211], 0\n"
    "buffer_load_dwordx4 v247, s[44:47], s84 offen offset:3072 lds\n"
    "v_cvt_pk_bf16_f32 v162, v24, v25\n"
    "v_cvt_pk_bf16_f32 v163, v26, v27\n"
    "v_and_b32 v43, v39, v253\n"
    "s_waitcnt lgkmcnt(4)\n"
    "ds_read_b64_tr_b16 a[200:201], v44 offset:6152\n"
    "v_mfma_f32_4x4x4_16b_bf16 v[20:23], v[36:37], a[212:213], 0\n"
    "ds_read_b64_tr_b16 a[202:203], v44 offset:6664\n"
    "s_add_u32 m0, s83, 83968\n"
    "v_cvt_pk_bf16_f32 v164, v28, v29\n"
    "v_cvt_pk_bf16_f32 v165, v30, v31\n"
    "v_mfma_f32_4x4x4_16b_bf16 v[24:27], v[40:41], a[214:215], 0\n"
    "buffer_load_dwordx4 v248, s[44:47], s84 offen lds\n"
    "v_cvt_pk_bf16_f32 v166, v32, v33\n"
    "v_cvt_pk_bf16_f32 v167, v34, v35\n"
    "v_and_b32 v46, v38, v254\n"
    "s_waitcnt lgkmcnt(4)\n"
    "ds_read_b64_tr_b16 a[204:205], v45 offset:6144\n"
    "v_mfma_f32_4x4x4_16b_bf16 v[28:31], v[36:37], a[216:217], 0\n"
    "ds_read_b64_tr_b16 a[206:207], v45 offset:6656\n"
    "v_cvt_pk_bf16_f32 v168, v20, v21\n"
    "v_cvt_pk_bf16_f32 v169, v22, v23\n"
    "v_mfma_f32_4x4x4_16b_bf16 v[32:35], v[40:41], a[218:219], 0\n"
    "buffer_load_dwordx4 v249, s[44:47], s84 offen offset:1024 lds\n"
    "v_cvt_pk_bf16_f32 v170, v24, v25\n"
    "v_cvt_pk_bf16_f32 v171, v26, v27\n"
    "v_and_b32 v47, v39, v255\n"
    "ds_read_b128 v[252:255], v18 offset:384\n"
    "s_waitcnt lgkmcnt(5)\n"
    "ds_read_b64_tr_b16 a[208:209], v45 offset:6152\n"
    "v_mfma_f32_4x4x4_16b_bf16 v[20:23], v[42:43], a[196:197], 0\n"
    "ds_read_b64_tr_b16 a[210:211], v45 offset:6664\n"
    "s_add_u32 m0, s83, 61440\n"
    "s_add_u32 s84, s58, s64\n"
    "v_cvt_pk_bf16_f32 v172, v28, v29\n"
    "v_cvt_pk_bf16_f32 v173, v30, v31\n"
    "v_mfma_f32_4x4x4_16b_bf16 v[24:27], v[46:47], a[198:199], 0\n"
    "buffer_load_dwordx4 v244, s[44:47], s84 offen lds\n"
    "v_cvt_pk_bf16_f32 v174, v32, v33\n"
    "v_cvt_pk_bf16_f32 v175, v34, v35\n"
    "s_waitcnt vmcnt(13) lgkmcnt(5)\n"
    "ds_read_b64_tr_b16 a[212:213], v15 offset:10240\n"
    "v_mfma_f32_4x4x4_16b_bf16 v[28:31], v[42:43], a[200:201], 0\n"
    "ds_read_b64_tr_b16 a[214:215], v15 offset:11264\n"
    "global_load_dwordx4 v[52:55], v16, s[54:55] sc1\n"
    "v_cvt_pk_bf16_f32 v176, v20, v21\n"
    "v_cvt_pk_bf16_f32 v177, v22, v23\n"
    "v_mfma_f32_4x4x4_16b_bf16 v[32:35], v[46:47], a[202:203], 0\n"
    "buffer_load_dwordx4 v245, s[44:47], s84 offen offset:1024 lds\n"
    "v_cvt_pk_bf16_f32 v178, v24, v25\n"
    "v_cvt_pk_bf16_f32 v179, v26, v27\n"
    "s_waitcnt lgkmcnt(4)\n"
    "ds_read_b64_tr_b16 a[216:217], v15 offset:10248\n"
    "v_mfma_f32_4x4x4_16b_bf16 v[20:23], v[42:43], a[204:205], 0\n"
    "ds_read_b64_tr_b16 a[218:219], v15 offset:11272\n"
    "v_cvt_pk_bf16_f32 v180, v28, v29\n"
    "v_cvt_pk_bf16_f32 v181, v30, v31\n"
    "v_mfma_f32_4x4x4_16b_bf16 v[24:27], v[46:47], a[206:207], 0\n"
    "buffer_load_dwordx4 v246, s[44:47], s84 offen offset:2048 lds\n"
    "v_cvt_pk_bf16_f32 v182, v32, v33\n"
    "v_cvt_pk_bf16_f32 v183, v34, v35\n"
    "v_and_b32 v36, v38, v252\n"
    "s_waitcnt vmcnt(12) lgkmcnt(4)\n"
    "ds_read_b64_tr_b16 a[196:197], v44 offset:12288\n"
    "v_mfma_f32_4x4x4_16b_bf16 v[28:31], v[42:43], a[208:209], 0\n"
    "ds_read_b64_tr_b16 a[198:199], v44 offset:12800\n"
    "v_cvt_pk_bf16_f32 v184, v20, v21\n"
    "v_cvt_pk_bf16_f32 v185, v22, v23\n"
    "v_mfma_f32_4x4x4_16b_bf16 v[32:35], v[46:47], a[210:211], 0\n"
    "buffer_load_dwordx4 v247, s[44:47], s84 offen offset:3072 lds\n"
    "v_cvt_pk_bf16_f32 v186, v24, v25\n"
    "v_cvt_pk_bf16_f32 v187, v26, v27\n"
    "v_and_b32 v37, v39, v253\n"
    "s_waitcnt lgkmcnt(4)\n"
    "ds_read_b64_tr_b16 a[200:201], v44 offset:12296\n"
    "v_mfma_f32_4x4x4_16b_bf16 v[20:23], v[42:43], a[212:213], 0\n"
    "ds_read_b64_tr_b16 a[202:203], v44 offset:12808\n"
    "s_add_u32 m0, s83, 65536\n"
    "v_cvt_pk_bf16_f32 v188, v28, v29\n"
    "v_cvt_pk_bf16_f32 v189, v30, v31\n"
    "v_mfma_f32_4x4x4_16b_bf16 v[24:27], v[46:47], a[214:215], 0\n"
    "buffer_load_dwordx4 v248, s[44:47], s84 offen lds\n"
    "v_cvt_pk_bf16_f32 v190, v32, v33\n"
    "v_cvt_pk_bf16_f32 v191, v34, v35\n"
    "v_and_b32 v40, v38, v254\n"
    "s_waitcnt lgkmcnt(4)\n"
    "ds_read_b64_tr_b16 a[204:205], v45 offset:12288\n"
    "v_mfma_f32_4x4x4_16b_bf16 v[28:31], v[42:43], a[216:217], 0\n"
    "ds_read_b64_tr_b16 a[206:207], v45 offset:12800\n"
    "v_cvt_pk_bf16_f32 v192, v20, v21\n"
    "v_cvt_pk_bf16_f32 v193, v22, v23\n"
    "v_mfma_f32_4x4x4_16b_bf16 v[32:35], v[46:47], a[218:219], 0\n"
    "buffer_load_dwordx4 v249, s[44:47], s84 offen offset:1024 lds\n"
    "v_cvt_pk_bf16_f32 v194, v24, v25\n"
    "v_cvt_pk_bf16_f32 v195, v26, v27\n"
    "v_and_b32 v41, v39, v255\n"
    "ds_read_b128 v[252:255], v18 offset:448\n"
    "s_waitcnt lgkmcnt(5)\n"
    "ds_read_b64_tr_b16 a[208:209], v45 offset:12296\n"
    "v_mfma_f32_4x4x4_16b_bf16 v[20:23], v[36:37], a[196:197], 0\n"
    "ds_read_b64_tr_b16 a[210:211], v45 offset:12808\n"
    "s_add_u32 m0, s83, 67584\n"
    "s_add_u32 s84, s59, s64\n"
    "v_cvt_pk_bf16_f32 v196, v28, v29\n"
    "v_cvt_pk_bf16_f32 v197, v30, v31\n"
    "v_mfma_f32_4x4x4_16b_bf16 v[24:27], v[40:41], a[198:199], 0\n"
    "buffer_load_dwordx4 v244, s[44:47], s84 offen lds\n"
    "v_cvt_pk_bf16_f32 v198, v32, v33\n"
    "v_cvt_pk_bf16_f32 v199, v34, v35\n"
    "s_waitcnt vmcnt(14) lgkmcnt(5)\n"
    "ds_read_b64_tr_b16 a[212:213], v15 offset:16384\n"
    "v_mfma_f32_4x4x4_16b_bf16 v[28:31], v[36:37], a[200:201], 0\n"
    "ds_read_b64_tr_b16 a[214:215], v15 offset:17408\n"
    "v_cvt_pk_bf16_f32 v200, v20, v21\n"
    "v_cvt_pk_bf16_f32 v201, v22, v23\n"
    "v_mfma_f32_4x4x4_16b_bf16 v[32:35], v[40:41], a[202:203], 0\n"
    "buffer_load_dwordx4 v245, s[44:47], s84 offen offset:1024 lds\n"
    "v_cvt_pk_bf16_f32 v202, v24, v25\n"
    "v_cvt_pk_bf16_f32 v203, v26, v27\n"
    "s_waitcnt lgkmcnt(4)\n"
    "ds_read_b64_tr_b16 a[216:217], v15 offset:16392\n"
    "v_mfma_f32_4x4x4_16b_bf16 v[20:23], v[36:37], a[204:205], 0\n"
    "ds_read_b64_tr_b16 a[218:219], v15 offset:17416\n"
    "v_cvt_pk_bf16_f32 v204, v28, v29\n"
    "v_cvt_pk_bf16_f32 v205, v30, v31\n"
    "v_mfma_f32_4x4x4_16b_bf16 v[24:27], v[40:41], a[206:207], 0\n"
    "buffer_load_dwordx4 v246, s[44:47], s84 offen offset:2048 lds\n"
    "v_cvt_pk_bf16_f32 v206, v32, v33\n"
    "v_cvt_pk_bf16_f32 v207, v34, v35\n"
    "v_and_b32 v42, v38, v252\n"
    "s_waitcnt vmcnt(12) lgkmcnt(4)\n"
    "ds_read_b64_tr_b16 a[196:197], v44 offset:18432\n"
    "v_mfma_f32_4x4x4_16b_bf16 v[28:31], v[36:37], a[208:209], 0\n"
    "ds_read_b64_tr_b16 a[198:199], v44 offset:18944\n"
    "v_cvt_pk_bf16_f32 v208, v20, v21\n"
    "v_cvt_pk_bf16_f32 v209, v22, v23\n"
    "v_mfma_f32_4x4x4_16b_bf16 v[32:35], v[40:41], a[210:211], 0\n"
    "buffer_load_dwordx4 v247, s[44:47], s84 offen offset:3072 lds\n"
    "v_cvt_pk_bf16_f32 v210, v24, v25\n"
    "v_cvt_pk_bf16_f32 v211, v26, v27\n"
    "v_and_b32 v43, v39, v253\n"
    "s_waitcnt lgkmcnt(4)\n"
    "ds_read_b64_tr_b16 a[200:201], v44 offset:18440\n"
    "v_mfma_f32_4x4x4_16b_bf16 v[20:23], v[36:37], a[212:213], 0\n"
    "ds_read_b64_tr_b16 a[202:203], v44 offset:18952\n"
    "s_add_u32 m0, s83, 71680\n"
    "v_cvt_pk_bf16_f32 v212, v28, v29\n"
    "v_cvt_pk_bf16_f32 v213, v30, v31\n"
    "v_mfma_f32_4x4x4_16b_bf16 v[24:27], v[40:41], a[214:215], 0\n"
    "buffer_load_dwordx4 v248, s[44:47], s84 offen lds\n"
    "v_cvt_pk_bf16_f32 v214, v32, v33\n"
    "v_cvt_pk_bf16_f32 v215, v34, v35\n"
    "v_and_b32 v46, v38, v254\n"
    "s_waitcnt vmcnt(10)\n"
    "v_xor_b32 v52, s16, v52\n"
    "v_xor_b32 v53, s17, v53\n"
    "v_xor_b32 v54, s16, v54\n"
    "v_xor_b32 v55, s17, v55\n"
    "v_or_b32 v52, v52, v54\n"
    "s_waitcnt lgkmcnt(4)\n"
    "ds_read_b64_tr_b16 a[204:205], v45 offset:18432\n"
    "v_mfma_f32_4x4x4_16b_bf16 v[28:31], v[36:37], a[216:217], 0\n"
    "ds_read_b64_tr_b16 a[206:207], v45 offset:18944\n"
    "v_cvt_pk_bf16_f32 v48, v20, v21\n"
    "v_cvt_pk_bf16_f32 v49, v22, v23\n"
    "v_accvgpr_write_b32 a0, v48\n"
    "v_accvgpr_write_b32 a1, v49\n"
    "v_mfma_f32_4x4x4_16b_bf16 v[32:35], v[40:41], a[218:219], 0\n"
    "buffer_load_dwordx4 v249, s[44:47], s84 offen offset:1024 lds\n"
    "v_cvt_pk_bf16_f32 v50, v24, v25\n"
    "v_cvt_pk_bf16_f32 v51, v26, v27\n"
    "v_accvgpr_write_b32 a2, v50\n"
    "v_accvgpr_write_b32 a3, v51\n"
    "v_and_b32 v47, v39, v255\n"
    "ds_read_b128 v[252:255], v18 offset:512\n"
    "v_or_b32 v54, v53, v55\n"
    "v_lshrrev_b32 v54, 4, v54\n"
    "v_or_b32 v52, v52, v54\n"
    "v_cmp_ne_u32 vcc, 0, v52\n"
    "v_and_b32 v52, 1, v53\n"
    "v_and_b32 v54, 1, v55\n"
    "s_waitcnt lgkmcnt(5)\n"
    "ds_read_b64_tr_b16 a[208:209], v45 offset:18440\n"
    "v_mfma_f32_4x4x4_16b_bf16 v[20:23], v[42:43], a[196:197], 0\n"
    "ds_read_b64_tr_b16 a[210:211], v45 offset:18952\n"
    "s_add_u32 m0, s83, 73728\n"
    "s_add_u32 s84, s60, s64\n"
    "v_cvt_pk_bf16_f32 v48, v28, v29\n"
    "v_cvt_pk_bf16_f32 v49, v30, v31\n"
    "v_accvgpr_write_b32 a4, v48\n"
    "v_accvgpr_write_b32 a5, v49\n"
    "v_mfma_f32_4x4x4_16b_bf16 v[24:27], v[46:47], a[198:199], 0\n"
    "buffer_load_dwordx4 v244, s[44:47], s84 offen lds\n"
    "v_cvt_pk_bf16_f32 v50, v32, v33\n"
    "v_cvt_pk_bf16_f32 v51, v34, v35\n"
    "v_accvgpr_write_b32 a6, v50\n"
    "v_accvgpr_write_b32 a7, v51\n"
    "s_cbranch_vccnz 75f\n"
    "77:\n"
    "v_cmp_eq_u32 s[24:25], 1, v52\n"
    "v_cmp_eq_u32 s[26:27], 1, v54\n"
    "v_readlane_b32 s9, v53, 0\n"
    "s_lshr_b32 s9, s9, 1\n"
    "s_waitcnt vmcnt(14) lgkmcnt(5)\n"
    "ds_read_b64_tr_b16 a[212:213], v15 offset:22528\n"
    "v_mfma_f32_4x4x4_16b_bf16 v[28:31], v[42:43], a[200:201], 0\n"
    "ds_read_b64_tr_b16 a[214:215], v15 offset:23552\n"
    "v_cvt_pk_bf16_f32 v48, v20, v21\n"
    "v_cvt_pk_bf16_f32 v49, v22, v23\n"
    "v_accvgpr_write_b32 a8, v48\n"
    "v_accvgpr_write_b32 a9, v49\n"
    "v_mfma_f32_4x4x4_16b_bf16 v[32:35], v[46:47], a[202:203], 0\n"
    "buffer_load_dwordx4 v245, s[44:47], s84 offen offset:1024 lds\n"
    "v_cvt_pk_bf16_f32 v50, v24, v25\n"
    "v_cvt_pk_bf16_f32 v51, v26, v27\n"
    "v_accvgpr_write_b32 a10, v50\n"
    "v_accvgpr_write_b32 a11, v51\n"
    "s_and_b32 s10, s2, 3\n"
    "s_add_u32 s9, s9, s10\n"
    "s_lshr_b32 s10, s9, 2\n"
    "s_xor_b32 s29, s29, s10\n"
    "s_mul_i32 s10, s52, s53\n"
    "s_add_u32 s10, s10, s56\n"
    "s_waitcnt lgkmcnt(4)\n"
    "ds_read_b64_tr_b16 a[216:217], v15 offset:22536\n"
    "v_mfma_f32_4x4x4_16b_bf16 v[20:23], v[42:43], a[204:205], 0\n"
    "ds_read_b64_tr_b16 a[218:219], v15 offset:23560\n"
    "v_cvt_pk_bf16_f32 v48, v28, v29\n"
    "v_cvt_pk_bf16_f32 v49, v30, v31\n"
    "v_accvgpr_write_b32 a12, v48\n"
    "v_accvgpr_write_b32 a13, v49\n"
    "v_mfma_f32_4x4x4_16b_bf16 v[24:27], v[46:47], a[206:207], 0\n"
    "buffer_load_dwordx4 v246, s[44:47], s84 offen offset:2048 lds\n"
    "v_cvt_pk_bf16_f32 v50, v32, v33\n"
    "v_cvt_pk_bf16_f32 v51, v34, v35\n"
    "v_accvgpr_write_b32 a14, v50\n"
    "v_accvgpr_write_b32 a15, v51\n"
    "v_and_b32 v36, v38, v252\n"
    "s_mov_b32 s11, -2752512\n"
    "s_bitcmp1_b32 s2, 0\n"
    "s_cselect_b32 s11, -3538944, s11\n"
    "s_add_u32 s10, s10, s11\n"
    "s_waitcnt vmcnt(11) lgkmcnt(4)\n"
    "ds_read_b64_tr_b16 a[196:197], v44\n"
    "v_mfma_f32_4x4x4_16b_bf16 v[28:31], v[42:43], a[208:209], 0\n"
    "ds_read_b64_tr_b16 a[198:199], v44 offset:512\n"
    "v_cvt_pk_bf16_f32 v48, v20, v21\n"
    "v_cvt_pk_bf16_f32 v49, v22, v23\n"
    "v_accvgpr_write_b32 a16, v48\n"
    "v_accvgpr_write_b32 a17, v49\n"
    "v_mfma_f32_4x4x4_16b_bf16 v[32:35], v[46:47], a[210:211], 0\n"
    "buffer_load_dwordx4 v247, s[44:47], s84 offen offset:3072 lds\n"
    "v_cvt_pk_bf16_f32 v50, v24, v25\n"
    "v_cvt_pk_bf16_f32 v51, v26, v27\n"
    "v_accvgpr_write_b32 a18, v50\n"
    "v_accvgpr_write_b32 a19, v51\n"
    "v_and_b32 v37, v39, v253\n"
    "s_bitcmp1_b32 s9, 0\n"
    "s_cselect_b32 s10, s33, s10\n"
    "s_cselect_b32 s95, -1, 1\n"
    "s_subb_u32 s11, s52, 0\n"
    "s_waitcnt lgkmcnt(4)\n"
    "ds_read_b64_tr_b16 a[200:201], v44 offset:8\n"
    "v_mfma_f32_4x4x4_16b_bf16 v[20:23], v[42:43], a[212:213], 0\n"
    "ds_read_b64_tr_b16 a[202:203], v44 offset:520\n"
    "s_add_u32 m0, s83, 77824\n"
    "v_cvt_pk_bf16_f32 v48, v28, v29\n"
    "v_cvt_pk_bf16_f32 v49, v30, v31\n"
    "v_accvgpr_write_b32 a20, v48\n"
    "v_accvgpr_write_b32 a21, v49\n"
    "v_mfma_f32_4x4x4_16b_bf16 v[24:27], v[46:47], a[214:215], 0\n"
    "buffer_load_dwordx4 v248, s[44:47], s84 offen lds\n"
    "v_cvt_pk_bf16_f32 v50, v32, v33\n"
    "v_cvt_pk_bf16_f32 v51, v34, v35\n"
    "v_accvgpr_write_b32 a22, v50\n"
    "v_accvgpr_write_b32 a23, v51\n"
    "v_and_b32 v40, v38, v254\n"
    "s_cmp_lt_u32 s52, 12\n"
    "s_cselect_b32 s11, -1, s11\n"
    "s_cselect_b32 s95, 0, s95\n"
    "v_cmp_eq_u32 vcc, s11, v19\n"
    "v_mov_b32 v52, s10\n"
    "s_add_u32 s52, s52, s95\n"
    "s_waitcnt lgkmcnt(4)\n"
    "ds_read_b64_tr_b16 a[204:205], v45\n"
    "v_mfma_f32_4x4x4_16b_bf16 v[28:31], v[42:43], a[216:217], 0\n"
    "ds_read_b64_tr_b16 a[206:207], v45 offset:512\n"
    "v_cvt_pk_bf16_f32 v48, v20, v21\n"
    "v_cvt_pk_bf16_f32 v49, v22, v23\n"
    "v_accvgpr_write_b32 a24, v48\n"
    "v_accvgpr_write_b32 a25, v49\n"
    "v_mfma_f32_4x4x4_16b_bf16 v[32:35], v[46:47], a[218:219], 0\n"
    "buffer_load_dwordx4 v249, s[44:47], s84 offen offset:1024 lds\n"
    "v_cvt_pk_bf16_f32 v50, v24, v25\n"
    "v_cvt_pk_bf16_f32 v51, v26, v27\n"
    "v_accvgpr_write_b32 a26, v50\n"
    "v_accvgpr_write_b32 a27, v51\n"
    "v_and_b32 v41, v39, v255\n"
    "ds_read_b128 v[252:255], v18 offset:576\n"
    "v_cndmask_b32 v3, v3, v52, vcc\n"
    "v_add_u32 v52, s23, v3\n"
    "v_lshrrev_b32 v52, 21, v52\n"
    "s_xor_b64 s[48:49], s[24:25], s[26:27]\n"
    "s_andn2_b32 s49, s49, s96\n"
    "s_bcnt1_i32_b64 s10, s[48:49]\n"
    "s_waitcnt lgkmcnt(5)\n"
    "ds_read_b64_tr_b16 a[208:209], v45 offset:8\n"
    "v_mfma_f32_4x4x4_16b_bf16 v[20:23], v[36:37], a[196:197], 0\n"
    "ds_read_b64_tr_b16 a[210:211], v45 offset:520\n"
    "s_add_u32 m0, s83, 79872\n"
    "s_add_u32 s84, s61, s64\n"
    "v_cvt_pk_bf16_f32 v48, v28, v29\n"
    "v_cvt_pk_bf16_f32 v49, v30, v31\n"
    "v_accvgpr_write_b32 a28, v48\n"
    "v_accvgpr_write_b32 a29, v49\n"
    "v_mfma_f32_4x4x4_16b_bf16 v[24:27], v[40:41], a[198:199], 0\n"
    "buffer_load_dwordx4 v244, s[44:47], s84 offen lds\n"
    "v_cvt_pk_bf16_f32 v50, v32, v33\n"
    "v_cvt_pk_bf16_f32 v51, v34, v35\n"
    "v_accvgpr_write_b32 a30, v50\n"
    "v_accvgpr_write_b32 a31, v51\n"
    "s_cmp_ge_u32 s10, 35\n"
    "s_cselect_b64 s[50:51], s[34:35], 0\n"
    "s_cselect_b32 s96, 1, 0\n"
    "v_and_b32 v53, 56, v52\n"
    "v_lshrrev_b64 v[54:55], v53, s[26:27]\n"
    "v_and_b32 v53, 4, v52\n"
    "s_waitcnt vmcnt(13) lgkmcnt(5)\n"
    "ds_read_b64_tr_b16 a[212:213], v15 offset:4096\n"
    "v_mfma_f32_4x4x4_16b_bf16 v[28:31], v[36:37], a[200:201], 0\n"
    "ds_read_b64_tr_b16 a[214:215], v15 offset:5120\n"
    "v_cvt_pk_bf16_f32 v48, v20, v21\n"
    "v_cvt_pk_bf16_f32 v49, v22, v23\n"
    "v_accvgpr_write_b32 a32, v48\n"
    "v_accvgpr_write_b32 a33, v49\n"
    "v_mfma_f32_4x4x4_16b_bf16 v[32:35], v[40:41], a[202:203], 0\n"
    "buffer_load_dwordx4 v245, s[44:47], s84 offen offset:1024 lds\n"
    "v_cvt_pk_bf16_f32 v50, v24, v25\n"
    "v_cvt_pk_bf16_f32 v51, v26, v27\n"
    "v_accvgpr_write_b32 a34, v50\n"
    "v_accvgpr_write_b32 a35, v51\n"
    "v_and_b32 v55, 0xff, v54\n"
    "v_bfe_u32 v54, v54, v53, 4\n"
    "v_bcnt_u32_b32 v55, v55, 8\n"
    "v_bcnt_u32_b32 v54, v54, 0\n"
    "v_lshl_add_u32 v54, v54, 1, v55\n"
    "v_cmp_lt_u32 s[26:27], 15, v54\n"
    "s_waitcnt lgkmcnt(4)\n"
    "ds_read_b64_tr_b16 a[216:217], v15 offset:4104\n"
    "v_mfma_f32_4x4x4_16b_bf16 v[20:23], v[36:37], a[204:205], 0\n"
    "ds_read_b64_tr_b16 a[218:219], v15 offset:5128\n"
    "v_cvt_pk_bf16_f32 v48, v28, v29\n"
    "v_cvt_pk_bf16_f32 v49, v30, v31\n"
    "v_accvgpr_write_b32 a36, v48\n"
    "v_accvgpr_write_b32 a37, v49\n"
    "v_mfma_f32_4x4x4_16b_bf16 v[24:27], v[40:41], a[206:207], 0\n"
    "buffer_load_dwordx4 v246, s[44:47], s84 offen offset:2048 lds\n"
    "v_cvt_pk_bf16_f32 v50, v32, v33\n"
    "v_cvt_pk_bf16_f32 v51, v34, v35\n"
    "v_accvgpr_write_b32 a38, v50\n"
    "v_accvgpr_write_b32 a39, v51\n"
    "v_and_b32 v42, v38, v252\n"
    "v_and_b32 v53, 56, v52\n"
    "v_lshrrev_b64 v[54:55], v53, s[24:25]\n"
    "v_and_b32 v53, 4, v52\n"
    "v_and_b32 v55, 0xff, v54\n"
    "v_bfe_u32 v54, v54, v53, 4\n"
    "v_bcnt_u32_b32 v55, v55, 8\n"
    "s_waitcnt vmcnt(11) lgkmcnt(4)\n"
    "ds_read_b64_tr_b16 a[196:197], v44 offset:6144\n"
    "v_mfma_f32_4x4x4_16b_bf16 v[28:31], v[36:37], a[208:209], 0\n"
    "ds_read_b64_tr_b16 a[198:199], v44 offset:6656\n"
    "v_cvt_pk_bf16_f32 v48, v20, v21\n"
    "v_cvt_pk_bf16_f32 v49, v22, v23\n"
    "v_accvgpr_write_b32 a40, v48\n"
    "v_accvgpr_write_b32 a41, v49\n"
    "v_mfma_f32_4x4x4_16b_bf16 v[32:35], v[40:41], a[210:211], 0\n"
    "buffer_load_dwordx4 v247, s[44:47], s84 offen offset:3072 lds\n"
    "v_cvt_pk_bf16_f32 v50, v24, v25\n"
    "v_cvt_pk_bf16_f32 v51, v26, v27\n"
    "v_accvgpr_write_b32 a42, v50\n"
    "v_accvgpr_write_b32 a43, v51\n"
    "v_and_b32 v43, v39, v253\n"
    "v_bcnt_u32_b32 v54, v54, 0\n"
    "v_lshl_add_u32 v54, v54, 1, v55\n"
    "v_cmp_lt_u32 s[24:25], 15, v54\n"
    "v_xor_b32 v54, s29, v52\n"
    "s_xor_b64 s[26:27], s[26:27], s[24:25]\n"
    "s_and_b64 s[50:51], s[50:51], s[26:27]\n"
    "s_waitcnt lgkmcnt(4)\n"
    "ds_read_b64_tr_b16 a[200:201], v44 offset:6152\n"
    "v_mfma_f32_4x4x4_16b_bf16 v[20:23], v[36:37], a[212:213], 0\n"
    "ds_read_b64_tr_b16 a[202:203], v44 offset:6664\n"
    "s_add_u32 m0, s83, 83968\n"
    "v_cvt_pk_bf16_f32 v48, v28, v29\n"
    "v_cvt_pk_bf16_f32 v49, v30, v31\n"
    "v_accvgpr_write_b32 a44, v48\n"
    "v_accvgpr_write_b32 a45, v49\n"
    "v_mfma_f32_4x4x4_16b_bf16 v[24:27], v[40:41], a[214:215], 0\n"
    "buffer_load_dwordx4 v248, s[44:47], s84 offen lds\n"
    "v_cvt_pk_bf16_f32 v50, v32, v33\n"
    "v_cvt_pk_bf16_f32 v51, v34, v35\n"
    "v_accvgpr_write_b32 a46, v50\n"
    "v_accvgpr_write_b32 a47, v51\n"
    "v_and_b32 v46, v38, v254\n"
    "s_xor_b64 vcc, s[50:51], s[24:25]\n"
    "v_addc_co_u32 v54, vcc, 0, v54, vcc\n"
    "v_and_b32 v54, 1, v54\n"
    "v_mad_u32_u24 v3, v54, s88, v3\n"
    "s_nop 0\n"
    "v_readlane_b32 s72, v3, 0\n"
    "s_waitcnt lgkmcnt(4)\n"
    "ds_read_b64_tr_b16 a[204:205], v45 offset:6144\n"
    "v_mfma_f32_4x4x4_16b_bf16 v[28:31], v[36:37], a[216:217], 0\n"
    "ds_read_b64_tr_b16 a[206:207], v45 offset:6656\n"
    "v_cvt_pk_bf16_f32 v48, v20, v21\n"
    "v_cvt_pk_bf16_f32 v49, v22, v23\n"
    "v_accvgpr_write_b32 a48, v48\n"
    "v_accvgpr_write_b32 a49, v49\n"
    "v_mfma_f32_4x4x4_16b_bf16 v[32:35], v[40:41], a[218:219], 0\n"
    "buffer_load_dwordx4 v249, s[44:47], s84 offen offset:1024 lds\n"
    "v_cvt_pk_bf16_f32 v50, v24, v25\n"
    "v_cvt_pk_bf16_f32 v51, v26, v27\n"
    "v_accvgpr_write_b32 a50, v50\n"
    "v_accvgpr_write_b32 a51, v51\n"
    "v_and_b32 v47, v39, v255\n"
    "ds_read_b128 v[252:255], v18 offset:640\n"
    "v_readlane_b32 s73, v3, 1\n"
    "v_readlane_b32 s74, v3, 2\n"
    "v_readlane_b32 s75, v3, 3\n"
    "v_readlane_b32 s81, v3, 4\n"
    "s_waitcnt lgkmcnt(5)\n"
    "ds_read_b64_tr_b16 a[208:209], v45 offset:6152\n"
    "v_mfma_f32_4x4x4_16b_bf16 v[20:23], v[42:43], a[196:197], 0\n"
    "ds_read_b64_tr_b16 a[210:211], v45 offset:6664\n"
    "s_barrier\n"
    "s_mov_b32 m0, s30\n"
    "v_cvt_pk_bf16_f32 v48, v28, v29\n"
    "v_cvt_pk_bf16_f32 v49, v30, v31\n"
    "v_accvgpr_write_b32 a52, v48\n"
    "v_accvgpr_write_b32 a53, v49\n"
    "v_mfma_f32_4x4x4_16b_bf16 v[24:27], v[46:47], a[198:199], 0\n"
    "buffer_load_dwordx4 v0, s[36:39], s72 offen nt lds\n"
    "v_cvt_pk_bf16_f32 v50, v32, v33\n"
    "v_cvt_pk_bf16_f32 v51, v34, v35\n"
    "v_accvgpr_write_b32 a54, v50\n"
    "v_accvgpr_write_b32 a55, v51\n"
    "s_waitcnt vmcnt(13) lgkmcnt(5)\n"
    "ds_read_b64_tr_b16 a[212:213], v15 offset:10240\n"
    "v_mfma_f32_4x4x4_16b_bf16 v[28:31], v[42:43], a[200:201], 0\n"
    "ds_read_b64_tr_b16 a[214:215], v15 offset:11264\n"
    "v_cvt_pk_bf16_f32 v48, v20, v21\n"
    "v_cvt_pk_bf16_f32 v49, v22, v23\n"
    "v_accvgpr_write_b32 a56, v48\n"
    "v_accvgpr_write_b32 a57, v49\n"
    "v_mfma_f32_4x4x4_16b_bf16 v[32:35], v[46:47], a[202:203], 0\n"
    "buffer_load_dwordx4 v1, s[36:39], s72 offen offset:1024 nt lds\n"
    "v_cvt_pk_bf16_f32 v50, v24, v25\n"
    "v_cvt_pk_bf16_f32 v51, v26, v27\n"
    "v_accvgpr_write_b32 a58, v50\n"
    "v_accvgpr_write_b32 a59, v51\n"
    "s_waitcnt lgkmcnt(4)\n"
    "ds_read_b64_tr_b16 a[216:217], v15 offset:10248\n"
    "v_mfma_f32_4x4x4_16b_bf16 v[20:23], v[42:43], a[204:205], 0\n"
    "ds_read_b64_tr_b16 a[218:219], v15 offset:11272\n"
    "v_cvt_pk_bf16_f32 v48, v28, v29\n"
    "v_cvt_pk_bf16_f32 v49, v30, v31\n"
    "v_accvgpr_write_b32 a60, v48\n"
    "v_accvgpr_write_b32 a61, v49\n"
    "v_mfma_f32_4x4x4_16b_bf16 v[24:27], v[46:47], a[206:207], 0\n"
    "buffer_load_dwordx4 v2, s[36:39], s72 offen offset:2048 nt lds\n"
    "v_cvt_pk_bf16_f32 v50, v32, v33\n"
    "v_cvt_pk_bf16_f32 v51, v34, v35\n"
    "v_accvgpr_write_b32 a62, v50\n"
    "v_accvgpr_write_b32 a63, v51\n"
    "v_and_b32 v36, v38, v252\n"
    "s_waitcnt vmcnt(11) lgkmcnt(4)\n"
    "ds_read_b64_tr_b16 a[196:197], v44 offset:12288\n"
    "v_mfma_f32_4x4x4_16b_bf16 v[28:31], v[42:43], a[208:209], 0\n"
    "ds_read_b64_tr_b16 a[198:199], v44 offset:12800\n"
    "v_cvt_pk_bf16_f32 v48, v20, v21\n"
    "v_cvt_pk_bf16_f32 v49, v22, v23\n"
    "v_accvgpr_write_b32 a64, v48\n"
    "v_accvgpr_write_b32 a65, v49\n"
    "v_mfma_f32_4x4x4_16b_bf16 v[32:35], v[46:47], a[210:211], 0\n"
    "v_cvt_pk_bf16_f32 v50, v24, v25\n"
    "v_cvt_pk_bf16_f32 v51, v26, v27\n"
    "v_accvgpr_write_b32 a66, v50\n"
    "v_accvgpr_write_b32 a67, v51\n"
    "v_and_b32 v37, v39, v253\n"
    "s_waitcnt lgkmcnt(4)\n"
    "ds_read_b64_tr_b16 a[200:201], v44 offset:12296\n"
    "v_mfma_f32_4x4x4_16b_bf16 v[20:23], v[42:43], a[212:213], 0\n"
    "ds_read_b64_tr_b16 a[202:203], v44 offset:12808\n"
    "v_cvt_pk_bf16_f32 v48, v28, v29\n"
    "v_cvt_pk_bf16_f32 v49, v30, v31\n"
    "v_accvgpr_write_b32 a68, v48\n"
    "v_accvgpr_write_b32 a69, v49\n"
    "v_mfma_f32_4x4x4_16b_bf16 v[24:27], v[46:47], a[214:215], 0\n"
    "v_cvt_pk_bf16_f32 v50, v32, v33\n"
    "v_cvt_pk_bf16_f32 v51, v34, v35\n"
    "v_accvgpr_write_b32 a70, v50\n"
    "v_accvgpr_write_b32 a71, v51\n"
    "v_and_b32 v40, v38, v254\n"
    "s_waitcnt lgkmcnt(4)\n"
    "ds_read_b64_tr_b16 a[204:205], v45 offset:12288\n"
    "v_mfma_f32_4x4x4_16b_bf16 v[28:31], v[42:43], a[216:217], 0\n"
    "ds_read_b64_tr_b16 a[206:207], v45 offset:12800\n"
    "v_cvt_pk_bf16_f32 v48, v20, v21\n"
    "v_cvt_pk_bf16_f32 v49, v22, v23\n"
    "v_accvgpr_write_b32 a72, v48\n"
    "v_accvgpr_write_b32 a73, v49\n"
    "v_mfma_f32_4x4x4_16b_bf16 v[32:35], v[46:47], a[218:219], 0\n"
    "v_cvt_pk_bf16_f32 v50, v24, v25\n"
    "v_cvt_pk_bf16_f32 v51, v26, v27\n"
    "v_accvgpr_write_b32 a74, v50\n"
    "v_accvgpr_write_b32 a75, v51\n"
    "v_and_b32 v41, v39, v255\n"
    "ds_read_b128 v[252:255], v18 offset:704\n"
    "s_waitcnt lgkmcnt(5)\n"
    "ds_read_b64_tr_b16 a[208:209], v45 offset:12296\n"
    "v_mfma_f32_4x4x4_16b_bf16 v[20:23], v[36:37], a[196:197], 0\n"
    "ds_read_b64_tr_b16 a[210:211], v45 offset:12808\n"
    "v_cvt_pk_bf16_f32 v48, v28, v29\n"
    "v_cvt_pk_bf16_f32 v49, v30, v31\n"
    "v_accvgpr_write_b32 a76, v48\n"
    "v_accvgpr_write_b32 a77, v49\n"
    "v_mfma_f32_4x4x4_16b_bf16 v[24:27], v[40:41], a[198:199], 0\n"
    "v_cvt_pk_bf16_f32 v50, v32, v33\n"
    "v_cvt_pk_bf16_f32 v51, v34, v35\n"
    "v_accvgpr_write_b32 a78, v50\n"
    "v_accvgpr_write_b32 a79, v51\n"
    "s_waitcnt vmcnt(9) lgkmcnt(5)\n"
    "ds_read_b64_tr_b16 a[212:213], v15 offset:16384\n"
    "v_mfma_f32_4x4x4_16b_bf16 v[28:31], v[36:37], a[200:201], 0\n"
    "ds_read_b64_tr_b16 a[214:215], v15 offset:17408\n"
    "v_cvt_pk_bf16_f32 v48, v20, v21\n"
    "v_cvt_pk_bf16_f32 v49, v22, v23\n"
    "v_accvgpr_write_b32 a80, v48\n"
    "v_accvgpr_write_b32 a81, v49\n"
    "v_mfma_f32_4x4x4_16b_bf16 v[32:35], v[40:41], a[202:203], 0\n"
    "v_cvt_pk_bf16_f32 v50, v24, v25\n"
    "v_cvt_pk_bf16_f32 v51, v26, v27\n"
    "v_accvgpr_write_b32 a82, v50\n"
    "v_accvgpr_write_b32 a83, v51\n"
    "s_waitcnt lgkmcnt(4)\n"
    "ds_read_b64_tr_b16 a[216:217], v15 offset:16392\n"
    "v_mfma_f32_4x4x4_16b_bf16 v[20:23], v[36:37], a[204:205], 0\n"
    "ds_read_b64_tr_b16 a[218:219], v15 offset:17416\n"
    "v_cvt_pk_bf16_f32 v48, v28, v29\n"
    "v_cvt_pk_bf16_f32 v49, v30, v31\n"
    "v_accvgpr_write_b32 a84, v48\n"
    "v_accvgpr_write_b32 a85, v49\n"
    "v_mfma_f32_4x4x4_16b_bf16 v[24:27], v[40:41], a[206:207], 0\n"
    "v_cvt_pk_bf16_f32 v50, v32, v33\n"
    "v_cvt_pk_bf16_f32 v51, v34, v35\n"
    "v_accvgpr_write_b32 a86, v50\n"
    "v_accvgpr_write_b32 a87, v51\n"
    "v_and_b32 v42, v38, v252\n"
    "s_waitcnt vmcnt(5) lgkmcnt(4)\n"
    "ds_read_b64_tr_b16 a[196:197], v44 offset:18432\n"
    "v_mfma_f32_4x4x4_16b_bf16 v[28:31], v[36:37], a[208:209], 0\n"
    "ds_read_b64_tr_b16 a[198:199], v44 offset:18944\n"
    "s_add_u32 s30, s30, 12288\n"
    "s_mov_b32 m0, s30\n"
    "v_cvt_pk_bf16_f32 v216, v20, v21\n"
    "v_cvt_pk_bf16_f32 v217, v22, v23\n"
    "v_mfma_f32_4x4x4_16b_bf16 v[32:35], v[40:41], a[210:211], 0\n"
    "buffer_load_dwordx4 v0, s[36:39], s73 offen nt lds\n"
    "v_cvt_pk_bf16_f32 v218, v24, v25\n"
    "v_cvt_pk_bf16_f32 v219, v26, v27\n"
    "v_and_b32 v43, v39, v253\n"
    "s_waitcnt lgkmcnt(4)\n"
    "ds_read_b64_tr_b16 a[200:201], v44 offset:18440\n"
    "v_mfma_f32_4x4x4_16b_bf16 v[20:23], v[36:37], a[212:213], 0\n"
    "ds_read_b64_tr_b16 a[202:203], v44 offset:18952\n"
    "v_cvt_pk_bf16_f32 v220, v28, v29\n"
    "v_cvt_pk_bf16_f32 v221, v30, v31\n"
    "v_mfma_f32_4x4x4_16b_bf16 v[24:27], v[40:41], a[214:215], 0\n"
    "buffer_load_dwordx4 v1, s[36:39], s73 offen offset:1024 nt lds\n"
    "v_cvt_pk_bf16_f32 v222, v32, v33\n"
    "v_cvt_pk_bf16_f32 v223, v34, v35\n"
    "v_and_b32 v46, v38, v254\n"
    "s_waitcnt lgkmcnt(4)\n"
    "ds_read_b64_tr_b16 a[204:205], v45 offset:18432\n"
    "v_mfma_f32_4x4x4_16b_bf16 v[28:31], v[36:37], a[216:217], 0\n"
    "ds_read_b64_tr_b16 a[206:207], v45 offset:18944\n"
    "v_cvt_pk_bf16_f32 v224, v20, v21\n"
    "v_cvt_pk_bf16_f32 v225, v22, v23\n"
    "v_mfma_f32_4x4x4_16b_bf16 v[32:35], v[40:41], a[218:219], 0\n"
    "buffer_load_dwordx4 v2, s[36:39], s73 offen offset:2048 nt lds\n"
    "v_cvt_pk_bf16_f32 v226, v24, v25\n"
    "v_cvt_pk_bf16_f32 v227, v26, v27\n"
    "v_and_b32 v47, v39, v255\n"
    "s_waitcnt lgkmcnt(4)\n"
    "ds_read_b64_tr_b16 a[208:209], v45 offset:18440\n"
    "v_mfma_f32_4x4x4_16b_bf16 v[20:23], v[42:43], a[196:197], 0\n"
    "ds_read_b64_tr_b16 a[210:211], v45 offset:18952\n"
    "v_cvt_pk_bf16_f32 v228, v28, v29\n"
    "v_cvt_pk_bf16_f32 v229, v30, v31\n"
    "v_mfma_f32_4x4x4_16b_bf16 v[24:27], v[46:47], a[198:199], 0\n"
    "v_cvt_pk_bf16_f32 v230, v32, v33\n"
    "v_cvt_pk_bf16_f32 v231, v34, v35\n"
    "s_nop 0\n"
    "s_waitcnt vmcnt(6) lgkmcnt(4)\n"
    "ds_read_b64_tr_b16 a[212:213], v15 offset:22528\n"
    "v_mfma_f32_4x4x4_16b_bf16 v[28:31], v[42:43], a[200:201], 0\n"
    "ds_read_b64_tr_b16 a[214:215], v15 offset:23552\n"
    "v_cvt_pk_bf16_f32 v232, v20, v21\n"
    "v_cvt_pk_bf16_f32 v233, v22, v23\n"
    "v_mfma_f32_4x4x4_16b_bf16 v[32:35], v[46:47], a[202:203], 0\n"
    "v_cvt_pk_bf16_f32 v234, v24, v25\n"
    "v_cvt_pk_bf16_f32 v235, v26, v27\n"
    "s_nop 0\n"
    "s_waitcnt lgkmcnt(4)\n"
    "ds_read_b64_tr_b16 a[216:217], v15 offset:22536\n"
    "v_mfma_f32_4x4x4_16b_bf16 v[20:23], v[42:43], a[204:205], 0\n"
    "ds_read_b64_tr_b16 a[218:219], v15 offset:23560\n"
    "v_cvt_pk_bf16_f32 v236, v28, v29\n"
    "v_cvt_pk_bf16_f32 v237, v30, v31\n"
    "v_mfma_f32_4x4x4_16b_bf16 v[24:27], v[46:47], a[206:207], 0\n"
    "v_cvt_pk_bf16_f32 v238, v32, v33\n"
    "v_cvt_pk_bf16_f32 v239, v34, v35\n"
    "s_nop 0\n"
    "s_waitcnt lgkmcnt(4)\n"
    "v_mfma_f32_4x4x4_16b_bf16 v[28:31], v[42:43], a[208:209], 0\n"
    "v_cvt_pk_bf16_f32 v240, v20, v21\n"
    "v_cvt_pk_bf16_f32 v241, v22, v23\n"
    "s_nop 0\n"
    "v_mfma_f32_4x4x4_16b_bf16 v[32:35], v[46:47], a[210:211], 0\n"
    "v_cvt_pk_bf16_f32 v242, v24, v25\n"
    "v_cvt_pk_bf16_f32 v243, v26, v27\n"
    "s_nop 2\n"
    "s_waitcnt lgkmcnt(2)\n"
    "v_mfma_f32_4x4x4_16b_bf16 v[20:23], v[42:43], a[212:213], 0\n"
    "v_cvt_pk_bf16_f32 v244, v28, v29\n"
    "v_cvt_pk_bf16_f32 v245, v30, v31\n"
    "s_nop 0\n"
    "v_mfma_f32_4x4x4_16b_bf16 v[24:27], v[46:47], a[214:215], 0\n"
    "v_cvt_pk_bf16_f32 v246, v32, v33\n"
    "v_cvt_pk_bf16_f32 v247, v34, v35\n"
    "s_nop 2\n"
    "s_waitcnt lgkmcnt(0)\n"
    "v_mfma_f32_4x4x4_16b_bf16 v[28:31], v[42:43], a[216:217], 0\n"
    "v_cvt_pk_bf16_f32 v248, v20, v21\n"
    "v_cvt_pk_bf16_f32 v249, v22, v23\n"
    "s_nop 0\n"
    "v_mfma_f32_4x4x4_16b_bf16 v[32:35], v[46:47], a[218:219], 0\n"
    "v_cvt_pk_bf16_f32 v250, v24, v25\n"
    "v_cvt_pk_bf16_f32 v251, v26, v27\n"
    "s_nop 2\n"
    "s_nop 0\n"
    "s_nop 0\n"
    "s_nop 1\n"
    "v_cvt_pk_bf16_f32 v252, v28, v29\n"
    "v_cvt_pk_bf16_f32 v253, v30, v31\n"
    "v_cvt_pk_bf16_f32 v254, v32, v33\n"
    "v_cvt_pk_bf16_f32 v255, v34, v35\n"
    "s_waitcnt lgkmcnt(0)\n"
    "s_add_u32 s89, s92, 390\n"
    "s_waitcnt vmcnt(3)\n"
    "s_barrier\n"
    "ds_read_b128 a[88:91], v4\n"
    "ds_read_b128 a[92:95], v5\n"
    "ds_read_b128 a[96:99], v6\n"
    "ds_read_b128 a[100:103], v7\n"
    "ds_read_b128 a[104:107], v4 offset:256\n"
    "ds_read_b128 a[108:111], v5 offset:256\n"
    "ds_read_b128 a[112:115], v6 offset:256\n"
    "ds_read_b128 a[116:119], v7 offset:256\n"
    "ds_read_b128 a[120:123], v4 offset:512\n"
    "ds_read_b128 a[124:127], v5 offset:512\n"
    "ds_read_b128 a[128:131], v6 offset:512\n"
    "ds_read_b128 a[132:135], v7 offset:512\n"
    "s_waitcnt lgkmcnt(11)\n"
    "v_mfma_f32_16x16x32_bf16 v[20:23], v[56:59], a[88:91], 0\n"
    "v_add_u32 v14, 1, v14\n"
    "s_sub_i32 s46, s52, 5\n"
    "s_max_i32 s46, s46, 0\n"
    "s_waitcnt lgkmcnt(10)\n"
    "v_mfma_f32_16x16x32_bf16 v[20:23], v[80:83], a[92:95], v[20:23]\n"
    "s_add_u32 m0, s57, 24576\n"
    "s_bfm_b64 s[44:45], s46, 5\n"
    "s_mov_b64 s[48:49], s[26:27]\n"
    "s_waitcnt lgkmcnt(9)\n"
    "v_mfma_f32_16x16x32_bf16 v[20:23], v[104:107], a[96:99], v[20:23]\n"
    "buffer_load_dwordx4 v0, s[36:39], s74 offen nt lds\n"
    "v_subrev_u32 v33, 5, v19\n"
    "s_waitcnt lgkmcnt(8)\n"
    "v_mfma_f32_16x16x32_bf16 v[20:23], v[128:131], a[100:103], v[20:23]\n"
    "s_andn2_b64 s[50:51], s[44:45], s[48:49]\n"
    "s_and_b64 s[48:49], s[44:45], s[48:49]\n"
    "s_bcnt1_i32_b64 s47, s[50:51]\n"
    "s_waitcnt lgkmcnt(7)\n"
    "v_mfma_f32_16x16x32_bf16 v[20:23], v[152:155], a[104:107], v[20:23]\n"
    "buffer_load_dwordx4 v1, s[36:39], s74 offen offset:1024 nt lds\n"
    "s_sub_u32 s46, s46, s47\n"
    "s_waitcnt lgkmcnt(6)\n"
    "v_mfma_f32_16x16x32_bf16 v[20:23], v[176:179], a[108:111], v[20:23]\n"
    "s_add_u32 s58, s46, 5\n"
    "v_mov_b32 v34, s58\n"
    "v_mbcnt_lo_u32_b32 v35, s48, 5\n"
    "s_waitcnt lgkmcnt(5)\n"
    "v_mfma_f32_16x16x32_bf16 v[20:23], v[200:203], a[112:115], v[20:23]\n"
    "buffer_load_dwordx4 v2, s[36:39], s74 offen offset:2048 nt lds\n"
    "v_mbcnt_lo_u32_b32 v34, s50, v34\n"
    "s_waitcnt lgkmcnt(4)\n"
    "v_mfma_f32_16x16x32_bf16 v[20:23], a[8:11], a[116:119], v[20:23]\n"
    "v_mbcnt_hi_u32_b32 v35, s49, v35\n"
    "v_mbcnt_hi_u32_b32 v34, s51, v34\n"
    "s_mul_i32 s94, s46, 2\n"
    "s_waitcnt lgkmcnt(3)\n"
    "v_mfma_f32_16x16x32_bf16 v[20:23], a[32:35], a[120:123], v[20:23]\n"
    "s_lshr_b32 s94, s94, 4\n"
    "s_add_u32 s94, s94, 71\n"
    "s_waitcnt lgkmcnt(2)\n"
    "v_mfma_f32_16x16x32_bf16 v[20:23], a[56:59], a[124:127], v[20:23]\n"
    "s_cmp_lg_u32 s96, 0\n"
    "s_cselect_b32 s94, 76, s94\n"
    "s_waitcnt lgkmcnt(1)\n"
    "v_mfma_f32_16x16x32_bf16 v[20:23], a[80:83], a[128:131], v[20:23]\n"
    "s_cmp_lg_u32 s96, 0\n"
    "s_cselect_b32 s63, 42, 45\n"
    "s_add_u32 s76, s52, 4\n"
    "s_waitcnt lgkmcnt(0)\n"
    "v_mfma_f32_16x16x32_bf16 v[20:23], v[232:235], a[132:135], v[20:23]\n"
    "v_subrev_u32 v18, 6, v19\n"
    "s_sub_u32 s64, s52, 6\n"
    "v_mfma_f32_16x16x32_bf16 v[24:27], v[236:239], a[132:135], 0\n"
    "s_cmp_lt_u32 s47, 6\n"
    "s_cselect_b32 s64, 0x1000, s64\n"
    "v_subrev_u32 v36, s64, v18\n"
    "v_mfma_f32_16x16x32_bf16 v[24:27], a[84:87], a[128:131], v[24:27]\n"
    "v_cmp_gt_u32 s[60:61], 2, v36\n"
    "s_add_u32 s65, s64, 8\n"
    "s_mul_i32 s7, s64, 43\n"
    "v_mfma_f32_16x16x32_bf16 v[24:27], a[60:63], a[124:127], v[24:27]\n"
    "v_cndmask_b32 v18, v18, 63, s[60:61]\n"
    "s_lshr_b32 s7, s7, 9\n"
    "s_mul_i32 s7, s7, 12\n"
    "v_mfma_f32_16x16x32_bf16 v[24:27], a[36:39], a[120:123], v[24:27]\n"
    "s_sub_u32 s7, s64, s7\n"
    "s_mul_i32 s7, s7, 12288\n"
    "s_add_u32 s64, s7, s57\n"
    "v_mfma_f32_16x16x32_bf16 v[24:27], a[12:15], a[116:119], v[24:27]\n"
    "v_lshlrev_b32 v18, 2, v18\n"
    "s_mul_i32 s59, s95, 2\n"
    "s_sub_u32 s94, s94, s59\n"
    "v_mfma_f32_16x16x32_bf16 v[24:27], v[204:207], a[112:115], v[24:27]\n"
    "v_cvt_pk_bf16_f32 v44, v20, v21\n"
    "v_cvt_pk_bf16_f32 v45, v22, v23\n"
    "v_cndmask_b32 v35, v19, v35, s[48:49]\n"
    "v_mfma_f32_16x16x32_bf16 v[24:27], v[180:183], a[108:111], v[24:27]\n"
    "s_sub_i32 s59, s47, 6\n"
    "v_cndmask_b32 v35, v35, v34, s[50:51]\n"
    "s_max_i32 s59, s59, 0\n"
    "v_mfma_f32_16x16x32_bf16 v[24:27], v[156:159], a[104:107], v[24:27]\n"
    "v_lshlrev_b32 v35, 2, v35\n"
    "s_mul_i32 s59, s59, 10\n"
    "s_min_u32 s59, s59, 0x100\n"
    "v_mfma_f32_16x16x32_bf16 v[24:27], v[132:135], a[100:103], v[24:27]\n"
    "ds_permute_b32 v3, v35, v3\n"
    "v_mul_i32_i24 v28, s59, v33\n"
    "s_mul_i32 s62, s2, 9\n"
    "v_mfma_f32_16x16x32_bf16 v[24:27], v[108:111], a[96:99], v[24:27]\n"
    "s_and_b32 s62, s62, 0xff\n"
    "v_add_u32 v28, s62, v28\n"
    "v_subrev_u32 v32, s46, v33\n"
    "v_mfma_f32_16x16x32_bf16 v[24:27], v[84:87], a[92:95], v[24:27]\n"
    "s_waitcnt vmcnt(3)\n"
    "ds_write_b32 v13, v14\n"
    "v_mfma_f32_16x16x32_bf16 v[24:27], v[60:63], a[88:91], v[24:27]\n"
    "v_add_u32 v30, s59, v28\n"
    "v_ashrrev_i32 v28, 8, v28\n"
    "v_ashrrev_i32 v30, 8, v30\n"
    "v_mfma_f32_16x16x32_bf16 v[20:23], v[64:67], a[88:91], 0\n"
    "s_add_u32 m0, s57, 36864\n"
    "v_min_i32 v28, s47, v28\n"
    "v_min_i32 v30, s47, v30\n"
    "v_mfma_f32_16x16x32_bf16 v[20:23], v[88:91], a[92:95], v[20:23]\n"
    "buffer_load_dwordx4 v0, s[36:39], s75 offen nt lds\n"
    "v_max_i32 v28, v28, v32\n"
    "v_mfma_f32_16x16x32_bf16 v[20:23], v[112:115], a[96:99], v[20:23]\n"
    "v_add_u32 v32, 1, v32\n"
    "v_max_i32 v30, v30, v32\n"
    "v_cmp_gt_i32 s[60:61], v30, v28\n"
    "v_mfma_f32_16x16x32_bf16 v[20:23], v[136:139], a[100:103], v[20:23]\n"
    "buffer_load_dwordx4 v1, s[36:39], s75 offen offset:1024 nt lds\n"
    "v_add_u32 v30, s58, v28\n"
    "v_mfma_f32_16x16x32_bf16 v[20:23], v[160:163], a[104:107], v[20:23]\n"
    "ds_read_b32 v15, v12\n"
    "v_sub_u32 v28, v19, v28\n"
    "v_cndmask_b32 v28, v28, v30, s[60:61]\n"
    "v_mfma_f32_16x16x32_bf16 v[20:23], v[184:187], a[108:111], v[20:23]\n"
    "v_cvt_pk_bf16_f32 v46, v24, v25\n"
    "v_cvt_pk_bf16_f32 v47, v26, v27\n"
    "v_cndmask_b32 v28, v19, v28, s[44:45]\n"
    "v_mfma_f32_16x16x32_bf16 v[20:23], v[208:211], a[112:115], v[20:23]\n"
    "buffer_load_dwordx4 v2, s[36:39], s75 offen offset:2048 nt lds\n"
    "v_lshlrev_b32 v28, 2, v28\n"
    "v_mfma_f32_16x16x32_bf16 v[20:23], a[16:19], a[116:119], v[20:23]\n"
    "buffer_load_dword a224, off, s[36:39], s33\n"
    "s_waitcnt lgkmcnt(2)\n"
    "ds_bpermute_b32 v3, v28, v3\n"
    "v_mfma_f32_16x16x32_bf16 v[20:23], a[40:43], a[120:123], v[20:23]\n"
    "s_waitcnt lgkmcnt(1)\n"
    "v_cmp_lt_u32 vcc, v15, v14\n"
    "s_mov_b32 s62, 0\n"
    "v_mfma_f32_16x16x32_bf16 v[20:23], a[64:67], a[124:127], v[20:23]\n"
    "s_cbranch_vccnz 81b\n"
    "82:\n"
    "v_mfma_f32_16x16x32_bf16 v[20:23], v[216:219], a[128:131], v[20:23]\n"
    "ds_write_b64 v8, v[44:45]\n"
    "v_mfma_f32_16x16x32_bf16 v[20:23], v[240:243], a[132:135], v[20:23]\n"
    "ds_read_b128 a[136:139], v4 offset:12288\n"
    "v_mfma_f32_16x16x32_bf16 v[24:27], v[244:247], a[132:135], 0\n"
    "ds_write_b64 v8, v[46:47] offset:8\n"
    "v_mfma_f32_16x16x32_bf16 v[24:27], v[220:223], a[128:131], v[24:27]\n"
    "ds_read_b128 a[140:143], v5 offset:12288\n"
    "v_mfma_f32_16x16x32_bf16 v[24:27], a[68:71], a[124:127], v[24:27]\n"
    "buffer_load_dword a224, off, s[36:39], s33\n"
    "v_mfma_f32_16x16x32_bf16 v[24:27], a[44:47], a[120:123], v[24:27]\n"
    "ds_read_b128 a[144:147], v6 offset:12288\n"
    "v_mfma_f32_16x16x32_bf16 v[24:27], a[20:23], a[116:119], v[24:27]\n"
    "buffer_load_dword a224, off, s[36:39], s33\n"
    "v_mfma_f32_16x16x32_bf16 v[24:27], v[212:215], a[112:115], v[24:27]\n"
    "v_cvt_pk_bf16_f32 v48, v20, v21\n"
    "v_cvt_pk_bf16_f32 v49, v22, v23\n"
    "v_mfma_f32_16x16x32_bf16 v[24:27], v[188:191], a[108:111], v[24:27]\n"
    "ds_read_b128 a[148:151], v7 offset:12288\n"
    "v_mfma_f32_16x16x32_bf16 v[24:27], v[164:167], a[104:107], v[24:27]\n"
    "ds_write_b64 v9, v[48:49]\n"
    "v_mfma_f32_16x16x32_bf16 v[24:27], v[140:143], a[100:103], v[24:27]\n"
    "ds_read_b128 a[152:155], v4 offset:12544\n"
    "v_mfma_f32_16x16x32_bf16 v[24:27], v[116:119], a[96:99], v[24:27]\n"
    "v_mfma_f32_16x16x32_bf16 v[24:27], v[92:95], a[92:95], v[24:27]\n"
    "ds_read_b128 a[156:159], v5 offset:12544\n"
    "v_mfma_f32_16x16x32_bf16 v[24:27], v[68:71], a[88:91], v[24:27]\n"
    "v_mfma_f32_16x16x32_bf16 v[20:23], v[72:75], a[88:91], 0\n"
    "ds_read_b128 a[160:163], v6 offset:12544\n"
    "v_mfma_f32_16x16x32_bf16 v[20:23], v[96:99], a[92:95], v[20:23]\n"
    "v_mfma_f32_16x16x32_bf16 v[20:23], v[120:123], a[96:99], v[20:23]\n"
    "ds_read_b128 a[164:167], v7 offset:12544\n"
    "s_add_u32 m0, s57, 49152\n"
    "v_mfma_f32_16x16x32_bf16 v[20:23], v[144:147], a[100:103], v[20:23]\n"
    "buffer_load_dwordx4 v0, s[36:39], s81 offen nt lds\n"
    "v_mfma_f32_16x16x32_bf16 v[20:23], v[168:171], a[104:107], v[20:23]\n"
    "ds_read_b128 a[168:171], v4 offset:12800\n"
    "v_mfma_f32_16x16x32_bf16 v[20:23], v[192:195], a[108:111], v[20:23]\n"
    "v_cvt_pk_bf16_f32 v50, v24, v25\n"
    "v_cvt_pk_bf16_f32 v51, v26, v27\n"
    "v_add_u32 v4, s78, v4\n"
    "v_mfma_f32_16x16x32_bf16 v[20:23], a[0:3], a[112:115], v[20:23]\n"
    "ds_read_b128 a[172:175], v5 offset:12800\n"
    "v_mfma_f32_16x16x32_bf16 v[20:23], a[24:27], a[116:119], v[20:23]\n"
    "ds_write_b64 v9, v[50:51] offset:8\n"
    "v_add_u32 v5, s78, v5\n"
    "v_mfma_f32_16x16x32_bf16 v[20:23], a[48:51], a[120:123], v[20:23]\n"
    "buffer_load_dwordx4 v1, s[36:39], s81 offen offset:1024 nt lds\n"
    "v_mfma_f32_16x16x32_bf16 v[20:23], a[72:75], a[124:127], v[20:23]\n"
    "ds_read_b128 a[176:179], v6 offset:12800\n"
    "v_mfma_f32_16x16x32_bf16 v[20:23], v[224:227], a[128:131], v[20:23]\n"
    "buffer_load_dwordx4 v2, s[36:39], s81 offen offset:2048 nt lds\n"
    "v_add_u32 v6, s78, v6\n"
    "v_mfma_f32_16x16x32_bf16 v[20:23], v[248:251], a[132:135], v[20:23]\n"
    "ds_read_b128 a[180:183], v7 offset:12800\n"
    "s_add_u32 m0, s57, 61440\n"
    "v_mfma_f32_16x16x32_bf16 v[24:27], v[252:255], a[132:135], 0\n"
    "buffer_load_dword a224, off, s[36:39], s33\n"
    "v_add_u32 v7, s78, v7\n"
    "v_mfma_f32_16x16x32_bf16 v[24:27], v[228:231], a[128:131], v[24:27]\n"
    "buffer_load_dword a224, off, s[36:39], s33\n"
    "v_mfma_f32_16x16x32_bf16 v[24:27], a[76:79], a[124:127], v[24:27]\n"
    "buffer_load_dword a224, off, s[36:39], s33\n"
    "v_mfma_f32_16x16x32_bf16 v[24:27], a[52:55], a[120:123], v[24:27]\n"
    "s_memrealtime s[92:93]\n"
    "v_mfma_f32_16x16x32_bf16 v[24:27], a[28:31], a[116:119], v[24:27]\n"
    "v_mfma_f32_16x16x32_bf16 v[24:27], a[4:7], a[112:115], v[24:27]\n"
    "v_cvt_pk_bf16_f32 v52, v20, v21\n"
    "v_cvt_pk_bf16_f32 v53, v22, v23\n"
    "v_mfma_f32_16x16x32_bf16 v[24:27], v[196:199], a[108:111], v[24:27]\n"
    "ds_write_b64 v10, v[52:53]\n"
    "v_mfma_f32_16x16x32_bf16 v[24:27], v[172:175], a[104:107], v[24:27]\n"
    "v_mfma_f32_16x16x32_bf16 v[24:27], v[148:151], a[100:103], v[24:27]\n"
    "s_waitcnt vmcnt(12) lgkmcnt(0)\n"
    "s_sub_u32 s85, s92, s89\n"
    "ds_bpermute_b32 v18, v18, v3\n"
    "v_readlane_b32 s81, v3, s86\n"
    "v_mfma_f32_16x16x32_bf16 v[24:27], v[124:127], a[96:99], v[24:27]\n"
    "s_add_i32 s63, s63, s85\n"
    "s_max_i32 s63, s63, s62\n"
    "s_add_u32 s89, s89, s63\n"
    "v_mfma_f32_16x16x32_bf16 v[24:27], v[100:103], a[92:95], v[24:27]\n"
    "v_mfma_f32_16x16x32_bf16 v[24:27], v[76:79], a[88:91], v[24:27]\n"
    "s_branch 52f\n"
    ".p2align 6\n"
    "51:\n"
    "v_mfma_f32_16x16x32_bf16 v[20:23], v[56:59], a[88:91], 0\n"
    "s_cmp_ge_u32 s86, s65\n"
    "v_mfma_f32_16x16x32_bf16 v[20:23], v[80:83], a[92:95], v[20:23]\n"
    "s_cbranch_scc1 45b\n"
    "v_mfma_f32_16x16x32_bf16 v[20:23], v[104:107], a[96:99], v[20:23]\n"
    "v_add_u32 v14, 1, v14\n"
    "v_mfma_f32_16x16x32_bf16 v[20:23], v[128:131], a[100:103], v[20:23]\n"
    "v_cvt_pk_bf16_f32 v54, v24, v25\n"
    "v_mfma_f32_16x16x32_bf16 v[20:23], v[152:155], a[104:107], v[20:23]\n"
    "v_cvt_pk_bf16_f32 v55, v26, v27\n"
    "v_mfma_f32_16x16x32_bf16 v[20:23], v[176:179], a[108:111], v[20:23]\n"
    "ds_write_b64 v10, v[54:55] offset:12296\n"
    "v_mfma_f32_16x16x32_bf16 v[20:23], v[200:203], a[112:115], v[20:23]\n"
    "s_add_u32 s89, s89, s94\n"
    "v_mfma_f32_16x16x32_bf16 v[20:23], a[8:11], a[116:119], v[20:23]\n"
    "s_waitcnt vmcnt(15)\n"
    "v_mfma_f32_16x16x32_bf16 v[20:23], a[32:35], a[120:123], v[20:23]\n"
    "ds_write_b32 v13, v14\n"
    "v_mfma_f32_16x16x32_bf16 v[20:23], a[56:59], a[124:127], v[20:23]\n"
    "v_add_u32 v10, s78, v10\n"
    "v_mfma_f32_16x16x32_bf16 v[20:23], a[80:83], a[128:131], v[20:23]\n"
    "buffer_load_dwordx4 v0, s[36:39], s81 offen nt lds\n"
    "v_mfma_f32_16x16x32_bf16 v[20:23], v[232:235], a[132:135], v[20:23]\n"
    "s_add_u32 s86, s86, 1\n"
    "v_mfma_f32_16x16x32_bf16 v[24:27], v[236:239], a[132:135], 0\n"
    "v_mfma_f32_16x16x32_bf16 v[24:27], a[84:87], a[128:131], v[24:27]\n"
    "buffer_load_dwordx4 v1, s[36:39], s81 offen offset:1024 nt lds\n"
    "v_mfma_f32_16x16x32_bf16 v[24:27], a[60:63], a[124:127], v[24:27]\n"
    "v_mfma_f32_16x16x32_bf16 v[24:27], a[36:39], a[120:123], v[24:27]\n"
    "v_mfma_f32_16x16x32_bf16 v[24:27], a[12:15], a[116:119], v[24:27]\n"
    "buffer_load_dwordx4 v2, s[36:39], s81 offen offset:2048 nt lds\n"
    "46:\n"
    "v_mfma_f32_16x16x32_bf16 v[24:27], v[204:207], a[112:115], v[24:27]\n"
    "ds_read_b32 v15, v12\n"
    "v_mfma_f32_16x16x32_bf16 v[24:27], v[180:183], a[108:111], v[24:27]\n"
    "v_cvt_pk_bf16_f32 v44, v20, v21\n"
    "v_mfma_f32_16x16x32_bf16 v[24:27], v[156:159], a[104:107], v[24:27]\n"
    "v_cvt_pk_bf16_f32 v45, v22, v23\n"
    "v_mfma_f32_16x16x32_bf16 v[24:27], v[132:135], a[100:103], v[24:27]\n"
    "v_readlane_b32 s81, v3, s86\n"
    "v_mfma_f32_16x16x32_bf16 v[24:27], v[108:111], a[96:99], v[24:27]\n"
    "s_waitcnt lgkmcnt(0)\n"
    "v_cmp_lt_u32 vcc, v15, v14\n"
    "v_mfma_f32_16x16x32_bf16 v[24:27], v[84:87], a[92:95], v[24:27]\n"
    "s_cbranch_vccnz 41b\n"
    "42:\n"
    "v_mfma_f32_16x16x32_bf16 v[24:27], v[60:63], a[88:91], v[24:27]\n"
    "ds_read_b128 a[184:187], v11 offset:12288\n"
    "v_mfma_f32_16x16x32_bf16 v[20:23], v[64:67], a[88:91], 0\n"
    "ds_read_b128 a[188:191], v11 offset:13312\n"
    "v_mfma_f32_16x16x32_bf16 v[20:23], v[88:91], a[92:95], v[20:23]\n"
    "ds_read_b128 a[192:195], v11 offset:14336\n"
    "v_mfma_f32_16x16x32_bf16 v[20:23], v[112:115], a[96:99], v[20:23]\n"
    "ds_write_b64 v8, v[44:45]\n"
    "v_mfma_f32_16x16x32_bf16 v[20:23], v[136:139], a[100:103], v[20:23]\n"
    "ds_read_b128 a[136:139], v4 offset:12288\n"
    "v_mfma_f32_16x16x32_bf16 v[20:23], v[160:163], a[104:107], v[20:23]\n"
    "v_cvt_pk_bf16_f32 v46, v24, v25\n"
    "v_mfma_f32_16x16x32_bf16 v[20:23], v[184:187], a[108:111], v[20:23]\n"
    "v_cvt_pk_bf16_f32 v47, v26, v27\n"
    "v_mfma_f32_16x16x32_bf16 v[20:23], v[208:211], a[112:115], v[20:23]\n"
    "ds_read_b128 a[140:143], v5 offset:12288\n"
    "v_mfma_f32_16x16x32_bf16 v[20:23], a[16:19], a[116:119], v[20:23]\n"
    "ds_write_b64 v8, v[46:47] offset:8\n"
    "v_mfma_f32_16x16x32_bf16 v[20:23], a[40:43], a[120:123], v[20:23]\n"
    "v_readlane_b32 s82, v18, s86\n"
    "v_mfma_f32_16x16x32_bf16 v[20:23], a[64:67], a[124:127], v[20:23]\n"
    "ds_read_b128 a[144:147], v6 offset:12288\n"
    "v_mfma_f32_16x16x32_bf16 v[20:23], v[216:219], a[128:131], v[20:23]\n"
    "s_waitcnt lgkmcnt(5)\n"
    "v_mfma_f32_16x16x32_bf16 v[20:23], v[240:243], a[132:135], v[20:23]\n"
    "buffer_store_dwordx4 a[184:187], v0, s[40:43], s82 offen sc1\n"
    "v_mfma_f32_16x16x32_bf16 v[24:27], v[244:247], a[132:135], 0\n"
    "ds_read_b128 a[148:151], v7 offset:12288\n"
    "v_mfma_f32_16x16x32_bf16 v[24:27], v[220:223], a[128:131], v[24:27]\n"
    "s_addk_i32 m0, 12288\n"
    "v_mfma_f32_16x16x32_bf16 v[24:27], a[68:71], a[124:127], v[24:27]\n"
    "v_add_u32 v11, s78, v11\n"
    "v_mfma_f32_16x16x32_bf16 v[24:27], a[44:47], a[120:123], v[24:27]\n"
    "ds_read_b128 a[152:155], v4 offset:12544\n"
    "v_mfma_f32_16x16x32_bf16 v[24:27], a[20:23], a[116:119], v[24:27]\n"
    "v_cvt_pk_bf16_f32 v48, v20, v21\n"
    "v_mfma_f32_16x16x32_bf16 v[24:27], v[212:215], a[112:115], v[24:27]\n"
    "v_cvt_pk_bf16_f32 v49, v22, v23\n"
    "v_mfma_f32_16x16x32_bf16 v[24:27], v[188:191], a[108:111], v[24:27]\n"
    "ds_read_b128 a[156:159], v5 offset:12544\n"
    "v_mfma_f32_16x16x32_bf16 v[24:27], v[164:167], a[104:107], v[24:27]\n"
    "ds_write_b64 v9, v[48:49]\n"
    "v_mfma_f32_16x16x32_bf16 v[24:27], v[140:143], a[100:103], v[24:27]\n"
    "buffer_store_dwordx4 a[188:191], v1, s[40:43], s82 offen offset:1024 sc1\n"
    "v_mfma_f32_16x16x32_bf16 v[24:27], v[116:119], a[96:99], v[24:27]\n"
    "ds_read_b128 a[160:163], v6 offset:12544\n"
    "v_mfma_f32_16x16x32_bf16 v[24:27], v[92:95], a[92:95], v[24:27]\n"
    "v_mfma_f32_16x16x32_bf16 v[24:27], v[68:71], a[88:91], v[24:27]\n"
    "ds_read_b128 a[164:167], v7 offset:12544\n"
    "v_mfma_f32_16x16x32_bf16 v[20:23], v[72:75], a[88:91], 0\n"
    "v_mfma_f32_16x16x32_bf16 v[20:23], v[96:99], a[92:95], v[20:23]\n"
    "ds_read_b128 a[168:171], v4 offset:12800\n"
    "v_mfma_f32_16x16x32_bf16 v[20:23], v[120:123], a[96:99], v[20:23]\n"
    "v_mfma_f32_16x16x32_bf16 v[20:23], v[144:147], a[100:103], v[20:23]\n"
    "ds_read_b128 a[172:175], v5 offset:12800\n"
    "v_mfma_f32_16x16x32_bf16 v[20:23], v[168:171], a[104:107], v[20:23]\n"
    "v_cvt_pk_bf16_f32 v50, v24, v25\n"
    "v_mfma_f32_16x16x32_bf16 v[20:23], v[192:195], a[108:111], v[20:23]\n"
    "v_cvt_pk_bf16_f32 v51, v26, v27\n"
    "v_mfma_f32_16x16x32_bf16 v[20:23], a[0:3], a[112:115], v[20:23]\n"
    "ds_read_b128 a[176:179], v6 offset:12800\n"
    "v_mfma_f32_16x16x32_bf16 v[20:23], a[24:27], a[116:119], v[20:23]\n"
    "ds_write_b64 v9, v[50:51] offset:8\n"
    "v_mfma_f32_16x16x32_bf16 v[20:23], a[48:51], a[120:123], v[20:23]\n"
    "buffer_store_dwordx4 a[192:195], v2, s[40:43], s82 offen offset:2048 sc1\n"
    "v_mfma_f32_16x16x32_bf16 v[20:23], a[72:75], a[124:127], v[20:23]\n"
    "ds_read_b128 a[180:183], v7 offset:12800\n"
    "v_mfma_f32_16x16x32_bf16 v[20:23], v[224:227], a[128:131], v[20:23]\n"
    "s_memrealtime s[92:93]\n"
    "v_mfma_f32_16x16x32_bf16 v[20:23], v[248:251], a[132:135], v[20:23]\n"
    "s_cmp_eq_u32 m0, s77\n"
    "v_mfma_f32_16x16x32_bf16 v[24:27], v[252:255], a[132:135], 0\n"
    "s_cselect_b32 s78, s87, s88\n"
    "v_mfma_f32_16x16x32_bf16 v[24:27], v[228:231], a[128:131], v[24:27]\n"
    "v_add_u32 v4, s78, v4\n"
    "v_mfma_f32_16x16x32_bf16 v[24:27], a[76:79], a[124:127], v[24:27]\n"
    "v_add_u32 v5, s78, v5\n"
    "v_mfma_f32_16x16x32_bf16 v[24:27], a[52:55], a[120:123], v[24:27]\n"
    "v_add_u32 v6, s78, v6\n"
    "v_mfma_f32_16x16x32_bf16 v[24:27], a[28:31], a[116:119], v[24:27]\n"
    "s_waitcnt lgkmcnt(0)\n"
    "s_sub_u32 s85, s92, s89\n"
    "v_mfma_f32_16x16x32_bf16 v[24:27], a[4:7], a[112:115], v[24:27]\n"
    "s_cbranch_scc1 43b\n"
    "44:\n"
    "v_mfma_f32_16x16x32_bf16 v[24:27], v[196:199], a[108:111], v[24:27]\n"
    "v_cvt_pk_bf16_f32 v52, v20, v21\n"
    "v_mfma_f32_16x16x32_bf16 v[24:27], v[172:175], a[104:107], v[24:27]\n"
    "v_cvt_pk_bf16_f32 v53, v22, v23\n"
    "v_mfma_f32_16x16x32_bf16 v[24:27], v[148:151], a[100:103], v[24:27]\n"
    "ds_write_b64 v10, v[52:53]\n"
    "v_mfma_f32_16x16x32_bf16 v[24:27], v[124:127], a[96:99], v[24:27]\n"
    "v_add_u32 v7, s78, v7\n"
    "v_mfma_f32_16x16x32_bf16 v[24:27], v[100:103], a[92:95], v[24:27]\n"
    "s_cmp_lt_u32 s86, s76\n"
    "v_mfma_f32_16x16x32_bf16 v[24:27], v[76:79], a[88:91], v[24:27]\n"
    "s_cbranch_scc0 55f\n"
    "52:\n"
    "v_mfma_f32_16x16x32_bf16 v[20:23], v[56:59], a[136:139], 0\n"
    "s_cmp_ge_u32 s86, s65\n"
    "v_mfma_f32_16x16x32_bf16 v[20:23], v[80:83], a[140:143], v[20:23]\n"
    "s_cbranch_scc1 65b\n"
    "v_mfma_f32_16x16x32_bf16 v[20:23], v[104:107], a[144:147], v[20:23]\n"
    "v_add_u32 v14, 1, v14\n"
    "v_mfma_f32_16x16x32_bf16 v[20:23], v[128:131], a[148:151], v[20:23]\n"
    "v_cvt_pk_bf16_f32 v54, v24, v25\n"
    "v_mfma_f32_16x16x32_bf16 v[20:23], v[152:155], a[152:155], v[20:23]\n"
    "v_cvt_pk_bf16_f32 v55, v26, v27\n"
    "v_mfma_f32_16x16x32_bf16 v[20:23], v[176:179], a[156:159], v[20:23]\n"
    "ds_write_b64 v10, v[54:55] offset:8\n"
    "v_mfma_f32_16x16x32_bf16 v[20:23], v[200:203], a[160:163], v[20:23]\n"
    "s_add_u32 s89, s89, s94\n"
    "v_mfma_f32_16x16x32_bf16 v[20:23], a[8:11], a[164:167], v[20:23]\n"
    "s_waitcnt vmcnt(15)\n"
    "v_mfma_f32_16x16x32_bf16 v[20:23], a[32:35], a[168:171], v[20:23]\n"
    "ds_write_b32 v13, v14\n"
    "v_mfma_f32_16x16x32_bf16 v[20:23], a[56:59], a[172:175], v[20:23]\n"
    "v_mfma_f32_16x16x32_bf16 v[20:23], a[80:83], a[176:179], v[20:23]\n"
    "buffer_load_dwordx4 v0, s[36:39], s81 offen nt lds\n"
    "v_mfma_f32_16x16x32_bf16 v[20:23], v[232:235], a[180:183], v[20:23]\n"
    "s_add_u32 s86, s86, 1\n"
    "v_mfma_f32_16x16x32_bf16 v[24:27], v[236:239], a[180:183], 0\n"
    "v_mfma_f32_16x16x32_bf16 v[24:27], a[84:87], a[176:179], v[24:27]\n"
    "buffer_load_dwordx4 v1, s[36:39], s81 offen offset:1024 nt lds\n"
    "v_mfma_f32_16x16x32_bf16 v[24:27], a[60:63], a[172:175], v[24:27]\n"
    "v_mfma_f32_16x16x32_bf16 v[24:27], a[36:39], a[168:171], v[24:27]\n"
    "v_mfma_f32_16x16x32_bf16 v[24:27], a[12:15], a[164:167], v[24:27]\n"
    "buffer_load_dwordx4 v2, s[36:39], s81 offen offset:2048 nt lds\n"
    "66:\n"
    "v_mfma_f32_16x16x32_bf16 v[24:27], v[204:207], a[160:163], v[24:27]\n"
    "ds_read_b32 v15, v12\n"
    "v_mfma_f32_16x16x32_bf16 v[24:27], v[180:183], a[156:159], v[24:27]\n"
    "v_cvt_pk_bf16_f32 v44, v20, v21\n"
    "v_mfma_f32_16x16x32_bf16 v[24:27], v[156:159], a[152:155], v[24:27]\n"
    "v_cvt_pk_bf16_f32 v45, v22, v23\n"
    "v_mfma_f32_16x16x32_bf16 v[24:27], v[132:135], a[148:151], v[24:27]\n"
    "v_readlane_b32 s81, v3, s86\n"
    "v_mfma_f32_16x16x32_bf16 v[24:27], v[108:111], a[144:147], v[24:27]\n"
    "s_waitcnt lgkmcnt(0)\n"
    "v_cmp_lt_u32 vcc, v15, v14\n"
    "v_mfma_f32_16x16x32_bf16 v[24:27], v[84:87], a[140:143], v[24:27]\n"
    "s_cbranch_vccnz 61b\n"
    "62:\n"
    "v_mfma_f32_16x16x32_bf16 v[24:27], v[60:63], a[136:139], v[24:27]\n"
    "ds_read_b128 a[184:187], v11\n"
    "v_mfma_f32_16x16x32_bf16 v[20:23], v[64:67], a[136:139], 0\n"
    "ds_read_b128 a[188:191], v11 offset:1024\n"
    "v_mfma_f32_16x16x32_bf16 v[20:23], v[88:91], a[140:143], v[20:23]\n"
    "ds_read_b128 a[192:195], v11 offset:2048\n"
    "v_mfma_f32_16x16x32_bf16 v[20:23], v[112:115], a[144:147], v[20:23]\n"
    "ds_write_b64 v8, v[44:45] offset:12288\n"
    "v_mfma_f32_16x16x32_bf16 v[20:23], v[136:139], a[148:151], v[20:23]\n"
    "ds_read_b128 a[88:91], v4\n"
    "v_mfma_f32_16x16x32_bf16 v[20:23], v[160:163], a[152:155], v[20:23]\n"
    "v_cvt_pk_bf16_f32 v46, v24, v25\n"
    "v_mfma_f32_16x16x32_bf16 v[20:23], v[184:187], a[156:159], v[20:23]\n"
    "v_cvt_pk_bf16_f32 v47, v26, v27\n"
    "v_mfma_f32_16x16x32_bf16 v[20:23], v[208:211], a[160:163], v[20:23]\n"
    "ds_read_b128 a[92:95], v5\n"
    "v_mfma_f32_16x16x32_bf16 v[20:23], a[16:19], a[164:167], v[20:23]\n"
    "ds_write_b64 v8, v[46:47] offset:12296\n"
    "v_mfma_f32_16x16x32_bf16 v[20:23], a[40:43], a[168:171], v[20:23]\n"
    "v_readlane_b32 s82, v18, s86\n"
    "v_mfma_f32_16x16x32_bf16 v[20:23], a[64:67], a[172:175], v[20:23]\n"
    "ds_read_b128 a[96:99], v6\n"
    "v_mfma_f32_16x16x32_bf16 v[20:23], v[216:219], a[176:179], v[20:23]\n"
    "s_waitcnt lgkmcnt(5)\n"
    "v_mfma_f32_16x16x32_bf16 v[20:23], v[240:243], a[180:183], v[20:23]\n"
    "buffer_store_dwordx4 a[184:187], v0, s[40:43], s82 offen sc1\n"
    "v_mfma_f32_16x16x32_bf16 v[24:27], v[244:247], a[180:183], 0\n"
    "ds_read_b128 a[100:103], v7\n"
    "v_mfma_f32_16x16x32_bf16 v[24:27], v[220:223], a[176:179], v[24:27]\n"
    "s_addk_i32 m0, 12288\n"
    "v_mfma_f32_16x16x32_bf16 v[24:27], a[68:71], a[172:175], v[24:27]\n"
    "v_add_u32 v8, s78, v8\n"
    "v_mfma_f32_16x16x32_bf16 v[24:27], a[44:47], a[168:171], v[24:27]\n"
    "ds_read_b128 a[104:107], v4 offset:256\n"
    "v_mfma_f32_16x16x32_bf16 v[24:27], a[20:23], a[164:167], v[24:27]\n"
    "v_cvt_pk_bf16_f32 v48, v20, v21\n"
    "v_mfma_f32_16x16x32_bf16 v[24:27], v[212:215], a[160:163], v[24:27]\n"
    "v_cvt_pk_bf16_f32 v49, v22, v23\n"
    "v_mfma_f32_16x16x32_bf16 v[24:27], v[188:191], a[156:159], v[24:27]\n"
    "ds_read_b128 a[108:111], v5 offset:256\n"
    "v_mfma_f32_16x16x32_bf16 v[24:27], v[164:167], a[152:155], v[24:27]\n"
    "ds_write_b64 v9, v[48:49] offset:12288\n"
    "v_mfma_f32_16x16x32_bf16 v[24:27], v[140:143], a[148:151], v[24:27]\n"
    "buffer_store_dwordx4 a[188:191], v1, s[40:43], s82 offen offset:1024 sc1\n"
    "v_mfma_f32_16x16x32_bf16 v[24:27], v[116:119], a[144:147], v[24:27]\n"
    "ds_read_b128 a[112:115], v6 offset:256\n"
    "v_mfma_f32_16x16x32_bf16 v[24:27], v[92:95], a[140:143], v[24:27]\n"
    "v_mfma_f32_16x16x32_bf16 v[24:27], v[68:71], a[136:139], v[24:27]\n"
    "ds_read_b128 a[116:119], v7 offset:256\n"
    "v_mfma_f32_16x16x32_bf16 v[20:23], v[72:75], a[136:139], 0\n"
    "v_mfma_f32_16x16x32_bf16 v[20:23], v[96:99], a[140:143], v[20:23]\n"
    "ds_read_b128 a[120:123], v4 offset:512\n"
    "v_mfma_f32_16x16x32_bf16 v[20:23], v[120:123], a[144:147], v[20:23]\n"
    "v_mfma_f32_16x16x32_bf16 v[20:23], v[144:147], a[148:151], v[20:23]\n"
    "ds_read_b128 a[124:127], v5 offset:512\n"
    "v_mfma_f32_16x16x32_bf16 v[20:23], v[168:171], a[152:155], v[20:23]\n"
    "v_cvt_pk_bf16_f32 v50, v24, v25\n"
    "v_mfma_f32_16x16x32_bf16 v[20:23], v[192:195], a[156:159], v[20:23]\n"
    "v_cvt_pk_bf16_f32 v51, v26, v27\n"
    "v_mfma_f32_16x16x32_bf16 v[20:23], a[0:3], a[160:163], v[20:23]\n"
    "ds_read_b128 a[128:131], v6 offset:512\n"
    "v_mfma_f32_16x16x32_bf16 v[20:23], a[24:27], a[164:167], v[20:23]\n"
    "ds_write_b64 v9, v[50:51] offset:12296\n"
    "v_mfma_f32_16x16x32_bf16 v[20:23], a[48:51], a[168:171], v[20:23]\n"
    "buffer_store_dwordx4 a[192:195], v2, s[40:43], s82 offen offset:2048 sc1\n"
    "v_mfma_f32_16x16x32_bf16 v[20:23], a[72:75], a[172:175], v[20:23]\n"
    "ds_read_b128 a[132:135], v7 offset:512\n"
    "v_mfma_f32_16x16x32_bf16 v[20:23], v[224:227], a[176:179], v[20:23]\n"
    "s_memrealtime s[92:93]\n"
    "v_mfma_f32_16x16x32_bf16 v[20:23], v[248:251], a[180:183], v[20:23]\n"
    "s_cmp_eq_u32 m0, s80\n"
    "v_mfma_f32_16x16x32_bf16 v[24:27], v[252:255], a[180:183], 0\n"
    "s_cselect_b32 m0, s57, m0\n"
    "v_mfma_f32_16x16x32_bf16 v[24:27], v[228:231], a[176:179], v[24:27]\n"
    "v_add_u32 v9, s78, v9\n"
    "v_mfma_f32_16x16x32_bf16 v[24:27], a[76:79], a[172:175], v[24:27]\n"
    "v_mfma_f32_16x16x32_bf16 v[24:27], a[52:55], a[168:171], v[24:27]\n"
    "v_mfma_f32_16x16x32_bf16 v[24:27], a[28:31], a[164:167], v[24:27]\n"
    "s_waitcnt lgkmcnt(0)\n"
    "s_sub_u32 s85, s92, s89\n"
    "v_mfma_f32_16x16x32_bf16 v[24:27], a[4:7], a[160:163], v[24:27]\n"
    "s_cbranch_scc1 63b\n"
    "64:\n"
    "v_mfma_f32_16x16x32_bf16 v[24:27], v[196:199], a[156:159], v[24:27]\n"
    "v_cvt_pk_bf16_f32 v52, v20, v21\n"
    "v_mfma_f32_16x16x32_bf16 v[24:27], v[172:175], a[152:155], v[24:27]\n"
    "v_cvt_pk_bf16_f32 v53, v22, v23\n"
    "v_mfma_f32_16x16x32_bf16 v[24:27], v[148:151], a[148:151], v[24:27]\n"
    "ds_write_b64 v10, v[52:53] offset:12288\n"
    "v_mfma_f32_16x16x32_bf16 v[24:27], v[124:127], a[144:147], v[24:27]\n"
    "v_mfma_f32_16x16x32_bf16 v[24:27], v[100:103], a[140:143], v[24:27]\n"
    "s_cmp_lt_u32 s86, s76\n"
    "v_mfma_f32_16x16x32_bf16 v[24:27], v[76:79], a[136:139], v[24:27]\n"
    "s_cbranch_scc1 51b\n"
    "v_add_u32 v10, 12288, v10\n"
    "v_add_u32 v11, 12288, v11\n"
    "55:\n"
    "s_add_u32 s86, s86, 1\n"
    "v_readlane_b32 s82, v18, s86\n"
    "s_nop 4\n"
    "v_cvt_pk_bf16_f32 v54, v24, v25\n"
    "v_cvt_pk_bf16_f32 v55, v26, v27\n"
    "ds_write_b64 v10, v[54:55] offset:8\n"
    "s_waitcnt lgkmcnt(0)\n"
    "s_barrier\n"
    "ds_read_b128 a[184:187], v11\n"
    "ds_read_b128 a[188:191], v11 offset:1024\n"
    "ds_read_b128 a[192:195], v11 offset:2048\n"
    "s_waitcnt lgkmcnt(2)\n"
    "buffer_store_dwordx4 a[184:187], v0, s[40:43], s82 offen sc1\n"
    "s_waitcnt lgkmcnt(1)\n"
    "buffer_store_dwordx4 a[188:191], v1, s[40:43], s82 offen offset:1024 sc1\n"
    "s_waitcnt lgkmcnt(0)\n"
    "buffer_store_dwordx4 a[192:195], v2, s[40:43], s82 offen offset:2048 sc1\n"
    "s_waitcnt vmcnt(0)\n"
    "s_endpgm\n"
    "75:\n"
    "s_sleep 2\n"
    "global_load_dwordx4 v[52:55], v16, s[54:55] sc1\n"
    "s_waitcnt vmcnt(0)\n"
    "v_xor_b32 v52, s16, v52\n"
    "v_xor_b32 v53, s17, v53\n"
    "v_xor_b32 v54, s16, v54\n"
    "v_xor_b32 v55, s17, v55\n"
    "v_or_b32 v52, v52, v54\n"
    "v_or_b32 v54, v53, v55\n"
    "v_lshrrev_b32 v54, 4, v54\n"
    "v_or_b32 v52, v52, v54\n"
    "v_cmp_ne_u32 vcc, 0, v52\n"
    "v_and_b32 v52, 1, v53\n"
    "v_and_b32 v54, 1, v55\n"
    "s_cbranch_vccnz 75b\n"
    "s_branch 77b\n"
    "79:\n"
    "s_nop 0\n"
    : "+{s[0:1]}"(dp), "+{s[2:3]}"(ka), "+{s[4:5]}"(did), "+{s6}"(bid), "+{v0}"(tid)
    : [lds] "{s98}"(lds0)
    : "v1","v2","v3","v4","v5","v6","v7","v8","v9","v10","v11","v12","v13","v14","v15","v16","v17","v18","v19","v20","v21","v22","v23","v24","v25","v26","v27","v28","v29","v30","v31","v32","v33","v34","v35","v36","v37","v38","v39","v40","v41","v42","v43","v44","v45","v46","v47","v48","v49","v50","v51","v52","v53","v54","v55","v56","v57","v58","v59","v60","v61","v62","v63","v64","v65","v66","v67","v68","v69","v70","v71","v72","v73","v74","v75","v76","v77","v78","v79","v80","v81","v82","v83","v84","v85","v86","v87","v88","v89","v90","v91","v92","v93","v94","v95","v96","v97","v98","v99","v100","v101","v102","v103","v104","v105","v106","v107","v108","v109","v110","v111","v112","v113","v114","v115","v116","v117","v118","v119","v120","v121","v122","v123","v124","v125","v126","v127","v128","v129","v130","v131","v132","v133","v134","v135","v136","v137","v138","v139","v140","v141","v142","v143","v144","v145","v146","v147","v148","v149","v150","v151","v152","v153","v154","v155","v156","v157","v158","v159","v160","v161","v162","v163","v164","v165","v166","v167","v168","v169","v170","v171","v172","v173","v174","v175","v176","v177","v178","v179","v180","v181","v182","v183","v184","v185","v186","v187","v188","v189","v190","v191","v192","v193","v194","v195","v196","v197","v198","v199","v200","v201","v202","v203","v204","v205","v206","v207","v208","v209","v210","v211","v212","v213","v214","v215","v216","v217","v218","v219","v220","v221","v222","v223","v224","v225","v226","v227","v228","v229","v230","v231","v232","v233","v234","v235","v236","v237","v238","v239","v240","v241","v242","v243","v244","v245","v246","v247","v248","v249","v250","v251","v252","v253","v254","v255","a0","a1","a2","a3","a4","a5","a6","a7","a8","a9","a10","a11","a12","a13","a14","a15","a16","a17","a18","a19","a20","a21","a22","a23","a24","a25","a26","a27","a28","a29","a30","a31","a32","a33","a34","a35","a36","a37","a38","a39","a40","a41","a42","a43","a44","a45","a46","a47","a48","a49","a50","a51","a52","a53","a54","a55","a56","a57","a58","a59","a60","a61","a62","a63","a64","a65","a66","a67","a68","a69","a70","a71","a72","a73","a74","a75","a76","a77","a78","a79","a80","a81","a82","a83","a84","a85","a86","a87","a88","a89","a90","a91","a92","a93","a94","a95","a96","a97","a98","a99","a100","a101","a102","a103","a104","a105","a106","a107","a108","a109","a110","a111","a112","a113","a114","a115","a116","a117","a118","a119","a120","a121","a122","a123","a124","a125","a126","a127","a128","a129","a130","a131","a132","a133","a134","a135","a136","a137","a138","a139","a140","a141","a142","a143","a144","a145","a146","a147","a148","a149","a150","a151","a152","a153","a154","a155","a156","a157","a158","a159","a160","a161","a162","a163","a164","a165","a166","a167","a168","a169","a170","a171","a172","a173","a174","a175","a176","a177","a178","a179","a180","a181","a182","a183","a184","a185","a186","a187","a188","a189","a190","a191","a192","a193","a194","a195","a196","a197","a198","a199","a200","a201","a202","a203","a204","a205","a206","a207","a208","a209","a210","a211","a212","a213","a214","a215","a216","a217","a218","a219","a220","a221","a222","a223","a224","s7","s8","s9","s10","s11","s12","s13","s14","s15","s16","s17","s18","s19","s20","s21","s22","s23","s24","s25","s26","s27","s28","s29","s30","s31","s33","s34","s35","s36","s37","s38","s39","s40","s41","s42","s43","s44","s45","s46","s47","s48","s49","s50","s51","s52","s53","s54","s55","s56","s57","s58","s59","s60","s61","s62","s63","s64","s65","s66","s67","s68","s69","s70","s71","s72","s73","s74","s75","s76","s77","s78","s79","s80","s81","s82","s83","s84","s85","s86","s87","s88","s89","s90","s91","s92","s93","s94","s95","s96","s97","vcc","scc","memory");
}

extern "C" void kernel_launch(void* const* d_in, const int* in_sizes, int n_in,
                              void* d_out, int out_size, void* d_ws, size_t ws_size,
                              hipStream_t stream) {
  Shapes sh;
  for (int j = 0; j < 16; ++j)
    for (int w = 0; w < 4; ++w) {
      const int ty = j >= 11, per = ty ? 10 : 11, n = 16 * (j - 11 * ty);
      auto row = [&](int k) { return ty ? 64 * (k / 10) + 11 + k % 10 : 64 * (k / 22) + 21 * (k / 11 % 2) + k % 11; };
      const int n0 = n + 4 * w, bp = per - n0 % per;
      uint32_t* e = sh.v + 16 * j + 4 * w;
      e[0] = row(n0) * 768 - 512;
      e[1] = (row(n0 + bp) - bp) * 768 - 512;
      e[2] = bp * 768;
      e[3] = (row(n + 8) + 16) * 768 + ty;
    }
  const int grid = 256, maxr = 40;
  const size_t M = (size_t)in_sizes[0] / CH;
  const uint32_t fl = 0x00020000u;
  for (size_t r0 = 0; r0 < M; r0 += (size_t)maxr * grid * TM) {
    const size_t rows = M - r0 < (size_t)maxr * grid * TM ? M - r0 : (size_t)maxr * grid * TM;
    const int nr = (int)((rows + grid * TM - 1) / (grid * TM)), nit = nr < 2 ? 2 : nr;
    const uint32_t nbytes = (uint32_t)(rows * CH * 2);
    const uint32_t np = (nbytes >> 21) & ~1u;
    const uint64_t pages = (np >= 40 ? (uint32_t)(~0u << (((np - 3) & ~7u) - 32)) : 0u) | (uint64_t)(np >= 2 ? (np - 2) << 21 : 0u) << 32;
    dwsep_kernel<<<grid, 256, 0, stream>>>((const char*)d_in[0] + r0 * CH * 2, nbytes, fl, (char*)d_out + r0 * CH * 2, nbytes, fl,
                                           (const char*)d_in[2], CH * CH * 2, fl, (const char*)d_in[1], CH * 2, fl,
                                           (uint32_t)nit, (uint32_t)grid * TILE_BYTES, d_ws, pages, sh);
  }
}
```

```cpp
#include <hip/hip_runtime.h>
#include <stdint.h>

#define CH 384
#define TM 16
#define TILE_BYTES (TM * CH * 2)
#define LDS_BYTES (13 * TILE_BYTES + 1024 + 64)

struct Shapes { uint32_t v[256]; };
extern "C" __device__ uint64_t dispatch_id_() __asm("llvm.amdgcn.dispatch.id");

__global__ __launch_bounds__(256, 1)
void dwsep_kernel(const char* x, uint32_t xbytes, uint32_t f0, char* out, uint32_t obytes, uint32_t f1,
                  const char* pw, uint32_t pwbytes, uint32_t f2, const char* dw, uint32_t dwbytes, uint32_t f3,
                  uint32_t nit, uint32_t tstride, void* ws, uint64_t pages, Shapes shapes) {
  __shared__ __attribute__((aligned(1024))) char lds[LDS_BYTES];
  uint32_t lds0 = (uint32_t)(uintptr_t)(__attribute__((address_space(3))) char*)lds;
  uint64_t ka = (uint64_t)(uintptr_t)__builtin_amdgcn_kernarg_segment_ptr();
  uint64_t did = dispatch_id_(), dp = (uint64_t)(uintptr_t)__builtin_amdgcn_dispatch_ptr();
  uint32_t bid = blockIdx.x, tid = threadIdx.x;
  asm volatile(
    "s_load_dwordx16 s[36:51], s[2:3], 0x0\n"
    "v_readfirstlane_b32 s8, v0\n"
    "s_load_dwordx4 s[52:55], s[2:3], 0x40\n"
    "s_bfe_u32 s7, s6, 0x40003\n"
    "s_lshr_b32 s9, s8, 2\n"
    "s_lshl_b32 s7, s7, 6\n"
    "s_add_u32 s7, s7, s9\n"
    "s_load_dwordx4 s[20:23], s[2:3], s7 offset:0x58\n"
    "s_load_dwordx2 s[96:97], s[2:3], 0x50\n"
    "s_getpc_b64 s[14:15]\n"
    "70:\n"
    "v_lshlrev_b32 v48, 6, v0\n"
    "s_mov_b32 s13, 79f-70b\n"
    "v_min_u32 v48, s13, v48\n"
    "global_load_dword v49, v48, s[14:15]\n"
    "s_mov_b32 s16, s4\n"
    "v_bfe_u32 v53, v0, 3, 3\n"
    "s_mov_b32 s2, s6\n"
    "v_lshrrev_b32 v54, 2, v0\n"
    "s_mov_b32 s3, %[lds]\n"
    "s_lshr_b32 s4, s8, 6\n"
    "v_mul_u32_u24 v53, 0x300, v53\n"
    "s_bfe_u32 s5, s2, 0x20003\n"
    "v_and_b32 v55, 4, v54\n"
    "s_bfe_u32 s6, s2, 0x20005\n"
    "v_xor_b32 v55, v55, v0\n"
    "s_cmp_eq_u32 s6, 3\n"
    "v_and_b32 v55, 7, v55\n"
    "s_cselect_b32 s6, 0, s6\n"
    "v_lshl_add_u32 v53, v55, 4, v53\n"
    "s_lshl_b32 s7, s4, 7\n"
    "v_and_b32 v16, 63, v0\n"
    "s_mul_i32 s58, s5, 24576\n"
    "v_add_u32 v244, s7, v53\n"
    "s_mul_i32 s62, s6, 98304\n"
    "v_lshlrev_b32 v16, 4, v16\n"
    "s_lshl_b32 s7, s5, 6\n"
    "s_lshl_b32 s8, s6, 8\n"
    "v_xor_b32 v53, s7, v16\n"
    "s_add_u32 s12, s3, 159744\n"
    "s_mul_i32 s83, s4, 30720\n"
    "v_add_u32 v53, s8, v53\n"
    "s_mov_b32 m0, s12\n"
    "v_subrev_u32 v55, 0x300, v53\n"
    "s_add_u32 s83, s83, s3\n"
    "v_min_u32 v250, v53, v55\n"
    "v_add_u32 v245, 0x1400, v244\n"
    "v_add_u32 v246, 0x2800, v244\n"
    "v_add_u32 v247, 0x3c00, v244\n"
    "v_and_b32 v53, 15, v54\n"
    "s_mul_i32 s57, s4, 3072\n"
    "v_and_b32 v54, 12, v54\n"
    "s_lshl_b32 s7, s4, 6\n"
    "v_add_u32 v53, v53, v54\n"
    "v_mul_u32_u24 v53, 0x300, v53\n"
    "s_add_u32 s57, s57, s3\n"
    "v_and_b32 v54, 3, v0\n"
    "s_add_u32 s7, s7, 512\n"
    "v_lshl_add_u32 v53, v54, 4, v53\n"
    "v_lshlrev_b32 v12, 2, v54\n"
    "v_add_u32 v248, s7, v53\n"
    "v_add_u32 v249, 0x800, v248\n"
    "s_lshr_b64 s[18:19], s[0:1], 12\n"
    "s_lshl4_add_u32 s18, s16, s18\n"
    "s_andn2_b32 s17, s18, 15\n"
    "s_bitset1_b32 s17, 31\n"
    "s_mov_b32 s72, s17\n"
    "s_mov_b32 s18, s16\n"
    "s_or_b32 s19, s17, 15\n"
    "s_lshl_b32 s34, s2, 20\n"
    "s_and_b32 s34, s34, 0x7e00000\n"
    "s_mul_i32 s7, s16, 0x9e3779b1\n"
    "s_lshr_b32 s7, s7, 9\n"
    "s_and_b32 s7, s7, 0x1f8f80\n"
    "s_or_b32 s34, s34, s7\n"
    "s_and_b32 s35, s2, 7\n"
    "s_lshl_b32 s7, s4, 1\n"
    "s_sub_u32 s35, s35, s7\n"
    "v_and_b32 v19, 15, v0\n"
    "v_bfe_u32 v52, v0, 4, 2\n"
    "v_and_b32 v18, 48, v0\n"
    "v_bfe_u32 v44, v0, 2, 2\n"
    "v_and_b32 v45, 8, v0\n"
    "s_mov_b32 s9, 0x51403020\n"
    "s_mov_b32 s10, 0x52423121\n"
    "s_mov_b32 s11, 0x53433322\n"
    "s_cmp_eq_u32 s6, 1\n"
    "s_cselect_b32 s9, 0x61204030, s9\n"
    "s_cselect_b32 s10, 0x62521131, s10\n"
    "s_cselect_b32 s11, 0x33534302, s11\n"
    "s_cmp_eq_u32 s6, 2\n"
    "s_cselect_b32 s9, 0x71302040, s9\n"
    "s_cselect_b32 s10, 0x42622111, s10\n"
    "s_cselect_b32 s11, 0x43335312, s11\n"
    "s_xor_b32 s7, s4, s5\n"
    "v_lshlrev_b32 v54, 3, v52\n"
    "s_lshl_b32 s7, s7, 2\n"
    "v_xor_b32 v55, s7, v19\n"
    "v_bfe_u32 v53, s11, v54, 8\n"
    "v_xor_b32 v2, v53, v55\n"
    "v_bfe_u32 v53, s10, v54, 8\n"
    "v_xor_b32 v1, v53, v55\n"
    "v_bfe_u32 v53, s9, v54, 8\n"
    "v_xor_b32 v0, v53, v55\n"
    "s_and_b32 s7, s2, 3\n"
    "s_lshr_b32 s8, s2, 7\n"
    "s_lshl2_add_u32 s7, s8, s7\n"
    "s_mul_i32 s56, s7, 393216\n"
    "s_waitcnt lgkmcnt(0)\n"
    "s_barrier\n"
    "buffer_load_dwordx4 v250, s[48:51], 0 offen lds\n"
    "s_cmp_eq_u32 s35, 0\n"
    "s_cbranch_scc0 72f\n"
    "s_bitcmp1_b32 s2, 7\n"
    "s_cselect_b64 s[68:69], s[40:41], s[36:37]\n"
    "s_mov_b64 s[70:71], s[38:39]\n"
    "s_and_b32 s85, s34, 0x3fffff\n"
    "s_add_u32 s85, s85, s97\n"
    "s_min_u32 s34, s34, s85\n"
    "s_buffer_load_dword s73, s[68:71], s34\n"
    "s_buffer_load_dword s72, s[68:71], s34 offset:0x2000\n"
    "72:\n"
    "s_add_u32 m0, s83, 36864\n"
    "s_add_u32 s84, s58, s62\n"
    "s_lshr_b32 s29, s2, 2\n"
    "s_mov_b32 s30, s57\n"
    "s_xor_b32 s7, s5, 1\n"
    "buffer_load_dwordx4 v244, s[44:47], s84 offen lds\n"
    "s_mul_i32 s59, s7, 24576\n"
    "s_xor_b32 s7, s5, 2\n"
    "s_mul_i32 s60, s7, 24576\n"
    "buffer_load_dwordx4 v245, s[44:47], s84 offen offset:1024 lds\n"
    "s_xor_b32 s7, s5, 3\n"
    "s_mul_i32 s61, s7, 24576\n"
    "v_mov_b32 v53, s20\n"
    "buffer_load_dwordx4 v246, s[44:47], s84 offen offset:2048 lds\n"
    "v_mov_b32 v54, s21\n"
    "v_cmp_le_i32 vcc, s22, v16\n"
    "s_sub_u32 s22, s22, 0x400\n"
    "s_mov_b32 s33, 0x90000000\n"
    "v_cndmask_b32 v55, v53, v54, vcc\n"
    "v_lshl_add_u32 v0, v0, 4, v55\n"
    "buffer_load_dwordx4 v247, s[44:47], s84 offen offset:3072 lds\n"
    "s_add_u32 m0, s83, 40960\n"
    "v_cmp_le_i32 vcc, s22, v16\n"
    "s_sub_u32 s22, s22, 0x400\n"
    "s_nop 0\n"
    "v_cndmask_b32 v55, v53, v54, vcc\n"
    "v_lshl_add_u32 v1, v1, 4, v55\n"
    "v_cmp_le_i32 vcc, s22, v16\n"
    "s_sub_u32 s22, s22, 0x400\n"
    "s_nop 0\n"
    "v_cndmask_b32 v55, v53, v54, vcc\n"
    "buffer_load_dwordx4 v248, s[44:47], s84 offen lds\n"
    "v_lshl_add_u32 v2, v2, 4, v55\n"
    "s_add_u32 s7, s83, 36864\n"
    "v_add_u32 v18, s12, v18\n"
    "buffer_load_dwordx4 v249, s[44:47], s84 offen offset:1024 lds\n"
    "s_add_u32 m0, s83, 43008\n"
    "s_add_u32 s84, s59, s62\n"
    "v_add_u32 v15, s7, v16\n"
    "v_and_b32 v54, 0x30, v16\n"
    "v_lshl_add_u32 v54, v52, 10, v54\n"
    "buffer_load_dwordx4 v244, s[44:47], s84 offen lds\n"
    "v_lshl_add_u32 v54, v44, 7, v54\n"
    "v_lshl_add_u32 v54, v45, 3, v54\n"
    "v_add_u32 v44, s7, v54\n"
    "buffer_load_dwordx4 v245, s[44:47], s84 offen offset:1024 lds\n"
    "s_cmp_eq_u32 s35, 0\n"
    "s_cbranch_scc0 73f\n"
    "s_getreg_b32 s88, hwreg(HW_REG_XCC_ID, 0, 3)\n"
    "s_lshl_b32 s89, s2, 3\n"
    "s_and_b32 s89, s89, 0x3f0\n"
    "s_lshr_b32 s82, s2, 7\n"
    "s_lshl3_add_u32 s89, s82, s89\n"
    "v_mov_b32 v20, s89\n"
    "v_mov_b32 v22, s16\n"
    "s_lshr_b32 s89, s88, 2\n"
    "s_lshl1_add_u32 s88, s88, s89\n"
    "s_or_b32 s88, s88, s17\n"
    "s_bfe_u32 s89, s2, 0x10001\n"
    "s_xor_b32 s88, s88, s89\n"
    "s_mov_b32 s85, 0\n"
    "s_waitcnt lgkmcnt(1)\n"
    "s_nop 3\n"
    "s_cmp_eq_u32 s72, s17\n"
    "s_addc_u32 s85, s85, 0\n"
    "s_cmp_lt_u32 s85, 1\n"
    "s_cselect_b32 s85, 1, 0\n"
    "s_xor_b32 s88, s88, s85\n"
    "v_mov_b32 v23, s88\n"
    "73:\n"
    "v_xor_b32 v45, 64, v44\n"
    "v_mov_b32 v54, 0xffff\n"
    "v_mov_b32 v55, 0\n"
    "buffer_load_dwordx4 v246, s[44:47], s84 offen offset:2048 lds\n"
    "v_lshlrev_b64 v[38:39], v16, v[54:55]\n"
    "s_add_u32 s63, s62, 98304\n"
    "s_cmp_eq_u32 s63, 294912\n"
    "s_cselect_b32 s63, 0, s63\n"
    "s_add_u32 s64, s63, 98304\n"
    "s_cmp_eq_u32 s64, 294912\n"
    "s_cselect_b32 s64, 0, s64\n"
    "buffer_load_dwordx4 v247, s[44:47], s84 offen offset:3072 lds\n"
    "s_add_u32 m0, s83, 47104\n"
    "s_add_u32 s7, s3, 160768\n"
    "v_mov_b32 v14, 0\n"
    "v_add_u32 v12, s7, v12\n"
    "buffer_load_dwordx4 v248, s[44:47], s84 offen lds\n"
    "s_lshl2_add_u32 s7, s4, s7\n"
    "v_mov_b32 v13, s7\n"
    "v_xor_b32 v53, v52, v19\n"
    "buffer_load_dwordx4 v249, s[44:47], s84 offen offset:1024 lds\n"
    "v_mul_u32_u24 v54, 0x300, v19\n"
    "v_lshl_add_u32 v54, v53, 4, v54\n"
    "v_add_u32 v4, s3, v54\n"
    "v_xor_b32 v5, 64, v4\n"
    "v_xor_b32 v6, 128, v4\n"
    "v_xor_b32 v7, 192, v4\n"
    "v_add_u32 v11, s57, v16\n"
    "s_lshl_b32 s14, s4, 1\n"
    "s_and_b32 s7, s14, 3\n"
    "s_lshr_b32 s8, s14, 2\n"
    "s_xor_b32 s7, s7, s5\n"
    "s_sub_u32 s8, s8, s6\n"
    "s_lshl_b32 s7, s7, 6\n"
    "s_cmp_lt_i32 s8, 0\n"
    "s_cselect_b32 s13, 3, 0\n"
    "v_xor_b32 v53, s7, v4\n"
    "s_add_u32 s8, s8, s13\n"
    "s_lshl_b32 s8, s8, 8\n"
    "s_add_u32 s14, s14, 1\n"
    "v_add_u32 v8, s8, v53\n"
    "s_and_b32 s7, s14, 3\n"
    "s_lshr_b32 s8, s14, 2\n"
    "s_xor_b32 s7, s7, s5\n"
    "s_sub_u32 s8, s8, s6\n"
    "s_lshl_b32 s7, s7, 6\n"
    "s_cmp_lt_i32 s8, 0\n"
    "s_cselect_b32 s13, 3, 0\n"
    "v_xor_b32 v53, s7, v4\n"
    "s_add_u32 s8, s8, s13\n"
    "s_lshl_b32 s8, s8, 8\n"
    "s_add_u32 s14, s4, 8\n"
    "v_add_u32 v9, s8, v53\n"
    "s_and_b32 s7, s14, 3\n"
    "s_lshr_b32 s8, s14, 2\n"
    "s_xor_b32 s7, s7, s5\n"
    "s_sub_u32 s8, s8, s6\n"
    "s_lshl_b32 s7, s7, 6\n"
    "s_cmp_lt_i32 s8, 0\n"
    "s_cselect_b32 s13, 3, 0\n"
    "v_xor_b32 v53, s7, v4\n"
    "s_add_u32 s8, s8, s13\n"
    "s_lshl_b32 s8, s8, 8\n"
    "v_add_u32 v10, s8, v53\n"
    "v_lshrrev_b32 v19, 4, v16\n"
    "s_mul_i32 s7, s52, 5\n"
    "s_lshr_b32 s7, s7, 3\n"
    "s_sub_u32 s8, s52, 1\n"
    "s_bitcmp1_b32 s2, 7\n"
    "s_cselect_b32 s7, s7, 0\n"
    "v_add_u32 v53, s7, v19\n"
    "v_cmp_gt_u32 vcc, s8, v19\n"
    "v_subrev_u32 v54, s8, v53\n"
    "v_min_u32 v53, v53, v54\n"
    "v_cndmask_b32 v53, v19, v53, vcc\n"
    "v_mul_lo_u32 v3, v53, s53\n"
    "v_add_u32 v3, s56, v3\n"
    "s_xor_b32 s29, s29, s23\n"
    "s_mul_i32 s7, s52, s53\n"
    "s_add_u32 s7, s7, s56\n"
    "s_mov_b32 s8, -2752512\n"
    "s_bitcmp1_b32 s2, 0\n"
    "s_cselect_b32 s8, -3538944, s8\n"
    "s_add_u32 s7, s7, s8\n"
    "v_cmp_eq_u32 vcc, s52, v19\n"
    "v_mov_b32 v53, s7\n"
    "s_and_b32 s28, s2, 3\n"
    "v_cndmask_b32 v3, v3, v53, vcc\n"
    "v_add_u32 v53, s23, v3\n"
    "v_lshrrev_b32 v53, 21, v53\n"
    "v_and_b32 v232, 56, v53\n"
    "v_and_b32 v233, 4, v53\n"
    "v_xor_b32 v234, s29, v53\n"
    "s_mov_b32 s7, s4\n"
    "s_mov_b64 exec, 0\n"
    "s_cmp_eq_u32 s7, 1\n"
    "s_cbranch_scc1 201f\n"
    "s_cmp_eq_u32 s7, 2\n"
    "s_cbranch_scc1 202f\n"
    "s_cmp_eq_u32 s7, 3\n"
    "s_cbranch_scc1 203f\n"
    "s_branch 200f\n"
    "199:\n"
    "s_mov_b64 exec, -1\n"
    "s_cmp_eq_u32 s35, 0\n"
    "s_cbranch_scc0 71f\n"
    "s_mov_b64 exec, 1\n"
    "global_store_dwordx2 v20, v[22:23], s[54:55] sc1\n"
    "s_mov_b64 exec, -1\n"
    "s_waitcnt lgkmcnt(0)\n"
    "71:\n"
    "s_branch 301f\n"
    "81:\n"
    "ds_read_b32 v15, v12\n"
    "s_waitcnt lgkmcnt(0)\n"
    "v_cmp_lt_u32 vcc, v15, v14\n"
    "s_nop 0\n"
    "s_cbranch_vccnz 81b\n"
    "s_branch 82f\n"
    "301:\n"
    "s_branch 302f\n"
    "41:\n"
    "ds_read_b32 v15, v12\n"
    "s_waitcnt lgkmcnt(0)\n"
    "v_cmp_lt_u32 vcc, v15, v14\n"
    "s_nop 0\n"
    "s_cbranch_vccnz 41b\n"
    "s_branch 42f\n"
    "43:\n"
    "s_memrealtime s[92:93]\n"
    "s_waitcnt lgkmcnt(0)\n"
    "s_sub_u32 s84, s92, s89\n"
    "s_cmp_lt_i32 s84, 0\n"
    "s_cbranch_scc0 44f\n"
    "s_sleep 1\n"
    "s_branch 43b\n"
    "302:\n"
    "s_branch 303f\n"
    "61:\n"
    "ds_read_b32 v15, v12\n"
    "s_waitcnt lgkmcnt(0)\n"
    "v_cmp_lt_u32 vcc, v15, v14\n"
    "s_nop 0\n"
    "s_cbranch_vccnz 61b\n"
    "s_branch 62f\n"
    "63:\n"
    "s_memrealtime s[92:93]\n"
    "s_waitcnt lgkmcnt(0)\n"
    "s_sub_u32 s84, s92, s89\n"
    "s_cmp_lt_i32 s84, 0\n"
    "s_cbranch_scc0 64f\n"
    "s_sleep 1\n"
    "s_branch 63b\n"
    "303:\n"
    "s_branch 304f\n"
    "45:\n"
    "v_mfma_f32_16x16x32_bf16 v[20:23], a[48:51], a[96:99], v[20:23]\n"
    "v_add_u32 v14, 1, v14\n"
    "s_sub_u32 s7, s86, 8\n"
    "v_add_u32 v28, s64, v16\n"
    "s_mov_b32 s94, 40\n"
    "v_mfma_f32_16x16x32_bf16 v[20:23], a[72:75], a[100:103], v[20:23]\n"
    "v_cvt_pk_bf16_f32 v54, v24, v25\n"
    "ds_read_b128 a[196:199], v28\n"
    "v_mfma_f32_16x16x32_bf16 v[20:23], v[64:67], a[104:107], v[20:23]\n"
    "v_cvt_pk_bf16_f32 v55, v26, v27\n"
    "ds_read_b128 a[200:203], v28 offset:1024\n"
    "v_mfma_f32_16x16x32_bf16 v[20:23], v[88:91], a[108:111], v[20:23]\n"
    "ds_write_b64 v10, v[54:55] offset:12296\n"
    "ds_read_b128 a[204:207], v28 offset:2048\n"
    "v_mfma_f32_16x16x32_bf16 v[20:23], v[112:115], a[112:115], v[20:23]\n"
    "s_add_u32 s89, s89, s94\n"
    "v_readlane_b32 s9, v3, s7\n"
    "v_mfma_f32_16x16x32_bf16 v[20:23], v[136:139], a[116:119], v[20:23]\n"
    "s_waitcnt vmcnt(15)\n"
    "v_mfma_f32_16x16x32_bf16 v[20:23], v[160:163], a[120:123], v[20:23]\n"
    "ds_write_b32 v13, v14\n"
    "v_mfma_f32_16x16x32_bf16 v[20:23], v[184:187], a[124:127], v[20:23]\n"
    "v_add_u32 v10, s78, v10\n"
    "v_mfma_f32_16x16x32_bf16 v[20:23], v[208:211], a[128:131], v[20:23]\n"
    "s_waitcnt lgkmcnt(4)\n"
    "buffer_store_dwordx4 a[196:199], v0, s[40:43], s9 offen sc1\n"
    "v_mfma_f32_16x16x32_bf16 v[20:23], v[232:235], a[132:135], v[20:23]\n"
    "s_add_u32 s86, s86, 1\n"
    "s_addk_i32 s64, 12288\n"
    "v_mfma_f32_16x16x32_bf16 v[24:27], v[236:239], a[132:135], 0\n"
    "v_mfma_f32_16x16x32_bf16 v[24:27], v[212:215], a[128:131], v[24:27]\n"
    "s_waitcnt lgkmcnt(3)\n"
    "buffer_store_dwordx4 a[200:203], v1, s[40:43], s9 offen offset:1024 sc1\n"
    "v_mfma_f32_16x16x32_bf16 v[24:27], v[188:191], a[124:127], v[24:27]\n"
    "s_cmp_eq_u32 s64, s80\n"
    "s_cselect_b32 s64, s57, s64\n"
    "v_mfma_f32_16x16x32_bf16 v[24:27], v[164:167], a[120:123], v[24:27]\n"
    "v_mfma_f32_16x16x32_bf16 v[24:27], v[140:143], a[116:119], v[24:27]\n"
    "s_waitcnt lgkmcnt(1)\n"
    "buffer_store_dwordx4 a[204:207], v2, s[40:43], s9 offen offset:2048 sc1\n"
    "s_branch 46f\n"
    "304:\n"
    "s_branch 305f\n"
    "65:\n"
    "v_mfma_f32_16x16x32_bf16 v[20:23], a[48:51], a[144:147], v[20:23]\n"
    "v_add_u32 v14, 1, v14\n"
    "s_sub_u32 s7, s86, 8\n"
    "v_add_u32 v28, s64, v16\n"
    "s_mov_b32 s94, 40\n"
    "v_mfma_f32_16x16x32_bf16 v[20:23], a[72:75], a[148:151], v[20:23]\n"
    "v_cvt_pk_bf16_f32 v54, v24, v25\n"
    "ds_read_b128 a[196:199], v28\n"
    "v_mfma_f32_16x16x32_bf16 v[20:23], v[64:67], a[152:155], v[20:23]\n"
    "v_cvt_pk_bf16_f32 v55, v26, v27\n"
    "ds_read_b128 a[200:203], v28 offset:1024\n"
    "v_mfma_f32_16x16x32_bf16 v[20:23], v[88:91], a[156:159], v[20:23]\n"
    "ds_write_b64 v10, v[54:55] offset:8\n"
    "ds_read_b128 a[204:207], v28 offset:2048\n"
    "v_mfma_f32_16x16x32_bf16 v[20:23], v[112:115], a[160:163], v[20:23]\n"
    "s_add_u32 s89, s89, s94\n"
    "v_readlane_b32 s9, v3, s7\n"
    "v_mfma_f32_16x16x32_bf16 v[20:23], v[136:139], a[164:167], v[20:23]\n"
    "s_waitcnt vmcnt(15)\n"
    "v_mfma_f32_16x16x32_bf16 v[20:23], v[160:163], a[168:171], v[20:23]\n"
    "ds_write_b32 v13, v14\n"
    "v_mfma_f32_16x16x32_bf16 v[20:23], v[184:187], a[172:175], v[20:23]\n"
    "v_mfma_f32_16x16x32_bf16 v[20:23], v[208:211], a[176:179], v[20:23]\n"
    "s_waitcnt lgkmcnt(4)\n"
    "buffer_store_dwordx4 a[196:199], v0, s[40:43], s9 offen sc1\n"
    "v_mfma_f32_16x16x32_bf16 v[20:23], v[232:235], a[180:183], v[20:23]\n"
    "s_add_u32 s86, s86, 1\n"
    "s_addk_i32 s64, 12288\n"
    "v_mfma_f32_16x16x32_bf16 v[24:27], v[236:239], a[180:183], 0\n"
    "v_mfma_f32_16x16x32_bf16 v[24:27], v[212:215], a[176:179], v[24:27]\n"
    "s_waitcnt lgkmcnt(3)\n"
    "buffer_store_dwordx4 a[200:203], v1, s[40:43], s9 offen offset:1024 sc1\n"
    "v_mfma_f32_16x16x32_bf16 v[24:27], v[188:191], a[172:175], v[24:27]\n"
    "s_cmp_eq_u32 s64, s80\n"
    "s_cselect_b32 s64, s57, s64\n"
    "v_mfma_f32_16x16x32_bf16 v[24:27], v[164:167], a[168:171], v[24:27]\n"
    "v_mfma_f32_16x16x32_bf16 v[24:27], v[140:143], a[164:167], v[24:27]\n"
    "s_waitcnt lgkmcnt(1)\n"
    "buffer_store_dwordx4 a[204:207], v2, s[40:43], s9 offen offset:2048 sc1\n"
    "s_branch 66f\n"
    "305:\n"
    "s_waitcnt vmcnt(12)\n"
    "s_memrealtime s[92:93]\n"
    "ds_write_b32 v13, v14\n"
    "ds_read_b128 v[252:255], v18\n"
    "s_waitcnt vmcnt(8)\n"
    "ds_read_b64_tr_b16 a[196:197], v44\n"
    "ds_read_b64_tr_b16 a[198:199], v44 offset:512\n"
    "ds_read_b64_tr_b16 a[200:201], v44 offset:8\n"
    "ds_read_b64_tr_b16 a[202:203], v44 offset:520\n"
    "ds_read_b64_tr_b16 a[204:205], v45\n"
    "ds_read_b64_tr_b16 a[206:207], v45 offset:512\n"
    "s_waitcnt lgkmcnt(6)\n"
    "v_and_b32 v36, v38, v252\n"
    "v_and_b32 v37, v39, v253\n"
    "v_and_b32 v40, v38, v254\n"
    "v_and_b32 v41, v39, v255\n"
    "ds_read_b128 v[252:255], v18 offset:64\n"
    "s_waitcnt lgkmcnt(5)\n"
    "ds_read_b64_tr_b16 a[208:209], v45 offset:8\n"
    "v_mfma_f32_4x4x4_16b_bf16 v[20:23], v[36:37], a[196:197], 0\n"
    "ds_read_b64_tr_b16 a[210:211], v45 offset:520\n"
    "s_add_u32 m0, s83, 49152\n"
    "s_add_u32 s84, s60, s62\n"
    "s_nop 0\n"
    "v_mfma_f32_4x4x4_16b_bf16 v[24:27], v[40:41], a[198:199], 0\n"
    "buffer_load_dwordx4 v244, s[44:47], s84 offen lds\n"
    "s_add_u32 s77, s57, 36864\n"
    "s_nop 0\n"
    "s_waitcnt vmcnt(7) lgkmcnt(5)\n"
    "ds_read_b64_tr_b16 a[212:213], v15 offset:4096\n"
    "v_mfma_f32_4x4x4_16b_bf16 v[28:31], v[36:37], a[200:201], 0\n"
    "ds_read_b64_tr_b16 a[214:215], v15 offset:5120\n"
    "v_cvt_pk_bf16_f32 v48, v20, v21\n"
    "v_cvt_pk_bf16_f32 v49, v22, v23\n"
    "v_accvgpr_write_b32 a0, v48\n"
    "v_accvgpr_write_b32 a1, v49\n"
    "v_mfma_f32_4x4x4_16b_bf16 v[32:35], v[40:41], a[202:203], 0\n"
    "buffer_load_dwordx4 v245, s[44:47], s84 offen offset:1024 lds\n"
    "v_cvt_pk_bf16_f32 v50, v24, v25\n"
    "v_cvt_pk_bf16_f32 v51, v26, v27\n"
    "v_accvgpr_write_b32 a2, v50\n"
    "v_accvgpr_write_b32 a3, v51\n"
    "s_movk_i32 s88, 24576\n"
    "s_waitcnt lgkmcnt(4)\n"
    "ds_read_b64_tr_b16 a[216:217], v15 offset:4104\n"
    "v_mfma_f32_4x4x4_16b_bf16 v[20:23], v[36:37], a[204:205], 0\n"
    "ds_read_b64_tr_b16 a[218:219], v15 offset:5128\n"
    "v_cvt_pk_bf16_f32 v48, v28, v29\n"
    "v_cvt_pk_bf16_f32 v49, v30, v31\n"
    "v_accvgpr_write_b32 a4, v48\n"
    "v_accvgpr_write_b32 a5, v49\n"
    "v_mfma_f32_4x4x4_16b_bf16 v[24:27], v[40:41], a[206:207], 0\n"
    "buffer_load_dwordx4 v246, s[44:47], s84 offen offset:2048 lds\n"
    "v_cvt_pk_bf16_f32 v50, v32, v33\n"
    "v_cvt_pk_bf16_f32 v51, v34, v35\n"
    "v_accvgpr_write_b32 a6, v50\n"
    "v_accvgpr_write_b32 a7, v51\n"
    "v_and_b32 v42, v38, v252\n"
    "s_movk_i32 s78, 24576\n"
    "s_waitcnt vmcnt(5) lgkmcnt(4)\n"
    "ds_read_b64_tr_b16 a[196:197], v44 offset:6144\n"
    "v_mfma_f32_4x4x4_16b_bf16 v[28:31], v[36:37], a[208:209], 0\n"
    "ds_read_b64_tr_b16 a[198:199], v44 offset:6656\n"
    "v_cvt_pk_bf16_f32 v48, v20, v21\n"
    "v_cvt_pk_bf16_f32 v49, v22, v23\n"
    "v_accvgpr_write_b32 a8, v48\n"
    "v_accvgpr_write_b32 a9, v49\n"
    "v_mfma_f32_4x4x4_16b_bf16 v[32:35], v[40:41], a[210:211], 0\n"
    "buffer_load_dwordx4 v247, s[44:47], s84 offen offset:3072 lds\n"
    "v_cvt_pk_bf16_f32 v50, v24, v25\n"
    "v_cvt_pk_bf16_f32 v51, v26, v27\n"
    "v_accvgpr_write_b32 a10, v50\n"
    "v_accvgpr_write_b32 a11, v51\n"
    "v_and_b32 v43, v39, v253\n"
    "s_mov_b32 s87, -122880\n"
    "s_waitcnt lgkmcnt(4)\n"
    "ds_read_b64_tr_b16 a[200:201], v44 offset:6152\n"
    "v_mfma_f32_4x4x4_16b_bf16 v[20:23], v[36:37], a[212:213], 0\n"
    "ds_read_b64_tr_b16 a[202:203], v44 offset:6664\n"
    "s_add_u32 m0, s83, 53248\n"
    "v_cvt_pk_bf16_f32 v48, v28, v29\n"
    "v_cvt_pk_bf16_f32 v49, v30, v31\n"
    "v_accvgpr_write_b32 a12, v48\n"
    "v_accvgpr_write_b32 a13, v49\n"
    "v_mfma_f32_4x4x4_16b_bf16 v[24:27], v[40:41], a[214:215], 0\n"
    "buffer_load_dwordx4 v248, s[44:47], s84 offen lds\n"
    "v_cvt_pk_bf16_f32 v50, v32, v33\n"
    "v_cvt_pk_bf16_f32 v51, v34, v35\n"
    "v_accvgpr_write_b32 a14, v50\n"
    "v_accvgpr_write_b32 a15, v51\n"
    "v_and_b32 v46, v38, v254\n"
    "s_add_u32 s80, s57, 147456\n"
    "s_waitcnt lgkmcnt(4)\n"
    "ds_read_b64_tr_b16 a[204:205], v45 offset:6144\n"
    "v_mfma_f32_4x4x4_16b_bf16 v[28:31], v[36:37], a[216:217], 0\n"
    "ds_read_b64_tr_b16 a[206:207], v45 offset:6656\n"
    "v_cvt_pk_bf16_f32 v48, v20, v21\n"
    "v_cvt_pk_bf16_f32 v49, v22, v23\n"
    "v_accvgpr_write_b32 a16, v48\n"
    "v_accvgpr_write_b32 a17, v49\n"
    "v_mfma_f32_4x4x4_16b_bf16 v[32:35], v[40:41], a[218:219], 0\n"
    "buffer_load_dwordx4 v249, s[44:47], s84 offen offset:1024 lds\n"
    "v_cvt_pk_bf16_f32 v50, v24, v25\n"
    "v_cvt_pk_bf16_f32 v51, v26, v27\n"
    "v_accvgpr_write_b32 a18, v50\n"
    "v_accvgpr_write_b32 a19, v51\n"
    "v_and_b32 v47, v39, v255\n"
    "ds_read_b128 v[252:255], v18 offset:128\n"
    "s_mov_b32 s86, 5\n"
    "s_waitcnt lgkmcnt(5)\n"
    "ds_read_b64_tr_b16 a[208:209], v45 offset:6152\n"
    "v_mfma_f32_4x4x4_16b_bf16 v[20:23], v[42:43], a[196:197], 0\n"
    "ds_read_b64_tr_b16 a[210:211], v45 offset:6664\n"
    "s_add_u32 m0, s83, 55296\n"
    "s_add_u32 s84, s61, s62\n"
    "v_cvt_pk_bf16_f32 v48, v28, v29\n"
    "v_cvt_pk_bf16_f32 v49, v30, v31\n"
    "v_accvgpr_write_b32 a20, v48\n"
    "v_accvgpr_write_b32 a21, v49\n"
    "v_mfma_f32_4x4x4_16b_bf16 v[24:27], v[46:47], a[198:199], 0\n"
    "buffer_load_dwordx4 v244, s[44:47], s84 offen lds\n"
    "v_cvt_pk_bf16_f32 v50, v32, v33\n"
    "v_cvt_pk_bf16_f32 v51, v34, v35\n"
    "v_accvgpr_write_b32 a22, v50\n"
    "v_accvgpr_write_b32 a23, v51\n"
    "s_lshr_b32 s7, s2, 1\n"
    "s_waitcnt vmcnt(7) lgkmcnt(5)\n"
    "ds_read_b64_tr_b16 a[212:213], v15 offset:10240\n"
    "v_mfma_f32_4x4x4_16b_bf16 v[28:31], v[42:43], a[200:201], 0\n"
    "ds_read_b64_tr_b16 a[214:215], v15 offset:11264\n"
    "v_cvt_pk_bf16_f32 v48, v20, v21\n"
    "v_cvt_pk_bf16_f32 v49, v22, v23\n"
    "v_accvgpr_write_b32 a24, v48\n"
    "v_accvgpr_write_b32 a25, v49\n"
    "v_mfma_f32_4x4x4_16b_bf16 v[32:35], v[46:47], a[202:203], 0\n"
    "buffer_load_dwordx4 v245, s[44:47], s84 offen offset:1024 lds\n"
    "v_cvt_pk_bf16_f32 v50, v24, v25\n"
    "v_cvt_pk_bf16_f32 v51, v26, v27\n"
    "v_accvgpr_write_b32 a26, v50\n"
    "v_accvgpr_write_b32 a27, v51\n"
    "s_and_b32 s7, s7, 0x7c\n"
    "s_waitcnt lgkmcnt(4)\n"
    "ds_read_b64_tr_b16 a[216:217], v15 offset:10248\n"
    "v_mfma_f32_4x4x4_16b_bf16 v[20:23], v[42:43], a[204:205], 0\n"
    "ds_read_b64_tr_b16 a[218:219], v15 offset:11272\n"
    "v_cvt_pk_bf16_f32 v48, v28, v29\n"
    "v_cvt_pk_bf16_f32 v49, v30, v31\n"
    "v_accvgpr_write_b32 a28, v48\n"
    "v_accvgpr_write_b32 a29, v49\n"
    "v_mfma_f32_4x4x4_16b_bf16 v[24:27], v[46:47], a[206:207], 0\n"
    "buffer_load_dwordx4 v246, s[44:47], s84 offen offset:2048 lds\n"
    "v_cvt_pk_bf16_f32 v50, v32, v33\n"
    "v_cvt_pk_bf16_f32 v51, v34, v35\n"
    "v_accvgpr_write_b32 a30, v50\n"
    "v_accvgpr_write_b32 a31, v51\n"
    "buffer_load_dwordx4 v247, s[44:47], s84 offen offset:3072 lds\n"
    "v_and_b32 v36, v38, v252\n"
    "s_and_b32 s8, s2, 3\n"
    "s_waitcnt vmcnt(6) lgkmcnt(4)\n"
    "ds_read_b64_tr_b16 a[196:197], v44 offset:12288\n"
    "v_mfma_f32_4x4x4_16b_bf16 v[28:31], v[42:43], a[208:209], 0\n"
    "ds_read_b64_tr_b16 a[198:199], v44 offset:12800\n"
    "s_add_u32 m0, s83, 59392\n"
    "v_cvt_pk_bf16_f32 v48, v20, v21\n"
    "v_cvt_pk_bf16_f32 v49, v22, v23\n"
    "v_accvgpr_write_b32 a32, v48\n"
    "v_accvgpr_write_b32 a33, v49\n"
    "v_mfma_f32_4x4x4_16b_bf16 v[32:35], v[46:47], a[210:211], 0\n"
    "buffer_load_dwordx4 v248, s[44:47], s84 offen lds\n"
    "v_cvt_pk_bf16_f32 v50, v24, v25\n"
    "v_cvt_pk_bf16_f32 v51, v26, v27\n"
    "v_accvgpr_write_b32 a34, v50\n"
    "v_accvgpr_write_b32 a35, v51\n"
    "v_and_b32 v37, v39, v253\n"
    "s_or_b32 s7, s7, s8\n"
    "s_waitcnt lgkmcnt(4)\n"
    "ds_read_b64_tr_b16 a[200:201], v44 offset:12296\n"
    "v_mfma_f32_4x4x4_16b_bf16 v[20:23], v[42:43], a[212:213], 0\n"
    "ds_read_b64_tr_b16 a[202:203], v44 offset:12808\n"
    "v_cvt_pk_bf16_f32 v48, v28, v29\n"
    "v_cvt_pk_bf16_f32 v49, v30, v31\n"
    "v_accvgpr_write_b32 a36, v48\n"
    "v_accvgpr_write_b32 a37, v49\n"
    "v_mfma_f32_4x4x4_16b_bf16 v[24:27], v[46:47], a[214:215], 0\n"
    "buffer_load_dwordx4 v249, s[44:47], s84 offen offset:1024 lds\n"
    "v_cvt_pk_bf16_f32 v50, v32, v33\n"
    "v_cvt_pk_bf16_f32 v51, v34, v35\n"
    "v_accvgpr_write_b32 a38, v50\n"
    "v_accvgpr_write_b32 a39, v51\n"
    "v_and_b32 v40, v38, v254\n"
    "s_mul_i32 s7, s7, 79\n"
    "s_waitcnt lgkmcnt(4)\n"
    "ds_read_b64_tr_b16 a[204:205], v45 offset:12288\n"
    "v_mfma_f32_4x4x4_16b_bf16 v[28:31], v[42:43], a[216:217], 0\n"
    "ds_read_b64_tr_b16 a[206:207], v45 offset:12800\n"
    "s_add_u32 m0, s83, 61440\n"
    "s_add_u32 s84, s58, s63\n"
    "v_cvt_pk_bf16_f32 v48, v20, v21\n"
    "v_cvt_pk_bf16_f32 v49, v22, v23\n"
    "v_accvgpr_write_b32 a40, v48\n"
    "v_accvgpr_write_b32 a41, v49\n"
    "v_mfma_f32_4x4x4_16b_bf16 v[32:35], v[46:47], a[218:219], 0\n"
    "buffer_load_dwordx4 v244, s[44:47], s84 offen lds\n"
    "v_cvt_pk_bf16_f32 v50, v24, v25\n"
    "v_cvt_pk_bf16_f32 v51, v26, v27\n"
    "v_accvgpr_write_b32 a42, v50\n"
    "v_accvgpr_write_b32 a43, v51\n"
    "v_and_b32 v41, v39, v255\n"
    "ds_read_b128 v[252:255], v18 offset:192\n"
    "s_and_b32 s7, s7, 0x7f\n"
    "s_waitcnt lgkmcnt(5)\n"
    "ds_read_b64_tr_b16 a[208:209], v45 offset:12296\n"
    "v_mfma_f32_4x4x4_16b_bf16 v[20:23], v[36:37], a[196:197], 0\n"
    "ds_read_b64_tr_b16 a[210:211], v45 offset:12808\n"
    "v_cvt_pk_bf16_f32 v48, v28, v29\n"
    "v_cvt_pk_bf16_f32 v49, v30, v31\n"
    "v_accvgpr_write_b32 a44, v48\n"
    "v_accvgpr_write_b32 a45, v49\n"
    "v_mfma_f32_4x4x4_16b_bf16 v[24:27], v[40:41], a[198:199], 0\n"
    "buffer_load_dwordx4 v245, s[44:47], s84 offen offset:1024 lds\n"
    "v_cvt_pk_bf16_f32 v50, v32, v33\n"
    "v_cvt_pk_bf16_f32 v51, v34, v35\n"
    "v_accvgpr_write_b32 a46, v50\n"
    "v_accvgpr_write_b32 a47, v51\n"
    "buffer_load_dwordx4 v246, s[44:47], s84 offen offset:2048 lds\n"
    "s_mul_i32 s7, s7, 60\n"
    "s_waitcnt vmcnt(9) lgkmcnt(5)\n"
    "ds_read_b64_tr_b16 a[212:213], v15 offset:16384\n"
    "v_mfma_f32_4x4x4_16b_bf16 v[28:31], v[36:37], a[200:201], 0\n"
    "ds_read_b64_tr_b16 a[214:215], v15 offset:17408\n"
    "v_cvt_pk_bf16_f32 v48, v20, v21\n"
    "v_cvt_pk_bf16_f32 v49, v22, v23\n"
    "v_accvgpr_write_b32 a48, v48\n"
    "v_accvgpr_write_b32 a49, v49\n"
    "v_mfma_f32_4x4x4_16b_bf16 v[32:35], v[40:41], a[202:203], 0\n"
    "buffer_load_dwordx4 v247, s[44:47], s84 offen offset:3072 lds\n"
    "v_cvt_pk_bf16_f32 v50, v24, v25\n"
    "v_cvt_pk_bf16_f32 v51, v26, v27\n"
    "v_accvgpr_write_b32 a50, v50\n"
    "v_accvgpr_write_b32 a51, v51\n"
    "s_lshr_b32 s7, s7, 6\n"
    "s_waitcnt lgkmcnt(4)\n"
    "ds_read_b64_tr_b16 a[216:217], v15 offset:16392\n"
    "v_mfma_f32_4x4x4_16b_bf16 v[20:23], v[36:37], a[204:205], 0\n"
    "ds_read_b64_tr_b16 a[218:219], v15 offset:17416\n"
    "s_add_u32 m0, s83, 65536\n"
    "v_cvt_pk_bf16_f32 v48, v28, v29\n"
    "v_cvt_pk_bf16_f32 v49, v30, v31\n"
    "v_accvgpr_write_b32 a52, v48\n"
    "v_accvgpr_write_b32 a53, v49\n"
    "v_mfma_f32_4x4x4_16b_bf16 v[24:27], v[40:41], a[206:207], 0\n"
    "buffer_load_dwordx4 v248, s[44:47], s84 offen lds\n"
    "v_cvt_pk_bf16_f32 v50, v32, v33\n"
    "v_cvt_pk_bf16_f32 v51, v34, v35\n"
    "v_accvgpr_write_b32 a54, v50\n"
    "v_accvgpr_write_b32 a55, v51\n"
    "v_and_b32 v42, v38, v252\n"
    "s_sub_i32 s7, s7, 102\n"
    "s_waitcnt vmcnt(7) lgkmcnt(4)\n"
    "ds_read_b64_tr_b16 a[196:197], v44 offset:18432\n"
    "v_mfma_f32_4x4x4_16b_bf16 v[28:31], v[36:37], a[208:209], 0\n"
    "ds_read_b64_tr_b16 a[198:199], v44 offset:18944\n"
    "v_cvt_pk_bf16_f32 v48, v20, v21\n"
    "v_cvt_pk_bf16_f32 v49, v22, v23\n"
    "v_accvgpr_write_b32 a56, v48\n"
    "v_accvgpr_write_b32 a57, v49\n"
    "v_mfma_f32_4x4x4_16b_bf16 v[32:35], v[40:41], a[210:211], 0\n"
    "buffer_load_dwordx4 v249, s[44:47], s84 offen offset:1024 lds\n"
    "v_cvt_pk_bf16_f32 v50, v24, v25\n"
    "v_cvt_pk_bf16_f32 v51, v26, v27\n"
    "v_accvgpr_write_b32 a58, v50\n"
    "v_accvgpr_write_b32 a59, v51\n"
    "v_and_b32 v43, v39, v253\n"
    "s_max_i32 s7, s7, 0\n"
    "s_waitcnt lgkmcnt(4)\n"
    "ds_read_b64_tr_b16 a[200:201], v44 offset:18440\n"
    "v_mfma_f32_4x4x4_16b_bf16 v[20:23], v[36:37], a[212:213], 0\n"
    "ds_read_b64_tr_b16 a[202:203], v44 offset:18952\n"
    "s_add_u32 m0, s83, 36864\n"
    "s_add_u32 s84, s59, s63\n"
    "v_cvt_pk_bf16_f32 v48, v28, v29\n"
    "v_cvt_pk_bf16_f32 v49, v30, v31\n"
    "v_accvgpr_write_b32 a60, v48\n"
    "v_accvgpr_write_b32 a61, v49\n"
    "v_mfma_f32_4x4x4_16b_bf16 v[24:27], v[40:41], a[214:215], 0\n"
    "buffer_load_dwordx4 v244, s[44:47], s84 offen lds\n"
    "v_cvt_pk_bf16_f32 v50, v32, v33\n"
    "v_cvt_pk_bf16_f32 v51, v34, v35\n"
    "v_accvgpr_write_b32 a62, v50\n"
    "v_accvgpr_write_b32 a63, v51\n"
    "buffer_load_dwordx4 v245, s[44:47], s84 offen offset:1024 lds\n"
    "v_and_b32 v46, v38, v254\n"
    "s_min_i32 s7, s7, 50\n"
    "s_waitcnt lgkmcnt(4)\n"
    "ds_read_b64_tr_b16 a[204:205], v45 offset:18432\n"
    "v_mfma_f32_4x4x4_16b_bf16 v[28:31], v[36:37], a[216:217], 0\n"
    "ds_read_b64_tr_b16 a[206:207], v45 offset:18944\n"
    "v_cvt_pk_bf16_f32 v48, v20, v21\n"
    "v_cvt_pk_bf16_f32 v49, v22, v23\n"
    "v_accvgpr_write_b32 a64, v48\n"
    "v_accvgpr_write_b32 a65, v49\n"
    "v_mfma_f32_4x4x4_16b_bf16 v[32:35], v[40:41], a[218:219], 0\n"
    "buffer_load_dwordx4 v246, s[44:47], s84 offen offset:2048 lds\n"
    "v_cvt_pk_bf16_f32 v50, v24, v25\n"
    "v_cvt_pk_bf16_f32 v51, v26, v27\n"
    "v_accvgpr_write_b32 a66, v50\n"
    "v_accvgpr_write_b32 a67, v51\n"
    "v_and_b32 v47, v39, v255\n"
    "ds_read_b128 v[252:255], v18 offset:256\n"
    "s_bfm_b64 s[34:35], s7, 2\n"
    "s_waitcnt lgkmcnt(5)\n"
    "ds_read_b64_tr_b16 a[208:209], v45 offset:18440\n"
    "v_mfma_f32_4x4x4_16b_bf16 v[20:23], v[42:43], a[196:197], 0\n"
    "ds_read_b64_tr_b16 a[210:211], v45 offset:18952\n"
    "v_cvt_pk_bf16_f32 v48, v28, v29\n"
    "v_cvt_pk_bf16_f32 v49, v30, v31\n"
    "v_accvgpr_write_b32 a68, v48\n"
    "v_accvgpr_write_b32 a69, v49\n"
    "v_mfma_f32_4x4x4_16b_bf16 v[24:27], v[46:47], a[198:199], 0\n"
    "buffer_load_dwordx4 v247, s[44:47], s84 offen offset:3072 lds\n"
    "v_cvt_pk_bf16_f32 v50, v32, v33\n"
    "v_cvt_pk_bf16_f32 v51, v34, v35\n"
    "v_accvgpr_write_b32 a70, v50\n"
    "v_accvgpr_write_b32 a71, v51\n"
    "s_waitcnt vmcnt(10) lgkmcnt(5)\n"
    "ds_read_b64_tr_b16 a[212:213], v15 offset:22528\n"
    "v_mfma_f32_4x4x4_16b_bf16 v[28:31], v[42:43], a[200:201], 0\n"
    "ds_read_b64_tr_b16 a[214:215], v15 offset:23552\n"
    "s_add_u32 m0, s83, 40960\n"
    "v_cvt_pk_bf16_f32 v48, v20, v21\n"
    "v_cvt_pk_bf16_f32 v49, v22, v23\n"
    "v_accvgpr_write_b32 a72, v48\n"
    "v_accvgpr_write_b32 a73, v49\n"
    "v_mfma_f32_4x4x4_16b_bf16 v[32:35], v[46:47], a[202:203], 0\n"
    "buffer_load_dwordx4 v248, s[44:47], s84 offen lds\n"
    "v_cvt_pk_bf16_f32 v50, v24, v25\n"
    "v_cvt_pk_bf16_f32 v51, v26, v27\n"
    "v_accvgpr_write_b32 a74, v50\n"
    "v_accvgpr_write_b32 a75, v51\n"
    "s_waitcnt lgkmcnt(4)\n"
    "ds_read_b64_tr_b16 a[216:217], v15 offset:22536\n"
    "v_mfma_f32_4x4x4_16b_bf16 v[20:23], v[42:43], a[204:205], 0\n"
    "ds_read_b64_tr_b16 a[218:219], v15 offset:23560\n"
    "v_cvt_pk_bf16_f32 v48, v28, v29\n"
    "v_cvt_pk_bf16_f32 v49, v30, v31\n"
    "v_accvgpr_write_b32 a76, v48\n"
    "v_accvgpr_write_b32 a77, v49\n"
    "v_mfma_f32_4x4x4_16b_bf16 v[24:27], v[46:47], a[206:207], 0\n"
    "buffer_load_dwordx4 v249, s[44:47], s84 offen offset:1024 lds\n"
    "s_add_u32 m0, s83, 43008\n"
    "s_add_u32 s84, s60, s63\n"
    "v_cvt_pk_bf16_f32 v50, v32, v33\n"
    "v_cvt_pk_bf16_f32 v51, v34, v35\n"
    "v_accvgpr_write_b32 a78, v50\n"
    "v_accvgpr_write_b32 a79, v51\n"
    "buffer_load_dwordx4 v244, s[44:47], s84 offen lds\n"
    "v_and_b32 v36, v38, v252\n"
    "s_waitcnt vmcnt(9) lgkmcnt(4)\n"
    "ds_read_b64_tr_b16 a[196:197], v44 offset:24576\n"
    "v_mfma_f32_4x4x4_16b_bf16 v[28:31], v[42:43], a[208:209], 0\n"
    "ds_read_b64_tr_b16 a[198:199], v44 offset:25088\n"
    "v_cvt_pk_bf16_f32 v48, v20, v21\n"
    "v_cvt_pk_bf16_f32 v49, v22, v23\n"
    "v_accvgpr_write_b32 a80, v48\n"
    "v_accvgpr_write_b32 a81, v49\n"
    "v_mfma_f32_4x4x4_16b_bf16 v[32:35], v[46:47], a[210:211], 0\n"
    "buffer_load_dwordx4 v245, s[44:47], s84 offen offset:1024 lds\n"
    "v_cvt_pk_bf16_f32 v50, v24, v25\n"
    "v_cvt_pk_bf16_f32 v51, v26, v27\n"
    "v_accvgpr_write_b32 a82, v50\n"
    "v_accvgpr_write_b32 a83, v51\n"
    "v_and_b32 v37, v39, v253\n"
    "s_waitcnt lgkmcnt(4)\n"
    "ds_read_b64_tr_b16 a[200:201], v44 offset:24584\n"
    "v_mfma_f32_4x4x4_16b_bf16 v[20:23], v[42:43], a[212:213], 0\n"
    "ds_read_b64_tr_b16 a[202:203], v44 offset:25096\n"
    "v_cvt_pk_bf16_f32 v48, v28, v29\n"
    "v_cvt_pk_bf16_f32 v49, v30, v31\n"
    "v_accvgpr_write_b32 a84, v48\n"
    "v_accvgpr_write_b32 a85, v49\n"
    "v_mfma_f32_4x4x4_16b_bf16 v[24:27], v[46:47], a[214:215], 0\n"
    "buffer_load_dwordx4 v246, s[44:47], s84 offen offset:2048 lds\n"
    "v_cvt_pk_bf16_f32 v50, v32, v33\n"
    "v_cvt_pk_bf16_f32 v51, v34, v35\n"
    "v_accvgpr_write_b32 a86, v50\n"
    "v_accvgpr_write_b32 a87, v51\n"
    "v_and_b32 v40, v38, v254\n"
    "s_waitcnt lgkmcnt(4)\n"
    "ds_read_b64_tr_b16 a[204:205], v45 offset:24576\n"
    "v_mfma_f32_4x4x4_16b_bf16 v[28:31], v[42:43], a[216:217], 0\n"
    "ds_read_b64_tr_b16 a[206:207], v45 offset:25088\n"
    "v_cvt_pk_bf16_f32 v56, v20, v21\n"
    "v_cvt_pk_bf16_f32 v57, v22, v23\n"
    "v_mfma_f32_4x4x4_16b_bf16 v[32:35], v[46:47], a[218:219], 0\n"
    "buffer_load_dwordx4 v247, s[44:47], s84 offen offset:3072 lds\n"
    "v_cvt_pk_bf16_f32 v58, v24, v25\n"
    "v_cvt_pk_bf16_f32 v59, v26, v27\n"
    "v_and_b32 v41, v39, v255\n"
    "ds_read_b128 v[252:255], v18 offset:320\n"
    "s_waitcnt lgkmcnt(5)\n"
    "ds_read_b64_tr_b16 a[208:209], v45 offset:24584\n"
    "v_mfma_f32_4x4x4_16b_bf16 v[20:23], v[36:37], a[196:197], 0\n"
    "ds_read_b64_tr_b16 a[210:211], v45 offset:25096\n"
    "s_add_u32 m0, s83, 47104\n"
    "v_cvt_pk_bf16_f32 v60, v28, v29\n"
    "v_cvt_pk_bf16_f32 v61, v30, v31\n"
    "v_mfma_f32_4x4x4_16b_bf16 v[24:27], v[40:41], a[198:199], 0\n"
    "buffer_load_dwordx4 v248, s[44:47], s84 offen lds\n"
    "v_cvt_pk_bf16_f32 v62, v32, v33\n"
    "v_cvt_pk_bf16_f32 v63, v34, v35\n"
    "buffer_load_dwordx4 v249, s[44:47], s84 offen offset:1024 lds\n"
    "s_waitcnt vmcnt(12) lgkmcnt(5)\n"
    "ds_read_b64_tr_b16 a[212:213], v15 offset:28672\n"
    "v_mfma_f32_4x4x4_16b_bf16 v[28:31], v[36:37], a[200:201], 0\n"
    "ds_read_b64_tr_b16 a[214:215], v15 offset:29696\n"
    "s_add_u32 m0, s83, 49152\n"
    "s_add_u32 s84, s61, s63\n"
    "v_cvt_pk_bf16_f32 v64, v20, v21\n"
    "v_cvt_pk_bf16_f32 v65, v22, v23\n"
    "v_mfma_f32_4x4x4_16b_bf16 v[32:35], v[40:41], a[202:203], 0\n"
    "buffer_load_dwordx4 v244, s[44:47], s84 offen lds\n"
    "v_cvt_pk_bf16_f32 v66, v24, v25\n"
    "v_cvt_pk_bf16_f32 v67, v26, v27\n"
    "s_waitcnt lgkmcnt(4)\n"
    "ds_read_b64_tr_b16 a[216:217], v15 offset:28680\n"
    "v_mfma_f32_4x4x4_16b_bf16 v[20:23], v[36:37], a[204:205], 0\n"
    "ds_read_b64_tr_b16 a[218:219], v15 offset:29704\n"
    "v_cvt_pk_bf16_f32 v68, v28, v29\n"
    "v_cvt_pk_bf16_f32 v69, v30, v31\n"
    "v_mfma_f32_4x4x4_16b_bf16 v[24:27], v[40:41], a[206:207], 0\n"
    "buffer_load_dwordx4 v245, s[44:47], s84 offen offset:1024 lds\n"
    "v_cvt_pk_bf16_f32 v70, v32, v33\n"
    "v_cvt_pk_bf16_f32 v71, v34, v35\n"
    "v_and_b32 v42, v38, v252\n"
    "s_waitcnt vmcnt(10) lgkmcnt(4)\n"
    "ds_read_b64_tr_b16 a[196:197], v44\n"
    "v_mfma_f32_4x4x4_16b_bf16 v[28:31], v[36:37], a[208:209], 0\n"
    "ds_read_b64_tr_b16 a[198:199], v44 offset:512\n"
    "v_cvt_pk_bf16_f32 v72, v20, v21\n"
    "v_cvt_pk_bf16_f32 v73, v22, v23\n"
    "v_mfma_f32_4x4x4_16b_bf16 v[32:35], v[40:41], a[210:211], 0\n"
    "buffer_load_dwordx4 v246, s[44:47], s84 offen offset:2048 lds\n"
    "v_cvt_pk_bf16_f32 v74, v24, v25\n"
    "v_cvt_pk_bf16_f32 v75, v26, v27\n"
    "v_and_b32 v43, v39, v253\n"
    "s_waitcnt lgkmcnt(4)\n"
    "ds_read_b64_tr_b16 a[200:201], v44 offset:8\n"
    "v_mfma_f32_4x4x4_16b_bf16 v[20:23], v[36:37], a[212:213], 0\n"
    "ds_read_b64_tr_b16 a[202:203], v44 offset:520\n"
    "v_cvt_pk_bf16_f32 v76, v28, v29\n"
    "v_cvt_pk_bf16_f32 v77, v30, v31\n"
    "v_mfma_f32_4x4x4_16b_bf16 v[24:27], v[40:41], a[214:215], 0\n"
    "buffer_load_dwordx4 v247, s[44:47], s84 offen offset:3072 lds\n"
    "s_add_u32 m0, s83, 53248\n"
    "v_cvt_pk_bf16_f32 v78, v32, v33\n"
    "v_cvt_pk_bf16_f32 v79, v34, v35\n"
    "buffer_load_dwordx4 v248, s[44:47], s84 offen lds\n"
    "v_and_b32 v46, v38, v254\n"
    "s_waitcnt lgkmcnt(4)\n"
    "ds_read_b64_tr_b16 a[204:205], v45\n"
    "v_mfma_f32_4x4x4_16b_bf16 v[28:31], v[36:37], a[216:217], 0\n"
    "ds_read_b64_tr_b16 a[206:207], v45 offset:512\n"
    "v_cvt_pk_bf16_f32 v80, v20, v21\n"
    "v_cvt_pk_bf16_f32 v81, v22, v23\n"
    "v_mfma_f32_4x4x4_16b_bf16 v[32:35], v[40:41], a[218:219], 0\n"
    "buffer_load_dwordx4 v249, s[44:47], s84 offen offset:1024 lds\n"
    "v_cvt_pk_bf16_f32 v82, v24, v25\n"
    "v_cvt_pk_bf16_f32 v83, v26, v27\n"
    "v_and_b32 v47, v39, v255\n"
    "ds_read_b128 v[252:255], v18 offset:384\n"
    "s_waitcnt lgkmcnt(5)\n"
    "ds_read_b64_tr_b16 a[208:209], v45 offset:8\n"
    "v_mfma_f32_4x4x4_16b_bf16 v[20:23], v[42:43], a[196:197], 0\n"
    "ds_read_b64_tr_b16 a[210:211], v45 offset:520\n"
    "s_add_u32 m0, s83, 55296\n"
    "s_add_u32 s84, s58, s64\n"
    "v_cvt_pk_bf16_f32 v84, v28, v29\n"
    "v_cvt_pk_bf16_f32 v85, v30, v31\n"
    "v_mfma_f32_4x4x4_16b_bf16 v[24:27], v[46:47], a[198:199], 0\n"
    "buffer_load_dwordx4 v244, s[44:47], s84 offen lds\n"
    "v_cvt_pk_bf16_f32 v86, v32, v33\n"
    "v_cvt_pk_bf16_f32 v87, v34, v35\n"
    "s_waitcnt vmcnt(13) lgkmcnt(5)\n"
    "ds_read_b64_tr_b16 a[212:213], v15 offset:4096\n"
    "v_mfma_f32_4x4x4_16b_bf16 v[28:31], v[42:43], a[200:201], 0\n"
    "ds_read_b64_tr_b16 a[214:215], v15 offset:5120\n"
    "v_cvt_pk_bf16_f32 v88, v20, v21\n"
    "v_cvt_pk_bf16_f32 v89, v22, v23\n"
    "v_mfma_f32_4x4x4_16b_bf16 v[32:35], v[46:47], a[202:203], 0\n"
    "buffer_load_dwordx4 v245, s[44:47], s84 offen offset:1024 lds\n"
    "v_cvt_pk_bf16_f32 v90, v24, v25\n"
    "v_cvt_pk_bf16_f32 v91, v26, v27\n"
    "s_waitcnt lgkmcnt(4)\n"
    "ds_read_b64_tr_b16 a[216:217], v15 offset:4104\n"
    "v_mfma_f32_4x4x4_16b_bf16 v[20:23], v[42:43], a[204:205], 0\n"
    "ds_read_b64_tr_b16 a[218:219], v15 offset:5128\n"
    "global_load_dwordx4 v[236:239], v16, s[54:55]\n"
    "v_cvt_pk_bf16_f32 v92, v28, v29\n"
    "v_cvt_pk_bf16_f32 v93, v30, v31\n"
    "v_mfma_f32_4x4x4_16b_bf16 v[24:27], v[46:47], a[206:207], 0\n"
    "buffer_load_dwordx4 v246, s[44:47], s84 offen offset:2048 lds\n"
    "v_cvt_pk_bf16_f32 v94, v32, v33\n"
    "v_cvt_pk_bf16_f32 v95, v34, v35\n"
    "buffer_load_dwordx4 v247, s[44:47], s84 offen offset:3072 lds\n"
    "v_and_b32 v36, v38, v252\n"
    "s_waitcnt vmcnt(13) lgkmcnt(4)\n"
    "ds_read_b64_tr_b16 a[196:197], v44 offset:6144\n"
    "v_mfma_f32_4x4x4_16b_bf16 v[28:31], v[42:43], a[208:209], 0\n"
    "ds_read_b64_tr_b16 a[198:199], v44 offset:6656\n"
    "s_add_u32 m0, s83, 59392\n"
    "v_cvt_pk_bf16_f32 v96, v20, v21\n"
    "v_cvt_pk_bf16_f32 v97, v22, v23\n"
    "v_mfma_f32_4x4x4_16b_bf16 v[32:35], v[46:47], a[210:211], 0\n"
    "buffer_load_dwordx4 v248, s[44:47], s84 offen lds\n"
    "v_cvt_pk_bf16_f32 v98, v24, v25\n"
    "v_cvt_pk_bf16_f32 v99, v26, v27\n"
    "v_and_b32 v37, v39, v253\n"
    "s_waitcnt lgkmcnt(4)\n"
    "ds_read_b64_tr_b16 a[200:201], v44 offset:6152\n"
    "v_mfma_f32_4x4x4_16b_bf16 v[20:23], v[42:43], a[212:213], 0\n"
    "ds_read_b64_tr_b16 a[202:203], v44 offset:6664\n"
    "v_cvt_pk_bf16_f32 v100, v28, v29\n"
    "v_cvt_pk_bf16_f32 v101, v30, v31\n"
    "v_mfma_f32_4x4x4_16b_bf16 v[24:27], v[46:47], a[214:215], 0\n"
    "buffer_load_dwordx4 v249, s[44:47], s84 offen offset:1024 lds\n"
    "v_cvt_pk_bf16_f32 v102, v32, v33\n"
    "v_cvt_pk_bf16_f32 v103, v34, v35\n"
    "v_and_b32 v40, v38, v254\n"
    "s_waitcnt lgkmcnt(4)\n"
    "ds_read_b64_tr_b16 a[204:205], v45 offset:6144\n"
    "v_mfma_f32_4x4x4_16b_bf16 v[28:31], v[42:43], a[216:217], 0\n"
    "ds_read_b64_tr_b16 a[206:207], v45 offset:6656\n"
    "s_add_u32 m0, s83, 61440\n"
    "s_add_u32 s84, s59, s64\n"
    "v_cvt_pk_bf16_f32 v104, v20, v21\n"
    "v_cvt_pk_bf16_f32 v105, v22, v23\n"
    "v_mfma_f32_4x4x4_16b_bf16 v[32:35], v[46:47], a[218:219], 0\n"
    "buffer_load_dwordx4 v244, s[44:47], s84 offen lds\n"
    "v_cvt_pk_bf16_f32 v106, v24, v25\n"
    "v_cvt_pk_bf16_f32 v107, v26, v27\n"
    "v_and_b32 v41, v39, v255\n"
    "ds_read_b128 v[252:255], v18 offset:448\n"
    "s_waitcnt lgkmcnt(5)\n"
    "ds_read_b64_tr_b16 a[208:209], v45 offset:6152\n"
    "v_mfma_f32_4x4x4_16b_bf16 v[20:23], v[36:37], a[196:197], 0\n"
    "ds_read_b64_tr_b16 a[210:211], v45 offset:6664\n"
    "v_cvt_pk_bf16_f32 v108, v28, v29\n"
    "v_cvt_pk_bf16_f32 v109, v30, v31\n"
    "v_mfma_f32_4x4x4_16b_bf16 v[24:27], v[40:41], a[198:199], 0\n"
    "buffer_load_dwordx4 v245, s[44:47], s84 offen offset:1024 lds\n"
    "v_cvt_pk_bf16_f32 v110, v32, v33\n"
    "v_cvt_pk_bf16_f32 v111, v34, v35\n"
    "buffer_load_dwordx4 v246, s[44:47], s84 offen offset:2048 lds\n"
    "s_waitcnt vmcnt(16) lgkmcnt(5)\n"
    "ds_read_b64_tr_b16 a[212:213], v15 offset:10240\n"
    "v_mfma_f32_4x4x4_16b_bf16 v[28:31], v[36:37], a[200:201], 0\n"
    "ds_read_b64_tr_b16 a[214:215], v15 offset:11264\n"
    "v_cvt_pk_bf16_f32 v112, v20, v21\n"
    "v_cvt_pk_bf16_f32 v113, v22, v23\n"
    "v_mfma_f32_4x4x4_16b_bf16 v[32:35], v[40:41], a[202:203], 0\n"
    "buffer_load_dwordx4 v247, s[44:47], s84 offen offset:3072 lds\n"
    "v_cvt_pk_bf16_f32 v114, v24, v25\n"
    "v_cvt_pk_bf16_f32 v115, v26, v27\n"
    "s_waitcnt lgkmcnt(4)\n"
    "ds_read_b64_tr_b16 a[216:217], v15 offset:10248\n"
    "v_mfma_f32_4x4x4_16b_bf16 v[20:23], v[36:37], a[204:205], 0\n"
    "ds_read_b64_tr_b16 a[218:219], v15 offset:11272\n"
    "s_add_u32 m0, s83, 65536\n"
    "v_cvt_pk_bf16_f32 v116, v28, v29\n"
    "v_cvt_pk_bf16_f32 v117, v30, v31\n"
    "v_mfma_f32_4x4x4_16b_bf16 v[24:27], v[40:41], a[206:207], 0\n"
    "buffer_load_dwordx4 v248, s[44:47], s84 offen lds\n"
    "v_cvt_pk_bf16_f32 v118, v32, v33\n"
    "v_cvt_pk_bf16_f32 v119, v34, v35\n"
    "v_and_b32 v42, v38, v252\n"
    "s_waitcnt vmcnt(14) lgkmcnt(4)\n"
    "ds_read_b64_tr_b16 a[196:197], v44 offset:12288\n"
    "v_mfma_f32_4x4x4_16b_bf16 v[28:31], v[36:37], a[208:209], 0\n"
    "ds_read_b64_tr_b16 a[198:199], v44 offset:12800\n"
    "v_cvt_pk_bf16_f32 v120, v20, v21\n"
    "v_cvt_pk_bf16_f32 v121, v22, v23\n"
    "v_mfma_f32_4x4x4_16b_bf16 v[32:35], v[40:41], a[210:211], 0\n"
    "buffer_load_dwordx4 v249, s[44:47], s84 offen offset:1024 lds\n"
    "v_cvt_pk_bf16_f32 v122, v24, v25\n"
    "v_cvt_pk_bf16_f32 v123, v26, v27\n"
    "v_and_b32 v43, v39, v253\n"
    "s_waitcnt lgkmcnt(4)\n"
    "ds_read_b64_tr_b16 a[200:201], v44 offset:12296\n"
    "v_mfma_f32_4x4x4_16b_bf16 v[20:23], v[36:37], a[212:213], 0\n"
    "ds_read_b64_tr_b16 a[202:203], v44 offset:12808\n"
    "s_add_u32 m0, s83, 36864\n"
    "s_add_u32 s84, s60, s64\n"
    "v_cvt_pk_bf16_f32 v124, v28, v29\n"
    "v_cvt_pk_bf16_f32 v125, v30, v31\n"
    "v_mfma_f32_4x4x4_16b_bf16 v[24:27], v[40:41], a[214:215], 0\n"
    "buffer_load_dwordx4 v244, s[44:47], s84 offen lds\n"
    "v_cvt_pk_bf16_f32 v126, v32, v33\n"
    "v_cvt_pk_bf16_f32 v127, v34, v35\n"
    "buffer_load_dwordx4 v245, s[44:47], s84 offen offset:1024 lds\n"
    "v_and_b32 v46, v38, v254\n"
    "s_waitcnt lgkmcnt(4)\n"
    "ds_read_b64_tr_b16 a[204:205], v45 offset:12288\n"
    "v_mfma_f32_4x4x4_16b_bf16 v[28:31], v[36:37], a[216:217], 0\n"
    "ds_read_b64_tr_b16 a[206:207], v45 offset:12800\n"
    "v_cvt_pk_bf16_f32 v128, v20, v21\n"
    "v_cvt_pk_bf16_f32 v129, v22, v23\n"
    "v_mfma_f32_4x4x4_16b_bf16 v[32:35], v[40:41], a[218:219], 0\n"
    "buffer_load_dwordx4 v246, s[44:47], s84 offen offset:2048 lds\n"
    "v_cvt_pk_bf16_f32 v130, v24, v25\n"
    "v_cvt_pk_bf16_f32 v131, v26, v27\n"
    "v_and_b32 v47, v39, v255\n"
    "ds_read_b128 v[252:255], v18 offset:512\n"
    "s_waitcnt vmcnt(13)\n"
    "v_and_b32 v242, 15, v237\n"
    "v_and_b32 v243, 1, v239\n"
    "v_or_b32 v237, 15, v237\n"
    "s_waitcnt lgkmcnt(5)\n"
    "ds_read_b64_tr_b16 a[208:209], v45 offset:12296\n"
    "v_mfma_f32_4x4x4_16b_bf16 v[20:23], v[42:43], a[196:197], 0\n"
    "ds_read_b64_tr_b16 a[210:211], v45 offset:12808\n"
    "v_cvt_pk_bf16_f32 v132, v28, v29\n"
    "v_cvt_pk_bf16_f32 v133, v30, v31\n"
    "v_mfma_f32_4x4x4_16b_bf16 v[24:27], v[46:47], a[198:199], 0\n"
    "buffer_load_dwordx4 v247, s[44:47], s84 offen offset:3072 lds\n"
    "v_cvt_pk_bf16_f32 v134, v32, v33\n"
    "v_cvt_pk_bf16_f32 v135, v34, v35\n"
    "v_or_b32 v239, 15, v239\n"
    "v_cmp_ne_u64 vcc, s[18:19], v[236:237]\n"
    "v_cmp_ne_u64 s[48:49], s[18:19], v[238:239]\n"
    "v_readlane_b32 s9, v242, 0\n"
    "s_waitcnt vmcnt(17) lgkmcnt(5)\n"
    "ds_read_b64_tr_b16 a[212:213], v15 offset:16384\n"
    "v_mfma_f32_4x4x4_16b_bf16 v[28:31], v[42:43], a[200:201], 0\n"
    "ds_read_b64_tr_b16 a[214:215], v15 offset:17408\n"
    "s_add_u32 m0, s83, 40960\n"
    "v_cvt_pk_bf16_f32 v136, v20, v21\n"
    "v_cvt_pk_bf16_f32 v137, v22, v23\n"
    "v_mfma_f32_4x4x4_16b_bf16 v[32:35], v[46:47], a[202:203], 0\n"
    "buffer_load_dwordx4 v248, s[44:47], s84 offen lds\n"
    "v_cvt_pk_bf16_f32 v138, v24, v25\n"
    "v_cvt_pk_bf16_f32 v139, v26, v27\n"
    "v_and_b32 v236, 1, v242\n"
    "s_or_b64 s[48:49], s[48:49], vcc\n"
    "s_cbranch_scc1 75f\n"
    "77:\n"
    "s_waitcnt lgkmcnt(4)\n"
    "ds_read_b64_tr_b16 a[216:217], v15 offset:16392\n"
    "v_mfma_f32_4x4x4_16b_bf16 v[20:23], v[42:43], a[204:205], 0\n"
    "ds_read_b64_tr_b16 a[218:219], v15 offset:17416\n"
    "v_cvt_pk_bf16_f32 v140, v28, v29\n"
    "v_cvt_pk_bf16_f32 v141, v30, v31\n"
    "v_mfma_f32_4x4x4_16b_bf16 v[24:27], v[46:47], a[206:207], 0\n"
    "buffer_load_dwordx4 v249, s[44:47], s84 offen offset:1024 lds\n"
    "s_add_u32 m0, s83, 43008\n"
    "s_add_u32 s84, s61, s64\n"
    "v_cvt_pk_bf16_f32 v142, v32, v33\n"
    "v_cvt_pk_bf16_f32 v143, v34, v35\n"
    "buffer_load_dwordx4 v244, s[44:47], s84 offen lds\n"
    "v_and_b32 v36, v38, v252\n"
    "v_cmp_eq_u32 s[24:25], 1, v236\n"
    "s_lshr_b32 s9, s9, 1\n"
    "s_add_u32 s9, s9, s28\n"
    "v_lshrrev_b64 v[240:241], v232, s[24:25]\n"
    "s_waitcnt vmcnt(15) lgkmcnt(4)\n"
    "ds_read_b64_tr_b16 a[196:197], v44 offset:18432\n"
    "v_mfma_f32_4x4x4_16b_bf16 v[28:31], v[42:43], a[208:209], 0\n"
    "ds_read_b64_tr_b16 a[198:199], v44 offset:18944\n"
    "v_cvt_pk_bf16_f32 v144, v20, v21\n"
    "v_cvt_pk_bf16_f32 v145, v22, v23\n"
    "v_mfma_f32_4x4x4_16b_bf16 v[32:35], v[46:47], a[210:211], 0\n"
    "buffer_load_dwordx4 v245, s[44:47], s84 offen offset:1024 lds\n"
    "v_cvt_pk_bf16_f32 v146, v24, v25\n"
    "v_cvt_pk_bf16_f32 v147, v26, v27\n"
    "v_and_b32 v37, v39, v253\n"
    "v_and_b32 v241, 0xff, v240\n"
    "v_bfe_u32 v240, v240, v233, 4\n"
    "v_bcnt_u32_b32 v241, v241, 8\n"
    "v_bcnt_u32_b32 v240, v240, 0\n"
    "s_waitcnt lgkmcnt(4)\n"
    "ds_read_b64_tr_b16 a[200:201], v44 offset:18440\n"
    "v_mfma_f32_4x4x4_16b_bf16 v[20:23], v[42:43], a[212:213], 0\n"
    "ds_read_b64_tr_b16 a[202:203], v44 offset:18952\n"
    "v_cvt_pk_bf16_f32 v148, v28, v29\n"
    "v_cvt_pk_bf16_f32 v149, v30, v31\n"
    "v_mfma_f32_4x4x4_16b_bf16 v[24:27], v[46:47], a[214:215], 0\n"
    "buffer_load_dwordx4 v246, s[44:47], s84 offen offset:2048 lds\n"
    "v_cvt_pk_bf16_f32 v150, v32, v33\n"
    "v_cvt_pk_bf16_f32 v151, v34, v35\n"
    "v_and_b32 v40, v38, v254\n"
    "v_lshl_add_u32 v240, v240, 1, v241\n"
    "s_lshr_b32 s10, s9, 2\n"
    "v_cmp_lt_u32 vcc, 15, v240\n"
    "v_xor_b32 v234, s10, v234\n"
    "s_waitcnt lgkmcnt(4)\n"
    "ds_read_b64_tr_b16 a[204:205], v45 offset:18432\n"
    "v_mfma_f32_4x4x4_16b_bf16 v[28:31], v[42:43], a[216:217], 0\n"
    "ds_read_b64_tr_b16 a[206:207], v45 offset:18944\n"
    "v_cvt_pk_bf16_f32 v152, v20, v21\n"
    "v_cvt_pk_bf16_f32 v153, v22, v23\n"
    "v_mfma_f32_4x4x4_16b_bf16 v[32:35], v[46:47], a[218:219], 0\n"
    "buffer_load_dwordx4 v247, s[44:47], s84 offen offset:3072 lds\n"
    "v_cvt_pk_bf16_f32 v154, v24, v25\n"
    "v_cvt_pk_bf16_f32 v155, v26, v27\n"
    "v_and_b32 v41, v39, v255\n"
    "ds_read_b128 v[252:255], v18 offset:576\n"
    "s_mov_b64 s[50:51], vcc\n"
    "v_addc_co_u32 v234, vcc, 0, v234, vcc\n"
    "v_and_b32 v234, 1, v234\n"
    "v_mad_u32_u24 v236, v234, s88, v3\n"
    "s_waitcnt lgkmcnt(5)\n"
    "ds_read_b64_tr_b16 a[208:209], v45 offset:18440\n"
    "v_mfma_f32_4x4x4_16b_bf16 v[20:23], v[36:37], a[196:197], 0\n"
    "ds_read_b64_tr_b16 a[210:211], v45 offset:18952\n"
    "s_add_u32 m0, s83, 47104\n"
    "v_cvt_pk_bf16_f32 v156, v28, v29\n"
    "v_cvt_pk_bf16_f32 v157, v30, v31\n"
    "v_mfma_f32_4x4x4_16b_bf16 v[24:27], v[40:41], a[198:199], 0\n"
    "buffer_load_dwordx4 v248, s[44:47], s84 offen lds\n"
    "v_cvt_pk_bf16_f32 v158, v32, v33\n"
    "v_cvt_pk_bf16_f32 v159, v34, v35\n"
    "v_cmp_eq_u32 s[26:27], 1, v243\n"
    "v_readlane_b32 s72, v236, 0\n"
    "v_readlane_b32 s73, v236, 1\n"
    "s_waitcnt vmcnt(17) lgkmcnt(5)\n"
    "ds_read_b64_tr_b16 a[212:213], v15 offset:22528\n"
    "v_mfma_f32_4x4x4_16b_bf16 v[28:31], v[36:37], a[200:201], 0\n"
    "ds_read_b64_tr_b16 a[214:215], v15 offset:23552\n"
    "v_cvt_pk_bf16_f32 v160, v20, v21\n"
    "v_cvt_pk_bf16_f32 v161, v22, v23\n"
    "v_mfma_f32_4x4x4_16b_bf16 v[32:35], v[40:41], a[202:203], 0\n"
    "buffer_load_dwordx4 v249, s[44:47], s84 offen offset:1024 lds\n"
    "v_cvt_pk_bf16_f32 v162, v24, v25\n"
    "v_cvt_pk_bf16_f32 v163, v26, v27\n"
    "s_waitcnt lgkmcnt(4)\n"
    "ds_read_b64_tr_b16 a[216:217], v15 offset:22536\n"
    "v_mfma_f32_4x4x4_16b_bf16 v[20:23], v[36:37], a[204:205], 0\n"
    "ds_read_b64_tr_b16 a[218:219], v15 offset:23560\n"
    "s_barrier\n"
    "s_mov_b32 m0, s30\n"
    "v_cvt_pk_bf16_f32 v164, v28, v29\n"
    "v_cvt_pk_bf16_f32 v165, v30, v31\n"
    "v_mfma_f32_4x4x4_16b_bf16 v[24:27], v[40:41], a[206:207], 0\n"
    "buffer_load_dwordx4 v0, s[36:39], s72 offen nt lds\n"
    "v_cvt_pk_bf16_f32 v166, v32, v33\n"
    "v_cvt_pk_bf16_f32 v167, v34, v35\n"
    "v_and_b32 v42, v38, v252\n"
    "s_waitcnt vmcnt(15) lgkmcnt(4)\n"
    "ds_read_b64_tr_b16 a[196:197], v44 offset:24576\n"
    "v_mfma_f32_4x4x4_16b_bf16 v[28:31], v[36:37], a[208:209], 0\n"
    "ds_read_b64_tr_b16 a[198:199], v44 offset:25088\n"
    "v_cvt_pk_bf16_f32 v168, v20, v21\n"
    "v_cvt_pk_bf16_f32 v169, v22, v23\n"
    "v_mfma_f32_4x4x4_16b_bf16 v[32:35], v[40:41], a[210:211], 0\n"
    "buffer_load_dwordx4 v1, s[36:39], s72 offen offset:1024 nt lds\n"
    "v_cvt_pk_bf16_f32 v170, v24, v25\n"
    "v_cvt_pk_bf16_f32 v171, v26, v27\n"
    "v_and_b32 v43, v39, v253\n"
    "s_xor_b64 s[48:49], s[24:25], s[26:27]\n"
    "s_andn2_b32 s49, s49, s96\n"
    "s_bcnt1_i32_b64 s10, s[48:49]\n"
    "s_waitcnt lgkmcnt(4)\n"
    "ds_read_b64_tr_b16 a[200:201], v44 offset:24584\n"
    "v_mfma_f32_4x4x4_16b_bf16 v[20:23], v[36:37], a[212:213], 0\n"
    "ds_read_b64_tr_b16 a[202:203], v44 offset:25096\n"
    "v_cvt_pk_bf16_f32 v172, v28, v29\n"
    "v_cvt_pk_bf16_f32 v173, v30, v31\n"
    "v_mfma_f32_4x4x4_16b_bf16 v[24:27], v[40:41], a[214:215], 0\n"
    "buffer_load_dwordx4 v2, s[36:39], s72 offen offset:2048 nt lds\n"
    "v_cvt_pk_bf16_f32 v174, v32, v33\n"
    "v_cvt_pk_bf16_f32 v175, v34, v35\n"
    "v_and_b32 v46, v38, v254\n"
    "s_cmp_ge_u32 s10, 35\n"
    "s_cselect_b64 s[34:35], s[34:35], 0\n"
    "s_cselect_b32 s96, 1, 0\n"
    "v_lshrrev_b64 v[240:241], v232, s[26:27]\n"
    "s_waitcnt lgkmcnt(4)\n"
    "ds_read_b64_tr_b16 a[204:205], v45 offset:24576\n"
    "v_mfma_f32_4x4x4_16b_bf16 v[28:31], v[36:37], a[216:217], 0\n"
    "ds_read_b64_tr_b16 a[206:207], v45 offset:25088\n"
    "v_cvt_pk_bf16_f32 v176, v20, v21\n"
    "v_cvt_pk_bf16_f32 v177, v22, v23\n"
    "v_mfma_f32_4x4x4_16b_bf16 v[32:35], v[40:41], a[218:219], 0\n"
    "v_cvt_pk_bf16_f32 v178, v24, v25\n"
    "v_cvt_pk_bf16_f32 v179, v26, v27\n"
    "v_and_b32 v47, v39, v255\n"
    "ds_read_b128 v[252:255], v18 offset:640\n"
    "v_and_b32 v241, 0xff, v240\n"
    "v_bfe_u32 v240, v240, v233, 4\n"
    "v_bcnt_u32_b32 v241, v241, 8\n"
    "v_bcnt_u32_b32 v240, v240, 0\n"
    "s_waitcnt lgkmcnt(5)\n"
    "ds_read_b64_tr_b16 a[208:209], v45 offset:24584\n"
    "v_mfma_f32_4x4x4_16b_bf16 v[20:23], v[42:43], a[196:197], 0\n"
    "ds_read_b64_tr_b16 a[210:211], v45 offset:25096\n"
    "v_cvt_pk_bf16_f32 v180, v28, v29\n"
    "v_cvt_pk_bf16_f32 v181, v30, v31\n"
    "v_mfma_f32_4x4x4_16b_bf16 v[24:27], v[46:47], a[198:199], 0\n"
    "v_cvt_pk_bf16_f32 v182, v32, v33\n"
    "v_cvt_pk_bf16_f32 v183, v34, v35\n"
    "v_lshl_add_u32 v240, v240, 1, v241\n"
    "v_cmp_lt_u32 s[26:27], 15, v240\n"
    "s_bitcmp1_b32 s9, 0\n"
    "s_cselect_b32 s95, -1, 1\n"
    "s_waitcnt vmcnt(15) lgkmcnt(5)\n"
    "ds_read_b64_tr_b16 a[212:213], v15 offset:28672\n"
    "v_mfma_f32_4x4x4_16b_bf16 v[28:31], v[42:43], a[200:201], 0\n"
    "ds_read_b64_tr_b16 a[214:215], v15 offset:29696\n"
    "v_cvt_pk_bf16_f32 v184, v20, v21\n"
    "v_cvt_pk_bf16_f32 v185, v22, v23\n"
    "v_mfma_f32_4x4x4_16b_bf16 v[32:35], v[46:47], a[202:203], 0\n"
    "v_cvt_pk_bf16_f32 v186, v24, v25\n"
    "v_cvt_pk_bf16_f32 v187, v26, v27\n"
    "s_cmp_lt_u32 s52, 12\n"
    "s_cselect_b32 s95, 0, s95\n"
    "s_add_u32 s52, s52, s95\n"
    "s_xor_b64 s[26:27], s[26:27], s[50:51]\n"
    "s_waitcnt lgkmcnt(4)\n"
    "ds_read_b64_tr_b16 a[216:217], v15 offset:28680\n"
    "v_mfma_f32_4x4x4_16b_bf16 v[20:23], v[42:43], a[204:205], 0\n"
    "ds_read_b64_tr_b16 a[218:219], v15 offset:29704\n"
    "v_cvt_pk_bf16_f32 v188, v28, v29\n"
    "v_cvt_pk_bf16_f32 v189, v30, v31\n"
    "v_mfma_f32_4x4x4_16b_bf16 v[24:27], v[46:47], a[206:207], 0\n"
    "v_cvt_pk_bf16_f32 v190, v32, v33\n"
    "v_cvt_pk_bf16_f32 v191, v34, v35\n"
    "v_and_b32 v36, v38, v252\n"
    "s_and_b64 vcc, s[34:35], s[26:27]\n"
    "v_addc_co_u32 v234, vcc, 0, v234, vcc\n"
    "v_cmp_le_u32 vcc, s52, v19\n"
    "v_subrev_u32 v240, 15, v240\n"
    "s_waitcnt vmcnt(11) lgkmcnt(4)\n"
    "ds_read_b64_tr_b16 a[196:197], v44\n"
    "v_mfma_f32_4x4x4_16b_bf16 v[28:31], v[42:43], a[208:209], 0\n"
    "ds_read_b64_tr_b16 a[198:199], v44 offset:512\n"
    "v_cvt_pk_bf16_f32 v192, v20, v21\n"
    "v_cvt_pk_bf16_f32 v193, v22, v23\n"
    "v_mfma_f32_4x4x4_16b_bf16 v[32:35], v[46:47], a[210:211], 0\n"
    "v_cvt_pk_bf16_f32 v194, v24, v25\n"
    "v_cvt_pk_bf16_f32 v195, v26, v27\n"
    "v_and_b32 v37, v39, v253\n"
    "v_cmp_gt_u32 s[48:49], 3, v240\n"
    "s_or_b64 s[26:27], s[26:27], s[48:49]\n"
    "v_and_b32 v234, 1, v234\n"
    "v_mov_b32 v236, s33\n"
    "s_waitcnt lgkmcnt(4)\n"
    "ds_read_b64_tr_b16 a[200:201], v44 offset:8\n"
    "v_mfma_f32_4x4x4_16b_bf16 v[20:23], v[42:43], a[212:213], 0\n"
    "ds_read_b64_tr_b16 a[202:203], v44 offset:520\n"
    "v_cvt_pk_bf16_f32 v196, v28, v29\n"
    "v_cvt_pk_bf16_f32 v197, v30, v31\n"
    "v_mfma_f32_4x4x4_16b_bf16 v[24:27], v[46:47], a[214:215], 0\n"
    "v_cvt_pk_bf16_f32 v198, v32, v33\n"
    "v_cvt_pk_bf16_f32 v199, v34, v35\n"
    "v_and_b32 v40, v38, v254\n"
    "v_mad_u32_u24 v3, v234, s88, v3\n"
    "v_cndmask_b32 v3, v3, v236, vcc\n"
    "s_nop 0\n"
    "v_readlane_b32 s74, v3, 2\n"
    "s_waitcnt lgkmcnt(4)\n"
    "ds_read_b64_tr_b16 a[204:205], v45\n"
    "v_mfma_f32_4x4x4_16b_bf16 v[28:31], v[42:43], a[216:217], 0\n"
    "ds_read_b64_tr_b16 a[206:207], v45 offset:512\n"
    "v_cvt_pk_bf16_f32 v200, v20, v21\n"
    "v_cvt_pk_bf16_f32 v201, v22, v23\n"
    "v_mfma_f32_4x4x4_16b_bf16 v[32:35], v[46:47], a[218:219], 0\n"
    "v_cvt_pk_bf16_f32 v202, v24, v25\n"
    "v_cvt_pk_bf16_f32 v203, v26, v27\n"
    "v_and_b32 v41, v39, v255\n"
    "ds_read_b128 v[252:255], v18 offset:704\n"
    "v_readlane_b32 s75, v3, 3\n"
    "v_readlane_b32 s81, v3, 4\n"
    "s_waitcnt lgkmcnt(5)\n"
    "ds_read_b64_tr_b16 a[208:209], v45 offset:8\n"
    "v_mfma_f32_4x4x4_16b_bf16 v[20:23], v[36:37], a[196:197], 0\n"
    "ds_read_b64_tr_b16 a[210:211], v45 offset:520\n"
    "s_add_u32 s30, s30, 12288\n"
    "s_mov_b32 m0, s30\n"
    "v_cvt_pk_bf16_f32 v204, v28, v29\n"
    "v_cvt_pk_bf16_f32 v205, v30, v31\n"
    "v_mfma_f32_4x4x4_16b_bf16 v[24:27], v[40:41], a[198:199], 0\n"
    "buffer_load_dwordx4 v0, s[36:39], s73 offen nt lds\n"
    "v_cvt_pk_bf16_f32 v206, v32, v33\n"
    "v_cvt_pk_bf16_f32 v207, v34, v35\n"
    "s_waitcnt vmcnt(10) lgkmcnt(5)\n"
    "ds_read_b64_tr_b16 a[212:213], v15 offset:4096\n"
    "v_mfma_f32_4x4x4_16b_bf16 v[28:31], v[36:37], a[200:201], 0\n"
    "ds_read_b64_tr_b16 a[214:215], v15 offset:5120\n"
    "v_cvt_pk_bf16_f32 v208, v20, v21\n"
    "v_cvt_pk_bf16_f32 v209, v22, v23\n"
    "v_mfma_f32_4x4x4_16b_bf16 v[32:35], v[40:41], a[202:203], 0\n"
    "buffer_load_dwordx4 v1, s[36:39], s73 offen offset:1024 nt lds\n"
    "v_cvt_pk_bf16_f32 v210, v24, v25\n"
    "v_cvt_pk_bf16_f32 v211, v26, v27\n"
    "s_waitcnt lgkmcnt(4)\n"
    "ds_read_b64_tr_b16 a[216:217], v15 offset:4104\n"
    "v_mfma_f32_4x4x4_16b_bf16 v[20:23], v[36:37], a[204:205], 0\n"
    "ds_read_b64_tr_b16 a[218:219], v15 offset:5128\n"
    "v_cvt_pk_bf16_f32 v212, v28, v29\n"
    "v_cvt_pk_bf16_f32 v213, v30, v31\n"
    "v_mfma_f32_4x4x4_16b_bf16 v[24:27], v[40:41], a[206:207], 0\n"
    "buffer_load_dwordx4 v2, s[36:39], s73 offen offset:2048 nt lds\n"
    "v_cvt_pk_bf16_f32 v214, v32, v33\n"
    "v_cvt_pk_bf16_f32 v215, v34, v35\n"
    "v_and_b32 v42, v38, v252\n"
    "s_waitcnt vmcnt(8) lgkmcnt(4)\n"
    "ds_read_b64_tr_b16 a[196:197], v44 offset:6144\n"
    "v_mfma_f32_4x4x4_16b_bf16 v[28:31], v[36:37], a[208:209], 0\n"
    "ds_read_b64_tr_b16 a[198:199], v44 offset:6656\n"
    "v_cvt_pk_bf16_f32 v216, v20, v21\n"
    "v_cvt_pk_bf16_f32 v217, v22, v23\n"
    "v_mfma_f32_4x4x4_16b_bf16 v[32:35], v[40:41], a[210:211], 0\n"
    "v_cvt_pk_bf16_f32 v218, v24, v25\n"
    "v_cvt_pk_bf16_f32 v219, v26, v27\n"
    "v_and_b32 v43, v39, v253\n"
    "s_waitcnt lgkmcnt(4)\n"
    "ds_read_b64_tr_b16 a[200:201], v44 offset:6152\n"
    "v_mfma_f32_4x4x4_16b_bf16 v[20:23], v[36:37], a[212:213], 0\n"
    "ds_read_b64_tr_b16 a[202:203], v44 offset:6664\n"
    "v_cvt_pk_bf16_f32 v220, v28, v29\n"
    "v_cvt_pk_bf16_f32 v221, v30, v31\n"
    "v_mfma_f32_4x4x4_16b_bf16 v[24:27], v[40:41], a[214:215], 0\n"
    "v_cvt_pk_bf16_f32 v222, v32, v33\n"
    "v_cvt_pk_bf16_f32 v223, v34, v35\n"
    "v_and_b32 v46, v38, v254\n"
    "s_waitcnt lgkmcnt(4)\n"
    "ds_read_b64_tr_b16 a[204:205], v45 offset:6144\n"
    "v_mfma_f32_4x4x4_16b_bf16 v[28:31], v[36:37], a[216:217], 0\n"
    "ds_read_b64_tr_b16 a[206:207], v45 offset:6656\n"
    "v_cvt_pk_bf16_f32 v224, v20, v21\n"
    "v_cvt_pk_bf16_f32 v225, v22, v23\n"
    "v_mfma_f32_4x4x4_16b_bf16 v[32:35], v[40:41], a[218:219], 0\n"
    "v_cvt_pk_bf16_f32 v226, v24, v25\n"
    "v_cvt_pk_bf16_f32 v227, v26, v27\n"
    "v_and_b32 v47, v39, v255\n"
    "s_waitcnt lgkmcnt(4)\n"
    "ds_read_b64_tr_b16 a[208:209], v45 offset:6152\n"
    "v_mfma_f32_4x4x4_16b_bf16 v[20:23], v[42:43], a[196:197], 0\n"
    "ds_read_b64_tr_b16 a[210:211], v45 offset:6664\n"
    "v_cvt_pk_bf16_f32 v228, v28, v29\n"
    "v_cvt_pk_bf16_f32 v229, v30, v31\n"
    "v_mfma_f32_4x4x4_16b_bf16 v[24:27], v[46:47], a[198:199], 0\n"
    "v_cvt_pk_bf16_f32 v230, v32, v33\n"
    "v_cvt_pk_bf16_f32 v231, v34, v35\n"
    "s_nop 0\n"
    "s_waitcnt vmcnt(6) lgkmcnt(4)\n"
    "ds_read_b64_tr_b16 a[212:213], v15 offset:10240\n"
    "v_mfma_f32_4x4x4_16b_bf16 v[28:31], v[42:43], a[200:201], 0\n"
    "ds_read_b64_tr_b16 a[214:215], v15 offset:11264\n"
    "v_cvt_pk_bf16_f32 v232, v20, v21\n"
    "v_cvt_pk_bf16_f32 v233, v22, v23\n"
    "v_mfma_f32_4x4x4_16b_bf16 v[32:35], v[46:47], a[202:203], 0\n"
    "v_cvt_pk_bf16_f32 v234, v24, v25\n"
    "v_cvt_pk_bf16_f32 v235, v26, v27\n"
    "s_nop 0\n"
    "s_waitcnt lgkmcnt(4)\n"
    "ds_read_b64_tr_b16 a[216:217], v15 offset:10248\n"
    "v_mfma_f32_4x4x4_16b_bf16 v[20:23], v[42:43], a[204:205], 0\n"
    "ds_read_b64_tr_b16 a[218:219], v15 offset:11272\n"
    "v_cvt_pk_bf16_f32 v236, v28, v29\n"
    "v_cvt_pk_bf16_f32 v237, v30, v31\n"
    "v_mfma_f32_4x4x4_16b_bf16 v[24:27], v[46:47], a[206:207], 0\n"
    "v_cvt_pk_bf16_f32 v238, v32, v33\n"
    "v_cvt_pk_bf16_f32 v239, v34, v35\n"
    "s_nop 0\n"
    "s_waitcnt lgkmcnt(4)\n"
    "v_mfma_f32_4x4x4_16b_bf16 v[28:31], v[42:43], a[208:209], 0\n"
    "v_cvt_pk_bf16_f32 v240, v20, v21\n"
    "v_cvt_pk_bf16_f32 v241, v22, v23\n"
    "s_nop 0\n"
    "v_mfma_f32_4x4x4_16b_bf16 v[32:35], v[46:47], a[210:211], 0\n"
    "v_cvt_pk_bf16_f32 v242, v24, v25\n"
    "v_cvt_pk_bf16_f32 v243, v26, v27\n"
    "s_nop 2\n"
    "s_waitcnt lgkmcnt(2)\n"
    "v_mfma_f32_4x4x4_16b_bf16 v[20:23], v[42:43], a[212:213], 0\n"
    "v_cvt_pk_bf16_f32 v244, v28, v29\n"
    "v_cvt_pk_bf16_f32 v245, v30, v31\n"
    "s_nop 0\n"
    "v_mfma_f32_4x4x4_16b_bf16 v[24:27], v[46:47], a[214:215], 0\n"
    "v_cvt_pk_bf16_f32 v246, v32, v33\n"
    "v_cvt_pk_bf16_f32 v247, v34, v35\n"
    "s_nop 2\n"
    "s_waitcnt lgkmcnt(0)\n"
    "200:\n"
    "s_cbranch_execz 199b\n"
    "v_mfma_f32_4x4x4_16b_bf16 v[28:31], v[42:43], a[216:217], 0\n"
    "v_cvt_pk_bf16_f32 v248, v20, v21\n"
    "v_cvt_pk_bf16_f32 v249, v22, v23\n"
    "s_nop 0\n"
    "v_mfma_f32_4x4x4_16b_bf16 v[32:35], v[46:47], a[218:219], 0\n"
    "v_cvt_pk_bf16_f32 v250, v24, v25\n"
    "v_cvt_pk_bf16_f32 v251, v26, v27\n"
    "s_nop 2\n"
    "s_nop 0\n"
    "s_nop 0\n"
    "s_nop 1\n"
    "v_cvt_pk_bf16_f32 v252, v28, v29\n"
    "v_cvt_pk_bf16_f32 v253, v30, v31\n"
    "v_cvt_pk_bf16_f32 v254, v32, v33\n"
    "v_cvt_pk_bf16_f32 v255, v34, v35\n"
    "s_waitcnt lgkmcnt(0)\n"
    "s_add_u32 s89, s92, 300\n"
    "s_waitcnt vmcnt(3)\n"
    "s_barrier\n"
    "ds_read_b128 a[88:91], v4\n"
    "ds_read_b128 a[92:95], v5\n"
    "ds_read_b128 a[96:99], v6\n"
    "ds_read_b128 a[100:103], v7\n"
    "ds_read_b128 a[104:107], v4 offset:256\n"
    "ds_read_b128 a[108:111], v5 offset:256\n"
    "ds_read_b128 a[112:115], v6 offset:256\n"
    "ds_read_b128 a[116:119], v7 offset:256\n"
    "ds_read_b128 a[120:123], v4 offset:512\n"
    "ds_read_b128 a[124:127], v5 offset:512\n"
    "ds_read_b128 a[128:131], v6 offset:512\n"
    "ds_read_b128 a[132:135], v7 offset:512\n"
    "s_waitcnt lgkmcnt(11)\n"
    "v_mfma_f32_16x16x32_bf16 v[20:23], a[0:3], a[88:91], 0\n"
    "v_add_u32 v14, 1, v14\n"
    "s_sub_i32 s46, s52, 5\n"
    "s_max_i32 s46, s46, 0\n"
    "s_waitcnt lgkmcnt(10)\n"
    "v_mfma_f32_16x16x32_bf16 v[20:23], a[24:27], a[92:95], v[20:23]\n"
    "s_add_u32 m0, s57, 24576\n"
    "s_bfm_b64 s[44:45], s46, 5\n"
    "s_mov_b64 s[48:49], s[26:27]\n"
    "s_waitcnt lgkmcnt(9)\n"
    "v_mfma_f32_16x16x32_bf16 v[20:23], a[48:51], a[96:99], v[20:23]\n"
    "buffer_load_dwordx4 v0, s[36:39], s74 offen nt lds\n"
    "v_subrev_u32 v33, 5, v19\n"
    "s_waitcnt lgkmcnt(8)\n"
    "v_mfma_f32_16x16x32_bf16 v[20:23], a[72:75], a[100:103], v[20:23]\n"
    "s_andn2_b64 s[50:51], s[44:45], s[48:49]\n"
    "s_and_b64 s[48:49], s[44:45], s[48:49]\n"
    "s_bcnt1_i32_b64 s47, s[50:51]\n"
    "s_waitcnt lgkmcnt(7)\n"
    "v_mfma_f32_16x16x32_bf16 v[20:23], v[64:67], a[104:107], v[20:23]\n"
    "buffer_load_dwordx4 v1, s[36:39], s74 offen offset:1024 nt lds\n"
    "s_sub_u32 s46, s46, s47\n"
    "s_waitcnt lgkmcnt(6)\n"
    "v_mfma_f32_16x16x32_bf16 v[20:23], v[88:91], a[108:111], v[20:23]\n"
    "s_add_u32 s58, s46, 5\n"
    "v_mov_b32 v34, s58\n"
    "v_mbcnt_lo_u32_b32 v35, s48, 5\n"
    "s_waitcnt lgkmcnt(5)\n"
    "v_mfma_f32_16x16x32_bf16 v[20:23], v[112:115], a[112:115], v[20:23]\n"
    "buffer_load_dwordx4 v2, s[36:39], s74 offen offset:2048 nt lds\n"
    "v_mbcnt_lo_u32_b32 v34, s50, v34\n"
    "s_waitcnt lgkmcnt(4)\n"
    "v_mfma_f32_16x16x32_bf16 v[20:23], v[136:139], a[116:119], v[20:23]\n"
    "v_mbcnt_hi_u32_b32 v35, s49, v35\n"
    "v_mbcnt_hi_u32_b32 v34, s51, v34\n"
    "s_mul_i32 s94, s46, 2\n"
    "s_waitcnt lgkmcnt(3)\n"
    "v_mfma_f32_16x16x32_bf16 v[20:23], v[160:163], a[120:123], v[20:23]\n"
    "s_lshr_b32 s94, s94, 4\n"
    "s_add_u32 s94, s94, 70\n"
    "s_waitcnt lgkmcnt(2)\n"
    "v_mfma_f32_16x16x32_bf16 v[20:23], v[184:187], a[124:127], v[20:23]\n"
    "s_cmp_lg_u32 s96, 0\n"
    "s_cselect_b32 s94, 77, s94\n"
    "s_waitcnt lgkmcnt(1)\n"
    "v_mfma_f32_16x16x32_bf16 v[20:23], v[208:211], a[128:131], v[20:23]\n"
    "s_cmp_lg_u32 s96, 0\n"
    "s_cselect_b32 s63, 42, 45\n"
    "s_lshr_b32 s62, s46, 1\n"
    "s_waitcnt lgkmcnt(0)\n"
    "v_mfma_f32_16x16x32_bf16 v[20:23], v[232:235], a[132:135], v[20:23]\n"
    "s_add_u32 s63, s63, s62\n"
    "s_add_u32 s76, s52, 4\n"
    "v_subrev_u32 v18, 6, v19\n"
    "v_mfma_f32_16x16x32_bf16 v[24:27], v[236:239], a[132:135], 0\n"
    "s_sub_u32 s64, s52, 6\n"
    "s_cmp_lt_u32 s47, 4\n"
    "s_cselect_b32 s64, 0x1000, s64\n"
    "v_mfma_f32_16x16x32_bf16 v[24:27], v[212:215], a[128:131], v[24:27]\n"
    "v_subrev_u32 v36, s64, v18\n"
    "v_cmp_gt_u32 s[60:61], 2, v36\n"
    "s_add_u32 s65, s64, 8\n"
    "v_mfma_f32_16x16x32_bf16 v[24:27], v[188:191], a[124:127], v[24:27]\n"
    "s_mul_i32 s7, s64, 43\n"
    "v_cndmask_b32 v18, v18, 63, s[60:61]\n"
    "s_lshr_b32 s7, s7, 9\n"
    "v_mfma_f32_16x16x32_bf16 v[24:27], v[164:167], a[120:123], v[24:27]\n"
    "s_mul_i32 s7, s7, 12\n"
    "s_sub_u32 s7, s64, s7\n"
    "s_mul_i32 s7, s7, 12288\n"
    "v_mfma_f32_16x16x32_bf16 v[24:27], v[140:143], a[116:119], v[24:27]\n"
    "s_add_u32 s64, s7, s57\n"
    "v_lshlrev_b32 v18, 2, v18\n"
    "s_mul_i32 s59, s95, 2\n"
    "v_mfma_f32_16x16x32_bf16 v[24:27], v[116:119], a[112:115], v[24:27]\n"
    "v_cvt_pk_bf16_f32 v44, v20, v21\n"
    "v_cvt_pk_bf16_f32 v45, v22, v23\n"
    "s_sub_u32 s94, s94, s59\n"
    "v_mfma_f32_16x16x32_bf16 v[24:27], v[92:95], a[108:111], v[24:27]\n"
    "v_cndmask_b32 v35, v19, v35, s[48:49]\n"
    "s_sub_i32 s59, s47, 4\n"
    "v_cndmask_b32 v35, v35, v34, s[50:51]\n"
    "v_mfma_f32_16x16x32_bf16 v[24:27], v[68:71], a[104:107], v[24:27]\n"
    "s_max_i32 s59, s59, 0\n"
    "v_lshlrev_b32 v35, 2, v35\n"
    "s_mul_i32 s59, s59, 9\n"
    "v_mfma_f32_16x16x32_bf16 v[24:27], a[76:79], a[100:103], v[24:27]\n"
    "s_min_u32 s59, s59, 256\n"
    "ds_permute_b32 v3, v35, v3\n"
    "v_mul_i32_i24 v28, s59, v33\n"
    "v_mfma_f32_16x16x32_bf16 v[24:27], a[52:55], a[96:99], v[24:27]\n"
    "s_mul_i32 s62, s2, 9\n"
    "s_and_b32 s62, s62, 0xff\n"
    "v_add_u32 v28, s62, v28\n"
    "v_mfma_f32_16x16x32_bf16 v[24:27], a[28:31], a[92:95], v[24:27]\n"
    "s_waitcnt vmcnt(3)\n"
    "ds_write_b32 v13, v14\n"
    "v_mfma_f32_16x16x32_bf16 v[24:27], a[4:7], a[88:91], v[24:27]\n"
    "v_subrev_u32 v32, s46, v33\n"
    "v_add_u32 v30, s59, v28\n"
    "v_ashrrev_i32 v28, 8, v28\n"
    "v_mfma_f32_16x16x32_bf16 v[20:23], a[8:11], a[88:91], 0\n"
    "s_add_u32 m0, s57, 36864\n"
    "v_ashrrev_i32 v30, 8, v30\n"
    "v_min_i32 v28, s47, v28\n"
    "v_mfma_f32_16x16x32_bf16 v[20:23], a[32:35], a[92:95], v[20:23]\n"
    "buffer_load_dwordx4 v0, s[36:39], s75 offen nt lds\n"
    "v_min_i32 v30, s47, v30\n"
    "v_mfma_f32_16x16x32_bf16 v[20:23], a[56:59], a[96:99], v[20:23]\n"
    "v_max_i32 v28, v28, v32\n"
    "v_add_u32 v32, 1, v32\n"
    "v_max_i32 v30, v30, v32\n"
    "v_mfma_f32_16x16x32_bf16 v[20:23], a[80:83], a[100:103], v[20:23]\n"
    "buffer_load_dwordx4 v1, s[36:39], s75 offen offset:1024 nt lds\n"
    "v_cmp_gt_i32 s[60:61], v30, v28\n"
    "v_mfma_f32_16x16x32_bf16 v[20:23], v[72:75], a[104:107], v[20:23]\n"
    "ds_read_b32 v15, v12\n"
    "v_add_u32 v30, s58, v28\n"
    "v_sub_u32 v28, v19, v28\n"
    "v_mfma_f32_16x16x32_bf16 v[20:23], v[96:99], a[108:111], v[20:23]\n"
    "v_cvt_pk_bf16_f32 v46, v24, v25\n"
    "v_cvt_pk_bf16_f32 v47, v26, v27\n"
    "v_cndmask_b32 v28, v28, v30, s[60:61]\n"
    "v_mfma_f32_16x16x32_bf16 v[20:23], v[120:123], a[112:115], v[20:23]\n"
    "buffer_load_dwordx4 v2, s[36:39], s75 offen offset:2048 nt lds\n"
    "v_cndmask_b32 v28, v19, v28, s[44:45]\n"
    "v_mfma_f32_16x16x32_bf16 v[20:23], v[144:147], a[116:119], v[20:23]\n"
    "buffer_load_dword a224, off, s[36:39], s33\n"
    "v_lshlrev_b32 v28, 2, v28\n"
    "v_mfma_f32_16x16x32_bf16 v[20:23], v[168:171], a[120:123], v[20:23]\n"
    "s_waitcnt lgkmcnt(0)\n"
    "v_cmp_lt_u32 vcc, v15, v14\n"
    "v_mfma_f32_16x16x32_bf16 v[20:23], v[192:195], a[124:127], v[20:23]\n"
    "s_cbranch_vccnz 81b\n"
    "82:\n"
    "ds_bpermute_b32 v3, v28, v3\n"
    "v_mfma_f32_16x16x32_bf16 v[20:23], v[216:219], a[128:131], v[20:23]\n"
    "ds_write_b64 v8, v[44:45]\n"
    "s_mov_b32 s62, 0\n"
    "v_mfma_f32_16x16x32_bf16 v[20:23], v[240:243], a[132:135], v[20:23]\n"
    "ds_read_b128 a[136:139], v4 offset:12288\n"
    "s_cmp_lg_u32 s96, 0\n"
    "s_cselect_b32 s31, 4, 6\n"
    "v_mfma_f32_16x16x32_bf16 v[24:27], v[244:247], a[132:135], 0\n"
    "ds_write_b64 v8, v[46:47] offset:8\n"
    "s_add_u32 s31, s94, s31\n"
    "v_mfma_f32_16x16x32_bf16 v[24:27], v[220:223], a[128:131], v[24:27]\n"
    "ds_read_b128 a[140:143], v5 offset:12288\n"
    "v_mfma_f32_16x16x32_bf16 v[24:27], v[196:199], a[124:127], v[24:27]\n"
    "buffer_load_dword a224, off, s[36:39], s33\n"
    "v_mfma_f32_16x16x32_bf16 v[24:27], v[172:175], a[120:123], v[24:27]\n"
    "ds_read_b128 a[144:147], v6 offset:12288\n"
    "v_mfma_f32_16x16x32_bf16 v[24:27], v[148:151], a[116:119], v[24:27]\n"
    "buffer_load_dword a224, off, s[36:39], s33\n"
    "v_mfma_f32_16x16x32_bf16 v[24:27], v[124:127], a[112:115], v[24:27]\n"
    "v_cvt_pk_bf16_f32 v48, v20, v21\n"
    "v_cvt_pk_bf16_f32 v49, v22, v23\n"
    "v_mfma_f32_16x16x32_bf16 v[24:27], v[100:103], a[108:111], v[24:27]\n"
    "ds_read_b128 a[148:151], v7 offset:12288\n"
    "v_mfma_f32_16x16x32_bf16 v[24:27], v[76:79], a[104:107], v[24:27]\n"
    "ds_write_b64 v9, v[48:49]\n"
    "v_mfma_f32_16x16x32_bf16 v[24:27], a[84:87], a[100:103], v[24:27]\n"
    "ds_read_b128 a[152:155], v4 offset:12544\n"
    "v_mfma_f32_16x16x32_bf16 v[24:27], a[60:63], a[96:99], v[24:27]\n"
    "201:\n"
    "s_cbranch_execz 199b\n"
    "v_mfma_f32_16x16x32_bf16 v[24:27], a[36:39], a[92:95], v[24:27]\n"
    "ds_read_b128 a[156:159], v5 offset:12544\n"
    "v_mfma_f32_16x16x32_bf16 v[24:27], a[12:15], a[88:91], v[24:27]\n"
    "v_mfma_f32_16x16x32_bf16 v[20:23], a[16:19], a[88:91], 0\n"
    "ds_read_b128 a[160:163], v6 offset:12544\n"
    "v_mfma_f32_16x16x32_bf16 v[20:23], a[40:43], a[92:95], v[20:23]\n"
    "v_mfma_f32_16x16x32_bf16 v[20:23], a[64:67], a[96:99], v[20:23]\n"
    "ds_read_b128 a[164:167], v7 offset:12544\n"
    "s_add_u32 m0, s57, 49152\n"
    "v_mfma_f32_16x16x32_bf16 v[20:23], v[56:59], a[100:103], v[20:23]\n"
    "buffer_load_dwordx4 v0, s[36:39], s81 offen nt lds\n"
    "v_mfma_f32_16x16x32_bf16 v[20:23], v[80:83], a[104:107], v[20:23]\n"
    "ds_read_b128 a[168:171], v4 offset:12800\n"
    "v_mfma_f32_16x16x32_bf16 v[20:23], v[104:107], a[108:111], v[20:23]\n"
    "v_cvt_pk_bf16_f32 v50, v24, v25\n"
    "v_cvt_pk_bf16_f32 v51, v26, v27\n"
    "v_add_u32 v4, s78, v4\n"
    "v_mfma_f32_16x16x32_bf16 v[20:23], v[128:131], a[112:115], v[20:23]\n"
    "ds_read_b128 a[172:175], v5 offset:12800\n"
    "v_mfma_f32_16x16x32_bf16 v[20:23], v[152:155], a[116:119], v[20:23]\n"
    "ds_write_b64 v9, v[50:51] offset:8\n"
    "v_add_u32 v5, s78, v5\n"
    "v_mfma_f32_16x16x32_bf16 v[20:23], v[176:179], a[120:123], v[20:23]\n"
    "buffer_load_dwordx4 v1, s[36:39], s81 offen offset:1024 nt lds\n"
    "v_mfma_f32_16x16x32_bf16 v[20:23], v[200:203], a[124:127], v[20:23]\n"
    "ds_read_b128 a[176:179], v6 offset:12800\n"
    "v_mfma_f32_16x16x32_bf16 v[20:23], v[224:227], a[128:131], v[20:23]\n"
    "buffer_load_dwordx4 v2, s[36:39], s81 offen offset:2048 nt lds\n"
    "v_add_u32 v6, s78, v6\n"
    "v_mfma_f32_16x16x32_bf16 v[20:23], v[248:251], a[132:135], v[20:23]\n"
    "ds_read_b128 a[180:183], v7 offset:12800\n"
    "s_add_u32 m0, s57, 61440\n"
    "v_mfma_f32_16x16x32_bf16 v[24:27], v[252:255], a[132:135], 0\n"
    "buffer_load_dword a224, off, s[36:39], s33\n"
    "v_add_u32 v7, s78, v7\n"
    "v_mfma_f32_16x16x32_bf16 v[24:27], v[228:231], a[128:131], v[24:27]\n"
    "buffer_load_dword a224, off, s[36:39], s33\n"
    "v_mfma_f32_16x16x32_bf16 v[24:27], v[204:207], a[124:127], v[24:27]\n"
    "buffer_load_dword a224, off, s[36:39], s33\n"
    "v_mfma_f32_16x16x32_bf16 v[24:27], v[180:183], a[120:123], v[24:27]\n"
    "s_memrealtime s[92:93]\n"
    "v_mfma_f32_16x16x32_bf16 v[24:27], v[156:159], a[116:119], v[24:27]\n"
    "v_mfma_f32_16x16x32_bf16 v[24:27], v[132:135], a[112:115], v[24:27]\n"
    "v_cvt_pk_bf16_f32 v52, v20, v21\n"
    "v_cvt_pk_bf16_f32 v53, v22, v23\n"
    "v_mfma_f32_16x16x32_bf16 v[24:27], v[108:111], a[108:111], v[24:27]\n"
    "ds_write_b64 v10, v[52:53]\n"
    "v_mfma_f32_16x16x32_bf16 v[24:27], v[84:87], a[104:107], v[24:27]\n"
    "v_mfma_f32_16x16x32_bf16 v[24:27], v[60:63], a[100:103], v[24:27]\n"
    "s_waitcnt vmcnt(12) lgkmcnt(0)\n"
    "s_sub_u32 s85, s92, s89\n"
    "ds_bpermute_b32 v18, v18, v3\n"
    "v_readlane_b32 s81, v3, s86\n"
    "v_mfma_f32_16x16x32_bf16 v[24:27], a[68:71], a[96:99], v[24:27]\n"
    "s_add_i32 s63, s63, s85\n"
    "s_max_i32 s63, s63, s62\n"
    "s_add_u32 s89, s89, s63\n"
    "v_mfma_f32_16x16x32_bf16 v[24:27], a[44:47], a[92:95], v[24:27]\n"
    "v_mfma_f32_16x16x32_bf16 v[24:27], a[20:23], a[88:91], v[24:27]\n"
    "s_branch 52f\n"
    ".p2align 6\n"
    "51:\n"
    "v_mfma_f32_16x16x32_bf16 v[20:23], a[0:3], a[88:91], 0\n"
    "s_cmp_ge_u32 s86, s65\n"
    "v_mfma_f32_16x16x32_bf16 v[20:23], a[24:27], a[92:95], v[20:23]\n"
    "s_cbranch_scc1 45b\n"
    "v_mfma_f32_16x16x32_bf16 v[20:23], a[48:51], a[96:99], v[20:23]\n"
    "v_add_u32 v14, 1, v14\n"
    "v_mfma_f32_16x16x32_bf16 v[20:23], a[72:75], a[100:103], v[20:23]\n"
    "v_cvt_pk_bf16_f32 v54, v24, v25\n"
    "v_mfma_f32_16x16x32_bf16 v[20:23], v[64:67], a[104:107], v[20:23]\n"
    "v_cvt_pk_bf16_f32 v55, v26, v27\n"
    "v_mfma_f32_16x16x32_bf16 v[20:23], v[88:91], a[108:111], v[20:23]\n"
    "ds_write_b64 v10, v[54:55] offset:12296\n"
    "v_mfma_f32_16x16x32_bf16 v[20:23], v[112:115], a[112:115], v[20:23]\n"
    "s_add_u32 s89, s89, s94\n"
    "v_mfma_f32_16x16x32_bf16 v[20:23], v[136:139], a[116:119], v[20:23]\n"
    "s_waitcnt vmcnt(15)\n"
    "v_mfma_f32_16x16x32_bf16 v[20:23], v[160:163], a[120:123], v[20:23]\n"
    "ds_write_b32 v13, v14\n"
    "v_mfma_f32_16x16x32_bf16 v[20:23], v[184:187], a[124:127], v[20:23]\n"
    "v_add_u32 v10, s78, v10\n"
    "v_mfma_f32_16x16x32_bf16 v[20:23], v[208:211], a[128:131], v[20:23]\n"
    "buffer_load_dwordx4 v0, s[36:39], s81 offen nt lds\n"
    "v_mfma_f32_16x16x32_bf16 v[20:23], v[232:235], a[132:135], v[20:23]\n"
    "s_add_u32 s86, s86, 1\n"
    "v_mfma_f32_16x16x32_bf16 v[24:27], v[236:239], a[132:135], 0\n"
    "v_mfma_f32_16x16x32_bf16 v[24:27], v[212:215], a[128:131], v[24:27]\n"
    "buffer_load_dwordx4 v1, s[36:39], s81 offen offset:1024 nt lds\n"
    "v_mfma_f32_16x16x32_bf16 v[24:27], v[188:191], a[124:127], v[24:27]\n"
    "v_mfma_f32_16x16x32_bf16 v[24:27], v[164:167], a[120:123], v[24:27]\n"
    "v_mfma_f32_16x16x32_bf16 v[24:27], v[140:143], a[116:119], v[24:27]\n"
    "buffer_load_dwordx4 v2, s[36:39], s81 offen offset:2048 nt lds\n"
    "46:\n"
    "v_mfma_f32_16x16x32_bf16 v[24:27], v[116:119], a[112:115], v[24:27]\n"
    "ds_read_b32 v15, v12\n"
    "v_mfma_f32_16x16x32_bf16 v[24:27], v[92:95], a[108:111], v[24:27]\n"
    "v_cvt_pk_bf16_f32 v44, v20, v21\n"
    "v_mfma_f32_16x16x32_bf16 v[24:27], v[68:71], a[104:107], v[24:27]\n"
    "v_cvt_pk_bf16_f32 v45, v22, v23\n"
    "v_mfma_f32_16x16x32_bf16 v[24:27], a[76:79], a[100:103], v[24:27]\n"
    "v_readlane_b32 s81, v3, s86\n"
    "v_mfma_f32_16x16x32_bf16 v[24:27], a[52:55], a[96:99], v[24:27]\n"
    "s_waitcnt lgkmcnt(0)\n"
    "v_cmp_lt_u32 vcc, v15, v14\n"
    "v_mfma_f32_16x16x32_bf16 v[24:27], a[28:31], a[92:95], v[24:27]\n"
    "s_cbranch_vccnz 41b\n"
    "42:\n"
    "v_mfma_f32_16x16x32_bf16 v[24:27], a[4:7], a[88:91], v[24:27]\n"
    "ds_read_b128 a[184:187], v11 offset:12288\n"
    "v_mfma_f32_16x16x32_bf16 v[20:23], a[8:11], a[88:91], 0\n"
    "ds_read_b128 a[188:191], v11 offset:13312\n"
    "v_mfma_f32_16x16x32_bf16 v[20:23], a[32:35], a[92:95], v[20:23]\n"
    "ds_read_b128 a[192:195], v11 offset:14336\n"
    "v_mfma_f32_16x16x32_bf16 v[20:23], a[56:59], a[96:99], v[20:23]\n"
    "ds_write_b64 v8, v[44:45]\n"
    "v_mfma_f32_16x16x32_bf16 v[20:23], a[80:83], a[100:103], v[20:23]\n"
    "ds_read_b128 a[136:139], v4 offset:12288\n"
    "v_mfma_f32_16x16x32_bf16 v[20:23], v[72:75], a[104:107], v[20:23]\n"
    "v_cvt_pk_bf16_f32 v46, v24, v25\n"
    "v_mfma_f32_16x16x32_bf16 v[20:23], v[96:99], a[108:111], v[20:23]\n"
    "v_cvt_pk_bf16_f32 v47, v26, v27\n"
    "v_mfma_f32_16x16x32_bf16 v[20:23], v[120:123], a[112:115], v[20:23]\n"
    "ds_read_b128 a[140:143], v5 offset:12288\n"
    "v_mfma_f32_16x16x32_bf16 v[20:23], v[144:147], a[116:119], v[20:23]\n"
    "ds_write_b64 v8, v[46:47] offset:8\n"
    "v_mfma_f32_16x16x32_bf16 v[20:23], v[168:171], a[120:123], v[20:23]\n"
    "v_readlane_b32 s82, v18, s86\n"
    "v_mfma_f32_16x16x32_bf16 v[20:23], v[192:195], a[124:127], v[20:23]\n"
    "ds_read_b128 a[144:147], v6 offset:12288\n"
    "v_mfma_f32_16x16x32_bf16 v[20:23], v[216:219], a[128:131], v[20:23]\n"
    "s_waitcnt lgkmcnt(5)\n"
    "v_mfma_f32_16x16x32_bf16 v[20:23], v[240:243], a[132:135], v[20:23]\n"
    "buffer_store_dwordx4 a[184:187], v0, s[40:43], s82 offen sc1\n"
    "v_mfma_f32_16x16x32_bf16 v[24:27], v[244:247], a[132:135], 0\n"
    "ds_read_b128 a[148:151], v7 offset:12288\n"
    "v_mfma_f32_16x16x32_bf16 v[24:27], v[220:223], a[128:131], v[24:27]\n"
    "s_addk_i32 m0, 12288\n"
    "v_mfma_f32_16x16x32_bf16 v[24:27], v[196:199], a[124:127], v[24:27]\n"
    "v_add_u32 v11, s78, v11\n"
    "v_mfma_f32_16x16x32_bf16 v[24:27], v[172:175], a[120:123], v[24:27]\n"
    "ds_read_b128 a[152:155], v4 offset:12544\n"
    "v_mfma_f32_16x16x32_bf16 v[24:27], v[148:151], a[116:119], v[24:27]\n"
    "v_cvt_pk_bf16_f32 v48, v20, v21\n"
    "v_mfma_f32_16x16x32_bf16 v[24:27], v[124:127], a[112:115], v[24:27]\n"
    "v_cvt_pk_bf16_f32 v49, v22, v23\n"
    "v_mfma_f32_16x16x32_bf16 v[24:27], v[100:103], a[108:111], v[24:27]\n"
    "ds_read_b128 a[156:159], v5 offset:12544\n"
    "v_mfma_f32_16x16x32_bf16 v[24:27], v[76:79], a[104:107], v[24:27]\n"
    "ds_write_b64 v9, v[48:49]\n"
    "v_mfma_f32_16x16x32_bf16 v[24:27], a[84:87], a[100:103], v[24:27]\n"
    "buffer_store_dwordx4 a[188:191], v1, s[40:43], s82 offen offset:1024 sc1\n"
    "v_mfma_f32_16x16x32_bf16 v[24:27], a[60:63], a[96:99], v[24:27]\n"
    "ds_read_b128 a[160:163], v6 offset:12544\n"
    "v_mfma_f32_16x16x32_bf16 v[24:27], a[36:39], a[92:95], v[24:27]\n"
    "202:\n"
    "s_cbranch_execz 199b\n"
    "v_mfma_f32_16x16x32_bf16 v[24:27], a[12:15], a[88:91], v[24:27]\n"
    "ds_read_b128 a[164:167], v7 offset:12544\n"
    "v_mfma_f32_16x16x32_bf16 v[20:23], a[16:19], a[88:91], 0\n"
    "v_mfma_f32_16x16x32_bf16 v[20:23], a[40:43], a[92:95], v[20:23]\n"
    "ds_read_b128 a[168:171], v4 offset:12800\n"
    "v_mfma_f32_16x16x32_bf16 v[20:23], a[64:67], a[96:99], v[20:23]\n"
    "v_mfma_f32_16x16x32_bf16 v[20:23], v[56:59], a[100:103], v[20:23]\n"
    "ds_read_b128 a[172:175], v5 offset:12800\n"
    "v_mfma_f32_16x16x32_bf16 v[20:23], v[80:83], a[104:107], v[20:23]\n"
    "v_cvt_pk_bf16_f32 v50, v24, v25\n"
    "v_mfma_f32_16x16x32_bf16 v[20:23], v[104:107], a[108:111], v[20:23]\n"
    "v_cvt_pk_bf16_f32 v51, v26, v27\n"
    "v_mfma_f32_16x16x32_bf16 v[20:23], v[128:131], a[112:115], v[20:23]\n"
    "ds_read_b128 a[176:179], v6 offset:12800\n"
    "v_mfma_f32_16x16x32_bf16 v[20:23], v[152:155], a[116:119], v[20:23]\n"
    "ds_write_b64 v9, v[50:51] offset:8\n"
    "v_mfma_f32_16x16x32_bf16 v[20:23], v[176:179], a[120:123], v[20:23]\n"
    "buffer_store_dwordx4 a[192:195], v2, s[40:43], s82 offen offset:2048 sc1\n"
    "v_mfma_f32_16x16x32_bf16 v[20:23], v[200:203], a[124:127], v[20:23]\n"
    "ds_read_b128 a[180:183], v7 offset:12800\n"
    "v_mfma_f32_16x16x32_bf16 v[20:23], v[224:227], a[128:131], v[20:23]\n"
    "s_memrealtime s[92:93]\n"
    "v_mfma_f32_16x16x32_bf16 v[20:23], v[248:251], a[132:135], v[20:23]\n"
    "s_cmp_eq_u32 m0, s77\n"
    "v_mfma_f32_16x16x32_bf16 v[24:27], v[252:255], a[132:135], 0\n"
    "s_cselect_b32 s78, s87, s88\n"
    "v_mfma_f32_16x16x32_bf16 v[24:27], v[228:231], a[128:131], v[24:27]\n"
    "v_add_u32 v4, s78, v4\n"
    "v_mfma_f32_16x16x32_bf16 v[24:27], v[204:207], a[124:127], v[24:27]\n"
    "v_add_u32 v5, s78, v5\n"
    "v_mfma_f32_16x16x32_bf16 v[24:27], v[180:183], a[120:123], v[24:27]\n"
    "v_add_u32 v6, s78, v6\n"
    "v_mfma_f32_16x16x32_bf16 v[24:27], v[156:159], a[116:119], v[24:27]\n"
    "s_waitcnt lgkmcnt(0)\n"
    "s_sub_u32 s85, s92, s89\n"
    "v_mfma_f32_16x16x32_bf16 v[24:27], v[132:135], a[112:115], v[24:27]\n"
    "s_cbranch_scc1 43b\n"
    "44:\n"
    "v_mfma_f32_16x16x32_bf16 v[24:27], v[108:111], a[108:111], v[24:27]\n"
    "v_cvt_pk_bf16_f32 v52, v20, v21\n"
    "v_mfma_f32_16x16x32_bf16 v[24:27], v[84:87], a[104:107], v[24:27]\n"
    "v_cvt_pk_bf16_f32 v53, v22, v23\n"
    "v_mfma_f32_16x16x32_bf16 v[24:27], v[60:63], a[100:103], v[24:27]\n"
    "ds_write_b64 v10, v[52:53]\n"
    "v_mfma_f32_16x16x32_bf16 v[24:27], a[68:71], a[96:99], v[24:27]\n"
    "v_add_u32 v7, s78, v7\n"
    "v_mfma_f32_16x16x32_bf16 v[24:27], a[44:47], a[92:95], v[24:27]\n"
    "s_cmp_lt_u32 s86, s76\n"
    "v_mfma_f32_16x16x32_bf16 v[24:27], a[20:23], a[88:91], v[24:27]\n"
    "s_cbranch_scc0 55f\n"
    "52:\n"
    "v_mfma_f32_16x16x32_bf16 v[20:23], a[0:3], a[136:139], 0\n"
    "s_cmp_ge_u32 s86, s65\n"
    "v_mfma_f32_16x16x32_bf16 v[20:23], a[24:27], a[140:143], v[20:23]\n"
    "s_cbranch_scc1 65b\n"
    "v_mfma_f32_16x16x32_bf16 v[20:23], a[48:51], a[144:147], v[20:23]\n"
    "v_add_u32 v14, 1, v14\n"
    "v_mfma_f32_16x16x32_bf16 v[20:23], a[72:75], a[148:151], v[20:23]\n"
    "v_cvt_pk_bf16_f32 v54, v24, v25\n"
    "v_mfma_f32_16x16x32_bf16 v[20:23], v[64:67], a[152:155], v[20:23]\n"
    "v_cvt_pk_bf16_f32 v55, v26, v27\n"
    "v_mfma_f32_16x16x32_bf16 v[20:23], v[88:91], a[156:159], v[20:23]\n"
    "ds_write_b64 v10, v[54:55] offset:8\n"
    "v_mfma_f32_16x16x32_bf16 v[20:23], v[112:115], a[160:163], v[20:23]\n"
    "s_add_u32 s89, s89, s94\n"
    "v_mfma_f32_16x16x32_bf16 v[20:23], v[136:139], a[164:167], v[20:23]\n"
    "s_waitcnt vmcnt(15)\n"
    "v_mfma_f32_16x16x32_bf16 v[20:23], v[160:163], a[168:171], v[20:23]\n"
    "ds_write_b32 v13, v14\n"
    "v_mfma_f32_16x16x32_bf16 v[20:23], v[184:187], a[172:175], v[20:23]\n"
    "v_mfma_f32_16x16x32_bf16 v[20:23], v[208:211], a[176:179], v[20:23]\n"
    "buffer_load_dwordx4 v0, s[36:39], s81 offen nt lds\n"
    "v_mfma_f32_16x16x32_bf16 v[20:23], v[232:235], a[180:183], v[20:23]\n"
    "s_add_u32 s86, s86, 1\n"
    "v_mfma_f32_16x16x32_bf16 v[24:27], v[236:239], a[180:183], 0\n"
    "203:\n"
    "s_cbranch_execz 199b\n"
    "v_mfma_f32_16x16x32_bf16 v[24:27], v[212:215], a[176:179], v[24:27]\n"
    "buffer_load_dwordx4 v1, s[36:39], s81 offen offset:1024 nt lds\n"
    "v_mfma_f32_16x16x32_bf16 v[24:27], v[188:191], a[172:175], v[24:27]\n"
    "v_mfma_f32_16x16x32_bf16 v[24:27], v[164:167], a[168:171], v[24:27]\n"
    "v_mfma_f32_16x16x32_bf16 v[24:27], v[140:143], a[164:167], v[24:27]\n"
    "buffer_load_dwordx4 v2, s[36:39], s81 offen offset:2048 nt lds\n"
    "66:\n"
    "v_mfma_f32_16x16x32_bf16 v[24:27], v[116:119], a[160:163], v[24:27]\n"
    "ds_read_b32 v15, v12\n"
    "v_mfma_f32_16x16x32_bf16 v[24:27], v[92:95], a[156:159], v[24:27]\n"
    "v_cvt_pk_bf16_f32 v44, v20, v21\n"
    "v_mfma_f32_16x16x32_bf16 v[24:27], v[68:71], a[152:155], v[24:27]\n"
    "v_cvt_pk_bf16_f32 v45, v22, v23\n"
    "v_mfma_f32_16x16x32_bf16 v[24:27], a[76:79], a[148:151], v[24:27]\n"
    "v_readlane_b32 s81, v3, s86\n"
    "v_mfma_f32_16x16x32_bf16 v[24:27], a[52:55], a[144:147], v[24:27]\n"
    "s_waitcnt lgkmcnt(0)\n"
    "v_cmp_lt_u32 vcc, v15, v14\n"
    "v_mfma_f32_16x16x32_bf16 v[24:27], a[28:31], a[140:143], v[24:27]\n"
    "s_cbranch_vccnz 61b\n"
    "62:\n"
    "v_mfma_f32_16x16x32_bf16 v[24:27], a[4:7], a[136:139], v[24:27]\n"
    "ds_read_b128 a[184:187], v11\n"
    "v_mfma_f32_16x16x32_bf16 v[20:23], a[8:11], a[136:139], 0\n"
    "ds_read_b128 a[188:191], v11 offset:1024\n"
    "v_mfma_f32_16x16x32_bf16 v[20:23], a[32:35], a[140:143], v[20:23]\n"
    "ds_read_b128 a[192:195], v11 offset:2048\n"
    "v_mfma_f32_16x16x32_bf16 v[20:23], a[56:59], a[144:147], v[20:23]\n"
    "ds_write_b64 v8, v[44:45] offset:12288\n"
    "v_mfma_f32_16x16x32_bf16 v[20:23], a[80:83], a[148:151], v[20:23]\n"
    "ds_read_b128 a[88:91], v4\n"
    "v_mfma_f32_16x16x32_bf16 v[20:23], v[72:75], a[152:155], v[20:23]\n"
    "v_cvt_pk_bf16_f32 v46, v24, v25\n"
    "v_mfma_f32_16x16x32_bf16 v[20:23], v[96:99], a[156:159], v[20:23]\n"
    "v_cvt_pk_bf16_f32 v47, v26, v27\n"
    "v_mfma_f32_16x16x32_bf16 v[20:23], v[120:123], a[160:163], v[20:23]\n"
    "ds_read_b128 a[92:95], v5\n"
    "v_mfma_f32_16x16x32_bf16 v[20:23], v[144:147], a[164:167], v[20:23]\n"
    "ds_write_b64 v8, v[46:47] offset:12296\n"
    "v_mfma_f32_16x16x32_bf16 v[20:23], v[168:171], a[168:171], v[20:23]\n"
    "v_readlane_b32 s82, v18, s86\n"
    "v_mfma_f32_16x16x32_bf16 v[20:23], v[192:195], a[172:175], v[20:23]\n"
    "ds_read_b128 a[96:99], v6\n"
    "v_mfma_f32_16x16x32_bf16 v[20:23], v[216:219], a[176:179], v[20:23]\n"
    "s_waitcnt lgkmcnt(5)\n"
    "v_mfma_f32_16x16x32_bf16 v[20:23], v[240:243], a[180:183], v[20:23]\n"
    "buffer_store_dwordx4 a[184:187], v0, s[40:43], s82 offen sc1\n"
    "v_mfma_f32_16x16x32_bf16 v[24:27], v[244:247], a[180:183], 0\n"
    "ds_read_b128 a[100:103], v7\n"
    "v_mfma_f32_16x16x32_bf16 v[24:27], v[220:223], a[176:179], v[24:27]\n"
    "s_addk_i32 m0, 12288\n"
    "v_mfma_f32_16x16x32_bf16 v[24:27], v[196:199], a[172:175], v[24:27]\n"
    "v_add_u32 v8, s78, v8\n"
    "v_mfma_f32_16x16x32_bf16 v[24:27], v[172:175], a[168:171], v[24:27]\n"
    "ds_read_b128 a[104:107], v4 offset:256\n"
    "v_mfma_f32_16x16x32_bf16 v[24:27], v[148:151], a[164:167], v[24:27]\n"
    "v_cvt_pk_bf16_f32 v48, v20, v21\n"
    "v_mfma_f32_16x16x32_bf16 v[24:27], v[124:127], a[160:163], v[24:27]\n"
    "v_cvt_pk_bf16_f32 v49, v22, v23\n"
    "v_mfma_f32_16x16x32_bf16 v[24:27], v[100:103], a[156:159], v[24:27]\n"
    "ds_read_b128 a[108:111], v5 offset:256\n"
    "v_mfma_f32_16x16x32_bf16 v[24:27], v[76:79], a[152:155], v[24:27]\n"
    "ds_write_b64 v9, v[48:49] offset:12288\n"
    "v_mfma_f32_16x16x32_bf16 v[24:27], a[84:87], a[148:151], v[24:27]\n"
    "buffer_store_dwordx4 a[188:191], v1, s[40:43], s82 offen offset:1024 sc1\n"
    "v_mfma_f32_16x16x32_bf16 v[24:27], a[60:63], a[144:147], v[24:27]\n"
    "ds_read_b128 a[112:115], v6 offset:256\n"
    "v_mfma_f32_16x16x32_bf16 v[24:27], a[36:39], a[140:143], v[24:27]\n"
    "v_mfma_f32_16x16x32_bf16 v[24:27], a[12:15], a[136:139], v[24:27]\n"
    "ds_read_b128 a[116:119], v7 offset:256\n"
    "v_mfma_f32_16x16x32_bf16 v[20:23], a[16:19], a[136:139], 0\n"
    "v_mfma_f32_16x16x32_bf16 v[20:23], a[40:43], a[140:143], v[20:23]\n"
    "ds_read_b128 a[120:123], v4 offset:512\n"
    "v_mfma_f32_16x16x32_bf16 v[20:23], a[64:67], a[144:147], v[20:23]\n"
    "v_mfma_f32_16x16x32_bf16 v[20:23], v[56:59], a[148:151], v[20:23]\n"
    "ds_read_b128 a[124:127], v5 offset:512\n"
    "v_mfma_f32_16x16x32_bf16 v[20:23], v[80:83], a[152:155], v[20:23]\n"
    "v_cvt_pk_bf16_f32 v50, v24, v25\n"
    "v_mfma_f32_16x16x32_bf16 v[20:23], v[104:107], a[156:159], v[20:23]\n"
    "v_cvt_pk_bf16_f32 v51, v26, v27\n"
    "v_mfma_f32_16x16x32_bf16 v[20:23], v[128:131], a[160:163], v[20:23]\n"
    "ds_read_b128 a[128:131], v6 offset:512\n"
    "v_mfma_f32_16x16x32_bf16 v[20:23], v[152:155], a[164:167], v[20:23]\n"
    "ds_write_b64 v9, v[50:51] offset:12296\n"
    "v_mfma_f32_16x16x32_bf16 v[20:23], v[176:179], a[168:171], v[20:23]\n"
    "buffer_store_dwordx4 a[192:195], v2, s[40:43], s82 offen offset:2048 sc1\n"
    "v_mfma_f32_16x16x32_bf16 v[20:23], v[200:203], a[172:175], v[20:23]\n"
    "ds_read_b128 a[132:135], v7 offset:512\n"
    "v_mfma_f32_16x16x32_bf16 v[20:23], v[224:227], a[176:179], v[20:23]\n"
    "s_memrealtime s[92:93]\n"
    "v_mfma_f32_16x16x32_bf16 v[20:23], v[248:251], a[180:183], v[20:23]\n"
    "s_cmp_eq_u32 m0, s80\n"
    "v_mfma_f32_16x16x32_bf16 v[24:27], v[252:255], a[180:183], 0\n"
    "s_cselect_b32 m0, s57, m0\n"
    "v_mfma_f32_16x16x32_bf16 v[24:27], v[228:231], a[176:179], v[24:27]\n"
    "v_add_u32 v9, s78, v9\n"
    "v_mfma_f32_16x16x32_bf16 v[24:27], v[204:207], a[172:175], v[24:27]\n"
    "s_cmp_eq_u32 s86, 22\n"
    "v_mfma_f32_16x16x32_bf16 v[24:27], v[180:183], a[168:171], v[24:27]\n"
    "s_cselect_b32 s94, s31, s94\n"
    "v_mfma_f32_16x16x32_bf16 v[24:27], v[156:159], a[164:167], v[24:27]\n"
    "s_waitcnt lgkmcnt(0)\n"
    "s_sub_u32 s85, s92, s89\n"
    "v_mfma_f32_16x16x32_bf16 v[24:27], v[132:135], a[160:163], v[24:27]\n"
    "s_cbranch_scc1 63b\n"
    "64:\n"
    "v_mfma_f32_16x16x32_bf16 v[24:27], v[108:111], a[156:159], v[24:27]\n"
    "v_cvt_pk_bf16_f32 v52, v20, v21\n"
    "v_mfma_f32_16x16x32_bf16 v[24:27], v[84:87], a[152:155], v[24:27]\n"
    "v_cvt_pk_bf16_f32 v53, v22, v23\n"
    "v_mfma_f32_16x16x32_bf16 v[24:27], v[60:63], a[148:151], v[24:27]\n"
    "ds_write_b64 v10, v[52:53] offset:12288\n"
    "v_mfma_f32_16x16x32_bf16 v[24:27], a[68:71], a[144:147], v[24:27]\n"
    "v_mfma_f32_16x16x32_bf16 v[24:27], a[44:47], a[140:143], v[24:27]\n"
    "s_cmp_lt_u32 s86, s76\n"
    "v_mfma_f32_16x16x32_bf16 v[24:27], a[20:23], a[136:139], v[24:27]\n"
    "s_cbranch_scc1 51b\n"
    "v_add_u32 v10, 12288, v10\n"
    "v_add_u32 v11, 12288, v11\n"
    "55:\n"
    "s_add_u32 s86, s86, 1\n"
    "v_readlane_b32 s82, v18, s86\n"
    "s_nop 4\n"
    "v_cvt_pk_bf16_f32 v54, v24, v25\n"
    "v_cvt_pk_bf16_f32 v55, v26, v27\n"
    "ds_write_b64 v10, v[54:55] offset:8\n"
    "s_waitcnt lgkmcnt(0)\n"
    "s_barrier\n"
    "ds_read_b128 a[184:187], v11\n"
    "ds_read_b128 a[188:191], v11 offset:1024\n"
    "ds_read_b128 a[192:195], v11 offset:2048\n"
    "s_waitcnt lgkmcnt(2)\n"
    "buffer_store_dwordx4 a[184:187], v0, s[40:43], s82 offen sc1\n"
    "s_waitcnt lgkmcnt(1)\n"
    "buffer_store_dwordx4 a[188:191], v1, s[40:43], s82 offen offset:1024 sc1\n"
    "s_waitcnt lgkmcnt(0)\n"
    "buffer_store_dwordx4 a[192:195], v2, s[40:43], s82 offen offset:2048 sc1\n"
    "s_waitcnt vmcnt(0)\n"
    "s_endpgm\n"
    "75:\n"
    "s_sleep 2\n"
    "global_load_dwordx4 v[236:239], v16, s[54:55] sc1\n"
    "s_waitcnt vmcnt(0)\n"
    "v_and_b32 v242, 15, v237\n"
    "v_and_b32 v243, 1, v239\n"
    "v_or_b32 v237, 15, v237\n"
    "v_or_b32 v239, 15, v239\n"
    "v_cmp_ne_u64 vcc, s[18:19], v[236:237]\n"
    "v_cmp_ne_u64 s[48:49], s[18:19], v[238:239]\n"
    "v_readlane_b32 s9, v242, 0\n"
    "v_and_b32 v236, 1, v242\n"
    "s_or_b64 s[48:49], s[48:49], vcc\n"
    "s_cbranch_scc1 75b\n"
    "s_branch 77b\n"
    "79:\n"
    "s_nop 0\n"
    : "+{s[0:1]}"(dp), "+{s[2:3]}"(ka), "+{s[4:5]}"(did), "+{s6}"(bid), "+{v0}"(tid)
    : [lds] "{s98}"(lds0)
    : "v1","v2","v3","v4","v5","v6","v7","v8","v9","v10","v11","v12","v13","v14","v15","v16","v17","v18","v19","v20","v21","v22","v23","v24","v25","v26","v27","v28","v29","v30","v31","v32","v33","v34","v35","v36","v37","v38","v39","v40","v41","v42","v43","v44","v45","v46","v47","v48","v49","v50","v51","v52","v53","v54","v55","v56","v57","v58","v59","v60","v61","v62","v63","v64","v65","v66","v67","v68","v69","v70","v71","v72","v73","v74","v75","v76","v77","v78","v79","v80","v81","v82","v83","v84","v85","v86","v87","v88","v89","v90","v91","v92","v93","v94","v95","v96","v97","v98","v99","v100","v101","v102","v103","v104","v105","v106","v107","v108","v109","v110","v111","v112","v113","v114","v115","v116","v117","v118","v119","v120","v121","v122","v123","v124","v125","v126","v127","v128","v129","v130","v131","v132","v133","v134","v135","v136","v137","v138","v139","v140","v141","v142","v143","v144","v145","v146","v147","v148","v149","v150","v151","v152","v153","v154","v155","v156","v157","v158","v159","v160","v161","v162","v163","v164","v165","v166","v167","v168","v169","v170","v171","v172","v173","v174","v175","v176","v177","v178","v179","v180","v181","v182","v183","v184","v185","v186","v187","v188","v189","v190","v191","v192","v193","v194","v195","v196","v197","v198","v199","v200","v201","v202","v203","v204","v205","v206","v207","v208","v209","v210","v211","v212","v213","v214","v215","v216","v217","v218","v219","v220","v221","v222","v223","v224","v225","v226","v227","v228","v229","v230","v231","v232","v233","v234","v235","v236","v237","v238","v239","v240","v241","v242","v243","v244","v245","v246","v247","v248","v249","v250","v251","v252","v253","v254","v255","a0","a1","a2","a3","a4","a5","a6","a7","a8","a9","a10","a11","a12","a13","a14","a15","a16","a17","a18","a19","a20","a21","a22","a23","a24","a25","a26","a27","a28","a29","a30","a31","a32","a33","a34","a35","a36","a37","a38","a39","a40","a41","a42","a43","a44","a45","a46","a47","a48","a49","a50","a51","a52","a53","a54","a55","a56","a57","a58","a59","a60","a61","a62","a63","a64","a65","a66","a67","a68","a69","a70","a71","a72","a73","a74","a75","a76","a77","a78","a79","a80","a81","a82","a83","a84","a85","a86","a87","a88","a89","a90","a91","a92","a93","a94","a95","a96","a97","a98","a99","a100","a101","a102","a103","a104","a105","a106","a107","a108","a109","a110","a111","a112","a113","a114","a115","a116","a117","a118","a119","a120","a121","a122","a123","a124","a125","a126","a127","a128","a129","a130","a131","a132","a133","a134","a135","a136","a137","a138","a139","a140","a141","a142","a143","a144","a145","a146","a147","a148","a149","a150","a151","a152","a153","a154","a155","a156","a157","a158","a159","a160","a161","a162","a163","a164","a165","a166","a167","a168","a169","a170","a171","a172","a173","a174","a175","a176","a177","a178","a179","a180","a181","a182","a183","a184","a185","a186","a187","a188","a189","a190","a191","a192","a193","a194","a195","a196","a197","a198","a199","a200","a201","a202","a203","a204","a205","a206","a207","a208","a209","a210","a211","a212","a213","a214","a215","a216","a217","a218","a219","a220","a221","a222","a223","a224","s7","s8","s9","s10","s11","s12","s13","s14","s15","s16","s17","s18","s19","s20","s21","s22","s23","s24","s25","s26","s27","s28","s29","s30","s31","s33","s34","s35","s36","s37","s38","s39","s40","s41","s42","s43","s44","s45","s46","s47","s48","s49","s50","s51","s52","s53","s54","s55","s56","s57","s58","s59","s60","s61","s62","s63","s64","s65","s66","s67","s68","s69","s70","s71","s72","s73","s74","s75","s76","s77","s78","s79","s80","s81","s82","s83","s84","s85","s86","s87","s88","s89","s90","s91","s92","s93","s94","s95","s96","s97","vcc","scc","memory");
}

extern "C" void kernel_launch(void* const* d_in, const int* in_sizes, int n_in,
                              void* d_out, int out_size, void* d_ws, size_t ws_size,
                              hipStream_t stream) {
  Shapes sh;
  for (int j = 0; j < 16; ++j)
    for (int w = 0; w < 4; ++w) {
      const int ty = j >= 11, per = ty ? 10 : 11, n = 16 * (j - 11 * ty);
      auto row = [&](int k) { return ty ? 64 * (k / 10) + 11 + k % 10 : 64 * (k / 22) + 21 * (k / 11 % 2) + k % 11; };
      const int n0 = n + 4 * w, bp = per - n0 % per;
      uint32_t* e = sh.v + 16 * j + 4 * w;
      e[0] = row(n0) * 768 - 512;
      e[1] = (row(n0 + bp) - bp) * 768 - 512;
      e[2] = bp * 768;
      e[3] = (row(n + 8) + 16) * 768 + ty;
    }
  const int grid = 256, maxr = 40;
  const size_t M = (size_t)in_sizes[0] / CH;
  const uint32_t fl = 0x00020000u;
  for (size_t r0 = 0; r0 < M; r0 += (size_t)maxr * grid * TM) {
    const size_t rows = M - r0 < (size_t)maxr * grid * TM ? M - r0 : (size_t)maxr * grid * TM;
    const int nr = (int)((rows + grid * TM - 1) / (grid * TM)), nit = nr < 2 ? 2 : nr;
    const uint32_t nbytes = (uint32_t)(rows * CH * 2);
    const uint32_t np = (nbytes >> 21) & ~1u;
    const uint64_t pages = (np >= 40 ? (uint32_t)(~0u << (((np - 3) & ~7u) - 32)) : 0u) | (uint64_t)(np >= 2 ? (np - 2) << 21 : 0u) << 32;
    dwsep_kernel<<<grid, 256, 0, stream>>>((const char*)d_in[0] + r0 * CH * 2, nbytes, fl, (char*)d_out + r0 * CH * 2, nbytes, fl,
                                           (const char*)d_in[2], CH * CH * 2, fl, (const char*)d_in[1], CH * 2, fl,
                                           (uint32_t)nit, (uint32_t)grid * TILE_BYTES, d_ws, pages, sh);
  }
}
```

```cpp
#include <hip/hip_runtime.h>
#include <stdint.h>

#define CH 384
#define TM 16
#define TILE_BYTES (TM * CH * 2)
#define LDS_BYTES (13 * TILE_BYTES + 1024 + 64)

struct Shapes { uint32_t v[256], rows[256]; };
extern "C" __device__ uint64_t dispatch_id_() __asm("llvm.amdgcn.dispatch.id");

__global__ __launch_bounds__(256, 1)
void dwsep_kernel(const char* x, uint32_t xbytes, uint32_t f0, char* out, uint32_t obytes, uint32_t f1,
                  const char* pw, uint32_t pwbytes, uint32_t f2, const char* dw, uint32_t dwbytes, uint32_t f3,
                  uint32_t nit, uint32_t tstride, void* ws, uint64_t pages, Shapes shapes) {
  __shared__ __attribute__((aligned(1024))) char lds[LDS_BYTES];
  uint32_t lds0 = (uint32_t)(uintptr_t)(__attribute__((address_space(3))) char*)lds;
  uint64_t ka = (uint64_t)(uintptr_t)__builtin_amdgcn_kernarg_segment_ptr();
  uint64_t did = dispatch_id_(), dp = (uint64_t)(uintptr_t)__builtin_amdgcn_dispatch_ptr();
  uint32_t bid = blockIdx.x, tid = threadIdx.x;
  asm volatile(
    "s_load_dwordx16 s[36:51], s[2:3], 0x0\n"
    "v_readfirstlane_b32 s8, v0\n"
    "s_load_dwordx4 s[52:55], s[2:3], 0x40\n"
    "s_bfe_u32 s7, s6, 0x40003\n"
    "s_lshr_b32 s9, s8, 2\n"
    "s_lshl_b32 s7, s7, 6\n"
    "s_add_u32 s9, s7, s9\n"
    "s_load_dwordx4 s[20:23], s[2:3], s9 offset:0x58\n"
    "s_load_dwordx2 s[96:97], s[2:3], 0x50\n"
    "s_getpc_b64 s[14:15]\n"
    "70:\n"
    "v_lshlrev_b32 v48, 6, v0\n"
    "s_mov_b32 s13, 79f-70b\n"
    "v_min_u32 v48, s13, v48\n"
    "global_load_dword v49, v48, s[14:15]\n"
    "v_and_b32 v18, 48, v0\n"
    "v_bfe_u32 v55, v0, 3, 1\n"
    "v_bfe_u32 v54, v0, 6, 1\n"
    "v_lshl_add_u32 v53, v55, 2, v18\n"
    "s_mov_b32 s16, s4\n"
    "v_add_u32 v53, s7, v53\n"
    "v_lshl_add_u32 v54, v54, 3, v53\n"
    "global_load_dword v8, v53, s[2:3] offset:1112\n"
    "global_load_dword v9, v53, s[2:3] offset:1120\n"
    "global_load_dword v10, v54, s[2:3] offset:1112\n"
    "s_mov_b32 s2, s6\n"
    "s_mov_b32 s3, %[lds]\n"
    "s_lshr_b32 s4, s8, 6\n"
    "v_and_b32 v53, 24, v0\n"
    "v_bfe_u32 v54, v0, 5, 1\n"
    "s_bfe_u32 s5, s2, 0x20003\n"
    "v_add_u32 v53, v53, v54\n"
    "s_bfe_u32 s6, s2, 0x20005\n"
    "v_mul_u32_u24 v53, 0x300, v53\n"
    "s_cmp_eq_u32 s6, 3\n"
    "v_and_b32 v55, 7, v0\n"
    "s_cselect_b32 s6, 0, s6\n"
    "v_lshl_add_u32 v53, v55, 4, v53\n"
    "s_lshl_b32 s7, s4, 7\n"
    "v_and_b32 v16, 63, v0\n"
    "s_mul_i32 s58, s5, 24576\n"
    "v_add_u32 v244, s7, v53\n"
    "s_mul_i32 s62, s6, 98304\n"
    "v_lshlrev_b32 v16, 4, v16\n"
    "s_lshl_b32 s7, s5, 6\n"
    "s_lshl_b32 s8, s6, 8\n"
    "v_xor_b32 v53, s7, v16\n"
    "s_add_u32 s12, s3, 159744\n"
    "s_mul_i32 s83, s4, 30720\n"
    "v_add_u32 v53, s8, v53\n"
    "s_mov_b32 m0, s12\n"
    "v_subrev_u32 v55, 0x300, v53\n"
    "s_add_u32 s83, s83, s3\n"
    "v_min_u32 v250, v53, v55\n"
    "v_add_u32 v245, 0x200, v244\n"
    "v_add_u32 v246, 0x400, v244\n"
    "v_add_u32 v247, 0x600, v244\n"
    "v_and_b32 v235, 12, v0\n"
    "s_mul_i32 s57, s4, 3072\n"
    "v_bfe_u32 v52, v0, 4, 2\n"
    "s_lshl_b32 s7, s4, 6\n"
    "v_lshl_add_u32 v53, v235, 1, v52\n"
    "v_mul_u32_u24 v53, 0x300, v53\n"
    "s_add_u32 s57, s57, s3\n"
    "v_and_b32 v54, 3, v0\n"
    "s_add_u32 s7, s7, 512\n"
    "v_lshl_add_u32 v53, v54, 4, v53\n"
    "v_lshlrev_b32 v12, 2, v54\n"
    "v_add_u32 v248, s7, v53\n"
    "v_add_u32 v249, 0x800, v248\n"
    "s_lshr_b64 s[18:19], s[0:1], 12\n"
    "s_lshl4_add_u32 s18, s16, s18\n"
    "s_andn2_b32 s17, s18, 15\n"
    "s_bitset1_b32 s17, 31\n"
    "s_mov_b32 s72, s17\n"
    "s_mov_b32 s18, s16\n"
    "s_or_b32 s19, s17, 15\n"
    "s_lshl_b32 s34, s2, 20\n"
    "s_and_b32 s34, s34, 0x7e00000\n"
    "s_mul_i32 s7, s16, 0x9e3779b1\n"
    "s_lshr_b32 s7, s7, 9\n"
    "s_and_b32 s7, s7, 0x1f8f80\n"
    "s_or_b32 s34, s34, s7\n"
    "s_and_b32 s35, s2, 7\n"
    "s_lshl_b32 s7, s4, 1\n"
    "s_sub_u32 s35, s35, s7\n"
    "v_and_b32 v19, 15, v0\n"
    "v_add_u32 v18, s12, v18\n"
    "v_and_b32 v54, 14, v0\n"
    "v_lshrrev_b32 v53, 1, v235\n"
    "v_add_u32 v235, v18, v54\n"
    "v_add_u32 v18, v18, v53\n"
    "s_mov_b32 s9, 0x51403020\n"
    "s_mov_b32 s10, 0x52423121\n"
    "s_mov_b32 s11, 0x53433322\n"
    "s_cmp_eq_u32 s6, 1\n"
    "s_cselect_b32 s9, 0x61204030, s9\n"
    "s_cselect_b32 s10, 0x62521131, s10\n"
    "s_cselect_b32 s11, 0x33534302, s11\n"
    "s_cmp_eq_u32 s6, 2\n"
    "s_cselect_b32 s9, 0x71302040, s9\n"
    "s_cselect_b32 s10, 0x42622111, s10\n"
    "s_cselect_b32 s11, 0x43335312, s11\n"
    "s_xor_b32 s7, s4, s5\n"
    "v_lshlrev_b32 v54, 3, v52\n"
    "s_lshl_b32 s7, s7, 2\n"
    "v_xor_b32 v55, s7, v19\n"
    "v_bfe_u32 v53, s11, v54, 8\n"
    "v_xor_b32 v2, v53, v55\n"
    "v_bfe_u32 v53, s10, v54, 8\n"
    "v_xor_b32 v1, v53, v55\n"
    "v_bfe_u32 v53, s9, v54, 8\n"
    "v_xor_b32 v0, v53, v55\n"
    "s_and_b32 s28, s2, 3\n"
    "s_lshr_b32 s8, s2, 7\n"
    "s_lshl2_add_u32 s7, s8, s28\n"
    "s_waitcnt lgkmcnt(0)\n"
    "s_barrier\n"
    "buffer_load_dwordx4 v250, s[48:51], 0 offen lds\n"
    "s_cmp_eq_u32 s35, 0\n"
    "s_cbranch_scc0 72f\n"
    "s_bitcmp1_b32 s2, 7\n"
    "s_cselect_b64 s[68:69], s[40:41], s[36:37]\n"
    "s_mov_b64 s[70:71], s[38:39]\n"
    "s_and_b32 s85, s34, 0x3fffff\n"
    "s_add_u32 s85, s85, s97\n"
    "s_min_u32 s34, s34, s85\n"
    "s_buffer_load_dword s73, s[68:71], s34\n"
    "s_buffer_load_dword s72, s[68:71], s34 offset:0x2000\n"
    "72:\n"
    "s_add_u32 m0, s83, 36864\n"
    "s_add_u32 s84, s58, s62\n"
    "s_mul_i32 s56, s7, 393216\n"
    "s_lshr_b32 s29, s2, 2\n"
    "s_mov_b32 s30, s57\n"
    "buffer_load_dwordx4 v244, s[44:47], s84 offen lds\n"
    "s_xor_b32 s7, s5, 1\n"
    "s_mul_i32 s59, s7, 24576\n"
    "s_xor_b32 s7, s5, 2\n"
    "buffer_load_dwordx4 v245, s[44:47], s84 offen offset:1024 lds\n"
    "s_mul_i32 s60, s7, 24576\n"
    "s_xor_b32 s7, s5, 3\n"
    "s_mul_i32 s61, s7, 24576\n"
    "buffer_load_dwordx4 v246, s[44:47], s84 offen offset:2048 lds\n"
    "v_mov_b32 v53, s20\n"
    "v_mov_b32 v54, s21\n"
    "v_cmp_le_i32 vcc, s22, v16\n"
    "s_sub_u32 s22, s22, 0x400\n"
    "s_mov_b32 s33, 0x90000000\n"
    "v_cndmask_b32 v55, v53, v54, vcc\n"
    "buffer_load_dwordx4 v247, s[44:47], s84 offen offset:3072 lds\n"
    "s_add_u32 m0, s83, 40960\n"
    "v_lshl_add_u32 v0, v0, 4, v55\n"
    "v_cmp_le_i32 vcc, s22, v16\n"
    "s_sub_u32 s22, s22, 0x400\n"
    "s_nop 0\n"
    "v_cndmask_b32 v55, v53, v54, vcc\n"
    "v_lshl_add_u32 v1, v1, 4, v55\n"
    "buffer_load_dwordx4 v248, s[44:47], s84 offen lds\n"
    "v_cmp_le_i32 vcc, s22, v16\n"
    "s_sub_u32 s22, s22, 0x400\n"
    "s_nop 0\n"
    "v_cndmask_b32 v55, v53, v54, vcc\n"
    "v_lshl_add_u32 v2, v2, 4, v55\n"
    "s_add_u32 s7, s83, 36864\n"
    "buffer_load_dwordx4 v249, s[44:47], s84 offen offset:1024 lds\n"
    "s_add_u32 m0, s83, 43008\n"
    "s_add_u32 s84, s59, s62\n"
    "v_and_b32 v240, 0x70, v16\n"
    "v_and_b32 v53, 8, v19\n"
    "v_lshl_add_u32 v53, v52, 7, v53\n"
    "buffer_load_dwordx4 v244, s[44:47], s84 offen lds\n"
    "v_lshrrev_b32 v54, 2, v16\n"
    "v_add3_u32 v15, s7, v53, v240\n"
    "v_add_u32 v38, s7, v54\n"
    "buffer_load_dwordx4 v245, s[44:47], s84 offen offset:1024 lds\n"
    "s_cmp_eq_u32 s35, 0\n"
    "s_cbranch_scc0 73f\n"
    "s_getreg_b32 s88, hwreg(HW_REG_XCC_ID, 0, 3)\n"
    "s_lshl_b32 s89, s2, 3\n"
    "s_and_b32 s89, s89, 0x3f0\n"
    "s_lshr_b32 s82, s2, 7\n"
    "s_lshl3_add_u32 s89, s82, s89\n"
    "v_mov_b32 v20, s89\n"
    "v_mov_b32 v22, s16\n"
    "s_lshr_b32 s89, s88, 2\n"
    "s_lshl1_add_u32 s88, s88, s89\n"
    "s_or_b32 s88, s88, s17\n"
    "s_bfe_u32 s89, s2, 0x10001\n"
    "s_xor_b32 s88, s88, s89\n"
    "s_waitcnt lgkmcnt(1)\n"
    "s_nop 3\n"
    "s_cmp_lg_u32 s72, s17\n"
    "s_cselect_b32 s85, 1, 0\n"
    "s_xor_b32 s88, s88, s85\n"
    "v_mov_b32 v23, s88\n"
    "73:\n"
    "s_lshr_b32 s8, s4, 1\n"
    "s_lshl_b32 s8, s8, 11\n"
    "s_add_u32 s8, s8, s3\n"
    "buffer_load_dwordx4 v246, s[44:47], s84 offen offset:2048 lds\n"
    "s_add_u32 s8, s8, 147456\n"
    "s_and_b32 s13, s4, 1\n"
    "s_lshl_b32 s14, s13, 8\n"
    "buffer_load_dwordx4 v247, s[44:47], s84 offen offset:3072 lds\n"
    "s_add_u32 m0, s83, 47104\n"
    "s_sub_u32 s7, s14, s7\n"
    "s_add_u32 s11, s7, s8\n"
    "s_lshl2_add_u32 s8, s14, s8\n"
    "buffer_load_dwordx4 v248, s[44:47], s84 offen lds\n"
    "v_and_b32 v53, 0x30, v16\n"
    "v_lshrrev_b32 v54, 2, v19\n"
    "v_lshl_add_u32 v53, v54, 8, v53\n"
    "buffer_load_dwordx4 v249, s[44:47], s84 offen offset:1024 lds\n"
    "v_lshl_add_u32 v53, v52, 6, v53\n"
    "v_add_u32 v39, s8, v53\n"
    "v_mov_b32 v251, 0\n"
    "v_mov_b32 v253, 0\n"
    "v_mov_b32 v255, 0\n"
    "s_add_u32 s63, s62, 98304\n"
    "s_cmp_eq_u32 s63, 294912\n"
    "s_cselect_b32 s63, 0, s63\n"
    "s_add_u32 s64, s63, 98304\n"
    "s_cmp_eq_u32 s64, 294912\n"
    "s_cselect_b32 s64, 0, s64\n"
    "s_add_u32 s7, s3, 160768\n"
    "v_mov_b32 v14, 0\n"
    "v_add_u32 v12, s7, v12\n"
    "s_lshl2_add_u32 s7, s4, s7\n"
    "v_mov_b32 v13, s7\n"
    "v_xor_b32 v53, v52, v19\n"
    "v_mul_u32_u24 v54, 0x300, v19\n"
    "v_lshl_add_u32 v54, v53, 4, v54\n"
    "v_add_u32 v4, s3, v54\n"
    "v_xor_b32 v5, 64, v4\n"
    "v_xor_b32 v6, 128, v4\n"
    "v_xor_b32 v7, 192, v4\n"
    "s_lshl_b32 s7, s4, 7\n"
    "s_lshr_b32 s8, s4, 1\n"
    "s_lshl_b32 s8, s8, 7\n"
    "s_add_u32 s8, s8, 512\n"
    "v_add_u32 v241, s8, v240\n"
    "v_add_u32 v240, s7, v240\n"
    "v_lshrrev_b32 v19, 4, v16\n"
    "s_mul_i32 s7, s52, 5\n"
    "s_lshr_b32 s7, s7, 3\n"
    "s_sub_u32 s8, s52, 1\n"
    "s_bitcmp1_b32 s2, 7\n"
    "s_cselect_b32 s7, s7, 0\n"
    "v_add_u32 v53, s7, v19\n"
    "v_cmp_gt_u32 vcc, s8, v19\n"
    "v_subrev_u32 v54, s8, v53\n"
    "v_min_u32 v53, v53, v54\n"
    "v_cndmask_b32 v53, v19, v53, vcc\n"
    "v_mul_lo_u32 v3, v53, s53\n"
    "v_add_u32 v3, s56, v3\n"
    "s_xor_b32 s29, s29, s23\n"
    "s_mul_i32 s7, s52, s53\n"
    "s_add_u32 s7, s7, s56\n"
    "s_mov_b32 s8, -2752512\n"
    "s_bitcmp1_b32 s2, 0\n"
    "s_cselect_b32 s8, -3538944, s8\n"
    "s_add_u32 s7, s7, s8\n"
    "v_cmp_eq_u32 vcc, s52, v19\n"
    "v_mov_b32 v53, s7\n"
    "s_nop 0\n"
    "v_cndmask_b32 v3, v3, v53, vcc\n"
    "v_add_u32 v53, s23, v3\n"
    "v_lshrrev_b32 v53, 21, v53\n"
    "v_and_b32 v232, 56, v53\n"
    "v_and_b32 v233, 4, v53\n"
    "v_xor_b32 v234, s29, v53\n"
    "s_cmp_eq_u32 s35, 0\n"
    "s_cbranch_scc0 71f\n"
    "s_mov_b64 exec, 1\n"
    "global_store_dwordx2 v20, v[22:23], s[54:55] sc1\n"
    "s_mov_b64 exec, -1\n"
    "s_waitcnt lgkmcnt(0)\n"
    "71:\n"
    "s_branch 301f\n"
    "81:\n"
    "ds_read_b32 v15, v12\n"
    "s_waitcnt lgkmcnt(0)\n"
    "v_cmp_lt_u32 vcc, v15, v14\n"
    "s_nop 0\n"
    "s_cbranch_vccnz 81b\n"
    "s_branch 82f\n"
    "301:\n"
    "s_branch 302f\n"
    "41:\n"
    "ds_read_b32 v15, v12\n"
    "s_waitcnt lgkmcnt(0)\n"
    "v_cmp_lt_u32 vcc, v15, v14\n"
    "s_nop 0\n"
    "s_cbranch_vccnz 41b\n"
    "s_branch 42f\n"
    "43:\n"
    "s_memrealtime s[92:93]\n"
    "s_waitcnt lgkmcnt(0)\n"
    "s_sub_u32 s84, s92, s89\n"
    "s_cmp_lt_i32 s84, 0\n"
    "s_cbranch_scc0 44f\n"
    "s_sleep 1\n"
    "s_branch 43b\n"
    "302:\n"
    "s_branch 303f\n"
    "61:\n"
    "ds_read_b32 v15, v12\n"
    "s_waitcnt lgkmcnt(0)\n"
    "v_cmp_lt_u32 vcc, v15, v14\n"
    "s_nop 0\n"
    "s_cbranch_vccnz 61b\n"
    "s_branch 62f\n"
    "63:\n"
    "s_memrealtime s[92:93]\n"
    "s_waitcnt lgkmcnt(0)\n"
    "s_sub_u32 s84, s92, s89\n"
    "s_cmp_lt_i32 s84, 0\n"
    "s_cbranch_scc0 64f\n"
    "s_sleep 1\n"
    "s_branch 63b\n"
    "303:\n"
    "s_branch 304f\n"
    "45:\n"
    "s_sub_u32 s7, s86, s71\n"
    "s_cmp_eq_u32 s7, 2\n"
    "s_cbranch_scc1 402f\n"
    "s_cmp_eq_u32 s7, 4\n"
    "s_cbranch_scc1 404f\n"
    "v_mfma_f32_16x16x32_bf16 v[24:27], a[96:99], a[68:71], v[24:27]\n"
    "s_add_u32 s7, s70, 0\n"
    "s_add_u32 s64, s64, 2\n"
    "ds_read_b128 a[136:139], v4 offset:12288\n"
    "v_mfma_f32_16x16x32_bf16 v[24:27], a[92:95], a[44:47], v[24:27]\n"
    "v_add_u32 v14, 1, v14\n"
    "v_accvgpr_write_b32 a188, v44\n"
    "v_mfma_f32_16x16x32_bf16 v[24:27], a[88:91], a[20:23], v[24:27]\n"
    "s_mov_b64 vcc, s[12:13]\n"
    "v_accvgpr_write_b32 a189, v45\n"
    "v_mfma_f32_16x16x32_bf16 v[28:31], a[88:91], a[0:3], 0\n"
    "buffer_load_dwordx4 v0, s[36:39], s81 offen nt lds\n"
    "v_mfma_f32_16x16x32_bf16 v[28:31], a[92:95], a[24:27], v[28:31]\n"
    "s_waitcnt lgkmcnt(1)\n"
    "ds_read_b128 a[140:143], v5 offset:12288\n"
    "v_mfma_f32_16x16x32_bf16 v[28:31], a[96:99], a[48:51], v[28:31]\n"
    "ds_read_b128 a[144:147], v6 offset:12288\n"
    "v_accvgpr_write_b32 a190, v46\n"
    "v_mfma_f32_16x16x32_bf16 v[28:31], a[100:103], a[72:75], v[28:31]\n"
    "v_cvt_pk_bf16_f32 v52, v20, v24\n"
    "v_accvgpr_write_b32 a191, v47\n"
    "v_mfma_f32_16x16x32_bf16 v[28:31], a[104:107], v[64:67], v[28:31]\n"
    "v_cvt_pk_bf16_f32 v53, v21, v25\n"
    "v_accvgpr_write_b32 a192, v48\n"
    "v_mfma_f32_16x16x32_bf16 v[28:31], a[108:111], v[88:91], v[28:31]\n"
    "v_cvt_pk_bf16_f32 v54, v22, v26\n"
    "v_accvgpr_write_b32 a193, v49\n"
    "v_mfma_f32_16x16x32_bf16 v[28:31], a[112:115], v[112:115], v[28:31]\n"
    "v_cvt_pk_bf16_f32 v55, v23, v27\n"
    "v_accvgpr_write_b32 a194, v50\n"
    "v_mfma_f32_16x16x32_bf16 v[28:31], a[116:119], v[136:139], v[28:31]\n"
    "ds_read_b128 a[148:151], v7 offset:12288\n"
    "v_accvgpr_write_b32 a195, v51\n"
    "v_mfma_f32_16x16x32_bf16 v[28:31], a[120:123], v[160:163], v[28:31]\n"
    "v_accvgpr_mov_b32 a196, a184\n"
    "v_readlane_b32 s9, v3, s7\n"
    "v_mfma_f32_16x16x32_bf16 v[28:31], a[124:127], v[184:187], v[28:31]\n"
    "buffer_load_dwordx4 v1, s[36:39], s81 offen offset:1024 nt lds\n"
    "v_mfma_f32_16x16x32_bf16 v[28:31], a[128:131], v[208:211], v[28:31]\n"
    "v_accvgpr_mov_b32 a197, a185\n"
    "v_mfma_f32_16x16x32_bf16 v[28:31], a[132:135], v[232:235], v[28:31]\n"
    "ds_read_b128 a[152:155], v4 offset:12544\n"
    "v_mfma_f32_16x16x32_bf16 v[32:35], a[132:135], v[236:239], 0\n"
    "v_accvgpr_mov_b32 a198, a186\n"
    "v_mfma_f32_16x16x32_bf16 v[32:35], a[128:131], v[212:215], v[32:35]\n"
    "ds_write2st64_b32 v38, v52, v53 offset0:0 offset1:2\n"
    "v_mfma_f32_16x16x32_bf16 v[32:35], a[124:127], v[188:191], v[32:35]\n"
    "v_accvgpr_mov_b32 a199, a187\n"
    "s_branch 46f\n"
    "304:\n"
    "s_branch 305f\n"
    "402:\n"
    "v_mfma_f32_16x16x32_bf16 v[24:27], a[96:99], a[68:71], v[24:27]\n"
    "s_add_u32 s64, s64, 2\n"
    "ds_read_b128 a[136:139], v4 offset:12288\n"
    "v_mfma_f32_16x16x32_bf16 v[24:27], a[92:95], a[44:47], v[24:27]\n"
    "v_add_u32 v14, 1, v14\n"
    "v_mfma_f32_16x16x32_bf16 v[24:27], a[88:91], a[20:23], v[24:27]\n"
    "s_mov_b64 vcc, s[12:13]\n"
    "v_mfma_f32_16x16x32_bf16 v[28:31], a[88:91], a[0:3], 0\n"
    "buffer_store_dwordx4 a[188:191], v8, s[40:43], s9 offen sc1\n"
    "v_mfma_f32_16x16x32_bf16 v[28:31], a[92:95], a[24:27], v[28:31]\n"
    "ds_read_b128 a[140:143], v5 offset:12288\n"
    "v_mfma_f32_16x16x32_bf16 v[28:31], a[96:99], a[48:51], v[28:31]\n"
    "ds_read_b128 a[144:147], v6 offset:12288\n"
    "v_mfma_f32_16x16x32_bf16 v[28:31], a[100:103], a[72:75], v[28:31]\n"
    "v_cvt_pk_bf16_f32 v52, v20, v24\n"
    "v_mfma_f32_16x16x32_bf16 v[28:31], a[104:107], v[64:67], v[28:31]\n"
    "v_cvt_pk_bf16_f32 v53, v21, v25\n"
    "v_mfma_f32_16x16x32_bf16 v[28:31], a[108:111], v[88:91], v[28:31]\n"
    "v_cvt_pk_bf16_f32 v54, v22, v26\n"
    "v_mfma_f32_16x16x32_bf16 v[28:31], a[112:115], v[112:115], v[28:31]\n"
    "v_cvt_pk_bf16_f32 v55, v23, v27\n"
    "v_mfma_f32_16x16x32_bf16 v[28:31], a[116:119], v[136:139], v[28:31]\n"
    "ds_read_b128 a[148:151], v7 offset:12288\n"
    "v_mfma_f32_16x16x32_bf16 v[28:31], a[120:123], v[160:163], v[28:31]\n"
    "v_mfma_f32_16x16x32_bf16 v[28:31], a[124:127], v[184:187], v[28:31]\n"
    "buffer_store_dwordx4 a[192:195], v9, s[40:43], s9 offen sc1\n"
    "v_mfma_f32_16x16x32_bf16 v[28:31], a[128:131], v[208:211], v[28:31]\n"
    "v_mfma_f32_16x16x32_bf16 v[28:31], a[132:135], v[232:235], v[28:31]\n"
    "ds_read_b128 a[152:155], v4 offset:12544\n"
    "v_mfma_f32_16x16x32_bf16 v[32:35], a[132:135], v[236:239], 0\n"
    "v_mfma_f32_16x16x32_bf16 v[32:35], a[128:131], v[212:215], v[32:35]\n"
    "ds_write2st64_b32 v38, v52, v53 offset0:0 offset1:2\n"
    "v_mfma_f32_16x16x32_bf16 v[32:35], a[124:127], v[188:191], v[32:35]\n"
    "s_branch 46f\n"
    "305:\n"
    "s_branch 306f\n"
    "404:\n"
    "v_mfma_f32_16x16x32_bf16 v[24:27], a[96:99], a[68:71], v[24:27]\n"
    "s_sub_u32 s89, s89, 36\n"
    "ds_read_b128 a[136:139], v4 offset:12288\n"
    "v_mfma_f32_16x16x32_bf16 v[24:27], a[92:95], a[44:47], v[24:27]\n"
    "v_add_u32 v14, 1, v14\n"
    "v_mfma_f32_16x16x32_bf16 v[24:27], a[88:91], a[20:23], v[24:27]\n"
    "s_mov_b64 vcc, s[12:13]\n"
    "v_mfma_f32_16x16x32_bf16 v[28:31], a[88:91], a[0:3], 0\n"
    "buffer_store_dwordx4 a[204:207], v9, s[40:43], s10 offen sc1\n"
    "v_mfma_f32_16x16x32_bf16 v[28:31], a[92:95], a[24:27], v[28:31]\n"
    "ds_read_b128 a[140:143], v5 offset:12288\n"
    "v_mfma_f32_16x16x32_bf16 v[28:31], a[96:99], a[48:51], v[28:31]\n"
    "ds_read_b128 a[144:147], v6 offset:12288\n"
    "v_mfma_f32_16x16x32_bf16 v[28:31], a[100:103], a[72:75], v[28:31]\n"
    "v_cvt_pk_bf16_f32 v52, v20, v24\n"
    "v_mfma_f32_16x16x32_bf16 v[28:31], a[104:107], v[64:67], v[28:31]\n"
    "v_cvt_pk_bf16_f32 v53, v21, v25\n"
    "v_mfma_f32_16x16x32_bf16 v[28:31], a[108:111], v[88:91], v[28:31]\n"
    "v_cvt_pk_bf16_f32 v54, v22, v26\n"
    "v_mfma_f32_16x16x32_bf16 v[28:31], a[112:115], v[112:115], v[28:31]\n"
    "v_cvt_pk_bf16_f32 v55, v23, v27\n"
    "v_mfma_f32_16x16x32_bf16 v[28:31], a[116:119], v[136:139], v[28:31]\n"
    "ds_read_b128 a[148:151], v7 offset:12288\n"
    "v_mfma_f32_16x16x32_bf16 v[28:31], a[120:123], v[160:163], v[28:31]\n"
    "v_mfma_f32_16x16x32_bf16 v[28:31], a[124:127], v[184:187], v[28:31]\n"
    "buffer_store_dwordx4 a[208:211], v10, s[40:43], s10 offen sc1\n"
    "v_mfma_f32_16x16x32_bf16 v[28:31], a[128:131], v[208:211], v[28:31]\n"
    "v_mfma_f32_16x16x32_bf16 v[28:31], a[132:135], v[232:235], v[28:31]\n"
    "ds_read_b128 a[152:155], v4 offset:12544\n"
    "v_mfma_f32_16x16x32_bf16 v[32:35], a[132:135], v[236:239], 0\n"
    "v_mfma_f32_16x16x32_bf16 v[32:35], a[128:131], v[212:215], v[32:35]\n"
    "ds_write2st64_b32 v38, v52, v53 offset0:0 offset1:2\n"
    "v_mfma_f32_16x16x32_bf16 v[32:35], a[124:127], v[188:191], v[32:35]\n"
    "s_branch 46f\n"
    "306:\n"
    "s_branch 307f\n"
    "65:\n"
    "s_sub_u32 s7, s86, s71\n"
    "s_cmp_eq_u32 s7, 3\n"
    "s_cbranch_scc1 403f\n"
    "v_mfma_f32_16x16x32_bf16 v[32:35], a[144:147], a[68:71], v[32:35]\n"
    "s_add_u32 s7, s70, 1\n"
    "s_add_u32 s65, s65, 2\n"
    "ds_read_b128 a[88:91], v4\n"
    "v_mfma_f32_16x16x32_bf16 v[32:35], a[140:143], a[44:47], v[32:35]\n"
    "v_add_u32 v7, s78, v7\n"
    "v_accvgpr_write_b32 a200, v44\n"
    "v_mfma_f32_16x16x32_bf16 v[32:35], a[136:139], a[20:23], v[32:35]\n"
    "v_add_u32 v14, 1, v14\n"
    "v_accvgpr_write_b32 a201, v45\n"
    "v_mfma_f32_16x16x32_bf16 v[20:23], a[136:139], a[0:3], 0\n"
    "buffer_load_dwordx4 v0, s[36:39], s81 offen nt lds\n"
    "v_mfma_f32_16x16x32_bf16 v[20:23], a[140:143], a[24:27], v[20:23]\n"
    "s_waitcnt lgkmcnt(1)\n"
    "ds_read_b128 a[92:95], v5\n"
    "v_mfma_f32_16x16x32_bf16 v[20:23], a[144:147], a[48:51], v[20:23]\n"
    "ds_read_b128 a[96:99], v6\n"
    "v_accvgpr_mov_b32 a211, a187\n"
    "v_mfma_f32_16x16x32_bf16 v[20:23], a[148:151], a[72:75], v[20:23]\n"
    "v_cvt_pk_bf16_f32 v52, v28, v32\n"
    "v_accvgpr_write_b32 a202, v46\n"
    "v_mfma_f32_16x16x32_bf16 v[20:23], a[152:155], v[64:67], v[20:23]\n"
    "v_cvt_pk_bf16_f32 v53, v29, v33\n"
    "v_accvgpr_write_b32 a203, v47\n"
    "v_mfma_f32_16x16x32_bf16 v[20:23], a[156:159], v[88:91], v[20:23]\n"
    "v_cvt_pk_bf16_f32 v54, v30, v34\n"
    "v_accvgpr_write_b32 a204, v48\n"
    "v_mfma_f32_16x16x32_bf16 v[20:23], a[160:163], v[112:115], v[20:23]\n"
    "v_cvt_pk_bf16_f32 v55, v31, v35\n"
    "v_accvgpr_write_b32 a205, v49\n"
    "v_mfma_f32_16x16x32_bf16 v[20:23], a[164:167], v[136:139], v[20:23]\n"
    "ds_read_b128 a[100:103], v7\n"
    "v_accvgpr_write_b32 a206, v50\n"
    "v_mfma_f32_16x16x32_bf16 v[20:23], a[168:171], v[160:163], v[20:23]\n"
    "s_mov_b64 vcc, s[12:13]\n"
    "v_accvgpr_write_b32 a207, v51\n"
    "v_mfma_f32_16x16x32_bf16 v[20:23], a[172:175], v[184:187], v[20:23]\n"
    "buffer_load_dwordx4 v1, s[36:39], s81 offen offset:1024 nt lds\n"
    "v_mfma_f32_16x16x32_bf16 v[20:23], a[176:179], v[208:211], v[20:23]\n"
    "v_accvgpr_mov_b32 a208, a184\n"
    "v_readlane_b32 s10, v3, s7\n"
    "v_mfma_f32_16x16x32_bf16 v[20:23], a[180:183], v[232:235], v[20:23]\n"
    "ds_read_b128 a[104:107], v4 offset:256\n"
    "v_mfma_f32_16x16x32_bf16 v[24:27], a[180:183], v[236:239], 0\n"
    "v_accvgpr_mov_b32 a209, a185\n"
    "v_mfma_f32_16x16x32_bf16 v[24:27], a[176:179], v[212:215], v[24:27]\n"
    "ds_write2st64_b32 v38, v52, v53 offset0:16 offset1:18\n"
    "v_mfma_f32_16x16x32_bf16 v[24:27], a[172:175], v[188:191], v[24:27]\n"
    "v_accvgpr_mov_b32 a210, a186\n"
    "s_branch 66f\n"
    "307:\n"
    "s_branch 308f\n"
    "403:\n"
    "v_mfma_f32_16x16x32_bf16 v[32:35], a[144:147], a[68:71], v[32:35]\n"
    "s_sub_u32 s89, s89, 36\n"
    "ds_read_b128 a[88:91], v4\n"
    "v_mfma_f32_16x16x32_bf16 v[32:35], a[140:143], a[44:47], v[32:35]\n"
    "v_add_u32 v7, s78, v7\n"
    "v_mfma_f32_16x16x32_bf16 v[32:35], a[136:139], a[20:23], v[32:35]\n"
    "v_add_u32 v14, 1, v14\n"
    "v_mfma_f32_16x16x32_bf16 v[20:23], a[136:139], a[0:3], 0\n"
    "buffer_store_dwordx4 a[196:199], v10, s[40:43], s9 offen sc1\n"
    "v_mfma_f32_16x16x32_bf16 v[20:23], a[140:143], a[24:27], v[20:23]\n"
    "ds_read_b128 a[92:95], v5\n"
    "v_mfma_f32_16x16x32_bf16 v[20:23], a[144:147], a[48:51], v[20:23]\n"
    "ds_read_b128 a[96:99], v6\n"
    "v_mfma_f32_16x16x32_bf16 v[20:23], a[148:151], a[72:75], v[20:23]\n"
    "v_cvt_pk_bf16_f32 v52, v28, v32\n"
    "v_mfma_f32_16x16x32_bf16 v[20:23], a[152:155], v[64:67], v[20:23]\n"
    "v_cvt_pk_bf16_f32 v53, v29, v33\n"
    "v_mfma_f32_16x16x32_bf16 v[20:23], a[156:159], v[88:91], v[20:23]\n"
    "v_cvt_pk_bf16_f32 v54, v30, v34\n"
    "v_mfma_f32_16x16x32_bf16 v[20:23], a[160:163], v[112:115], v[20:23]\n"
    "v_cvt_pk_bf16_f32 v55, v31, v35\n"
    "v_mfma_f32_16x16x32_bf16 v[20:23], a[164:167], v[136:139], v[20:23]\n"
    "ds_read_b128 a[100:103], v7\n"
    "v_mfma_f32_16x16x32_bf16 v[20:23], a[168:171], v[160:163], v[20:23]\n"
    "s_mov_b64 vcc, s[12:13]\n"
    "v_mfma_f32_16x16x32_bf16 v[20:23], a[172:175], v[184:187], v[20:23]\n"
    "buffer_store_dwordx4 a[200:203], v8, s[40:43], s10 offen sc1\n"
    "v_mfma_f32_16x16x32_bf16 v[20:23], a[176:179], v[208:211], v[20:23]\n"
    "v_mfma_f32_16x16x32_bf16 v[20:23], a[180:183], v[232:235], v[20:23]\n"
    "ds_read_b128 a[104:107], v4 offset:256\n"
    "v_mfma_f32_16x16x32_bf16 v[24:27], a[180:183], v[236:239], 0\n"
    "v_mfma_f32_16x16x32_bf16 v[24:27], a[176:179], v[212:215], v[24:27]\n"
    "ds_write2st64_b32 v38, v52, v53 offset0:16 offset1:18\n"
    "v_mfma_f32_16x16x32_bf16 v[24:27], a[172:175], v[188:191], v[24:27]\n"
    "s_branch 66f\n"
    "308:\n"
    "s_waitcnt vmcnt(12)\n"
    "ds_write_b32 v13, v14\n"
    "ds_read_u16 v250, v18\n"
    "ds_read_u16 v252, v18 offset:8\n"
    "ds_read_u16 v254, v235\n"
    "s_waitcnt vmcnt(11)\n"
    "ds_read2st64_b64 a[188:191], v15 offset0:0 offset1:1\n"
    "s_waitcnt vmcnt(10)\n"
    "ds_read2st64_b64 a[192:195], v15 offset0:2 offset1:3\n"
    "s_waitcnt vmcnt(9)\n"
    "ds_read2st64_b64 a[196:199], v15 offset0:4 offset1:5\n"
    "s_waitcnt lgkmcnt(3)\n"
    "v_lshlrev_b64 v[40:41], v16, v[250:251]\n"
    "v_lshlrev_b64 v[42:43], v16, v[252:253]\n"
    "v_lshlrev_b64 v[44:45], v16, v[254:255]\n"
    "s_waitcnt vmcnt(8) lgkmcnt(2)\n"
    "ds_read2st64_b64 a[200:203], v15 offset0:6 offset1:7\n"
    "s_nop 0\n"
    "v_mfma_f32_4x4x4_16b_bf16 v[20:23], v[40:41], a[188:189], 0 cbsz:2 abid:0\n"
    "ds_read_u16 v250, v18 offset:64\n"
    "s_add_u32 m0, s83, 49152\n"
    "s_add_u32 s84, s60, s62\n"
    "v_mfma_f32_4x4x4_16b_bf16 v[24:27], v[40:41], a[190:191], 0 cbsz:2 abid:1\n"
    "buffer_load_dwordx4 v244, s[44:47], s84 offen lds\n"
    "v_add_u32 v8, v8, v240\n"
    "s_waitcnt vmcnt(8) lgkmcnt(3)\n"
    "ds_read2st64_b32 a[204:205], v38 offset0:16 offset1:17\n"
    "v_mfma_f32_4x4x4_16b_bf16 v[28:31], v[40:41], a[192:193], 0 cbsz:2 abid:2\n"
    "ds_read2st64_b32 a[206:207], v38 offset0:18 offset1:19\n"
    "ds_read_u16 v252, v18 offset:72\n"
    "v_cvt_pk_bf16_f32 v52, v20, v24\n"
    "v_cvt_pk_bf16_f32 v53, v21, v25\n"
    "v_accvgpr_write_b32 a0, v52\n"
    "v_accvgpr_write_b32 a4, v53\n"
    "v_mfma_f32_4x4x4_16b_bf16 v[32:35], v[40:41], a[194:195], 0 cbsz:2 abid:3\n"
    "buffer_load_dwordx4 v245, s[44:47], s84 offen offset:1024 lds\n"
    "v_cvt_pk_bf16_f32 v54, v22, v26\n"
    "v_cvt_pk_bf16_f32 v55, v23, v27\n"
    "v_accvgpr_write_b32 a8, v54\n"
    "v_accvgpr_write_b32 a12, v55\n"
    "v_add_u32 v9, v9, v240\n"
    "s_waitcnt vmcnt(8) lgkmcnt(5)\n"
    "ds_read2st64_b32 a[208:209], v38 offset0:20 offset1:21\n"
    "v_mfma_f32_4x4x4_16b_bf16 v[20:23], v[42:43], a[196:197], 0 cbsz:2 abid:0\n"
    "ds_read2st64_b32 a[210:211], v38 offset0:22 offset1:23\n"
    "ds_read_u16 v254, v235 offset:64\n"
    "v_cvt_pk_bf16_f32 v52, v28, v32\n"
    "v_cvt_pk_bf16_f32 v53, v29, v33\n"
    "v_accvgpr_write_b32 a1, v52\n"
    "v_accvgpr_write_b32 a5, v53\n"
    "v_mfma_f32_4x4x4_16b_bf16 v[24:27], v[42:43], a[198:199], 0 cbsz:2 abid:1\n"
    "buffer_load_dwordx4 v246, s[44:47], s84 offen offset:2048 lds\n"
    "v_cvt_pk_bf16_f32 v54, v30, v34\n"
    "v_cvt_pk_bf16_f32 v55, v31, v35\n"
    "v_accvgpr_write_b32 a9, v54\n"
    "v_accvgpr_write_b32 a13, v55\n"
    "v_add_u32 v10, v10, v241\n"
    "s_waitcnt vmcnt(8) lgkmcnt(6)\n"
    "ds_read2st64_b64 a[188:191], v15 offset0:12 offset1:13\n"
    "v_mfma_f32_4x4x4_16b_bf16 v[28:31], v[42:43], a[200:201], 0 cbsz:2 abid:2\n"
    "v_cvt_pk_bf16_f32 v52, v20, v24\n"
    "v_cvt_pk_bf16_f32 v53, v21, v25\n"
    "v_accvgpr_write_b32 a2, v52\n"
    "v_accvgpr_write_b32 a6, v53\n"
    "v_mfma_f32_4x4x4_16b_bf16 v[32:35], v[42:43], a[202:203], 0 cbsz:2 abid:3\n"
    "buffer_load_dwordx4 v247, s[44:47], s84 offen offset:3072 lds\n"
    "v_cvt_pk_bf16_f32 v54, v22, v26\n"
    "v_cvt_pk_bf16_f32 v55, v23, v27\n"
    "v_accvgpr_write_b32 a10, v54\n"
    "v_accvgpr_write_b32 a14, v55\n"
    "v_lshlrev_b64 v[46:47], v16, v[250:251]\n"
    "s_mov_b32 s12, 0xff00ff\n"
    "s_waitcnt vmcnt(8) lgkmcnt(4)\n"
    "ds_read2st64_b64 a[192:195], v15 offset0:14 offset1:15\n"
    "v_mfma_f32_4x4x4_16b_bf16 v[20:23], v[44:45], a[204:205], 0 cbsz:2 abid:0\n"
    "s_add_u32 m0, s83, 53248\n"
    "v_cvt_pk_bf16_f32 v52, v28, v32\n"
    "v_cvt_pk_bf16_f32 v53, v29, v33\n"
    "v_accvgpr_write_b32 a3, v52\n"
    "v_accvgpr_write_b32 a7, v53\n"
    "v_mfma_f32_4x4x4_16b_bf16 v[24:27], v[44:45], a[206:207], 0 cbsz:2 abid:1\n"
    "buffer_load_dwordx4 v248, s[44:47], s84 offen lds\n"
    "v_cvt_pk_bf16_f32 v54, v30, v34\n"
    "v_cvt_pk_bf16_f32 v55, v31, v35\n"
    "v_accvgpr_write_b32 a11, v54\n"
    "v_accvgpr_write_b32 a15, v55\n"
    "v_lshlrev_b64 v[48:49], v16, v[252:253]\n"
    "s_mov_b32 s13, 0xff00ff\n"
    "s_waitcnt vmcnt(8) lgkmcnt(2)\n"
    "ds_read2st64_b64 a[196:199], v15 offset0:16 offset1:17\n"
    "v_mfma_f32_4x4x4_16b_bf16 v[28:31], v[44:45], a[208:209], 0 cbsz:2 abid:2\n"
    "v_cvt_pk_bf16_f32 v52, v20, v22\n"
    "v_cvt_pk_bf16_f32 v53, v21, v23\n"
    "v_accvgpr_write_b32 a16, v52\n"
    "v_accvgpr_write_b32 a20, v53\n"
    "v_mfma_f32_4x4x4_16b_bf16 v[32:35], v[44:45], a[210:211], 0 cbsz:2 abid:3\n"
    "buffer_load_dwordx4 v249, s[44:47], s84 offen offset:1024 lds\n"
    "v_cvt_pk_bf16_f32 v54, v24, v26\n"
    "v_cvt_pk_bf16_f32 v55, v25, v27\n"
    "v_accvgpr_write_b32 a17, v54\n"
    "v_accvgpr_write_b32 a21, v55\n"
    "v_lshlrev_b64 v[50:51], v16, v[254:255]\n"
    "s_add_u32 s77, s57, 36864\n"
    "s_waitcnt vmcnt(8) lgkmcnt(2)\n"
    "ds_read2st64_b64 a[200:203], v15 offset0:18 offset1:19\n"
    "v_mfma_f32_4x4x4_16b_bf16 v[20:23], v[46:47], a[188:189], 0 cbsz:2 abid:0\n"
    "ds_read_u16 v250, v18 offset:128\n"
    "s_add_u32 m0, s83, 55296\n"
    "s_add_u32 s84, s61, s62\n"
    "v_cvt_pk_bf16_f32 v52, v28, v30\n"
    "v_cvt_pk_bf16_f32 v53, v29, v31\n"
    "v_accvgpr_write_b32 a18, v52\n"
    "v_accvgpr_write_b32 a22, v53\n"
    "v_mfma_f32_4x4x4_16b_bf16 v[24:27], v[46:47], a[190:191], 0 cbsz:2 abid:1\n"
    "buffer_load_dwordx4 v244, s[44:47], s84 offen lds\n"
    "v_cvt_pk_bf16_f32 v54, v32, v34\n"
    "v_cvt_pk_bf16_f32 v55, v33, v35\n"
    "v_accvgpr_write_b32 a19, v54\n"
    "v_accvgpr_write_b32 a23, v55\n"
    "s_movk_i32 s88, 24576\n"
    "s_waitcnt vmcnt(8) lgkmcnt(3)\n"
    "ds_read2st64_b32 a[204:205], v38 offset0:40 offset1:41\n"
    "v_mfma_f32_4x4x4_16b_bf16 v[28:31], v[46:47], a[192:193], 0 cbsz:2 abid:2\n"
    "ds_read2st64_b32 a[206:207], v38 offset0:42 offset1:43\n"
    "ds_read_u16 v252, v18 offset:136\n"
    "v_cvt_pk_bf16_f32 v52, v20, v24\n"
    "v_cvt_pk_bf16_f32 v53, v21, v25\n"
    "v_accvgpr_write_b32 a24, v52\n"
    "v_accvgpr_write_b32 a28, v53\n"
    "v_mfma_f32_4x4x4_16b_bf16 v[32:35], v[46:47], a[194:195], 0 cbsz:2 abid:3\n"
    "buffer_load_dwordx4 v245, s[44:47], s84 offen offset:1024 lds\n"
    "v_cvt_pk_bf16_f32 v54, v22, v26\n"
    "v_cvt_pk_bf16_f32 v55, v23, v27\n"
    "v_accvgpr_write_b32 a32, v54\n"
    "v_accvgpr_write_b32 a36, v55\n"
    "s_movk_i32 s78, 24576\n"
    "s_waitcnt vmcnt(8) lgkmcnt(5)\n"
    "ds_read2st64_b32 a[208:209], v38 offset0:44 offset1:45\n"
    "v_mfma_f32_4x4x4_16b_bf16 v[20:23], v[48:49], a[196:197], 0 cbsz:2 abid:0\n"
    "ds_read2st64_b32 a[210:211], v38 offset0:46 offset1:47\n"
    "ds_read_u16 v254, v235 offset:128\n"
    "v_cvt_pk_bf16_f32 v52, v28, v32\n"
    "v_cvt_pk_bf16_f32 v53, v29, v33\n"
    "v_accvgpr_write_b32 a25, v52\n"
    "v_accvgpr_write_b32 a29, v53\n"
    "v_mfma_f32_4x4x4_16b_bf16 v[24:27], v[48:49], a[198:199], 0 cbsz:2 abid:1\n"
    "buffer_load_dwordx4 v246, s[44:47], s84 offen offset:2048 lds\n"
    "v_cvt_pk_bf16_f32 v54, v30, v34\n"
    "v_cvt_pk_bf16_f32 v55, v31, v35\n"
    "v_accvgpr_write_b32 a33, v54\n"
    "v_accvgpr_write_b32 a37, v55\n"
    "buffer_load_dwordx4 v247, s[44:47], s84 offen offset:3072 lds\n"
    "s_mov_b32 s87, -122880\n"
    "s_waitcnt vmcnt(9) lgkmcnt(6)\n"
    "ds_read2st64_b64 a[188:191], v15 offset0:24 offset1:25\n"
    "v_mfma_f32_4x4x4_16b_bf16 v[28:31], v[48:49], a[200:201], 0 cbsz:2 abid:2\n"
    "s_add_u32 m0, s83, 59392\n"
    "v_cvt_pk_bf16_f32 v52, v20, v24\n"
    "v_cvt_pk_bf16_f32 v53, v21, v25\n"
    "v_accvgpr_write_b32 a26, v52\n"
    "v_accvgpr_write_b32 a30, v53\n"
    "v_mfma_f32_4x4x4_16b_bf16 v[32:35], v[48:49], a[202:203], 0 cbsz:2 abid:3\n"
    "buffer_load_dwordx4 v248, s[44:47], s84 offen lds\n"
    "v_cvt_pk_bf16_f32 v54, v22, v26\n"
    "v_cvt_pk_bf16_f32 v55, v23, v27\n"
    "v_accvgpr_write_b32 a34, v54\n"
    "v_accvgpr_write_b32 a38, v55\n"
    "v_lshlrev_b64 v[40:41], v16, v[250:251]\n"
    "s_add_u32 s80, s57, 147456\n"
    "s_waitcnt vmcnt(9) lgkmcnt(4)\n"
    "ds_read2st64_b64 a[192:195], v15 offset0:26 offset1:27\n"
    "v_mfma_f32_4x4x4_16b_bf16 v[20:23], v[50:51], a[204:205], 0 cbsz:2 abid:0\n"
    "v_cvt_pk_bf16_f32 v52, v28, v32\n"
    "v_cvt_pk_bf16_f32 v53, v29, v33\n"
    "v_accvgpr_write_b32 a27, v52\n"
    "v_accvgpr_write_b32 a31, v53\n"
    "v_mfma_f32_4x4x4_16b_bf16 v[24:27], v[50:51], a[206:207], 0 cbsz:2 abid:1\n"
    "buffer_load_dwordx4 v249, s[44:47], s84 offen offset:1024 lds\n"
    "v_cvt_pk_bf16_f32 v54, v30, v34\n"
    "v_cvt_pk_bf16_f32 v55, v31, v35\n"
    "v_accvgpr_write_b32 a35, v54\n"
    "v_accvgpr_write_b32 a39, v55\n"
    "v_lshlrev_b64 v[42:43], v16, v[252:253]\n"
    "s_mov_b32 s86, 5\n"
    "s_waitcnt vmcnt(9) lgkmcnt(2)\n"
    "ds_read2st64_b64 a[196:199], v15 offset0:28 offset1:29\n"
    "v_mfma_f32_4x4x4_16b_bf16 v[28:31], v[50:51], a[208:209], 0 cbsz:2 abid:2\n"
    "s_add_u32 m0, s83, 61440\n"
    "s_add_u32 s84, s58, s63\n"
    "v_cvt_pk_bf16_f32 v52, v20, v22\n"
    "v_cvt_pk_bf16_f32 v53, v21, v23\n"
    "v_accvgpr_write_b32 a40, v52\n"
    "v_accvgpr_write_b32 a44, v53\n"
    "v_mfma_f32_4x4x4_16b_bf16 v[32:35], v[50:51], a[210:211], 0 cbsz:2 abid:3\n"
    "buffer_load_dwordx4 v244, s[44:47], s84 offen lds\n"
    "v_cvt_pk_bf16_f32 v54, v24, v26\n"
    "v_cvt_pk_bf16_f32 v55, v25, v27\n"
    "v_accvgpr_write_b32 a41, v54\n"
    "v_accvgpr_write_b32 a45, v55\n"
    "v_lshlrev_b64 v[44:45], v16, v[254:255]\n"
    "s_lshr_b32 s7, s2, 1\n"
    "s_waitcnt vmcnt(9) lgkmcnt(2)\n"
    "ds_read2st64_b64 a[200:203], v15 offset0:30 offset1:31\n"
    "v_mfma_f32_4x4x4_16b_bf16 v[20:23], v[40:41], a[188:189], 0 cbsz:2 abid:0\n"
    "ds_read_u16 v250, v18 offset:192\n"
    "v_cvt_pk_bf16_f32 v52, v28, v30\n"
    "v_cvt_pk_bf16_f32 v53, v29, v31\n"
    "v_accvgpr_write_b32 a42, v52\n"
    "v_accvgpr_write_b32 a46, v53\n"
    "v_mfma_f32_4x4x4_16b_bf16 v[24:27], v[40:41], a[190:191], 0 cbsz:2 abid:1\n"
    "buffer_load_dwordx4 v245, s[44:47], s84 offen offset:1024 lds\n"
    "v_cvt_pk_bf16_f32 v54, v32, v34\n"
    "v_cvt_pk_bf16_f32 v55, v33, v35\n"
    "v_accvgpr_write_b32 a43, v54\n"
    "v_accvgpr_write_b32 a47, v55\n"
    "buffer_load_dwordx4 v246, s[44:47], s84 offen offset:2048 lds\n"
    "s_and_b32 s7, s7, 0x7c\n"
    "s_waitcnt vmcnt(10) lgkmcnt(3)\n"
    "ds_read2st64_b32 a[204:205], v38 offset0:64 offset1:65\n"
    "v_mfma_f32_4x4x4_16b_bf16 v[28:31], v[40:41], a[192:193], 0 cbsz:2 abid:2\n"
    "ds_read2st64_b32 a[206:207], v38 offset0:66 offset1:67\n"
    "ds_read_u16 v252, v18 offset:200\n"
    "v_cvt_pk_bf16_f32 v52, v20, v24\n"
    "v_cvt_pk_bf16_f32 v53, v21, v25\n"
    "v_accvgpr_write_b32 a48, v52\n"
    "v_accvgpr_write_b32 a52, v53\n"
    "v_mfma_f32_4x4x4_16b_bf16 v[32:35], v[40:41], a[194:195], 0 cbsz:2 abid:3\n"
    "buffer_load_dwordx4 v247, s[44:47], s84 offen offset:3072 lds\n"
    "v_cvt_pk_bf16_f32 v54, v22, v26\n"
    "v_cvt_pk_bf16_f32 v55, v23, v27\n"
    "v_accvgpr_write_b32 a56, v54\n"
    "v_accvgpr_write_b32 a60, v55\n"
    "s_or_b32 s7, s7, s28\n"
    "s_waitcnt vmcnt(10) lgkmcnt(5)\n"
    "ds_read2st64_b32 a[208:209], v38 offset0:68 offset1:69\n"
    "v_mfma_f32_4x4x4_16b_bf16 v[20:23], v[42:43], a[196:197], 0 cbsz:2 abid:0\n"
    "ds_read2st64_b32 a[210:211], v38 offset0:70 offset1:71\n"
    "ds_read_u16 v254, v235 offset:192\n"
    "s_add_u32 m0, s83, 65536\n"
    "v_cvt_pk_bf16_f32 v52, v28, v32\n"
    "v_cvt_pk_bf16_f32 v53, v29, v33\n"
    "v_accvgpr_write_b32 a49, v52\n"
    "v_accvgpr_write_b32 a53, v53\n"
    "v_mfma_f32_4x4x4_16b_bf16 v[24:27], v[42:43], a[198:199], 0 cbsz:2 abid:1\n"
    "buffer_load_dwordx4 v248, s[44:47], s84 offen lds\n"
    "v_cvt_pk_bf16_f32 v54, v30, v34\n"
    "v_cvt_pk_bf16_f32 v55, v31, v35\n"
    "v_accvgpr_write_b32 a57, v54\n"
    "v_accvgpr_write_b32 a61, v55\n"
    "s_mul_i32 s7, s7, 79\n"
    "s_waitcnt vmcnt(10) lgkmcnt(6)\n"
    "ds_read2st64_b64 a[188:191], v15 offset0:36 offset1:37\n"
    "v_mfma_f32_4x4x4_16b_bf16 v[28:31], v[42:43], a[200:201], 0 cbsz:2 abid:2\n"
    "v_cvt_pk_bf16_f32 v52, v20, v24\n"
    "v_cvt_pk_bf16_f32 v53, v21, v25\n"
    "v_accvgpr_write_b32 a50, v52\n"
    "v_accvgpr_write_b32 a54, v53\n"
    "v_mfma_f32_4x4x4_16b_bf16 v[32:35], v[42:43], a[202:203], 0 cbsz:2 abid:3\n"
    "buffer_load_dwordx4 v249, s[44:47], s84 offen offset:1024 lds\n"
    "v_cvt_pk_bf16_f32 v54, v22, v26\n"
    "v_cvt_pk_bf16_f32 v55, v23, v27\n"
    "v_accvgpr_write_b32 a58, v54\n"
    "v_accvgpr_write_b32 a62, v55\n"
    "v_lshlrev_b64 v[46:47], v16, v[250:251]\n"
    "s_and_b32 s7, s7, 0x7f\n"
    "s_waitcnt vmcnt(10) lgkmcnt(4)\n"
    "ds_read2st64_b64 a[192:195], v15 offset0:38 offset1:39\n"
    "v_mfma_f32_4x4x4_16b_bf16 v[20:23], v[44:45], a[204:205], 0 cbsz:2 abid:0\n"
    "s_add_u32 m0, s83, 36864\n"
    "s_add_u32 s84, s59, s63\n"
    "v_cvt_pk_bf16_f32 v52, v28, v32\n"
    "v_cvt_pk_bf16_f32 v53, v29, v33\n"
    "v_accvgpr_write_b32 a51, v52\n"
    "v_accvgpr_write_b32 a55, v53\n"
    "v_mfma_f32_4x4x4_16b_bf16 v[24:27], v[44:45], a[206:207], 0 cbsz:2 abid:1\n"
    "buffer_load_dwordx4 v244, s[44:47], s84 offen lds\n"
    "v_cvt_pk_bf16_f32 v54, v30, v34\n"
    "v_cvt_pk_bf16_f32 v55, v31, v35\n"
    "v_accvgpr_write_b32 a59, v54\n"
    "v_accvgpr_write_b32 a63, v55\n"
    "buffer_load_dwordx4 v245, s[44:47], s84 offen offset:1024 lds\n"
    "v_lshlrev_b64 v[48:49], v16, v[252:253]\n"
    "s_mul_i32 s7, s7, 60\n"
    "s_waitcnt vmcnt(11) lgkmcnt(2)\n"
    "ds_read2st64_b64 a[196:199], v15 offset0:40 offset1:41\n"
    "v_mfma_f32_4x4x4_16b_bf16 v[28:31], v[44:45], a[208:209], 0 cbsz:2 abid:2\n"
    "v_cvt_pk_bf16_f32 v52, v20, v22\n"
    "v_cvt_pk_bf16_f32 v53, v21, v23\n"
    "v_accvgpr_write_b32 a64, v52\n"
    "v_accvgpr_write_b32 a68, v53\n"
    "v_mfma_f32_4x4x4_16b_bf16 v[32:35], v[44:45], a[210:211], 0 cbsz:2 abid:3\n"
    "buffer_load_dwordx4 v246, s[44:47], s84 offen offset:2048 lds\n"
    "v_cvt_pk_bf16_f32 v54, v24, v26\n"
    "v_cvt_pk_bf16_f32 v55, v25, v27\n"
    "v_accvgpr_write_b32 a65, v54\n"
    "v_accvgpr_write_b32 a69, v55\n"
    "v_lshlrev_b64 v[50:51], v16, v[254:255]\n"
    "s_lshr_b32 s7, s7, 6\n"
    "s_waitcnt vmcnt(11) lgkmcnt(2)\n"
    "ds_read2st64_b64 a[200:203], v15 offset0:42 offset1:43\n"
    "v_mfma_f32_4x4x4_16b_bf16 v[20:23], v[46:47], a[188:189], 0 cbsz:2 abid:0\n"
    "ds_read_u16 v250, v18 offset:256\n"
    "v_cvt_pk_bf16_f32 v52, v28, v30\n"
    "v_cvt_pk_bf16_f32 v53, v29, v31\n"
    "v_accvgpr_write_b32 a66, v52\n"
    "v_accvgpr_write_b32 a70, v53\n"
    "v_mfma_f32_4x4x4_16b_bf16 v[24:27], v[46:47], a[190:191], 0 cbsz:2 abid:1\n"
    "buffer_load_dwordx4 v247, s[44:47], s84 offen offset:3072 lds\n"
    "v_cvt_pk_bf16_f32 v54, v32, v34\n"
    "v_cvt_pk_bf16_f32 v55, v33, v35\n"
    "v_accvgpr_write_b32 a67, v54\n"
    "v_accvgpr_write_b32 a71, v55\n"
    "s_sub_i32 s7, s7, 102\n"
    "s_waitcnt vmcnt(11) lgkmcnt(3)\n"
    "ds_read2st64_b32 a[204:205], v38 offset0:88 offset1:89\n"
    "v_mfma_f32_4x4x4_16b_bf16 v[28:31], v[46:47], a[192:193], 0 cbsz:2 abid:2\n"
    "ds_read2st64_b32 a[206:207], v38 offset0:90 offset1:91\n"
    "ds_read_u16 v252, v18 offset:264\n"
    "s_add_u32 m0, s83, 40960\n"
    "v_cvt_pk_bf16_f32 v52, v20, v24\n"
    "v_cvt_pk_bf16_f32 v53, v21, v25\n"
    "v_accvgpr_write_b32 a72, v52\n"
    "v_accvgpr_write_b32 a76, v53\n"
    "v_mfma_f32_4x4x4_16b_bf16 v[32:35], v[46:47], a[194:195], 0 cbsz:2 abid:3\n"
    "buffer_load_dwordx4 v248, s[44:47], s84 offen lds\n"
    "v_cvt_pk_bf16_f32 v54, v22, v26\n"
    "v_cvt_pk_bf16_f32 v55, v23, v27\n"
    "v_accvgpr_write_b32 a80, v54\n"
    "v_accvgpr_write_b32 a84, v55\n"
    "s_max_i32 s7, s7, 0\n"
    "s_waitcnt vmcnt(11) lgkmcnt(5)\n"
    "ds_read2st64_b32 a[208:209], v38 offset0:92 offset1:93\n"
    "v_mfma_f32_4x4x4_16b_bf16 v[20:23], v[48:49], a[196:197], 0 cbsz:2 abid:0\n"
    "ds_read2st64_b32 a[210:211], v38 offset0:94 offset1:95\n"
    "ds_read_u16 v254, v235 offset:256\n"
    "v_cvt_pk_bf16_f32 v52, v28, v32\n"
    "v_cvt_pk_bf16_f32 v53, v29, v33\n"
    "v_accvgpr_write_b32 a73, v52\n"
    "v_accvgpr_write_b32 a77, v53\n"
    "v_mfma_f32_4x4x4_16b_bf16 v[24:27], v[48:49], a[198:199], 0 cbsz:2 abid:1\n"
    "buffer_load_dwordx4 v249, s[44:47], s84 offen offset:1024 lds\n"
    "s_add_u32 m0, s83, 43008\n"
    "s_add_u32 s84, s60, s63\n"
    "v_cvt_pk_bf16_f32 v54, v30, v34\n"
    "v_cvt_pk_bf16_f32 v55, v31, v35\n"
    "v_accvgpr_write_b32 a81, v54\n"
    "v_accvgpr_write_b32 a85, v55\n"
    "buffer_load_dwordx4 v244, s[44:47], s84 offen lds\n"
    "s_min_i32 s7, s7, 50\n"
    "s_waitcnt vmcnt(12) lgkmcnt(6)\n"
    "ds_read2st64_b64 a[188:191], v15 offset0:48 offset1:49\n"
    "v_mfma_f32_4x4x4_16b_bf16 v[28:31], v[48:49], a[200:201], 0 cbsz:2 abid:2\n"
    "v_cvt_pk_bf16_f32 v52, v20, v24\n"
    "v_cvt_pk_bf16_f32 v53, v21, v25\n"
    "v_accvgpr_write_b32 a74, v52\n"
    "v_accvgpr_write_b32 a78, v53\n"
    "v_mfma_f32_4x4x4_16b_bf16 v[32:35], v[48:49], a[202:203], 0 cbsz:2 abid:3\n"
    "buffer_load_dwordx4 v245, s[44:47], s84 offen offset:1024 lds\n"
    "v_cvt_pk_bf16_f32 v54, v22, v26\n"
    "v_cvt_pk_bf16_f32 v55, v23, v27\n"
    "v_accvgpr_write_b32 a82, v54\n"
    "v_accvgpr_write_b32 a86, v55\n"
    "v_lshlrev_b64 v[40:41], v16, v[250:251]\n"
    "s_bfm_b64 s[34:35], s7, 2\n"
    "s_waitcnt vmcnt(12) lgkmcnt(4)\n"
    "ds_read2st64_b64 a[192:195], v15 offset0:50 offset1:51\n"
    "v_mfma_f32_4x4x4_16b_bf16 v[20:23], v[50:51], a[204:205], 0 cbsz:2 abid:0\n"
    "v_cvt_pk_bf16_f32 v52, v28, v32\n"
    "v_cvt_pk_bf16_f32 v53, v29, v33\n"
    "v_accvgpr_write_b32 a75, v52\n"
    "v_accvgpr_write_b32 a79, v53\n"
    "v_mfma_f32_4x4x4_16b_bf16 v[24:27], v[50:51], a[206:207], 0 cbsz:2 abid:1\n"
    "buffer_load_dwordx4 v246, s[44:47], s84 offen offset:2048 lds\n"
    "v_cvt_pk_bf16_f32 v54, v30, v34\n"
    "v_cvt_pk_bf16_f32 v55, v31, v35\n"
    "v_accvgpr_write_b32 a83, v54\n"
    "v_accvgpr_write_b32 a87, v55\n"
    "v_lshlrev_b64 v[42:43], v16, v[252:253]\n"
    "s_waitcnt vmcnt(12) lgkmcnt(2)\n"
    "ds_read2st64_b64 a[196:199], v15 offset0:52 offset1:53\n"
    "v_mfma_f32_4x4x4_16b_bf16 v[28:31], v[50:51], a[208:209], 0 cbsz:2 abid:2\n"
    "v_cvt_pk_bf16_f32 v56, v20, v22\n"
    "v_cvt_pk_bf16_f32 v60, v21, v23\n"
    "v_mfma_f32_4x4x4_16b_bf16 v[32:35], v[50:51], a[210:211], 0 cbsz:2 abid:3\n"
    "buffer_load_dwordx4 v247, s[44:47], s84 offen offset:3072 lds\n"
    "v_cvt_pk_bf16_f32 v57, v24, v26\n"
    "v_cvt_pk_bf16_f32 v61, v25, v27\n"
    "v_lshlrev_b64 v[44:45], v16, v[254:255]\n"
    "s_waitcnt vmcnt(12) lgkmcnt(2)\n"
    "ds_read2st64_b64 a[200:203], v15 offset0:54 offset1:55\n"
    "v_mfma_f32_4x4x4_16b_bf16 v[20:23], v[40:41], a[188:189], 0 cbsz:2 abid:0\n"
    "ds_read_u16 v250, v18 offset:320\n"
    "s_add_u32 m0, s83, 47104\n"
    "v_cvt_pk_bf16_f32 v58, v28, v30\n"
    "v_cvt_pk_bf16_f32 v62, v29, v31\n"
    "v_mfma_f32_4x4x4_16b_bf16 v[24:27], v[40:41], a[190:191], 0 cbsz:2 abid:1\n"
    "buffer_load_dwordx4 v248, s[44:47], s84 offen lds\n"
    "v_cvt_pk_bf16_f32 v59, v32, v34\n"
    "v_cvt_pk_bf16_f32 v63, v33, v35\n"
    "buffer_load_dwordx4 v249, s[44:47], s84 offen offset:1024 lds\n"
    "s_waitcnt vmcnt(13) lgkmcnt(3)\n"
    "ds_read2st64_b32 a[204:205], v38 offset0:112 offset1:113\n"
    "v_mfma_f32_4x4x4_16b_bf16 v[28:31], v[40:41], a[192:193], 0 cbsz:2 abid:2\n"
    "ds_read2st64_b32 a[206:207], v38 offset0:114 offset1:115\n"
    "ds_read_u16 v252, v18 offset:328\n"
    "s_add_u32 m0, s83, 49152\n"
    "s_add_u32 s84, s61, s63\n"
    "v_cvt_pk_bf16_f32 v64, v20, v24\n"
    "v_cvt_pk_bf16_f32 v68, v21, v25\n"
    "v_mfma_f32_4x4x4_16b_bf16 v[32:35], v[40:41], a[194:195], 0 cbsz:2 abid:3\n"
    "buffer_load_dwordx4 v244, s[44:47], s84 offen lds\n"
    "v_cvt_pk_bf16_f32 v72, v22, v26\n"
    "v_cvt_pk_bf16_f32 v76, v23, v27\n"
    "s_waitcnt vmcnt(13) lgkmcnt(5)\n"
    "ds_read2st64_b32 a[208:209], v38 offset0:116 offset1:117\n"
    "v_mfma_f32_4x4x4_16b_bf16 v[20:23], v[42:43], a[196:197], 0 cbsz:2 abid:0\n"
    "ds_read2st64_b32 a[210:211], v38 offset0:118 offset1:119\n"
    "ds_read_u16 v254, v235 offset:320\n"
    "v_cvt_pk_bf16_f32 v65, v28, v32\n"
    "v_cvt_pk_bf16_f32 v69, v29, v33\n"
    "v_mfma_f32_4x4x4_16b_bf16 v[24:27], v[42:43], a[198:199], 0 cbsz:2 abid:1\n"
    "buffer_load_dwordx4 v245, s[44:47], s84 offen offset:1024 lds\n"
    "v_cvt_pk_bf16_f32 v73, v30, v34\n"
    "v_cvt_pk_bf16_f32 v77, v31, v35\n"
    "s_waitcnt vmcnt(13) lgkmcnt(6)\n"
    "ds_read2st64_b64 a[188:191], v15 offset0:0 offset1:1\n"
    "v_mfma_f32_4x4x4_16b_bf16 v[28:31], v[42:43], a[200:201], 0 cbsz:2 abid:2\n"
    "v_cvt_pk_bf16_f32 v66, v20, v24\n"
    "v_cvt_pk_bf16_f32 v70, v21, v25\n"
    "v_mfma_f32_4x4x4_16b_bf16 v[32:35], v[42:43], a[202:203], 0 cbsz:2 abid:3\n"
    "buffer_load_dwordx4 v246, s[44:47], s84 offen offset:2048 lds\n"
    "v_cvt_pk_bf16_f32 v74, v22, v26\n"
    "v_cvt_pk_bf16_f32 v78, v23, v27\n"
    "v_lshlrev_b64 v[46:47], v16, v[250:251]\n"
    "s_waitcnt vmcnt(13) lgkmcnt(4)\n"
    "ds_read2st64_b64 a[192:195], v15 offset0:2 offset1:3\n"
    "v_mfma_f32_4x4x4_16b_bf16 v[20:23], v[44:45], a[204:205], 0 cbsz:2 abid:0\n"
    "v_cvt_pk_bf16_f32 v67, v28, v32\n"
    "v_cvt_pk_bf16_f32 v71, v29, v33\n"
    "v_mfma_f32_4x4x4_16b_bf16 v[24:27], v[44:45], a[206:207], 0 cbsz:2 abid:1\n"
    "buffer_load_dwordx4 v247, s[44:47], s84 offen offset:3072 lds\n"
    "s_add_u32 m0, s83, 53248\n"
    "v_cvt_pk_bf16_f32 v75, v30, v34\n"
    "v_cvt_pk_bf16_f32 v79, v31, v35\n"
    "buffer_load_dwordx4 v248, s[44:47], s84 offen lds\n"
    "v_lshlrev_b64 v[48:49], v16, v[252:253]\n"
    "s_waitcnt vmcnt(14) lgkmcnt(2)\n"
    "ds_read2st64_b64 a[196:199], v15 offset0:4 offset1:5\n"
    "v_mfma_f32_4x4x4_16b_bf16 v[28:31], v[44:45], a[208:209], 0 cbsz:2 abid:2\n"
    "v_cvt_pk_bf16_f32 v80, v20, v22\n"
    "v_cvt_pk_bf16_f32 v84, v21, v23\n"
    "v_mfma_f32_4x4x4_16b_bf16 v[32:35], v[44:45], a[210:211], 0 cbsz:2 abid:3\n"
    "buffer_load_dwordx4 v249, s[44:47], s84 offen offset:1024 lds\n"
    "v_cvt_pk_bf16_f32 v81, v24, v26\n"
    "v_cvt_pk_bf16_f32 v85, v25, v27\n"
    "v_lshlrev_b64 v[50:51], v16, v[254:255]\n"
    "s_waitcnt vmcnt(14) lgkmcnt(2)\n"
    "ds_read2st64_b64 a[200:203], v15 offset0:6 offset1:7\n"
    "v_mfma_f32_4x4x4_16b_bf16 v[20:23], v[46:47], a[188:189], 0 cbsz:2 abid:0\n"
    "ds_read_u16 v250, v18 offset:384\n"
    "s_memrealtime s[92:93]\n"
    "s_add_u32 m0, s83, 55296\n"
    "s_add_u32 s84, s58, s64\n"
    "v_cvt_pk_bf16_f32 v82, v28, v30\n"
    "v_cvt_pk_bf16_f32 v86, v29, v31\n"
    "v_mfma_f32_4x4x4_16b_bf16 v[24:27], v[46:47], a[190:191], 0 cbsz:2 abid:1\n"
    "buffer_load_dwordx4 v244, s[44:47], s84 offen lds\n"
    "v_cvt_pk_bf16_f32 v83, v32, v34\n"
    "v_cvt_pk_bf16_f32 v87, v33, v35\n"
    "s_waitcnt vmcnt(14) lgkmcnt(3)\n"
    "ds_read2st64_b32 a[204:205], v38 offset0:16 offset1:17\n"
    "v_mfma_f32_4x4x4_16b_bf16 v[28:31], v[46:47], a[192:193], 0 cbsz:2 abid:2\n"
    "ds_read2st64_b32 a[206:207], v38 offset0:18 offset1:19\n"
    "ds_read_u16 v252, v18 offset:392\n"
    "v_cvt_pk_bf16_f32 v88, v20, v24\n"
    "v_cvt_pk_bf16_f32 v92, v21, v25\n"
    "v_mfma_f32_4x4x4_16b_bf16 v[32:35], v[46:47], a[194:195], 0 cbsz:2 abid:3\n"
    "buffer_load_dwordx4 v245, s[44:47], s84 offen offset:1024 lds\n"
    "v_cvt_pk_bf16_f32 v96, v22, v26\n"
    "v_cvt_pk_bf16_f32 v100, v23, v27\n"
    "s_waitcnt vmcnt(14) lgkmcnt(5)\n"
    "ds_read2st64_b32 a[208:209], v38 offset0:20 offset1:21\n"
    "v_mfma_f32_4x4x4_16b_bf16 v[20:23], v[48:49], a[196:197], 0 cbsz:2 abid:0\n"
    "ds_read2st64_b32 a[210:211], v38 offset0:22 offset1:23\n"
    "ds_read_u16 v254, v235 offset:384\n"
    "global_load_dwordx4 v[236:239], v16, s[54:55]\n"
    "v_cvt_pk_bf16_f32 v89, v28, v32\n"
    "v_cvt_pk_bf16_f32 v93, v29, v33\n"
    "v_mfma_f32_4x4x4_16b_bf16 v[24:27], v[48:49], a[198:199], 0 cbsz:2 abid:1\n"
    "buffer_load_dwordx4 v246, s[44:47], s84 offen offset:2048 lds\n"
    "v_cvt_pk_bf16_f32 v97, v30, v34\n"
    "v_cvt_pk_bf16_f32 v101, v31, v35\n"
    "buffer_load_dwordx4 v247, s[44:47], s84 offen offset:3072 lds\n"
    "s_waitcnt vmcnt(16) lgkmcnt(6)\n"
    "ds_read2st64_b64 a[188:191], v15 offset0:12 offset1:13\n"
    "v_mfma_f32_4x4x4_16b_bf16 v[28:31], v[48:49], a[200:201], 0 cbsz:2 abid:2\n"
    "s_add_u32 m0, s83, 59392\n"
    "v_cvt_pk_bf16_f32 v90, v20, v24\n"
    "v_cvt_pk_bf16_f32 v94, v21, v25\n"
    "v_mfma_f32_4x4x4_16b_bf16 v[32:35], v[48:49], a[202:203], 0 cbsz:2 abid:3\n"
    "buffer_load_dwordx4 v248, s[44:47], s84 offen lds\n"
    "v_cvt_pk_bf16_f32 v98, v22, v26\n"
    "v_cvt_pk_bf16_f32 v102, v23, v27\n"
    "v_lshlrev_b64 v[40:41], v16, v[250:251]\n"
    "s_waitcnt vmcnt(16) lgkmcnt(4)\n"
    "ds_read2st64_b64 a[192:195], v15 offset0:14 offset1:15\n"
    "v_mfma_f32_4x4x4_16b_bf16 v[20:23], v[50:51], a[204:205], 0 cbsz:2 abid:0\n"
    "v_cvt_pk_bf16_f32 v91, v28, v32\n"
    "v_cvt_pk_bf16_f32 v95, v29, v33\n"
    "v_mfma_f32_4x4x4_16b_bf16 v[24:27], v[50:51], a[206:207], 0 cbsz:2 abid:1\n"
    "buffer_load_dwordx4 v249, s[44:47], s84 offen offset:1024 lds\n"
    "v_cvt_pk_bf16_f32 v99, v30, v34\n"
    "v_cvt_pk_bf16_f32 v103, v31, v35\n"
    "v_lshlrev_b64 v[42:43], v16, v[252:253]\n"
    "s_waitcnt vmcnt(16) lgkmcnt(2)\n"
    "ds_read2st64_b64 a[196:199], v15 offset0:16 offset1:17\n"
    "v_mfma_f32_4x4x4_16b_bf16 v[28:31], v[50:51], a[208:209], 0 cbsz:2 abid:2\n"
    "s_add_u32 m0, s83, 61440\n"
    "s_add_u32 s84, s59, s64\n"
    "v_cvt_pk_bf16_f32 v104, v20, v22\n"
    "v_cvt_pk_bf16_f32 v108, v21, v23\n"
    "v_mfma_f32_4x4x4_16b_bf16 v[32:35], v[50:51], a[210:211], 0 cbsz:2 abid:3\n"
    "buffer_load_dwordx4 v244, s[44:47], s84 offen lds\n"
    "v_cvt_pk_bf16_f32 v105, v24, v26\n"
    "v_cvt_pk_bf16_f32 v109, v25, v27\n"
    "v_lshlrev_b64 v[44:45], v16, v[254:255]\n"
    "s_waitcnt vmcnt(16) lgkmcnt(2)\n"
    "ds_read2st64_b64 a[200:203], v15 offset0:18 offset1:19\n"
    "v_mfma_f32_4x4x4_16b_bf16 v[20:23], v[40:41], a[188:189], 0 cbsz:2 abid:0\n"
    "ds_read_u16 v250, v18 offset:448\n"
    "v_cvt_pk_bf16_f32 v106, v28, v30\n"
    "v_cvt_pk_bf16_f32 v110, v29, v31\n"
    "v_mfma_f32_4x4x4_16b_bf16 v[24:27], v[40:41], a[190:191], 0 cbsz:2 abid:1\n"
    "buffer_load_dwordx4 v245, s[44:47], s84 offen offset:1024 lds\n"
    "v_cvt_pk_bf16_f32 v107, v32, v34\n"
    "v_cvt_pk_bf16_f32 v111, v33, v35\n"
    "buffer_load_dwordx4 v246, s[44:47], s84 offen offset:2048 lds\n"
    "s_waitcnt vmcnt(17) lgkmcnt(3)\n"
    "ds_read2st64_b32 a[204:205], v38 offset0:40 offset1:41\n"
    "v_mfma_f32_4x4x4_16b_bf16 v[28:31], v[40:41], a[192:193], 0 cbsz:2 abid:2\n"
    "ds_read2st64_b32 a[206:207], v38 offset0:42 offset1:43\n"
    "ds_read_u16 v252, v18 offset:456\n"
    "v_cvt_pk_bf16_f32 v112, v20, v24\n"
    "v_cvt_pk_bf16_f32 v116, v21, v25\n"
    "v_mfma_f32_4x4x4_16b_bf16 v[32:35], v[40:41], a[194:195], 0 cbsz:2 abid:3\n"
    "buffer_load_dwordx4 v247, s[44:47], s84 offen offset:3072 lds\n"
    "v_cvt_pk_bf16_f32 v120, v22, v26\n"
    "v_cvt_pk_bf16_f32 v124, v23, v27\n"
    "s_waitcnt vmcnt(17) lgkmcnt(5)\n"
    "ds_read2st64_b32 a[208:209], v38 offset0:44 offset1:45\n"
    "v_mfma_f32_4x4x4_16b_bf16 v[20:23], v[42:43], a[196:197], 0 cbsz:2 abid:0\n"
    "ds_read2st64_b32 a[210:211], v38 offset0:46 offset1:47\n"
    "ds_read_u16 v254, v235 offset:448\n"
    "s_add_u32 m0, s83, 65536\n"
    "v_cvt_pk_bf16_f32 v113, v28, v32\n"
    "v_cvt_pk_bf16_f32 v117, v29, v33\n"
    "v_mfma_f32_4x4x4_16b_bf16 v[24:27], v[42:43], a[198:199], 0 cbsz:2 abid:1\n"
    "buffer_load_dwordx4 v248, s[44:47], s84 offen lds\n"
    "v_cvt_pk_bf16_f32 v121, v30, v34\n"
    "v_cvt_pk_bf16_f32 v125, v31, v35\n"
    "s_waitcnt vmcnt(17) lgkmcnt(6)\n"
    "ds_read2st64_b64 a[188:191], v15 offset0:24 offset1:25\n"
    "v_mfma_f32_4x4x4_16b_bf16 v[28:31], v[42:43], a[200:201], 0 cbsz:2 abid:2\n"
    "v_cvt_pk_bf16_f32 v114, v20, v24\n"
    "v_cvt_pk_bf16_f32 v118, v21, v25\n"
    "v_mfma_f32_4x4x4_16b_bf16 v[32:35], v[42:43], a[202:203], 0 cbsz:2 abid:3\n"
    "buffer_load_dwordx4 v249, s[44:47], s84 offen offset:1024 lds\n"
    "v_cvt_pk_bf16_f32 v122, v22, v26\n"
    "v_cvt_pk_bf16_f32 v126, v23, v27\n"
    "v_lshlrev_b64 v[46:47], v16, v[250:251]\n"
    "s_waitcnt vmcnt(17) lgkmcnt(4)\n"
    "ds_read2st64_b64 a[192:195], v15 offset0:26 offset1:27\n"
    "v_mfma_f32_4x4x4_16b_bf16 v[20:23], v[44:45], a[204:205], 0 cbsz:2 abid:0\n"
    "s_add_u32 m0, s83, 36864\n"
    "s_add_u32 s84, s60, s64\n"
    "v_cvt_pk_bf16_f32 v115, v28, v32\n"
    "v_cvt_pk_bf16_f32 v119, v29, v33\n"
    "v_mfma_f32_4x4x4_16b_bf16 v[24:27], v[44:45], a[206:207], 0 cbsz:2 abid:1\n"
    "buffer_load_dwordx4 v244, s[44:47], s84 offen lds\n"
    "v_cvt_pk_bf16_f32 v123, v30, v34\n"
    "v_cvt_pk_bf16_f32 v127, v31, v35\n"
    "buffer_load_dwordx4 v245, s[44:47], s84 offen offset:1024 lds\n"
    "v_lshlrev_b64 v[48:49], v16, v[252:253]\n"
    "s_waitcnt vmcnt(18) lgkmcnt(2)\n"
    "ds_read2st64_b64 a[196:199], v15 offset0:28 offset1:29\n"
    "v_mfma_f32_4x4x4_16b_bf16 v[28:31], v[44:45], a[208:209], 0 cbsz:2 abid:2\n"
    "v_cvt_pk_bf16_f32 v128, v20, v22\n"
    "v_cvt_pk_bf16_f32 v132, v21, v23\n"
    "v_mfma_f32_4x4x4_16b_bf16 v[32:35], v[44:45], a[210:211], 0 cbsz:2 abid:3\n"
    "buffer_load_dwordx4 v246, s[44:47], s84 offen offset:2048 lds\n"
    "v_cvt_pk_bf16_f32 v129, v24, v26\n"
    "v_cvt_pk_bf16_f32 v133, v25, v27\n"
    "v_lshlrev_b64 v[50:51], v16, v[254:255]\n"
    "s_waitcnt vmcnt(13)\n"
    "v_and_b32 v242, 15, v237\n"
    "v_and_b32 v243, 1, v239\n"
    "v_or_b32 v237, 15, v237\n"
    "s_waitcnt vmcnt(18) lgkmcnt(2)\n"
    "ds_read2st64_b64 a[200:203], v15 offset0:30 offset1:31\n"
    "v_mfma_f32_4x4x4_16b_bf16 v[20:23], v[46:47], a[188:189], 0 cbsz:2 abid:0\n"
    "ds_read_u16 v250, v18 offset:512\n"
    "v_cvt_pk_bf16_f32 v130, v28, v30\n"
    "v_cvt_pk_bf16_f32 v134, v29, v31\n"
    "v_mfma_f32_4x4x4_16b_bf16 v[24:27], v[46:47], a[190:191], 0 cbsz:2 abid:1\n"
    "buffer_load_dwordx4 v247, s[44:47], s84 offen offset:3072 lds\n"
    "v_cvt_pk_bf16_f32 v131, v32, v34\n"
    "v_cvt_pk_bf16_f32 v135, v33, v35\n"
    "v_or_b32 v239, 15, v239\n"
    "v_cmp_ne_u64 vcc, s[18:19], v[236:237]\n"
    "v_cmp_ne_u64 s[48:49], s[18:19], v[238:239]\n"
    "v_readlane_b32 s9, v242, 0\n"
    "s_waitcnt vmcnt(18) lgkmcnt(3)\n"
    "ds_read2st64_b32 a[204:205], v38 offset0:64 offset1:65\n"
    "v_mfma_f32_4x4x4_16b_bf16 v[28:31], v[46:47], a[192:193], 0 cbsz:2 abid:2\n"
    "ds_read2st64_b32 a[206:207], v38 offset0:66 offset1:67\n"
    "ds_read_u16 v252, v18 offset:520\n"
    "s_add_u32 m0, s83, 40960\n"
    "v_cvt_pk_bf16_f32 v136, v20, v24\n"
    "v_cvt_pk_bf16_f32 v140, v21, v25\n"
    "v_mfma_f32_4x4x4_16b_bf16 v[32:35], v[46:47], a[194:195], 0 cbsz:2 abid:3\n"
    "buffer_load_dwordx4 v248, s[44:47], s84 offen lds\n"
    "v_cvt_pk_bf16_f32 v144, v22, v26\n"
    "v_cvt_pk_bf16_f32 v148, v23, v27\n"
    "v_and_b32 v236, 1, v242\n"
    "s_or_b64 s[48:49], s[48:49], vcc\n"
    "s_cbranch_scc1 75f\n"
    "77:\n"
    "s_waitcnt vmcnt(18) lgkmcnt(5)\n"
    "ds_read2st64_b32 a[208:209], v38 offset0:68 offset1:69\n"
    "v_mfma_f32_4x4x4_16b_bf16 v[20:23], v[48:49], a[196:197], 0 cbsz:2 abid:0\n"
    "ds_read2st64_b32 a[210:211], v38 offset0:70 offset1:71\n"
    "ds_read_u16 v254, v235 offset:512\n"
    "v_cvt_pk_bf16_f32 v137, v28, v32\n"
    "v_cvt_pk_bf16_f32 v141, v29, v33\n"
    "v_mfma_f32_4x4x4_16b_bf16 v[24:27], v[48:49], a[198:199], 0 cbsz:2 abid:1\n"
    "buffer_load_dwordx4 v249, s[44:47], s84 offen offset:1024 lds\n"
    "s_add_u32 m0, s83, 43008\n"
    "s_add_u32 s84, s61, s64\n"
    "v_cvt_pk_bf16_f32 v145, v30, v34\n"
    "v_cvt_pk_bf16_f32 v149, v31, v35\n"
    "buffer_load_dwordx4 v244, s[44:47], s84 offen lds\n"
    "v_cmp_eq_u32 s[24:25], 1, v236\n"
    "s_lshr_b32 s9, s9, 1\n"
    "s_add_u32 s9, s9, s28\n"
    "v_lshrrev_b64 v[240:241], v232, s[24:25]\n"
    "s_waitcnt vmcnt(19) lgkmcnt(6)\n"
    "ds_read2st64_b64 a[188:191], v15 offset0:36 offset1:37\n"
    "v_mfma_f32_4x4x4_16b_bf16 v[28:31], v[48:49], a[200:201], 0 cbsz:2 abid:2\n"
    "v_cvt_pk_bf16_f32 v138, v20, v24\n"
    "v_cvt_pk_bf16_f32 v142, v21, v25\n"
    "v_mfma_f32_4x4x4_16b_bf16 v[32:35], v[48:49], a[202:203], 0 cbsz:2 abid:3\n"
    "buffer_load_dwordx4 v245, s[44:47], s84 offen offset:1024 lds\n"
    "v_cvt_pk_bf16_f32 v146, v22, v26\n"
    "v_cvt_pk_bf16_f32 v150, v23, v27\n"
    "v_lshlrev_b64 v[40:41], v16, v[250:251]\n"
    "v_and_b32 v241, 0xff, v240\n"
    "v_bfe_u32 v240, v240, v233, 4\n"
    "v_bcnt_u32_b32 v241, v241, 8\n"
    "v_bcnt_u32_b32 v240, v240, 0\n"
    "s_waitcnt vmcnt(19) lgkmcnt(4)\n"
    "ds_read2st64_b64 a[192:195], v15 offset0:38 offset1:39\n"
    "v_mfma_f32_4x4x4_16b_bf16 v[20:23], v[50:51], a[204:205], 0 cbsz:2 abid:0\n"
    "v_cvt_pk_bf16_f32 v139, v28, v32\n"
    "v_cvt_pk_bf16_f32 v143, v29, v33\n"
    "v_mfma_f32_4x4x4_16b_bf16 v[24:27], v[50:51], a[206:207], 0 cbsz:2 abid:1\n"
    "buffer_load_dwordx4 v246, s[44:47], s84 offen offset:2048 lds\n"
    "v_cvt_pk_bf16_f32 v147, v30, v34\n"
    "v_cvt_pk_bf16_f32 v151, v31, v35\n"
    "v_lshlrev_b64 v[42:43], v16, v[252:253]\n"
    "v_lshl_add_u32 v240, v240, 1, v241\n"
    "s_lshr_b32 s10, s9, 2\n"
    "v_cmp_lt_u32 vcc, 15, v240\n"
    "v_xor_b32 v234, s10, v234\n"
    "s_waitcnt vmcnt(18) lgkmcnt(2)\n"
    "ds_read2st64_b64 a[196:199], v15 offset0:40 offset1:41\n"
    "v_mfma_f32_4x4x4_16b_bf16 v[28:31], v[50:51], a[208:209], 0 cbsz:2 abid:2\n"
    "v_cvt_pk_bf16_f32 v152, v20, v22\n"
    "v_cvt_pk_bf16_f32 v156, v21, v23\n"
    "v_mfma_f32_4x4x4_16b_bf16 v[32:35], v[50:51], a[210:211], 0 cbsz:2 abid:3\n"
    "buffer_load_dwordx4 v247, s[44:47], s84 offen offset:3072 lds\n"
    "v_cvt_pk_bf16_f32 v153, v24, v26\n"
    "v_cvt_pk_bf16_f32 v157, v25, v27\n"
    "v_lshlrev_b64 v[44:45], v16, v[254:255]\n"
    "s_mov_b64 s[50:51], vcc\n"
    "v_addc_co_u32 v234, vcc, 0, v234, vcc\n"
    "v_and_b32 v234, 1, v234\n"
    "v_mad_u32_u24 v236, v234, s88, v3\n"
    "s_waitcnt vmcnt(18) lgkmcnt(2)\n"
    "ds_read2st64_b64 a[200:203], v15 offset0:42 offset1:43\n"
    "v_mfma_f32_4x4x4_16b_bf16 v[20:23], v[40:41], a[188:189], 0 cbsz:2 abid:0\n"
    "ds_read_u16 v250, v18 offset:576\n"
    "s_add_u32 m0, s83, 47104\n"
    "v_cvt_pk_bf16_f32 v154, v28, v30\n"
    "v_cvt_pk_bf16_f32 v158, v29, v31\n"
    "v_mfma_f32_4x4x4_16b_bf16 v[24:27], v[40:41], a[190:191], 0 cbsz:2 abid:1\n"
    "buffer_load_dwordx4 v248, s[44:47], s84 offen lds\n"
    "v_cvt_pk_bf16_f32 v155, v32, v34\n"
    "v_cvt_pk_bf16_f32 v159, v33, v35\n"
    "v_cmp_eq_u32 s[26:27], 1, v243\n"
    "v_readlane_b32 s82, v236, 0\n"
    "v_readlane_b32 s73, v236, 1\n"
    "s_waitcnt vmcnt(18) lgkmcnt(3)\n"
    "ds_read2st64_b32 a[204:205], v38 offset0:88 offset1:89\n"
    "v_mfma_f32_4x4x4_16b_bf16 v[28:31], v[40:41], a[192:193], 0 cbsz:2 abid:2\n"
    "ds_read2st64_b32 a[206:207], v38 offset0:90 offset1:91\n"
    "ds_read_u16 v252, v18 offset:584\n"
    "v_cvt_pk_bf16_f32 v160, v20, v24\n"
    "v_cvt_pk_bf16_f32 v164, v21, v25\n"
    "v_mfma_f32_4x4x4_16b_bf16 v[32:35], v[40:41], a[194:195], 0 cbsz:2 abid:3\n"
    "buffer_load_dwordx4 v249, s[44:47], s84 offen offset:1024 lds\n"
    "v_cvt_pk_bf16_f32 v168, v22, v26\n"
    "v_cvt_pk_bf16_f32 v172, v23, v27\n"
    "s_waitcnt vmcnt(18) lgkmcnt(5)\n"
    "ds_read2st64_b32 a[208:209], v38 offset0:92 offset1:93\n"
    "v_mfma_f32_4x4x4_16b_bf16 v[20:23], v[42:43], a[196:197], 0 cbsz:2 abid:0\n"
    "ds_read2st64_b32 a[210:211], v38 offset0:94 offset1:95\n"
    "ds_read_u16 v254, v235 offset:576\n"
    "s_barrier\n"
    "s_mov_b32 m0, s57\n"
    "v_cvt_pk_bf16_f32 v161, v28, v32\n"
    "v_cvt_pk_bf16_f32 v165, v29, v33\n"
    "v_mfma_f32_4x4x4_16b_bf16 v[24:27], v[42:43], a[198:199], 0 cbsz:2 abid:1\n"
    "buffer_load_dwordx4 v0, s[36:39], s82 offen nt lds\n"
    "v_cvt_pk_bf16_f32 v169, v30, v34\n"
    "v_cvt_pk_bf16_f32 v173, v31, v35\n"
    "s_waitcnt vmcnt(18) lgkmcnt(6)\n"
    "ds_read2st64_b64 a[188:191], v15 offset0:48 offset1:49\n"
    "v_mfma_f32_4x4x4_16b_bf16 v[28:31], v[42:43], a[200:201], 0 cbsz:2 abid:2\n"
    "v_cvt_pk_bf16_f32 v162, v20, v24\n"
    "v_cvt_pk_bf16_f32 v166, v21, v25\n"
    "v_mfma_f32_4x4x4_16b_bf16 v[32:35], v[42:43], a[202:203], 0 cbsz:2 abid:3\n"
    "buffer_load_dwordx4 v1, s[36:39], s82 offen offset:1024 nt lds\n"
    "v_cvt_pk_bf16_f32 v170, v22, v26\n"
    "v_cvt_pk_bf16_f32 v174, v23, v27\n"
    "v_lshlrev_b64 v[46:47], v16, v[250:251]\n"
    "v_cmp_gt_u32 s[48:49], s96, v232\n"
    "v_lshrrev_b64 v[240:241], v232, s[26:27]\n"
    "v_and_b32 v241, 0xff, v240\n"
    "v_bfe_u32 v240, v240, v233, 4\n"
    "s_waitcnt vmcnt(18) lgkmcnt(4)\n"
    "ds_read2st64_b64 a[192:195], v15 offset0:50 offset1:51\n"
    "v_mfma_f32_4x4x4_16b_bf16 v[20:23], v[44:45], a[204:205], 0 cbsz:2 abid:0\n"
    "v_cvt_pk_bf16_f32 v163, v28, v32\n"
    "v_cvt_pk_bf16_f32 v167, v29, v33\n"
    "v_mfma_f32_4x4x4_16b_bf16 v[24:27], v[44:45], a[206:207], 0 cbsz:2 abid:1\n"
    "buffer_load_dwordx4 v2, s[36:39], s82 offen offset:2048 nt lds\n"
    "v_cvt_pk_bf16_f32 v171, v30, v34\n"
    "v_cvt_pk_bf16_f32 v175, v31, v35\n"
    "v_lshlrev_b64 v[48:49], v16, v[252:253]\n"
    "v_bcnt_u32_b32 v241, v241, 8\n"
    "v_bcnt_u32_b32 v240, v240, 0\n"
    "v_lshl_add_u32 v240, v240, 1, v241\n"
    "v_cmp_lt_u32 s[26:27], 15, v240\n"
    "s_waitcnt vmcnt(18) lgkmcnt(2)\n"
    "ds_read2st64_b64 a[196:199], v15 offset0:52 offset1:53\n"
    "v_mfma_f32_4x4x4_16b_bf16 v[28:31], v[44:45], a[208:209], 0 cbsz:2 abid:2\n"
    "v_cvt_pk_bf16_f32 v176, v20, v22\n"
    "v_cvt_pk_bf16_f32 v180, v21, v23\n"
    "v_mfma_f32_4x4x4_16b_bf16 v[32:35], v[44:45], a[210:211], 0 cbsz:2 abid:3\n"
    "v_cvt_pk_bf16_f32 v177, v24, v26\n"
    "v_cvt_pk_bf16_f32 v181, v25, v27\n"
    "v_lshlrev_b64 v[50:51], v16, v[254:255]\n"
    "s_bfm_b64 s[24:25], s52, 0\n"
    "s_bitcmp1_b32 s9, 0\n"
    "s_cselect_b32 s95, -1, 1\n"
    "s_waitcnt vmcnt(17) lgkmcnt(2)\n"
    "ds_read2st64_b64 a[200:203], v15 offset0:54 offset1:55\n"
    "v_mfma_f32_4x4x4_16b_bf16 v[20:23], v[46:47], a[188:189], 0 cbsz:2 abid:0\n"
    "ds_read_u16 v250, v18 offset:640\n"
    "v_cvt_pk_bf16_f32 v178, v28, v30\n"
    "v_cvt_pk_bf16_f32 v182, v29, v31\n"
    "v_mfma_f32_4x4x4_16b_bf16 v[24:27], v[46:47], a[190:191], 0 cbsz:2 abid:1\n"
    "v_cvt_pk_bf16_f32 v179, v32, v34\n"
    "v_cvt_pk_bf16_f32 v183, v33, v35\n"
    "s_cmp_lt_u32 s52, 12\n"
    "s_cselect_b32 s95, 0, s95\n"
    "s_add_u32 s52, s52, s95\n"
    "s_xor_b64 s[26:27], s[26:27], s[50:51]\n"
    "s_waitcnt vmcnt(16) lgkmcnt(3)\n"
    "ds_read2st64_b32 a[204:205], v38 offset0:112 offset1:113\n"
    "v_mfma_f32_4x4x4_16b_bf16 v[28:31], v[46:47], a[192:193], 0 cbsz:2 abid:2\n"
    "ds_read2st64_b32 a[206:207], v38 offset0:114 offset1:115\n"
    "ds_read_u16 v252, v18 offset:648\n"
    "v_cvt_pk_bf16_f32 v184, v20, v24\n"
    "v_cvt_pk_bf16_f32 v188, v21, v25\n"
    "v_mfma_f32_4x4x4_16b_bf16 v[32:35], v[46:47], a[194:195], 0 cbsz:2 abid:3\n"
    "v_cvt_pk_bf16_f32 v192, v22, v26\n"
    "v_cvt_pk_bf16_f32 v196, v23, v27\n"
    "s_and_b64 s[48:49], s[48:49], s[24:25]\n"
    "s_andn2_b64 s[48:49], s[48:49], s[26:27]\n"
    "s_bcnt1_i32_b64 s10, s[48:49]\n"
    "s_waitcnt vmcnt(15) lgkmcnt(5)\n"
    "ds_read2st64_b32 a[208:209], v38 offset0:116 offset1:117\n"
    "v_mfma_f32_4x4x4_16b_bf16 v[20:23], v[48:49], a[196:197], 0 cbsz:2 abid:0\n"
    "ds_read2st64_b32 a[210:211], v38 offset0:118 offset1:119\n"
    "ds_read_u16 v254, v235 offset:640\n"
    "v_cvt_pk_bf16_f32 v185, v28, v32\n"
    "v_cvt_pk_bf16_f32 v189, v29, v33\n"
    "v_mfma_f32_4x4x4_16b_bf16 v[24:27], v[48:49], a[198:199], 0 cbsz:2 abid:1\n"
    "v_cvt_pk_bf16_f32 v193, v30, v34\n"
    "v_cvt_pk_bf16_f32 v197, v31, v35\n"
    "s_cmp_lt_u32 s10, 4\n"
    "s_cselect_b64 s[34:35], s[34:35], 0\n"
    "s_cselect_b32 s96, 1, 0\n"
    "s_and_b64 vcc, s[34:35], s[26:27]\n"
    "s_waitcnt vmcnt(14) lgkmcnt(6)\n"
    "ds_read2st64_b64 a[188:191], v15 offset0:0 offset1:1\n"
    "v_mfma_f32_4x4x4_16b_bf16 v[28:31], v[48:49], a[200:201], 0 cbsz:2 abid:2\n"
    "v_cvt_pk_bf16_f32 v186, v20, v24\n"
    "v_cvt_pk_bf16_f32 v190, v21, v25\n"
    "v_mfma_f32_4x4x4_16b_bf16 v[32:35], v[48:49], a[202:203], 0 cbsz:2 abid:3\n"
    "v_cvt_pk_bf16_f32 v194, v22, v26\n"
    "v_cvt_pk_bf16_f32 v198, v23, v27\n"
    "v_lshlrev_b64 v[40:41], v16, v[250:251]\n"
    "v_addc_co_u32 v234, vcc, 0, v234, vcc\n"
    "v_cmp_le_u32 vcc, s52, v19\n"
    "v_subrev_u32 v240, 15, v240\n"
    "v_cmp_gt_u32 s[48:49], 3, v240\n"
    "s_waitcnt vmcnt(13) lgkmcnt(4)\n"
    "ds_read2st64_b64 a[192:195], v15 offset0:2 offset1:3\n"
    "v_mfma_f32_4x4x4_16b_bf16 v[20:23], v[50:51], a[204:205], 0 cbsz:2 abid:0\n"
    "v_cvt_pk_bf16_f32 v187, v28, v32\n"
    "v_cvt_pk_bf16_f32 v191, v29, v33\n"
    "v_mfma_f32_4x4x4_16b_bf16 v[24:27], v[50:51], a[206:207], 0 cbsz:2 abid:1\n"
    "v_cvt_pk_bf16_f32 v195, v30, v34\n"
    "v_cvt_pk_bf16_f32 v199, v31, v35\n"
    "v_lshlrev_b64 v[42:43], v16, v[252:253]\n"
    "s_or_b64 s[26:27], s[26:27], s[48:49]\n"
    "v_and_b32 v234, 1, v234\n"
    "v_mov_b32 v236, s33\n"
    "v_mad_u32_u24 v3, v234, s88, v3\n"
    "s_waitcnt vmcnt(12) lgkmcnt(2)\n"
    "ds_read2st64_b64 a[196:199], v15 offset0:4 offset1:5\n"
    "v_mfma_f32_4x4x4_16b_bf16 v[28:31], v[50:51], a[208:209], 0 cbsz:2 abid:2\n"
    "v_cvt_pk_bf16_f32 v200, v20, v22\n"
    "v_cvt_pk_bf16_f32 v204, v21, v23\n"
    "v_mfma_f32_4x4x4_16b_bf16 v[32:35], v[50:51], a[210:211], 0 cbsz:2 abid:3\n"
    "v_cvt_pk_bf16_f32 v201, v24, v26\n"
    "v_cvt_pk_bf16_f32 v205, v25, v27\n"
    "v_lshlrev_b64 v[44:45], v16, v[254:255]\n"
    "v_cndmask_b32 v3, v3, v236, vcc\n"
    "s_nop 0\n"
    "v_readlane_b32 s74, v3, 2\n"
    "v_readlane_b32 s75, v3, 3\n"
    "s_waitcnt vmcnt(11) lgkmcnt(2)\n"
    "ds_read2st64_b64 a[200:203], v15 offset0:6 offset1:7\n"
    "v_mfma_f32_4x4x4_16b_bf16 v[20:23], v[40:41], a[188:189], 0 cbsz:2 abid:0\n"
    "ds_read_u16 v250, v18 offset:704\n"
    "v_cvt_pk_bf16_f32 v202, v28, v30\n"
    "v_cvt_pk_bf16_f32 v206, v29, v31\n"
    "v_mfma_f32_4x4x4_16b_bf16 v[24:27], v[40:41], a[190:191], 0 cbsz:2 abid:1\n"
    "v_cvt_pk_bf16_f32 v203, v32, v34\n"
    "v_cvt_pk_bf16_f32 v207, v33, v35\n"
    "v_readlane_b32 s81, v3, 4\n"
    "s_waitcnt vmcnt(10) lgkmcnt(3)\n"
    "ds_read2st64_b32 a[204:205], v38 offset0:16 offset1:17\n"
    "v_mfma_f32_4x4x4_16b_bf16 v[28:31], v[40:41], a[192:193], 0 cbsz:2 abid:2\n"
    "ds_read2st64_b32 a[206:207], v38 offset0:18 offset1:19\n"
    "ds_read_u16 v252, v18 offset:712\n"
    "v_cvt_pk_bf16_f32 v208, v20, v24\n"
    "v_cvt_pk_bf16_f32 v212, v21, v25\n"
    "v_mfma_f32_4x4x4_16b_bf16 v[32:35], v[40:41], a[194:195], 0 cbsz:2 abid:3\n"
    "v_cvt_pk_bf16_f32 v216, v22, v26\n"
    "v_cvt_pk_bf16_f32 v220, v23, v27\n"
    "s_waitcnt vmcnt(9) lgkmcnt(5)\n"
    "ds_read2st64_b32 a[208:209], v38 offset0:20 offset1:21\n"
    "v_mfma_f32_4x4x4_16b_bf16 v[20:23], v[42:43], a[196:197], 0 cbsz:2 abid:0\n"
    "ds_read2st64_b32 a[210:211], v38 offset0:22 offset1:23\n"
    "ds_read_u16 v254, v235 offset:704\n"
    "v_cvt_pk_bf16_f32 v209, v28, v32\n"
    "v_cvt_pk_bf16_f32 v213, v29, v33\n"
    "v_mfma_f32_4x4x4_16b_bf16 v[24:27], v[42:43], a[198:199], 0 cbsz:2 abid:1\n"
    "v_cvt_pk_bf16_f32 v217, v30, v34\n"
    "v_cvt_pk_bf16_f32 v221, v31, v35\n"
    "s_waitcnt vmcnt(8) lgkmcnt(6)\n"
    "ds_read2st64_b64 a[188:191], v15 offset0:12 offset1:13\n"
    "v_mfma_f32_4x4x4_16b_bf16 v[28:31], v[42:43], a[200:201], 0 cbsz:2 abid:2\n"
    "s_nop 0\n"
    "v_cvt_pk_bf16_f32 v210, v20, v24\n"
    "v_cvt_pk_bf16_f32 v214, v21, v25\n"
    "v_mfma_f32_4x4x4_16b_bf16 v[32:35], v[42:43], a[202:203], 0 cbsz:2 abid:3\n"
    "v_cvt_pk_bf16_f32 v218, v22, v26\n"
    "v_cvt_pk_bf16_f32 v222, v23, v27\n"
    "v_lshlrev_b64 v[46:47], v16, v[250:251]\n"
    "s_waitcnt vmcnt(7) lgkmcnt(4)\n"
    "ds_read2st64_b64 a[192:195], v15 offset0:14 offset1:15\n"
    "v_mfma_f32_4x4x4_16b_bf16 v[20:23], v[44:45], a[204:205], 0 cbsz:2 abid:0\n"
    "s_add_u32 m0, s57, 12288\n"
    "v_cvt_pk_bf16_f32 v211, v28, v32\n"
    "v_cvt_pk_bf16_f32 v215, v29, v33\n"
    "v_mfma_f32_4x4x4_16b_bf16 v[24:27], v[44:45], a[206:207], 0 cbsz:2 abid:1\n"
    "buffer_load_dwordx4 v0, s[36:39], s73 offen nt lds\n"
    "v_cvt_pk_bf16_f32 v219, v30, v34\n"
    "v_cvt_pk_bf16_f32 v223, v31, v35\n"
    "v_lshlrev_b64 v[48:49], v16, v[252:253]\n"
    "s_waitcnt vmcnt(7) lgkmcnt(2)\n"
    "ds_read2st64_b64 a[196:199], v15 offset0:16 offset1:17\n"
    "v_mfma_f32_4x4x4_16b_bf16 v[28:31], v[44:45], a[208:209], 0 cbsz:2 abid:2\n"
    "v_cvt_pk_bf16_f32 v224, v20, v22\n"
    "v_cvt_pk_bf16_f32 v228, v21, v23\n"
    "v_mfma_f32_4x4x4_16b_bf16 v[32:35], v[44:45], a[210:211], 0 cbsz:2 abid:3\n"
    "buffer_load_dwordx4 v1, s[36:39], s73 offen offset:1024 nt lds\n"
    "v_cvt_pk_bf16_f32 v225, v24, v26\n"
    "v_cvt_pk_bf16_f32 v229, v25, v27\n"
    "v_lshlrev_b64 v[50:51], v16, v[254:255]\n"
    "s_waitcnt vmcnt(7) lgkmcnt(2)\n"
    "ds_read2st64_b64 a[200:203], v15 offset0:18 offset1:19\n"
    "v_mfma_f32_4x4x4_16b_bf16 v[20:23], v[46:47], a[188:189], 0 cbsz:2 abid:0\n"
    "v_cvt_pk_bf16_f32 v226, v28, v30\n"
    "v_cvt_pk_bf16_f32 v230, v29, v31\n"
    "v_mfma_f32_4x4x4_16b_bf16 v[24:27], v[46:47], a[190:191], 0 cbsz:2 abid:1\n"
    "buffer_load_dwordx4 v2, s[36:39], s73 offen offset:2048 nt lds\n"
    "v_cvt_pk_bf16_f32 v227, v32, v34\n"
    "v_cvt_pk_bf16_f32 v231, v33, v35\n"
    "s_waitcnt vmcnt(7) lgkmcnt(2)\n"
    "ds_read2st64_b32 a[204:205], v38 offset0:40 offset1:41\n"
    "v_mfma_f32_4x4x4_16b_bf16 v[28:31], v[46:47], a[192:193], 0 cbsz:2 abid:2\n"
    "ds_read2st64_b32 a[206:207], v38 offset0:42 offset1:43\n"
    "v_cvt_pk_bf16_f32 v232, v20, v24\n"
    "v_cvt_pk_bf16_f32 v236, v21, v25\n"
    "v_mfma_f32_4x4x4_16b_bf16 v[32:35], v[46:47], a[194:195], 0 cbsz:2 abid:3\n"
    "v_cvt_pk_bf16_f32 v240, v22, v26\n"
    "v_cvt_pk_bf16_f32 v244, v23, v27\n"
    "s_waitcnt vmcnt(6) lgkmcnt(3)\n"
    "ds_read2st64_b32 a[208:209], v38 offset0:44 offset1:45\n"
    "v_mfma_f32_4x4x4_16b_bf16 v[20:23], v[48:49], a[196:197], 0 cbsz:2 abid:0\n"
    "ds_read2st64_b32 a[210:211], v38 offset0:46 offset1:47\n"
    "v_cvt_pk_bf16_f32 v233, v28, v32\n"
    "v_cvt_pk_bf16_f32 v237, v29, v33\n"
    "v_mfma_f32_4x4x4_16b_bf16 v[24:27], v[48:49], a[198:199], 0 cbsz:2 abid:1\n"
    "v_cvt_pk_bf16_f32 v241, v30, v34\n"
    "v_cvt_pk_bf16_f32 v245, v31, v35\n"
    "s_waitcnt lgkmcnt(4)\n"
    "v_mfma_f32_4x4x4_16b_bf16 v[28:31], v[48:49], a[200:201], 0 cbsz:2 abid:2\n"
    "s_nop 1\n"
    "v_cvt_pk_bf16_f32 v234, v20, v24\n"
    "v_cvt_pk_bf16_f32 v238, v21, v25\n"
    "v_mfma_f32_4x4x4_16b_bf16 v[32:35], v[48:49], a[202:203], 0 cbsz:2 abid:3\n"
    "v_cvt_pk_bf16_f32 v242, v22, v26\n"
    "v_cvt_pk_bf16_f32 v246, v23, v27\n"
    "s_waitcnt lgkmcnt(2)\n"
    "v_mfma_f32_4x4x4_16b_bf16 v[20:23], v[50:51], a[204:205], 0 cbsz:2 abid:0\n"
    "s_nop 1\n"
    "v_cvt_pk_bf16_f32 v235, v28, v32\n"
    "v_cvt_pk_bf16_f32 v239, v29, v33\n"
    "v_mfma_f32_4x4x4_16b_bf16 v[24:27], v[50:51], a[206:207], 0 cbsz:2 abid:1\n"
    "v_cvt_pk_bf16_f32 v243, v30, v34\n"
    "v_cvt_pk_bf16_f32 v247, v31, v35\n"
    "s_waitcnt lgkmcnt(0)\n"
    "v_mfma_f32_4x4x4_16b_bf16 v[28:31], v[50:51], a[208:209], 0 cbsz:2 abid:2\n"
    "s_nop 1\n"
    "v_cvt_pk_bf16_f32 v248, v20, v22\n"
    "v_cvt_pk_bf16_f32 v252, v21, v23\n"
    "v_mfma_f32_4x4x4_16b_bf16 v[32:35], v[50:51], a[210:211], 0 cbsz:2 abid:3\n"
    "v_cvt_pk_bf16_f32 v249, v24, v26\n"
    "v_cvt_pk_bf16_f32 v253, v25, v27\n"
    "s_waitcnt lgkmcnt(0)\n"
    "s_add_u32 s89, s92, 255\n"
    "v_add_u32 v38, s11, v38\n"
    "s_nop 0\n"
    "v_cvt_pk_bf16_f32 v250, v28, v30\n"
    "v_cvt_pk_bf16_f32 v254, v29, v31\n"
    "v_cvt_pk_bf16_f32 v251, v32, v34\n"
    "v_cvt_pk_bf16_f32 v255, v33, v35\n"
    "s_waitcnt vmcnt(3)\n"
    "s_barrier\n"
    "ds_read_b128 a[88:91], v4\n"
    "ds_read_b128 a[92:95], v5\n"
    "ds_read_b128 a[96:99], v6\n"
    "ds_read_b128 a[100:103], v7\n"
    "ds_read_b128 a[104:107], v4 offset:256\n"
    "ds_read_b128 a[108:111], v5 offset:256\n"
    "ds_read_b128 a[112:115], v6 offset:256\n"
    "ds_read_b128 a[116:119], v7 offset:256\n"
    "ds_read_b128 a[120:123], v4 offset:512\n"
    "ds_read_b128 a[124:127], v5 offset:512\n"
    "ds_read_b128 a[128:131], v6 offset:512\n"
    "ds_read_b128 a[132:135], v7 offset:512\n"
    "s_waitcnt lgkmcnt(11)\n"
    "v_mfma_f32_16x16x32_bf16 v[20:23], a[88:91], a[16:19], 0\n"
    "v_add_u32 v14, 1, v14\n"
    "s_sub_i32 s46, s52, 5\n"
    "s_max_i32 s46, s46, 0\n"
    "s_waitcnt lgkmcnt(10)\n"
    "v_mfma_f32_16x16x32_bf16 v[20:23], a[92:95], a[40:43], v[20:23]\n"
    "s_add_u32 m0, s57, 24576\n"
    "s_bfm_b64 s[44:45], s46, 5\n"
    "v_subrev_u32 v43, 5, v19\n"
    "s_waitcnt lgkmcnt(9)\n"
    "v_mfma_f32_16x16x32_bf16 v[20:23], a[96:99], a[64:67], v[20:23]\n"
    "buffer_load_dwordx4 v0, s[36:39], s74 offen nt lds\n"
    "s_andn2_b64 s[50:51], s[44:45], s[26:27]\n"
    "s_waitcnt lgkmcnt(8)\n"
    "v_mfma_f32_16x16x32_bf16 v[20:23], a[100:103], v[56:59], v[20:23]\n"
    "s_and_b64 s[48:49], s[44:45], s[26:27]\n"
    "s_bcnt1_i32_b64 s47, s[50:51]\n"
    "s_sub_u32 s46, s46, s47\n"
    "s_waitcnt lgkmcnt(7)\n"
    "v_mfma_f32_16x16x32_bf16 v[20:23], a[104:107], v[80:83], v[20:23]\n"
    "buffer_load_dwordx4 v1, s[36:39], s74 offen offset:1024 nt lds\n"
    "s_add_u32 s58, s46, 5\n"
    "s_waitcnt lgkmcnt(6)\n"
    "v_mfma_f32_16x16x32_bf16 v[20:23], a[108:111], v[104:107], v[20:23]\n"
    "v_mov_b32 v45, s58\n"
    "v_mbcnt_lo_u32_b32 v47, s48, 5\n"
    "v_mbcnt_lo_u32_b32 v45, s50, v45\n"
    "s_waitcnt lgkmcnt(5)\n"
    "v_mfma_f32_16x16x32_bf16 v[20:23], a[112:115], v[128:131], v[20:23]\n"
    "buffer_load_dwordx4 v2, s[36:39], s74 offen offset:2048 nt lds\n"
    "v_mbcnt_hi_u32_b32 v47, s49, v47\n"
    "s_waitcnt lgkmcnt(4)\n"
    "v_mfma_f32_16x16x32_bf16 v[20:23], a[116:119], v[152:155], v[20:23]\n"
    "v_mbcnt_hi_u32_b32 v45, s51, v45\n"
    "v_cndmask_b32 v47, v19, v47, s[48:49]\n"
    "s_sub_i32 s59, s47, 4\n"
    "s_waitcnt lgkmcnt(3)\n"
    "v_mfma_f32_16x16x32_bf16 v[20:23], a[120:123], v[176:179], v[20:23]\n"
    "v_cndmask_b32 v47, v47, v45, s[50:51]\n"
    "s_max_i32 s59, s59, 0\n"
    "v_lshlrev_b32 v47, 2, v47\n"
    "s_waitcnt lgkmcnt(2)\n"
    "v_mfma_f32_16x16x32_bf16 v[20:23], a[124:127], v[200:203], v[20:23]\n"
    "s_mul_i32 s59, s59, 9\n"
    "s_min_u32 s59, s59, 256\n"
    "ds_permute_b32 v3, v47, v3\n"
    "s_waitcnt lgkmcnt(2)\n"
    "v_mfma_f32_16x16x32_bf16 v[20:23], a[128:131], v[224:227], v[20:23]\n"
    "s_cmp_lg_u32 s96, 0\n"
    "s_cselect_b32 s62, 56, 34\n"
    "s_lshr_b32 s63, s46, 5\n"
    "s_waitcnt lgkmcnt(1)\n"
    "v_mfma_f32_16x16x32_bf16 v[20:23], a[132:135], v[248:251], v[20:23]\n"
    "s_add_u32 s62, s62, s63\n"
    "s_sub_u32 s63, s62, 20\n"
    "v_mul_i32_i24 v40, s59, v43\n"
    "v_mfma_f32_16x16x32_bf16 v[24:27], a[132:135], v[252:255], 0\n"
    "s_mul_i32 s7, s2, 9\n"
    "s_and_b32 s7, s7, 0xff\n"
    "v_add_u32 v40, s7, v40\n"
    "v_mfma_f32_16x16x32_bf16 v[24:27], a[128:131], v[228:231], v[24:27]\n"
    "v_subrev_u32 v42, s46, v43\n"
    "v_add_u32 v41, s59, v40\n"
    "v_ashrrev_i32 v40, 8, v40\n"
    "v_mfma_f32_16x16x32_bf16 v[24:27], a[124:127], v[204:207], v[24:27]\n"
    "v_ashrrev_i32 v41, 8, v41\n"
    "v_min_i32 v40, s47, v40\n"
    "v_min_i32 v41, s47, v41\n"
    "v_mfma_f32_16x16x32_bf16 v[24:27], a[120:123], v[180:183], v[24:27]\n"
    "v_max_i32 v40, v40, v42\n"
    "v_add_u32 v42, 1, v42\n"
    "v_max_i32 v41, v41, v42\n"
    "v_mfma_f32_16x16x32_bf16 v[24:27], a[116:119], v[156:159], v[24:27]\n"
    "v_cmp_gt_i32 s[60:61], v41, v40\n"
    "v_add_u32 v41, s58, v40\n"
    "v_sub_u32 v40, v19, v40\n"
    "v_mfma_f32_16x16x32_bf16 v[24:27], a[112:115], v[132:135], v[24:27]\n"
    "v_cndmask_b32 v40, v40, v41, s[60:61]\n"
    "v_cndmask_b32 v40, v19, v40, s[44:45]\n"
    "v_lshlrev_b32 v40, 2, v40\n"
    "v_mfma_f32_16x16x32_bf16 v[24:27], a[108:111], v[108:111], v[24:27]\n"
    "s_waitcnt lgkmcnt(0)\n"
    "ds_bpermute_b32 v3, v40, v3\n"
    "s_mul_i32 s94, s46, 4\n"
    "v_mfma_f32_16x16x32_bf16 v[24:27], a[104:107], v[84:87], v[24:27]\n"
    "s_lshr_b32 s94, s94, 4\n"
    "s_add_u32 s94, s94, 67\n"
    "v_mfma_f32_16x16x32_bf16 v[24:27], a[100:103], v[60:63], v[24:27]\n"
    "s_cmp_lg_u32 s96, 0\n"
    "s_cselect_b32 s94, 77, s94\n"
    "s_mul_i32 s7, s95, 3\n"
    "v_mfma_f32_16x16x32_bf16 v[24:27], a[96:99], a[68:71], v[24:27]\n"
    "s_waitcnt vmcnt(3)\n"
    "s_sub_u32 s94, s94, s7\n"
    "s_mul_i32 s7, s46, -1\n"
    "v_mfma_f32_16x16x32_bf16 v[24:27], a[92:95], a[44:47], v[24:27]\n"
    "ds_write_b32 v13, v14\n"
    "s_ashr_i32 s7, s7, 4\n"
    "v_mfma_f32_16x16x32_bf16 v[24:27], a[88:91], a[20:23], v[24:27]\n"
    "s_add_u32 m0, s57, 36864\n"
    "s_add_u32 s7, s7, 6\n"
    "v_mfma_f32_16x16x32_bf16 v[28:31], a[88:91], a[0:3], 0\n"
    "buffer_load_dwordx4 v0, s[36:39], s75 offen nt lds\n"
    "v_mfma_f32_16x16x32_bf16 v[28:31], a[92:95], a[24:27], v[28:31]\n"
    "s_cmp_lg_u32 s96, 0\n"
    "s_cselect_b32 s31, 3, s7\n"
    "s_add_u32 s31, s94, s31\n"
    "v_mfma_f32_16x16x32_bf16 v[28:31], a[96:99], a[48:51], v[28:31]\n"
    "buffer_load_dwordx4 v1, s[36:39], s75 offen offset:1024 nt lds\n"
    "s_add_u32 s76, s52, 4\n"
    "v_mfma_f32_16x16x32_bf16 v[28:31], a[100:103], a[72:75], v[28:31]\n"
    "v_cvt_pk_bf16_f32 v52, v20, v24\n"
    "v_subrev_u32 v18, 5, v19\n"
    "s_mov_b32 s30, s94\n"
    "v_mfma_f32_16x16x32_bf16 v[28:31], a[104:107], v[64:67], v[28:31]\n"
    "ds_read_b32 v15, v12\n"
    "s_cmp_lg_u32 s96, 0\n"
    "s_cselect_b32 s7, 10, 12\n"
    "v_mfma_f32_16x16x32_bf16 v[28:31], a[108:111], v[88:91], v[28:31]\n"
    "v_cvt_pk_bf16_f32 v53, v21, v25\n"
    "s_add_u32 s94, s94, s7\n"
    "s_sub_u32 s7, s52, 5\n"
    "v_mfma_f32_16x16x32_bf16 v[28:31], a[112:115], v[112:115], v[28:31]\n"
    "v_cvt_pk_bf16_f32 v54, v22, v26\n"
    "s_andn2_b32 s7, s7, 1\n"
    "v_mfma_f32_16x16x32_bf16 v[28:31], a[116:119], v[136:139], v[28:31]\n"
    "v_cvt_pk_bf16_f32 v55, v23, v27\n"
    "s_waitcnt lgkmcnt(0)\n"
    "v_cmp_lt_u32 vcc, v15, v14\n"
    "v_mfma_f32_16x16x32_bf16 v[28:31], a[120:123], v[160:163], v[28:31]\n"
    "s_cmp_lt_i32 s7, 2\n"
    "s_cselect_b32 s7, 0x1000, s7\n"
    "v_mfma_f32_16x16x32_bf16 v[28:31], a[124:127], v[184:187], v[28:31]\n"
    "s_cbranch_vccnz 81b\n"
    "82:\n"
    "v_mfma_f32_16x16x32_bf16 v[28:31], a[128:131], v[208:211], v[28:31]\n"
    "ds_write2st64_b32 v38, v52, v53 offset0:0 offset1:2\n"
    "v_mfma_f32_16x16x32_bf16 v[28:31], a[132:135], v[232:235], v[28:31]\n"
    "ds_read_b128 a[136:139], v4 offset:12288\n"
    "s_mov_b64 vcc, s[12:13]\n"
    "v_mfma_f32_16x16x32_bf16 v[32:35], a[132:135], v[236:239], 0\n"
    "ds_write2st64_b32 v38, v54, v55 offset0:4 offset1:6\n"
    "v_mfma_f32_16x16x32_bf16 v[32:35], a[128:131], v[212:215], v[32:35]\n"
    "ds_read_b128 a[140:143], v5 offset:12288\n"
    "v_add_u32 v14, 1, v14\n"
    "v_mfma_f32_16x16x32_bf16 v[32:35], a[124:127], v[188:191], v[32:35]\n"
    "buffer_load_dwordx4 v2, s[36:39], s75 offen offset:2048 nt lds\n"
    "v_mfma_f32_16x16x32_bf16 v[32:35], a[120:123], v[164:167], v[32:35]\n"
    "ds_read_b128 a[144:147], v6 offset:12288\n"
    "buffer_load_dword a212, off, s[36:39], s33\n"
    "v_mfma_f32_16x16x32_bf16 v[32:35], a[116:119], v[140:143], v[32:35]\n"
    "buffer_load_dword a212, off, s[36:39], s33\n"
    "buffer_load_dword a212, off, s[36:39], s33\n"
    "v_mfma_f32_16x16x32_bf16 v[32:35], a[112:115], v[116:119], v[32:35]\n"
    "s_cmp_lt_u32 s47, 4\n"
    "s_cselect_b32 s7, 0x1000, s7\n"
    "v_mfma_f32_16x16x32_bf16 v[32:35], a[108:111], v[92:95], v[32:35]\n"
    "ds_read_b128 a[148:151], v7 offset:12288\n"
    "s_cmp_lg_u32 s96, 0\n"
    "s_cselect_b32 s7, 0x1000, s7\n"
    "v_mfma_f32_16x16x32_bf16 v[32:35], a[104:107], v[68:71], v[32:35]\n"
    "s_add_u32 s70, s7, -1\n"
    "v_subrev_u32 v40, s70, v18\n"
    "v_cmp_gt_u32 s[60:61], 2, v40\n"
    "v_mfma_f32_16x16x32_bf16 v[32:35], a[100:103], a[76:79], v[32:35]\n"
    "ds_read_b128 a[152:155], v4 offset:12544\n"
    "s_add_u32 s64, s7, 5\n"
    "s_add_u32 s65, s7, 6\n"
    "v_mfma_f32_16x16x32_bf16 v[32:35], a[96:99], a[52:55], v[32:35]\n"
    "v_cndmask_b32 v18, v18, 63, s[60:61]\n"
    "s_add_u32 s71, s7, 5\n"
    "v_lshlrev_b32 v18, 2, v18\n"
    "v_mfma_f32_16x16x32_bf16 v[32:35], a[92:95], a[28:31], v[32:35]\n"
    "ds_read_b128 a[156:159], v5 offset:12544\n"
    "ds_bpermute_b32 v18, v18, v3\n"
    "v_mfma_f32_16x16x32_bf16 v[32:35], a[88:91], a[4:7], v[32:35]\n"
    "v_readlane_b32 s84, v3, s86\n"
    "v_mfma_f32_16x16x32_bf16 v[20:23], a[88:91], a[8:11], 0\n"
    "ds_read_b128 a[160:163], v6 offset:12544\n"
    "v_mfma_f32_16x16x32_bf16 v[20:23], a[92:95], a[32:35], v[20:23]\n"
    "v_mfma_f32_16x16x32_bf16 v[20:23], a[96:99], a[56:59], v[20:23]\n"
    "ds_read_b128 a[164:167], v7 offset:12544\n"
    "v_mfma_f32_16x16x32_bf16 v[20:23], a[100:103], a[80:83], v[20:23]\n"
    "v_cvt_pk_bf16_f32 v36, v28, v32\n"
    "v_mfma_f32_16x16x32_bf16 v[20:23], a[104:107], v[72:75], v[20:23]\n"
    "ds_read_b128 a[168:171], v4 offset:12800\n"
    "v_mfma_f32_16x16x32_bf16 v[20:23], a[108:111], v[96:99], v[20:23]\n"
    "v_cvt_pk_bf16_f32 v46, v29, v33\n"
    "v_mfma_f32_16x16x32_bf16 v[20:23], a[112:115], v[120:123], v[20:23]\n"
    "ds_read_b128 a[172:175], v5 offset:12800\n"
    "s_add_u32 m0, s57, 49152\n"
    "v_mfma_f32_16x16x32_bf16 v[20:23], a[116:119], v[144:147], v[20:23]\n"
    "buffer_load_dwordx4 v0, s[36:39], s81 offen nt lds\n"
    "v_mfma_f32_16x16x32_bf16 v[20:23], a[120:123], v[168:171], v[20:23]\n"
    "v_cvt_pk_bf16_f32 v37, v30, v34\n"
    "v_mfma_f32_16x16x32_bf16 v[20:23], a[124:127], v[192:195], v[20:23]\n"
    "ds_read_b128 a[176:179], v6 offset:12800\n"
    "v_mfma_f32_16x16x32_bf16 v[20:23], a[128:131], v[216:219], v[20:23]\n"
    "v_cvt_pk_bf16_f32 v50, v31, v35\n"
    "v_mfma_f32_16x16x32_bf16 v[20:23], a[132:135], v[240:243], v[20:23]\n"
    "ds_read_b128 a[180:183], v7 offset:12800\n"
    "v_mfma_f32_16x16x32_bf16 v[24:27], a[132:135], v[244:247], 0\n"
    "buffer_load_dwordx4 v1, s[36:39], s81 offen offset:1024 nt lds\n"
    "v_mfma_f32_16x16x32_bf16 v[24:27], a[128:131], v[220:223], v[24:27]\n"
    "v_cndmask_b32_dpp v44, v46, v36, vcc row_ror:8 row_mask:0xf bank_mask:0xf\n"
    "v_mfma_f32_16x16x32_bf16 v[24:27], a[124:127], v[196:199], v[24:27]\n"
    "buffer_load_dwordx4 v2, s[36:39], s81 offen offset:2048 nt lds\n"
    "v_mfma_f32_16x16x32_bf16 v[24:27], a[120:123], v[172:175], v[24:27]\n"
    "s_memrealtime s[92:93]\n"
    "buffer_load_dword a212, off, s[36:39], s33\n"
    "v_mfma_f32_16x16x32_bf16 v[24:27], a[116:119], v[148:151], v[24:27]\n"
    "v_cndmask_b32_dpp v48, v50, v37, vcc row_ror:8 row_mask:0xf bank_mask:0xf\n"
    "v_mfma_f32_16x16x32_bf16 v[24:27], a[112:115], v[124:127], v[24:27]\n"
    "v_mov_b32_dpp v46, v36 row_ror:8 row_mask:0xf bank_mask:0x3\n"
    "v_mfma_f32_16x16x32_bf16 v[24:27], a[108:111], v[100:103], v[24:27]\n"
    "v_mov_b32_dpp v50, v37 row_ror:8 row_mask:0xf bank_mask:0x3\n"
    "v_mfma_f32_16x16x32_bf16 v[24:27], a[104:107], v[76:79], v[24:27]\n"
    "v_mfma_f32_16x16x32_bf16 v[24:27], a[100:103], a[84:87], v[24:27]\n"
    "s_waitcnt vmcnt(10) lgkmcnt(0)\n"
    "s_sub_u32 s85, s92, s89\n"
    "v_mfma_f32_16x16x32_bf16 v[24:27], a[96:99], a[60:63], v[24:27]\n"
    "s_add_i32 s63, s63, s85\n"
    "s_max_i32 s63, s63, s62\n"
    "s_add_u32 s89, s89, s63\n"
    "v_mfma_f32_16x16x32_bf16 v[24:27], a[92:95], a[36:39], v[24:27]\n"
    "s_mov_b32 s81, s84\n"
    "s_add_u32 m0, s57, 61440\n"
    "v_mfma_f32_16x16x32_bf16 v[24:27], a[88:91], a[12:15], v[24:27]\n"
    "s_branch 52f\n"
    ".p2align 6\n"
    "51:\n"
    "v_mfma_f32_16x16x32_bf16 v[20:23], a[88:91], a[16:19], 0\n"
    "s_add_u32 s86, s86, 1\n"
    "v_mfma_f32_16x16x32_bf16 v[20:23], a[92:95], a[40:43], v[20:23]\n"
    "s_waitcnt vmcnt(13)\n"
    "v_mfma_f32_16x16x32_bf16 v[20:23], a[96:99], a[64:67], v[20:23]\n"
    "ds_write_b32 v13, v14\n"
    "v_mfma_f32_16x16x32_bf16 v[20:23], a[100:103], v[56:59], v[20:23]\n"
    "v_cvt_pk_bf16_f32 v36, v28, v32\n"
    "v_mfma_f32_16x16x32_bf16 v[20:23], a[104:107], v[80:83], v[20:23]\n"
    "v_cvt_pk_bf16_f32 v47, v29, v33\n"
    "v_mfma_f32_16x16x32_bf16 v[20:23], a[108:111], v[104:107], v[20:23]\n"
    "v_cvt_pk_bf16_f32 v37, v30, v34\n"
    "v_mfma_f32_16x16x32_bf16 v[20:23], a[112:115], v[128:131], v[20:23]\n"
    "v_cvt_pk_bf16_f32 v51, v31, v35\n"
    "v_mfma_f32_16x16x32_bf16 v[20:23], a[116:119], v[152:155], v[20:23]\n"
    "v_cndmask_b32_dpp v45, v47, v36, vcc row_ror:8 row_mask:0xf bank_mask:0xf\n"
    "v_mfma_f32_16x16x32_bf16 v[20:23], a[120:123], v[176:179], v[20:23]\n"
    "v_cndmask_b32_dpp v49, v51, v37, vcc row_ror:8 row_mask:0xf bank_mask:0xf\n"
    "v_mfma_f32_16x16x32_bf16 v[20:23], a[124:127], v[200:203], v[20:23]\n"
    "v_mov_b32_dpp v47, v36 row_ror:8 row_mask:0xf bank_mask:0x3\n"
    "v_mfma_f32_16x16x32_bf16 v[20:23], a[128:131], v[224:227], v[20:23]\n"
    "v_mov_b32_dpp v51, v37 row_ror:8 row_mask:0xf bank_mask:0x3\n"
    "v_mfma_f32_16x16x32_bf16 v[20:23], a[132:135], v[248:251], v[20:23]\n"
    "ds_read_b32 v15, v12\n"
    "v_mfma_f32_16x16x32_bf16 v[24:27], a[132:135], v[252:255], 0\n"
    "s_add_u32 s89, s89, s94\n"
    "v_mfma_f32_16x16x32_bf16 v[24:27], a[128:131], v[228:231], v[24:27]\n"
    "s_sub_u32 s94, s94, 6\n"
    "v_mfma_f32_16x16x32_bf16 v[24:27], a[124:127], v[204:207], v[24:27]\n"
    "buffer_store_dwordx4 v[44:47], v8, s[40:43], s82 offen sc1\n"
    "v_mfma_f32_16x16x32_bf16 v[24:27], a[120:123], v[180:183], v[24:27]\n"
    "s_max_u32 s94, s94, s30\n"
    "v_mfma_f32_16x16x32_bf16 v[24:27], a[116:119], v[156:159], v[24:27]\n"
    "s_waitcnt lgkmcnt(0)\n"
    "v_cmp_lt_u32 vcc, v15, v14\n"
    "v_mfma_f32_16x16x32_bf16 v[24:27], a[112:115], v[132:135], v[24:27]\n"
    "v_mfma_f32_16x16x32_bf16 v[24:27], a[108:111], v[108:111], v[24:27]\n"
    "s_cbranch_vccnz 41b\n"
    "42:\n"
    "v_mfma_f32_16x16x32_bf16 v[24:27], a[104:107], v[84:87], v[24:27]\n"
    "ds_read_b128 a[184:187], v39 offset:4096\n"
    "v_mfma_f32_16x16x32_bf16 v[24:27], a[100:103], v[60:63], v[24:27]\n"
    "s_cmp_eq_u32 s86, s64\n"
    "s_cbranch_scc1 45b\n"
    "v_mfma_f32_16x16x32_bf16 v[24:27], a[96:99], a[68:71], v[24:27]\n"
    "ds_read_b128 a[136:139], v4 offset:12288\n"
    "v_mfma_f32_16x16x32_bf16 v[24:27], a[92:95], a[44:47], v[24:27]\n"
    "v_add_u32 v14, 1, v14\n"
    "v_mfma_f32_16x16x32_bf16 v[24:27], a[88:91], a[20:23], v[24:27]\n"
    "s_mov_b64 vcc, s[12:13]\n"
    "v_mfma_f32_16x16x32_bf16 v[28:31], a[88:91], a[0:3], 0\n"
    "buffer_load_dwordx4 v0, s[36:39], s81 offen nt lds\n"
    "v_mfma_f32_16x16x32_bf16 v[28:31], a[92:95], a[24:27], v[28:31]\n"
    "ds_read_b128 a[140:143], v5 offset:12288\n"
    "v_mfma_f32_16x16x32_bf16 v[28:31], a[96:99], a[48:51], v[28:31]\n"
    "ds_read_b128 a[144:147], v6 offset:12288\n"
    "v_mfma_f32_16x16x32_bf16 v[28:31], a[100:103], a[72:75], v[28:31]\n"
    "v_cvt_pk_bf16_f32 v52, v20, v24\n"
    "v_mfma_f32_16x16x32_bf16 v[28:31], a[104:107], v[64:67], v[28:31]\n"
    "v_cvt_pk_bf16_f32 v53, v21, v25\n"
    "v_mfma_f32_16x16x32_bf16 v[28:31], a[108:111], v[88:91], v[28:31]\n"
    "v_cvt_pk_bf16_f32 v54, v22, v26\n"
    "v_mfma_f32_16x16x32_bf16 v[28:31], a[112:115], v[112:115], v[28:31]\n"
    "v_cvt_pk_bf16_f32 v55, v23, v27\n"
    "v_mfma_f32_16x16x32_bf16 v[28:31], a[116:119], v[136:139], v[28:31]\n"
    "ds_read_b128 a[148:151], v7 offset:12288\n"
    "v_mfma_f32_16x16x32_bf16 v[28:31], a[120:123], v[160:163], v[28:31]\n"
    "v_mfma_f32_16x16x32_bf16 v[28:31], a[124:127], v[184:187], v[28:31]\n"
    "buffer_load_dwordx4 v1, s[36:39], s81 offen offset:1024 nt lds\n"
    "v_mfma_f32_16x16x32_bf16 v[28:31], a[128:131], v[208:211], v[28:31]\n"
    "v_mfma_f32_16x16x32_bf16 v[28:31], a[132:135], v[232:235], v[28:31]\n"
    "ds_read_b128 a[152:155], v4 offset:12544\n"
    "v_mfma_f32_16x16x32_bf16 v[32:35], a[132:135], v[236:239], 0\n"
    "v_mfma_f32_16x16x32_bf16 v[32:35], a[128:131], v[212:215], v[32:35]\n"
    "ds_write2st64_b32 v38, v52, v53 offset0:0 offset1:2\n"
    "v_mfma_f32_16x16x32_bf16 v[32:35], a[124:127], v[188:191], v[32:35]\n"
    "46:\n"
    "v_mfma_f32_16x16x32_bf16 v[32:35], a[120:123], v[164:167], v[32:35]\n"
    "ds_read_b128 a[156:159], v5 offset:12544\n"
    "v_mfma_f32_16x16x32_bf16 v[32:35], a[116:119], v[140:143], v[32:35]\n"
    "v_mfma_f32_16x16x32_bf16 v[32:35], a[112:115], v[116:119], v[32:35]\n"
    "v_mfma_f32_16x16x32_bf16 v[32:35], a[108:111], v[92:95], v[32:35]\n"
    "buffer_store_dwordx4 v[48:51], v9, s[40:43], s82 offen sc1\n"
    "v_mfma_f32_16x16x32_bf16 v[32:35], a[104:107], v[68:71], v[32:35]\n"
    "v_mfma_f32_16x16x32_bf16 v[32:35], a[100:103], a[76:79], v[32:35]\n"
    "ds_read_b128 a[160:163], v6 offset:12544\n"
    "v_mfma_f32_16x16x32_bf16 v[32:35], a[96:99], a[52:55], v[32:35]\n"
    "ds_read_b128 a[164:167], v7 offset:12544\n"
    "v_mfma_f32_16x16x32_bf16 v[32:35], a[92:95], a[28:31], v[32:35]\n"
    "ds_read_b128 a[168:171], v4 offset:12800\n"
    "v_mfma_f32_16x16x32_bf16 v[32:35], a[88:91], a[4:7], v[32:35]\n"
    "v_mfma_f32_16x16x32_bf16 v[20:23], a[88:91], a[8:11], 0\n"
    "ds_write2st64_b32 v38, v54, v55 offset0:4 offset1:6\n"
    "v_mfma_f32_16x16x32_bf16 v[20:23], a[92:95], a[32:35], v[20:23]\n"
    "v_mfma_f32_16x16x32_bf16 v[20:23], a[96:99], a[56:59], v[20:23]\n"
    "ds_read_b128 a[172:175], v5 offset:12800\n"
    "v_mfma_f32_16x16x32_bf16 v[20:23], a[100:103], a[80:83], v[20:23]\n"
    "v_cvt_pk_bf16_f32 v36, v28, v32\n"
    "v_mfma_f32_16x16x32_bf16 v[20:23], a[104:107], v[72:75], v[20:23]\n"
    "v_cvt_pk_bf16_f32 v46, v29, v33\n"
    "v_mfma_f32_16x16x32_bf16 v[20:23], a[108:111], v[96:99], v[20:23]\n"
    "ds_read_b128 a[176:179], v6 offset:12800\n"
    "v_mfma_f32_16x16x32_bf16 v[20:23], a[112:115], v[120:123], v[20:23]\n"
    "v_cvt_pk_bf16_f32 v37, v30, v34\n"
    "v_mfma_f32_16x16x32_bf16 v[20:23], a[116:119], v[144:147], v[20:23]\n"
    "v_cvt_pk_bf16_f32 v50, v31, v35\n"
    "v_mfma_f32_16x16x32_bf16 v[20:23], a[120:123], v[168:171], v[20:23]\n"
    "buffer_load_dwordx4 v2, s[36:39], s81 offen offset:2048 nt lds\n"
    "v_mfma_f32_16x16x32_bf16 v[20:23], a[124:127], v[192:195], v[20:23]\n"
    "v_cndmask_b32_dpp v44, v46, v36, vcc row_ror:8 row_mask:0xf bank_mask:0xf\n"
    "v_mfma_f32_16x16x32_bf16 v[20:23], a[128:131], v[216:219], v[20:23]\n"
    "ds_read_b128 a[180:183], v7 offset:12800\n"
    "v_mfma_f32_16x16x32_bf16 v[20:23], a[132:135], v[240:243], v[20:23]\n"
    "v_cndmask_b32_dpp v48, v50, v37, vcc row_ror:8 row_mask:0xf bank_mask:0xf\n"
    "v_mfma_f32_16x16x32_bf16 v[24:27], a[132:135], v[244:247], 0\n"
    "s_memrealtime s[92:93]\n"
    "v_mfma_f32_16x16x32_bf16 v[24:27], a[128:131], v[220:223], v[24:27]\n"
    "v_mov_b32_dpp v46, v36 row_ror:8 row_mask:0xf bank_mask:0x3\n"
    "v_mfma_f32_16x16x32_bf16 v[24:27], a[124:127], v[196:199], v[24:27]\n"
    "v_mov_b32_dpp v50, v37 row_ror:8 row_mask:0xf bank_mask:0x3\n"
    "v_mfma_f32_16x16x32_bf16 v[24:27], a[120:123], v[172:175], v[24:27]\n"
    "v_readlane_b32 s81, v3, s86\n"
    "v_mfma_f32_16x16x32_bf16 v[24:27], a[116:119], v[148:151], v[24:27]\n"
    "s_waitcnt lgkmcnt(14)\n"
    "buffer_store_dwordx4 a[184:187], v10, s[40:43], s82 offen sc1\n"
    "v_mfma_f32_16x16x32_bf16 v[24:27], a[112:115], v[124:127], v[24:27]\n"
    "s_addk_i32 m0, 12288\n"
    "v_mfma_f32_16x16x32_bf16 v[24:27], a[108:111], v[100:103], v[24:27]\n"
    "v_readlane_b32 s82, v18, s86\n"
    "v_mfma_f32_16x16x32_bf16 v[24:27], a[104:107], v[76:79], v[24:27]\n"
    "s_waitcnt lgkmcnt(0)\n"
    "s_sub_u32 s85, s92, s89\n"
    "v_mfma_f32_16x16x32_bf16 v[24:27], a[100:103], a[84:87], v[24:27]\n"
    "s_cbranch_scc1 43b\n"
    "44:\n"
    "v_mfma_f32_16x16x32_bf16 v[24:27], a[96:99], a[60:63], v[24:27]\n"
    "s_cmp_eq_u32 m0, s77\n"
    "s_cselect_b32 s78, s87, s88\n"
    "v_mfma_f32_16x16x32_bf16 v[24:27], a[92:95], a[36:39], v[24:27]\n"
    "s_cmp_lt_u32 s86, s76\n"
    "v_mfma_f32_16x16x32_bf16 v[24:27], a[88:91], a[12:15], v[24:27]\n"
    "s_cbranch_scc0 55f\n"
    "52:\n"
    "v_mfma_f32_16x16x32_bf16 v[28:31], a[136:139], a[16:19], 0\n"
    "s_add_u32 s86, s86, 1\n"
    "v_mfma_f32_16x16x32_bf16 v[28:31], a[140:143], a[40:43], v[28:31]\n"
    "s_waitcnt vmcnt(13)\n"
    "v_mfma_f32_16x16x32_bf16 v[28:31], a[144:147], a[64:67], v[28:31]\n"
    "ds_write_b32 v13, v14\n"
    "v_mfma_f32_16x16x32_bf16 v[28:31], a[148:151], v[56:59], v[28:31]\n"
    "v_cvt_pk_bf16_f32 v36, v20, v24\n"
    "v_mfma_f32_16x16x32_bf16 v[28:31], a[152:155], v[80:83], v[28:31]\n"
    "v_cvt_pk_bf16_f32 v47, v21, v25\n"
    "v_mfma_f32_16x16x32_bf16 v[28:31], a[156:159], v[104:107], v[28:31]\n"
    "v_cvt_pk_bf16_f32 v37, v22, v26\n"
    "v_mfma_f32_16x16x32_bf16 v[28:31], a[160:163], v[128:131], v[28:31]\n"
    "v_cvt_pk_bf16_f32 v51, v23, v27\n"
    "v_mfma_f32_16x16x32_bf16 v[28:31], a[164:167], v[152:155], v[28:31]\n"
    "v_cndmask_b32_dpp v45, v47, v36, vcc row_ror:8 row_mask:0xf bank_mask:0xf\n"
    "v_mfma_f32_16x16x32_bf16 v[28:31], a[168:171], v[176:179], v[28:31]\n"
    "v_cndmask_b32_dpp v49, v51, v37, vcc row_ror:8 row_mask:0xf bank_mask:0xf\n"
    "v_mfma_f32_16x16x32_bf16 v[28:31], a[172:175], v[200:203], v[28:31]\n"
    "v_mov_b32_dpp v47, v36 row_ror:8 row_mask:0xf bank_mask:0x3\n"
    "v_mfma_f32_16x16x32_bf16 v[28:31], a[176:179], v[224:227], v[28:31]\n"
    "v_mov_b32_dpp v51, v37 row_ror:8 row_mask:0xf bank_mask:0x3\n"
    "v_mfma_f32_16x16x32_bf16 v[28:31], a[180:183], v[248:251], v[28:31]\n"
    "ds_read_b32 v15, v12\n"
    "v_mfma_f32_16x16x32_bf16 v[32:35], a[180:183], v[252:255], 0\n"
    "s_add_u32 s89, s89, s94\n"
    "v_mfma_f32_16x16x32_bf16 v[32:35], a[176:179], v[228:231], v[32:35]\n"
    "v_add_u32 v4, s78, v4\n"
    "v_mfma_f32_16x16x32_bf16 v[32:35], a[172:175], v[204:207], v[32:35]\n"
    "buffer_store_dwordx4 v[44:47], v8, s[40:43], s82 offen sc1\n"
    "v_mfma_f32_16x16x32_bf16 v[32:35], a[168:171], v[180:183], v[32:35]\n"
    "v_add_u32 v5, s78, v5\n"
    "v_mfma_f32_16x16x32_bf16 v[32:35], a[164:167], v[156:159], v[32:35]\n"
    "s_waitcnt lgkmcnt(0)\n"
    "v_cmp_lt_u32 vcc, v15, v14\n"
    "v_mfma_f32_16x16x32_bf16 v[32:35], a[160:163], v[132:135], v[32:35]\n"
    "v_add_u32 v6, s78, v6\n"
    "v_mfma_f32_16x16x32_bf16 v[32:35], a[156:159], v[108:111], v[32:35]\n"
    "s_cbranch_vccnz 61b\n"
    "62:\n"
    "v_mfma_f32_16x16x32_bf16 v[32:35], a[152:155], v[84:87], v[32:35]\n"
    "ds_read_b128 a[184:187], v39\n"
    "v_mfma_f32_16x16x32_bf16 v[32:35], a[148:151], v[60:63], v[32:35]\n"
    "s_cmp_eq_u32 s86, s65\n"
    "s_cbranch_scc1 65b\n"
    "v_mfma_f32_16x16x32_bf16 v[32:35], a[144:147], a[68:71], v[32:35]\n"
    "ds_read_b128 a[88:91], v4\n"
    "v_mfma_f32_16x16x32_bf16 v[32:35], a[140:143], a[44:47], v[32:35]\n"
    "v_add_u32 v7, s78, v7\n"
    "v_mfma_f32_16x16x32_bf16 v[32:35], a[136:139], a[20:23], v[32:35]\n"
    "v_add_u32 v14, 1, v14\n"
    "v_mfma_f32_16x16x32_bf16 v[20:23], a[136:139], a[0:3], 0\n"
    "buffer_load_dwordx4 v0, s[36:39], s81 offen nt lds\n"
    "v_mfma_f32_16x16x32_bf16 v[20:23], a[140:143], a[24:27], v[20:23]\n"
    "ds_read_b128 a[92:95], v5\n"
    "v_mfma_f32_16x16x32_bf16 v[20:23], a[144:147], a[48:51], v[20:23]\n"
    "ds_read_b128 a[96:99], v6\n"
    "v_mfma_f32_16x16x32_bf16 v[20:23], a[148:151], a[72:75], v[20:23]\n"
    "v_cvt_pk_bf16_f32 v52, v28, v32\n"
    "v_mfma_f32_16x16x32_bf16 v[20:23], a[152:155], v[64:67], v[20:23]\n"
    "v_cvt_pk_bf16_f32 v53, v29, v33\n"
    "v_mfma_f32_16x16x32_bf16 v[20:23], a[156:159], v[88:91], v[20:23]\n"
    "v_cvt_pk_bf16_f32 v54, v30, v34\n"
    "v_mfma_f32_16x16x32_bf16 v[20:23], a[160:163], v[112:115], v[20:23]\n"
    "v_cvt_pk_bf16_f32 v55, v31, v35\n"
    "v_mfma_f32_16x16x32_bf16 v[20:23], a[164:167], v[136:139], v[20:23]\n"
    "ds_read_b128 a[100:103], v7\n"
    "v_mfma_f32_16x16x32_bf16 v[20:23], a[168:171], v[160:163], v[20:23]\n"
    "s_mov_b64 vcc, s[12:13]\n"
    "v_mfma_f32_16x16x32_bf16 v[20:23], a[172:175], v[184:187], v[20:23]\n"
    "buffer_load_dwordx4 v1, s[36:39], s81 offen offset:1024 nt lds\n"
    "v_mfma_f32_16x16x32_bf16 v[20:23], a[176:179], v[208:211], v[20:23]\n"
    "v_mfma_f32_16x16x32_bf16 v[20:23], a[180:183], v[232:235], v[20:23]\n"
    "ds_read_b128 a[104:107], v4 offset:256\n"
    "v_mfma_f32_16x16x32_bf16 v[24:27], a[180:183], v[236:239], 0\n"
    "v_mfma_f32_16x16x32_bf16 v[24:27], a[176:179], v[212:215], v[24:27]\n"
    "ds_write2st64_b32 v38, v52, v53 offset0:16 offset1:18\n"
    "v_mfma_f32_16x16x32_bf16 v[24:27], a[172:175], v[188:191], v[24:27]\n"
    "66:\n"
    "v_mfma_f32_16x16x32_bf16 v[24:27], a[168:171], v[164:167], v[24:27]\n"
    "ds_read_b128 a[108:111], v5 offset:256\n"
    "v_mfma_f32_16x16x32_bf16 v[24:27], a[164:167], v[140:143], v[24:27]\n"
    "s_cmp_eq_u32 s86, 20\n"
    "v_mfma_f32_16x16x32_bf16 v[24:27], a[160:163], v[116:119], v[24:27]\n"
    "s_cselect_b32 s30, s31, s30\n"
    "v_mfma_f32_16x16x32_bf16 v[24:27], a[156:159], v[92:95], v[24:27]\n"
    "buffer_store_dwordx4 v[48:51], v9, s[40:43], s82 offen sc1\n"
    "v_mfma_f32_16x16x32_bf16 v[24:27], a[152:155], v[68:71], v[24:27]\n"
    "v_mfma_f32_16x16x32_bf16 v[24:27], a[148:151], a[76:79], v[24:27]\n"
    "ds_read_b128 a[112:115], v6 offset:256\n"
    "v_mfma_f32_16x16x32_bf16 v[24:27], a[144:147], a[52:55], v[24:27]\n"
    "ds_read_b128 a[116:119], v7 offset:256\n"
    "v_mfma_f32_16x16x32_bf16 v[24:27], a[140:143], a[28:31], v[24:27]\n"
    "ds_read_b128 a[120:123], v4 offset:512\n"
    "v_mfma_f32_16x16x32_bf16 v[24:27], a[136:139], a[4:7], v[24:27]\n"
    "v_mfma_f32_16x16x32_bf16 v[28:31], a[136:139], a[8:11], 0\n"
    "ds_write2st64_b32 v38, v54, v55 offset0:20 offset1:22\n"
    "v_mfma_f32_16x16x32_bf16 v[28:31], a[140:143], a[32:35], v[28:31]\n"
    "v_mfma_f32_16x16x32_bf16 v[28:31], a[144:147], a[56:59], v[28:31]\n"
    "ds_read_b128 a[124:127], v5 offset:512\n"
    "v_mfma_f32_16x16x32_bf16 v[28:31], a[148:151], a[80:83], v[28:31]\n"
    "v_cvt_pk_bf16_f32 v36, v20, v24\n"
    "v_mfma_f32_16x16x32_bf16 v[28:31], a[152:155], v[72:75], v[28:31]\n"
    "v_cvt_pk_bf16_f32 v46, v21, v25\n"
    "v_mfma_f32_16x16x32_bf16 v[28:31], a[156:159], v[96:99], v[28:31]\n"
    "ds_read_b128 a[128:131], v6 offset:512\n"
    "v_mfma_f32_16x16x32_bf16 v[28:31], a[160:163], v[120:123], v[28:31]\n"
    "v_cvt_pk_bf16_f32 v37, v22, v26\n"
    "v_mfma_f32_16x16x32_bf16 v[28:31], a[164:167], v[144:147], v[28:31]\n"
    "v_cvt_pk_bf16_f32 v50, v23, v27\n"
    "v_mfma_f32_16x16x32_bf16 v[28:31], a[168:171], v[168:171], v[28:31]\n"
    "buffer_load_dwordx4 v2, s[36:39], s81 offen offset:2048 nt lds\n"
    "v_mfma_f32_16x16x32_bf16 v[28:31], a[172:175], v[192:195], v[28:31]\n"
    "v_cndmask_b32_dpp v44, v46, v36, vcc row_ror:8 row_mask:0xf bank_mask:0xf\n"
    "v_mfma_f32_16x16x32_bf16 v[28:31], a[176:179], v[216:219], v[28:31]\n"
    "ds_read_b128 a[132:135], v7 offset:512\n"
    "v_mfma_f32_16x16x32_bf16 v[28:31], a[180:183], v[240:243], v[28:31]\n"
    "v_cndmask_b32_dpp v48, v50, v37, vcc row_ror:8 row_mask:0xf bank_mask:0xf\n"
    "v_mfma_f32_16x16x32_bf16 v[32:35], a[180:183], v[244:247], 0\n"
    "s_memrealtime s[92:93]\n"
    "v_mfma_f32_16x16x32_bf16 v[32:35], a[176:179], v[220:223], v[32:35]\n"
    "v_mov_b32_dpp v46, v36 row_ror:8 row_mask:0xf bank_mask:0x3\n"
    "v_mfma_f32_16x16x32_bf16 v[32:35], a[172:175], v[196:199], v[32:35]\n"
    "v_mov_b32_dpp v50, v37 row_ror:8 row_mask:0xf bank_mask:0x3\n"
    "v_mfma_f32_16x16x32_bf16 v[32:35], a[168:171], v[172:175], v[32:35]\n"
    "v_readlane_b32 s81, v3, s86\n"
    "v_mfma_f32_16x16x32_bf16 v[32:35], a[164:167], v[148:151], v[32:35]\n"
    "s_waitcnt lgkmcnt(14)\n"
    "buffer_store_dwordx4 a[184:187], v10, s[40:43], s82 offen sc1\n"
    "v_mfma_f32_16x16x32_bf16 v[32:35], a[160:163], v[124:127], v[32:35]\n"
    "s_addk_i32 m0, 12288\n"
    "v_mfma_f32_16x16x32_bf16 v[32:35], a[156:159], v[100:103], v[32:35]\n"
    "v_readlane_b32 s82, v18, s86\n"
    "v_mfma_f32_16x16x32_bf16 v[32:35], a[152:155], v[76:79], v[32:35]\n"
    "s_waitcnt lgkmcnt(0)\n"
    "s_sub_u32 s85, s92, s89\n"
    "v_mfma_f32_16x16x32_bf16 v[32:35], a[148:151], a[84:87], v[32:35]\n"
    "s_cbranch_scc1 63b\n"
    "64:\n"
    "v_mfma_f32_16x16x32_bf16 v[32:35], a[144:147], a[60:63], v[32:35]\n"
    "s_cmp_eq_u32 m0, s80\n"
    "s_cselect_b32 m0, s57, m0\n"
    "v_mfma_f32_16x16x32_bf16 v[32:35], a[140:143], a[36:39], v[32:35]\n"
    "s_cmp_lt_u32 s86, s76\n"
    "v_mfma_f32_16x16x32_bf16 v[32:35], a[136:139], a[12:15], v[32:35]\n"
    "s_cbranch_scc1 51b\n"
    "s_barrier\n"
    "ds_read_b128 a[184:187], v39 offset:4096\n"
    "s_nop 4\n"
    "v_cvt_pk_bf16_f32 v36, v28, v32\n"
    "v_cvt_pk_bf16_f32 v47, v29, v33\n"
    "v_cvt_pk_bf16_f32 v37, v30, v34\n"
    "v_cvt_pk_bf16_f32 v51, v31, v35\n"
    "s_branch 56f\n"
    "55:\n"
    "s_barrier\n"
    "ds_read_b128 a[184:187], v39\n"
    "s_nop 4\n"
    "v_cvt_pk_bf16_f32 v36, v20, v24\n"
    "v_cvt_pk_bf16_f32 v47, v21, v25\n"
    "v_cvt_pk_bf16_f32 v37, v22, v26\n"
    "v_cvt_pk_bf16_f32 v51, v23, v27\n"
    "56:\n"
    "v_cndmask_b32_dpp v45, v47, v36, vcc row_ror:8 row_mask:0xf bank_mask:0xf\n"
    "v_mov_b32_dpp v47, v36 row_ror:8 row_mask:0xf bank_mask:0x3\n"
    "v_cndmask_b32_dpp v49, v51, v37, vcc row_ror:8 row_mask:0xf bank_mask:0xf\n"
    "buffer_store_dwordx4 v[44:47], v8, s[40:43], s82 offen sc1\n"
    "v_mov_b32_dpp v51, v37 row_ror:8 row_mask:0xf bank_mask:0x3\n"
    "s_waitcnt lgkmcnt(0)\n"
    "buffer_store_dwordx4 a[184:187], v10, s[40:43], s82 offen sc1\n"
    "buffer_store_dwordx4 v[48:51], v9, s[40:43], s82 offen sc1\n"
    "s_waitcnt vmcnt(0) lgkmcnt(0)\n"
    "s_endpgm\n"
    "75:\n"
    "s_sleep 2\n"
    "global_load_dwordx4 v[236:239], v16, s[54:55] sc1\n"
    "s_waitcnt vmcnt(0)\n"
    "v_and_b32 v242, 15, v237\n"
    "v_and_b32 v243, 1, v239\n"
    "v_or_b32 v237, 15, v237\n"
    "v_or_b32 v239, 15, v239\n"
    "v_cmp_ne_u64 vcc, s[18:19], v[236:237]\n"
    "v_cmp_ne_u64 s[48:49], s[18:19], v[238:239]\n"
    "v_readlane_b32 s9, v242, 0\n"
    "v_and_b32 v236, 1, v242\n"
    "s_or_b64 s[48:49], s[48:49], vcc\n"
    "s_cbranch_scc1 75b\n"
    "s_branch 77b\n"
    "79:\n"
    "s_nop 0\n"
    : "+{s[0:1]}"(dp), "+{s[2:3]}"(ka), "+{s[4:5]}"(did), "+{s6}"(bid), "+{v0}"(tid)
    : [lds] "{s98}"(lds0)
    : "v1","v2","v3","v4","v5","v6","v7","v8","v9","v10","v11","v12","v13","v14","v15","v16","v17","v18","v19","v20","v21","v22","v23","v24","v25","v26","v27","v28","v29","v30","v31","v32","v33","v34","v35","v36","v37","v38","v39","v40","v41","v42","v43","v44","v45","v46","v47","v48","v49","v50","v51","v52","v53","v54","v55","v56","v57","v58","v59","v60","v61","v62","v63","v64","v65","v66","v67","v68","v69","v70","v71","v72","v73","v74","v75","v76","v77","v78","v79","v80","v81","v82","v83","v84","v85","v86","v87","v88","v89","v90","v91","v92","v93","v94","v95","v96","v97","v98","v99","v100","v101","v102","v103","v104","v105","v106","v107","v108","v109","v110","v111","v112","v113","v114","v115","v116","v117","v118","v119","v120","v121","v122","v123","v124","v125","v126","v127","v128","v129","v130","v131","v132","v133","v134","v135","v136","v137","v138","v139","v140","v141","v142","v143","v144","v145","v146","v147","v148","v149","v150","v151","v152","v153","v154","v155","v156","v157","v158","v159","v160","v161","v162","v163","v164","v165","v166","v167","v168","v169","v170","v171","v172","v173","v174","v175","v176","v177","v178","v179","v180","v181","v182","v183","v184","v185","v186","v187","v188","v189","v190","v191","v192","v193","v194","v195","v196","v197","v198","v199","v200","v201","v202","v203","v204","v205","v206","v207","v208","v209","v210","v211","v212","v213","v214","v215","v216","v217","v218","v219","v220","v221","v222","v223","v224","v225","v226","v227","v228","v229","v230","v231","v232","v233","v234","v235","v236","v237","v238","v239","v240","v241","v242","v243","v244","v245","v246","v247","v248","v249","v250","v251","v252","v253","v254","v255","a0","a1","a2","a3","a4","a5","a6","a7","a8","a9","a10","a11","a12","a13","a14","a15","a16","a17","a18","a19","a20","a21","a22","a23","a24","a25","a26","a27","a28","a29","a30","a31","a32","a33","a34","a35","a36","a37","a38","a39","a40","a41","a42","a43","a44","a45","a46","a47","a48","a49","a50","a51","a52","a53","a54","a55","a56","a57","a58","a59","a60","a61","a62","a63","a64","a65","a66","a67","a68","a69","a70","a71","a72","a73","a74","a75","a76","a77","a78","a79","a80","a81","a82","a83","a84","a85","a86","a87","a88","a89","a90","a91","a92","a93","a94","a95","a96","a97","a98","a99","a100","a101","a102","a103","a104","a105","a106","a107","a108","a109","a110","a111","a112","a113","a114","a115","a116","a117","a118","a119","a120","a121","a122","a123","a124","a125","a126","a127","a128","a129","a130","a131","a132","a133","a134","a135","a136","a137","a138","a139","a140","a141","a142","a143","a144","a145","a146","a147","a148","a149","a150","a151","a152","a153","a154","a155","a156","a157","a158","a159","a160","a161","a162","a163","a164","a165","a166","a167","a168","a169","a170","a171","a172","a173","a174","a175","a176","a177","a178","a179","a180","a181","a182","a183","a184","a185","a186","a187","a188","a189","a190","a191","a192","a193","a194","a195","a196","a197","a198","a199","a200","a201","a202","a203","a204","a205","a206","a207","a208","a209","a210","a211","a212","s7","s8","s9","s10","s11","s12","s13","s14","s15","s16","s17","s18","s19","s20","s21","s22","s23","s24","s25","s26","s27","s28","s29","s30","s31","s33","s34","s35","s36","s37","s38","s39","s40","s41","s42","s43","s44","s45","s46","s47","s48","s49","s50","s51","s52","s53","s54","s55","s56","s57","s58","s59","s60","s61","s62","s63","s64","s65","s66","s67","s68","s69","s70","s71","s72","s73","s74","s75","s76","s77","s78","s79","s80","s81","s82","s83","s84","s85","s86","s87","s88","s89","s90","s91","s92","s93","s94","s95","s96","s97","vcc","scc","memory");
}

extern "C" void kernel_launch(void* const* d_in, const int* in_sizes, int n_in,
                              void* d_out, int out_size, void* d_ws, size_t ws_size,
                              hipStream_t stream) {
  Shapes sh;
  for (int j = 0; j < 16; ++j)
    for (int w = 0; w < 4; ++w) {
      const int ty = j >= 11, per = ty ? 10 : 11, n = 16 * (j - 11 * ty);
      auto row = [&](int k) { return ty ? 64 * (k / 10) + 11 + k % 10 : 64 * (k / 22) + 21 * (k / 11 % 2) + k % 11; };
      const int n0 = n + 4 * w, bp = per - n0 % per;
      uint32_t* e = sh.v + 16 * j + 4 * w;
      e[0] = row(n0) * 768 - 512;
      e[1] = (row(n0 + bp) - bp) * 768 - 512;
      e[2] = bp * 768;
      e[3] = (row(n + 8) + 16) * 768 + ty;
      for (int i = 0; i < 4; ++i) sh.rows[16 * j + 4 * w + i] = row(n0 + i) * 768;
    }
  const int grid = 256, maxr = 40;
  const size_t M = (size_t)in_sizes[0] / CH;
  const uint32_t fl = 0x00020000u;
  for (size_t r0 = 0; r0 < M; r0 += (size_t)maxr * grid * TM) {
    const size_t rows = M - r0 < (size_t)maxr * grid * TM ? M - r0 : (size_t)maxr * grid * TM;
    const int nr = (int)((rows + grid * TM - 1) / (grid * TM)), nit = nr < 2 ? 2 : nr;
    const uint32_t nbytes = (uint32_t)(rows * CH * 2);
    const uint32_t np = (nbytes >> 21) & ~1u;
    const uint64_t pages = (np >= 24 ? (np - 14) & ~7u : 64u) | (uint64_t)(np >= 2 ? (np - 2) << 21 : 0u) << 32;
    dwsep_kernel<<<grid, 256, 0, stream>>>((const char*)d_in[0] + r0 * CH * 2, nbytes, fl, (char*)d_out + r0 * CH * 2, nbytes, fl,
                                           (const char*)d_in[2], CH * CH * 2, fl, (const char*)d_in[1], CH * 2, fl,
                                           (uint32_t)nit, (uint32_t)grid * TILE_BYTES, d_ws, pages, sh);
  }
}
```

```cpp
#include <hip/hip_runtime.h>
#include <stdint.h>

#define CH 384
#define TM 16
#define TILE_BYTES (TM * CH * 2)
#define LDS_BYTES (13 * TILE_BYTES + 1024 + 64)

struct Shapes { uint32_t v[256], rows[256]; };
extern "C" __device__ uint64_t dispatch_id_() __asm("llvm.amdgcn.dispatch.id");

__global__ __launch_bounds__(256, 1)
void dwsep_kernel(const char* x, uint32_t xbytes, uint32_t f0, char* out, uint32_t obytes, uint32_t f1,
                  const char* pw, uint32_t pwbytes, uint32_t f2, const char* dw, uint32_t dwbytes, uint32_t f3,
                  uint32_t nit, uint32_t tstride, void* ws, uint64_t pages, Shapes shapes) {
  __shared__ __attribute__((aligned(1024))) char lds[LDS_BYTES];
  uint32_t lds0 = (uint32_t)(uintptr_t)(__attribute__((address_space(3))) char*)lds;
  uint64_t ka = (uint64_t)(uintptr_t)__builtin_amdgcn_kernarg_segment_ptr();
  uint64_t did = dispatch_id_(), dp = (uint64_t)(uintptr_t)__builtin_amdgcn_dispatch_ptr();
  uint32_t bid = blockIdx.x, tid = threadIdx.x;
  asm volatile(
    "s_load_dwordx16 s[36:51], s[2:3], 0x0\n"
    "v_readfirstlane_b32 s8, v0\n"
    "s_load_dwordx4 s[52:55], s[2:3], 0x40\n"
    "s_bfe_u32 s7, s6, 0x40003\n"
    "s_lshr_b32 s9, s8, 2\n"
    "s_lshl_b32 s7, s7, 6\n"
    "s_add_u32 s9, s7, s9\n"
    "s_load_dwordx4 s[20:23], s[2:3], s9 offset:0x58\n"
    "s_load_dwordx2 s[96:97], s[2:3], 0x50\n"
    "s_getpc_b64 s[14:15]\n"
    "70:\n"
    "v_lshlrev_b32 v48, 6, v0\n"
    "s_mov_b32 s13, 79f-70b\n"
    "v_min_u32 v48, s13, v48\n"
    "global_load_dword v49, v48, s[14:15]\n"
    "v_and_b32 v18, 48, v0\n"
    "v_bfe_u32 v55, v0, 3, 1\n"
    "v_bfe_u32 v54, v0, 6, 1\n"
    "v_lshl_add_u32 v53, v55, 2, v18\n"
    "s_mov_b32 s16, s4\n"
    "v_add_u32 v53, s7, v53\n"
    "v_lshl_add_u32 v54, v54, 3, v53\n"
    "global_load_dword v8, v53, s[2:3] offset:1112\n"
    "global_load_dword v9, v53, s[2:3] offset:1120\n"
    "global_load_dword v10, v54, s[2:3] offset:1112\n"
    "s_mov_b32 s2, s6\n"
    "s_mov_b32 s3, %[lds]\n"
    "s_lshr_b32 s4, s8, 6\n"
    "v_and_b32 v53, 24, v0\n"
    "v_bfe_u32 v54, v0, 5, 1\n"
    "s_bfe_u32 s5, s2, 0x20003\n"
    "v_add_u32 v53, v53, v54\n"
    "s_bfe_u32 s6, s2, 0x20005\n"
    "v_mul_u32_u24 v53, 0x300, v53\n"
    "s_cmp_eq_u32 s6, 3\n"
    "v_and_b32 v55, 7, v0\n"
    "s_cselect_b32 s6, 0, s6\n"
    "v_lshl_add_u32 v53, v55, 4, v53\n"
    "s_lshl_b32 s7, s4, 7\n"
    "v_and_b32 v16, 63, v0\n"
    "s_mul_i32 s58, s5, 24576\n"
    "v_add_u32 v244, s7, v53\n"
    "s_mul_i32 s62, s6, 98304\n"
    "v_lshlrev_b32 v16, 4, v16\n"
    "s_lshl_b32 s7, s5, 6\n"
    "s_lshl_b32 s8, s6, 8\n"
    "v_xor_b32 v53, s7, v16\n"
    "s_add_u32 s12, s3, 159744\n"
    "s_mul_i32 s83, s4, 30720\n"
    "v_add_u32 v53, s8, v53\n"
    "s_mov_b32 m0, s12\n"
    "v_subrev_u32 v55, 0x300, v53\n"
    "s_add_u32 s83, s83, s3\n"
    "v_min_u32 v250, v53, v55\n"
    "v_add_u32 v245, 0x200, v244\n"
    "v_add_u32 v246, 0x400, v244\n"
    "v_add_u32 v247, 0x600, v244\n"
    "v_and_b32 v235, 12, v0\n"
    "s_mul_i32 s57, s4, 3072\n"
    "v_bfe_u32 v52, v0, 4, 2\n"
    "s_lshl_b32 s7, s4, 6\n"
    "v_lshl_add_u32 v53, v235, 1, v52\n"
    "v_mul_u32_u24 v53, 0x300, v53\n"
    "s_add_u32 s57, s57, s3\n"
    "v_and_b32 v54, 3, v0\n"
    "s_add_u32 s7, s7, 512\n"
    "v_lshl_add_u32 v53, v54, 4, v53\n"
    "v_lshlrev_b32 v12, 2, v54\n"
    "v_add_u32 v248, s7, v53\n"
    "v_add_u32 v249, 0x800, v248\n"
    "s_lshr_b64 s[18:19], s[0:1], 12\n"
    "s_lshl4_add_u32 s18, s16, s18\n"
    "s_andn2_b32 s17, s18, 15\n"
    "s_bitset1_b32 s17, 31\n"
    "s_mov_b32 s72, s17\n"
    "s_mov_b32 s18, s16\n"
    "s_or_b32 s19, s17, 15\n"
    "s_lshl_b32 s34, s2, 20\n"
    "s_and_b32 s34, s34, 0x7e00000\n"
    "s_mul_i32 s7, s16, 0x9e3779b1\n"
    "s_lshr_b32 s7, s7, 9\n"
    "s_and_b32 s7, s7, 0x1f8f80\n"
    "s_or_b32 s34, s34, s7\n"
    "s_and_b32 s35, s2, 7\n"
    "s_lshl_b32 s7, s4, 1\n"
    "s_sub_u32 s35, s35, s7\n"
    "v_and_b32 v19, 15, v0\n"
    "v_add_u32 v18, s12, v18\n"
    "v_and_b32 v54, 14, v0\n"
    "v_lshrrev_b32 v53, 1, v235\n"
    "v_add_u32 v235, v18, v54\n"
    "v_add_u32 v18, v18, v53\n"
    "s_mov_b32 s9, 0x51403020\n"
    "s_mov_b32 s10, 0x52423121\n"
    "s_mov_b32 s11, 0x53433322\n"
    "s_cmp_eq_u32 s6, 1\n"
    "s_cselect_b32 s9, 0x61204030, s9\n"
    "s_cselect_b32 s10, 0x62521131, s10\n"
    "s_cselect_b32 s11, 0x33534302, s11\n"
    "s_cmp_eq_u32 s6, 2\n"
    "s_cselect_b32 s9, 0x71302040, s9\n"
    "s_cselect_b32 s10, 0x42622111, s10\n"
    "s_cselect_b32 s11, 0x43335312, s11\n"
    "s_xor_b32 s7, s4, s5\n"
    "v_lshlrev_b32 v54, 3, v52\n"
    "s_lshl_b32 s7, s7, 2\n"
    "v_xor_b32 v55, s7, v19\n"
    "v_bfe_u32 v53, s11, v54, 8\n"
    "v_xor_b32 v2, v53, v55\n"
    "v_bfe_u32 v53, s10, v54, 8\n"
    "v_xor_b32 v1, v53, v55\n"
    "v_bfe_u32 v53, s9, v54, 8\n"
    "v_xor_b32 v0, v53, v55\n"
    "s_and_b32 s28, s2, 3\n"
    "s_lshr_b32 s8, s2, 7\n"
    "s_lshl2_add_u32 s7, s8, s28\n"
    "s_waitcnt lgkmcnt(0)\n"
    "s_barrier\n"
    "buffer_load_dwordx4 v250, s[48:51], 0 offen lds\n"
    "s_cmp_eq_u32 s35, 0\n"
    "s_cbranch_scc0 72f\n"
    "s_bitcmp1_b32 s2, 7\n"
    "s_cselect_b64 s[68:69], s[40:41], s[36:37]\n"
    "s_mov_b64 s[70:71], s[38:39]\n"
    "s_and_b32 s85, s34, 0x3fffff\n"
    "s_add_u32 s85, s85, s97\n"
    "s_min_u32 s34, s34, s85\n"
    "s_buffer_load_dword s73, s[68:71], s34\n"
    "s_buffer_load_dword s72, s[68:71], s34 offset:0x2000\n"
    "72:\n"
    "s_add_u32 m0, s83, 36864\n"
    "s_add_u32 s84, s58, s62\n"
    "s_mul_i32 s56, s7, 393216\n"
    "s_lshr_b32 s29, s2, 2\n"
    "s_mov_b32 s30, s57\n"
    "buffer_load_dwordx4 v244, s[44:47], s84 offen lds\n"
    "s_xor_b32 s7, s5, 1\n"
    "s_mul_i32 s59, s7, 24576\n"
    "s_xor_b32 s7, s5, 2\n"
    "buffer_load_dwordx4 v245, s[44:47], s84 offen offset:1024 lds\n"
    "s_mul_i32 s60, s7, 24576\n"
    "s_xor_b32 s7, s5, 3\n"
    "s_mul_i32 s61, s7, 24576\n"
    "buffer_load_dwordx4 v246, s[44:47], s84 offen offset:2048 lds\n"
    "v_mov_b32 v53, s20\n"
    "v_mov_b32 v54, s21\n"
    "v_cmp_le_i32 vcc, s22, v16\n"
    "s_sub_u32 s22, s22, 0x400\n"
    "s_mov_b32 s33, 0x90000000\n"
    "v_cndmask_b32 v55, v53, v54, vcc\n"
    "buffer_load_dwordx4 v247, s[44:47], s84 offen offset:3072 lds\n"
    "s_add_u32 m0, s83, 40960\n"
    "v_lshl_add_u32 v0, v0, 4, v55\n"
    "v_cmp_le_i32 vcc, s22, v16\n"
    "s_sub_u32 s22, s22, 0x400\n"
    "s_nop 0\n"
    "v_cndmask_b32 v55, v53, v54, vcc\n"
    "v_lshl_add_u32 v1, v1, 4, v55\n"
    "buffer_load_dwordx4 v248, s[44:47], s84 offen lds\n"
    "v_cmp_le_i32 vcc, s22, v16\n"
    "s_sub_u32 s22, s22, 0x400\n"
    "s_nop 0\n"
    "v_cndmask_b32 v55, v53, v54, vcc\n"
    "v_lshl_add_u32 v2, v2, 4, v55\n"
    "s_add_u32 s7, s83, 36864\n"
    "buffer_load_dwordx4 v249, s[44:47], s84 offen offset:1024 lds\n"
    "s_add_u32 m0, s83, 43008\n"
    "s_add_u32 s84, s59, s62\n"
    "v_and_b32 v240, 0x70, v16\n"
    "v_and_b32 v53, 8, v19\n"
    "v_lshl_add_u32 v53, v52, 7, v53\n"
    "buffer_load_dwordx4 v244, s[44:47], s84 offen lds\n"
    "v_lshrrev_b32 v54, 2, v16\n"
    "v_add3_u32 v15, s7, v53, v240\n"
    "v_add_u32 v38, s7, v54\n"
    "buffer_load_dwordx4 v245, s[44:47], s84 offen offset:1024 lds\n"
    "s_cmp_eq_u32 s35, 0\n"
    "s_cbranch_scc0 73f\n"
    "s_getreg_b32 s88, hwreg(HW_REG_XCC_ID, 0, 3)\n"
    "s_lshl_b32 s89, s2, 3\n"
    "s_and_b32 s89, s89, 0x3f0\n"
    "s_lshr_b32 s82, s2, 7\n"
    "s_lshl3_add_u32 s89, s82, s89\n"
    "v_mov_b32 v20, s89\n"
    "v_mov_b32 v22, s16\n"
    "s_lshr_b32 s89, s88, 2\n"
    "s_lshl1_add_u32 s88, s88, s89\n"
    "s_or_b32 s88, s88, s17\n"
    "s_bfe_u32 s89, s2, 0x10001\n"
    "s_xor_b32 s88, s88, s89\n"
    "s_waitcnt lgkmcnt(1)\n"
    "s_nop 3\n"
    "s_cmp_lg_u32 s72, s17\n"
    "s_cselect_b32 s85, 1, 0\n"
    "s_xor_b32 s88, s88, s85\n"
    "v_mov_b32 v23, s88\n"
    "73:\n"
    "s_lshr_b32 s8, s4, 1\n"
    "s_lshl_b32 s8, s8, 11\n"
    "s_add_u32 s8, s8, s3\n"
    "buffer_load_dwordx4 v246, s[44:47], s84 offen offset:2048 lds\n"
    "s_add_u32 s8, s8, 147456\n"
    "s_and_b32 s13, s4, 1\n"
    "s_lshl_b32 s14, s13, 8\n"
    "buffer_load_dwordx4 v247, s[44:47], s84 offen offset:3072 lds\n"
    "s_add_u32 m0, s83, 47104\n"
    "s_sub_u32 s7, s14, s7\n"
    "s_add_u32 s11, s7, s8\n"
    "s_lshl2_add_u32 s8, s14, s8\n"
    "buffer_load_dwordx4 v248, s[44:47], s84 offen lds\n"
    "v_and_b32 v53, 0x30, v16\n"
    "v_lshrrev_b32 v54, 2, v19\n"
    "v_lshl_add_u32 v53, v54, 8, v53\n"
    "buffer_load_dwordx4 v249, s[44:47], s84 offen offset:1024 lds\n"
    "v_lshl_add_u32 v53, v52, 6, v53\n"
    "v_add_u32 v39, s8, v53\n"
    "v_mov_b32 v251, 0\n"
    "v_mov_b32 v253, 0\n"
    "v_mov_b32 v255, 0\n"
    "s_add_u32 s63, s62, 98304\n"
    "s_cmp_eq_u32 s63, 294912\n"
    "s_cselect_b32 s63, 0, s63\n"
    "s_add_u32 s64, s63, 98304\n"
    "s_cmp_eq_u32 s64, 294912\n"
    "s_cselect_b32 s64, 0, s64\n"
    "s_add_u32 s7, s3, 160768\n"
    "v_mov_b32 v14, 0\n"
    "v_add_u32 v12, s7, v12\n"
    "s_lshl2_add_u32 s7, s4, s7\n"
    "v_mov_b32 v13, s7\n"
    "v_xor_b32 v53, v52, v19\n"
    "v_mul_u32_u24 v54, 0x300, v19\n"
    "v_lshl_add_u32 v54, v53, 4, v54\n"
    "v_add_u32 v4, s3, v54\n"
    "v_xor_b32 v5, 64, v4\n"
    "v_xor_b32 v6, 128, v4\n"
    "v_xor_b32 v7, 192, v4\n"
    "s_lshl_b32 s7, s4, 7\n"
    "s_lshr_b32 s8, s4, 1\n"
    "s_lshl_b32 s8, s8, 7\n"
    "s_add_u32 s8, s8, 512\n"
    "v_add_u32 v241, s8, v240\n"
    "v_add_u32 v240, s7, v240\n"
    "v_lshrrev_b32 v19, 4, v16\n"
    "s_mul_i32 s7, s52, 5\n"
    "s_lshr_b32 s7, s7, 3\n"
    "s_sub_u32 s8, s52, 1\n"
    "s_bitcmp1_b32 s2, 7\n"
    "s_cselect_b32 s7, s7, 0\n"
    "v_add_u32 v53, s7, v19\n"
    "v_cmp_gt_u32 vcc, s8, v19\n"
    "v_subrev_u32 v54, s8, v53\n"
    "v_min_u32 v53, v53, v54\n"
    "v_cndmask_b32 v53, v19, v53, vcc\n"
    "v_mul_lo_u32 v3, v53, s53\n"
    "v_add_u32 v3, s56, v3\n"
    "s_xor_b32 s29, s29, s23\n"
    "s_mul_i32 s7, s52, s53\n"
    "s_add_u32 s7, s7, s56\n"
    "s_mov_b32 s8, -2752512\n"
    "s_bitcmp1_b32 s2, 0\n"
    "s_cselect_b32 s8, -3538944, s8\n"
    "s_add_u32 s7, s7, s8\n"
    "v_cmp_eq_u32 vcc, s52, v19\n"
    "v_mov_b32 v53, s7\n"
    "s_nop 0\n"
    "v_cndmask_b32 v3, v3, v53, vcc\n"
    "v_add_u32 v53, s23, v3\n"
    "v_lshrrev_b32 v53, 21, v53\n"
    "v_and_b32 v232, 56, v53\n"
    "v_and_b32 v233, 4, v53\n"
    "v_xor_b32 v234, s29, v53\n"
    "s_cmp_eq_u32 s35, 0\n"
    "s_cbranch_scc0 71f\n"
    "s_mov_b64 exec, 1\n"
    "global_store_dwordx2 v20, v[22:23], s[54:55] sc1\n"
    "s_mov_b64 exec, -1\n"
    "s_waitcnt lgkmcnt(0)\n"
    "71:\n"
    "s_branch 301f\n"
    "81:\n"
    "ds_read_b32 v15, v12\n"
    "s_waitcnt lgkmcnt(0)\n"
    "v_cmp_lt_u32 vcc, v15, v14\n"
    "s_nop 0\n"
    "s_cbranch_vccnz 81b\n"
    "s_branch 82f\n"
    "301:\n"
    "s_branch 302f\n"
    "41:\n"
    "ds_read_b32 v15, v12\n"
    "s_waitcnt lgkmcnt(0)\n"
    "v_cmp_lt_u32 vcc, v15, v14\n"
    "s_nop 0\n"
    "s_cbranch_vccnz 41b\n"
    "s_branch 42f\n"
    "43:\n"
    "s_memrealtime s[92:93]\n"
    "s_waitcnt lgkmcnt(0)\n"
    "s_sub_u32 s84, s92, s89\n"
    "s_cmp_lt_i32 s84, 0\n"
    "s_cbranch_scc0 44f\n"
    "s_sleep 1\n"
    "s_branch 43b\n"
    "302:\n"
    "s_branch 303f\n"
    "61:\n"
    "ds_read_b32 v15, v12\n"
    "s_waitcnt lgkmcnt(0)\n"
    "v_cmp_lt_u32 vcc, v15, v14\n"
    "s_nop 0\n"
    "s_cbranch_vccnz 61b\n"
    "s_branch 62f\n"
    "63:\n"
    "s_memrealtime s[92:93]\n"
    "s_waitcnt lgkmcnt(0)\n"
    "s_sub_u32 s84, s92, s89\n"
    "s_cmp_lt_i32 s84, 0\n"
    "s_cbranch_scc0 64f\n"
    "s_sleep 1\n"
    "s_branch 63b\n"
    "303:\n"
    "s_branch 304f\n"
    "45:\n"
    "s_sub_u32 s7, s86, s71\n"
    "s_cmp_eq_u32 s7, 2\n"
    "s_cbranch_scc1 402f\n"
    "s_cmp_eq_u32 s7, 4\n"
    "s_cbranch_scc1 404f\n"
    "v_mfma_f32_16x16x32_bf16 v[24:27], a[96:99], a[68:71], v[24:27]\n"
    "s_add_u32 s7, s70, 0\n"
    "s_add_u32 s64, s64, 2\n"
    "ds_read_b128 a[136:139], v4 offset:12288\n"
    "v_mfma_f32_16x16x32_bf16 v[24:27], a[92:95], a[44:47], v[24:27]\n"
    "v_add_u32 v14, 1, v14\n"
    "v_accvgpr_write_b32 a188, v44\n"
    "v_mfma_f32_16x16x32_bf16 v[24:27], a[88:91], a[20:23], v[24:27]\n"
    "s_mov_b64 vcc, s[12:13]\n"
    "v_accvgpr_write_b32 a189, v45\n"
    "v_mfma_f32_16x16x32_bf16 v[28:31], a[88:91], a[0:3], 0\n"
    "buffer_load_dwordx4 v0, s[36:39], s81 offen nt lds\n"
    "v_mfma_f32_16x16x32_bf16 v[28:31], a[92:95], a[24:27], v[28:31]\n"
    "s_waitcnt lgkmcnt(1)\n"
    "ds_read_b128 a[140:143], v5 offset:12288\n"
    "v_mfma_f32_16x16x32_bf16 v[28:31], a[96:99], a[48:51], v[28:31]\n"
    "ds_read_b128 a[144:147], v6 offset:12288\n"
    "v_accvgpr_write_b32 a190, v46\n"
    "v_mfma_f32_16x16x32_bf16 v[28:31], a[100:103], a[72:75], v[28:31]\n"
    "v_cvt_pk_bf16_f32 v52, v20, v24\n"
    "v_accvgpr_write_b32 a191, v47\n"
    "v_mfma_f32_16x16x32_bf16 v[28:31], a[104:107], v[64:67], v[28:31]\n"
    "v_cvt_pk_bf16_f32 v53, v21, v25\n"
    "v_accvgpr_write_b32 a192, v48\n"
    "v_mfma_f32_16x16x32_bf16 v[28:31], a[108:111], v[88:91], v[28:31]\n"
    "v_cvt_pk_bf16_f32 v54, v22, v26\n"
    "v_accvgpr_write_b32 a193, v49\n"
    "v_mfma_f32_16x16x32_bf16 v[28:31], a[112:115], v[112:115], v[28:31]\n"
    "v_cvt_pk_bf16_f32 v55, v23, v27\n"
    "v_accvgpr_write_b32 a194, v50\n"
    "v_mfma_f32_16x16x32_bf16 v[28:31], a[116:119], v[136:139], v[28:31]\n"
    "ds_read_b128 a[148:151], v7 offset:12288\n"
    "v_accvgpr_write_b32 a195, v51\n"
    "v_mfma_f32_16x16x32_bf16 v[28:31], a[120:123], v[160:163], v[28:31]\n"
    "v_accvgpr_mov_b32 a196, a184\n"
    "v_readlane_b32 s9, v3, s7\n"
    "v_mfma_f32_16x16x32_bf16 v[28:31], a[124:127], v[184:187], v[28:31]\n"
    "buffer_load_dwordx4 v1, s[36:39], s81 offen offset:1024 nt lds\n"
    "v_mfma_f32_16x16x32_bf16 v[28:31], a[128:131], v[208:211], v[28:31]\n"
    "v_accvgpr_mov_b32 a197, a185\n"
    "v_mfma_f32_16x16x32_bf16 v[28:31], a[132:135], v[232:235], v[28:31]\n"
    "ds_read_b128 a[152:155], v4 offset:12544\n"
    "v_mfma_f32_16x16x32_bf16 v[32:35], a[132:135], v[236:239], 0\n"
    "v_accvgpr_mov_b32 a198, a186\n"
    "v_mfma_f32_16x16x32_bf16 v[32:35], a[128:131], v[212:215], v[32:35]\n"
    "ds_write2st64_b32 v38, v52, v53 offset0:0 offset1:2\n"
    "v_mfma_f32_16x16x32_bf16 v[32:35], a[124:127], v[188:191], v[32:35]\n"
    "v_accvgpr_mov_b32 a199, a187\n"
    "s_branch 46f\n"
    "304:\n"
    "s_branch 305f\n"
    "402:\n"
    "v_mfma_f32_16x16x32_bf16 v[24:27], a[96:99], a[68:71], v[24:27]\n"
    "s_add_u32 s64, s64, 2\n"
    "ds_read_b128 a[136:139], v4 offset:12288\n"
    "v_mfma_f32_16x16x32_bf16 v[24:27], a[92:95], a[44:47], v[24:27]\n"
    "v_add_u32 v14, 1, v14\n"
    "v_mfma_f32_16x16x32_bf16 v[24:27], a[88:91], a[20:23], v[24:27]\n"
    "s_mov_b64 vcc, s[12:13]\n"
    "v_mfma_f32_16x16x32_bf16 v[28:31], a[88:91], a[0:3], 0\n"
    "buffer_store_dwordx4 a[188:191], v8, s[40:43], s9 offen sc1\n"
    "v_mfma_f32_16x16x32_bf16 v[28:31], a[92:95], a[24:27], v[28:31]\n"
    "ds_read_b128 a[140:143], v5 offset:12288\n"
    "v_mfma_f32_16x16x32_bf16 v[28:31], a[96:99], a[48:51], v[28:31]\n"
    "ds_read_b128 a[144:147], v6 offset:12288\n"
    "v_mfma_f32_16x16x32_bf16 v[28:31], a[100:103], a[72:75], v[28:31]\n"
    "v_cvt_pk_bf16_f32 v52, v20, v24\n"
    "v_mfma_f32_16x16x32_bf16 v[28:31], a[104:107], v[64:67], v[28:31]\n"
    "v_cvt_pk_bf16_f32 v53, v21, v25\n"
    "v_mfma_f32_16x16x32_bf16 v[28:31], a[108:111], v[88:91], v[28:31]\n"
    "v_cvt_pk_bf16_f32 v54, v22, v26\n"
    "v_mfma_f32_16x16x32_bf16 v[28:31], a[112:115], v[112:115], v[28:31]\n"
    "v_cvt_pk_bf16_f32 v55, v23, v27\n"
    "v_mfma_f32_16x16x32_bf16 v[28:31], a[116:119], v[136:139], v[28:31]\n"
    "ds_read_b128 a[148:151], v7 offset:12288\n"
    "v_mfma_f32_16x16x32_bf16 v[28:31], a[120:123], v[160:163], v[28:31]\n"
    "v_mfma_f32_16x16x32_bf16 v[28:31], a[124:127], v[184:187], v[28:31]\n"
    "buffer_store_dwordx4 a[192:195], v9, s[40:43], s9 offen sc1\n"
    "v_mfma_f32_16x16x32_bf16 v[28:31], a[128:131], v[208:211], v[28:31]\n"
    "v_mfma_f32_16x16x32_bf16 v[28:31], a[132:135], v[232:235], v[28:31]\n"
    "ds_read_b128 a[152:155], v4 offset:12544\n"
    "v_mfma_f32_16x16x32_bf16 v[32:35], a[132:135], v[236:239], 0\n"
    "v_mfma_f32_16x16x32_bf16 v[32:35], a[128:131], v[212:215], v[32:35]\n"
    "ds_write2st64_b32 v38, v52, v53 offset0:0 offset1:2\n"
    "v_mfma_f32_16x16x32_bf16 v[32:35], a[124:127], v[188:191], v[32:35]\n"
    "s_branch 46f\n"
    "305:\n"
    "s_branch 306f\n"
    "404:\n"
    "v_mfma_f32_16x16x32_bf16 v[24:27], a[96:99], a[68:71], v[24:27]\n"
    "s_sub_u32 s89, s89, 36\n"
    "ds_read_b128 a[136:139], v4 offset:12288\n"
    "v_mfma_f32_16x16x32_bf16 v[24:27], a[92:95], a[44:47], v[24:27]\n"
    "v_add_u32 v14, 1, v14\n"
    "v_mfma_f32_16x16x32_bf16 v[24:27], a[88:91], a[20:23], v[24:27]\n"
    "s_mov_b64 vcc, s[12:13]\n"
    "v_mfma_f32_16x16x32_bf16 v[28:31], a[88:91], a[0:3], 0\n"
    "buffer_store_dwordx4 a[204:207], v9, s[40:43], s10 offen sc1\n"
    "v_mfma_f32_16x16x32_bf16 v[28:31], a[92:95], a[24:27], v[28:31]\n"
    "ds_read_b128 a[140:143], v5 offset:12288\n"
    "v_mfma_f32_16x16x32_bf16 v[28:31], a[96:99], a[48:51], v[28:31]\n"
    "ds_read_b128 a[144:147], v6 offset:12288\n"
    "v_mfma_f32_16x16x32_bf16 v[28:31], a[100:103], a[72:75], v[28:31]\n"
    "v_cvt_pk_bf16_f32 v52, v20, v24\n"
    "v_mfma_f32_16x16x32_bf16 v[28:31], a[104:107], v[64:67], v[28:31]\n"
    "v_cvt_pk_bf16_f32 v53, v21, v25\n"
    "v_mfma_f32_16x16x32_bf16 v[28:31], a[108:111], v[88:91], v[28:31]\n"
    "v_cvt_pk_bf16_f32 v54, v22, v26\n"
    "v_mfma_f32_16x16x32_bf16 v[28:31], a[112:115], v[112:115], v[28:31]\n"
    "v_cvt_pk_bf16_f32 v55, v23, v27\n"
    "v_mfma_f32_16x16x32_bf16 v[28:31], a[116:119], v[136:139], v[28:31]\n"
    "ds_read_b128 a[148:151], v7 offset:12288\n"
    "v_mfma_f32_16x16x32_bf16 v[28:31], a[120:123], v[160:163], v[28:31]\n"
    "v_mfma_f32_16x16x32_bf16 v[28:31], a[124:127], v[184:187], v[28:31]\n"
    "buffer_store_dwordx4 a[208:211], v10, s[40:43], s10 offen sc1\n"
    "v_mfma_f32_16x16x32_bf16 v[28:31], a[128:131], v[208:211], v[28:31]\n"
    "v_mfma_f32_16x16x32_bf16 v[28:31], a[132:135], v[232:235], v[28:31]\n"
    "ds_read_b128 a[152:155], v4 offset:12544\n"
    "v_mfma_f32_16x16x32_bf16 v[32:35], a[132:135], v[236:239], 0\n"
    "v_mfma_f32_16x16x32_bf16 v[32:35], a[128:131], v[212:215], v[32:35]\n"
    "ds_write2st64_b32 v38, v52, v53 offset0:0 offset1:2\n"
    "v_mfma_f32_16x16x32_bf16 v[32:35], a[124:127], v[188:191], v[32:35]\n"
    "s_branch 46f\n"
    "306:\n"
    "s_branch 307f\n"
    "65:\n"
    "s_sub_u32 s7, s86, s71\n"
    "s_cmp_eq_u32 s7, 3\n"
    "s_cbranch_scc1 403f\n"
    "v_mfma_f32_16x16x32_bf16 v[32:35], a[144:147], a[68:71], v[32:35]\n"
    "s_add_u32 s7, s70, 1\n"
    "s_add_u32 s65, s65, 2\n"
    "ds_read_b128 a[88:91], v4\n"
    "v_mfma_f32_16x16x32_bf16 v[32:35], a[140:143], a[44:47], v[32:35]\n"
    "v_add_u32 v7, s78, v7\n"
    "v_accvgpr_write_b32 a200, v44\n"
    "v_mfma_f32_16x16x32_bf16 v[32:35], a[136:139], a[20:23], v[32:35]\n"
    "v_add_u32 v14, 1, v14\n"
    "v_accvgpr_write_b32 a201, v45\n"
    "v_mfma_f32_16x16x32_bf16 v[20:23], a[136:139], a[0:3], 0\n"
    "buffer_load_dwordx4 v0, s[36:39], s81 offen nt lds\n"
    "v_mfma_f32_16x16x32_bf16 v[20:23], a[140:143], a[24:27], v[20:23]\n"
    "s_waitcnt lgkmcnt(1)\n"
    "ds_read_b128 a[92:95], v5\n"
    "v_mfma_f32_16x16x32_bf16 v[20:23], a[144:147], a[48:51], v[20:23]\n"
    "ds_read_b128 a[96:99], v6\n"
    "v_accvgpr_mov_b32 a211, a187\n"
    "v_mfma_f32_16x16x32_bf16 v[20:23], a[148:151], a[72:75], v[20:23]\n"
    "v_cvt_pk_bf16_f32 v52, v28, v32\n"
    "v_accvgpr_write_b32 a202, v46\n"
    "v_mfma_f32_16x16x32_bf16 v[20:23], a[152:155], v[64:67], v[20:23]\n"
    "v_cvt_pk_bf16_f32 v53, v29, v33\n"
    "v_accvgpr_write_b32 a203, v47\n"
    "v_mfma_f32_16x16x32_bf16 v[20:23], a[156:159], v[88:91], v[20:23]\n"
    "v_cvt_pk_bf16_f32 v54, v30, v34\n"
    "v_accvgpr_write_b32 a204, v48\n"
    "v_mfma_f32_16x16x32_bf16 v[20:23], a[160:163], v[112:115], v[20:23]\n"
    "v_cvt_pk_bf16_f32 v55, v31, v35\n"
    "v_accvgpr_write_b32 a205, v49\n"
    "v_mfma_f32_16x16x32_bf16 v[20:23], a[164:167], v[136:139], v[20:23]\n"
    "ds_read_b128 a[100:103], v7\n"
    "v_accvgpr_write_b32 a206, v50\n"
    "v_mfma_f32_16x16x32_bf16 v[20:23], a[168:171], v[160:163], v[20:23]\n"
    "s_mov_b64 vcc, s[12:13]\n"
    "v_accvgpr_write_b32 a207, v51\n"
    "v_mfma_f32_16x16x32_bf16 v[20:23], a[172:175], v[184:187], v[20:23]\n"
    "buffer_load_dwordx4 v1, s[36:39], s81 offen offset:1024 nt lds\n"
    "v_mfma_f32_16x16x32_bf16 v[20:23], a[176:179], v[208:211], v[20:23]\n"
    "v_accvgpr_mov_b32 a208, a184\n"
    "v_readlane_b32 s10, v3, s7\n"
    "v_mfma_f32_16x16x32_bf16 v[20:23], a[180:183], v[232:235], v[20:23]\n"
    "ds_read_b128 a[104:107], v4 offset:256\n"
    "v_mfma_f32_16x16x32_bf16 v[24:27], a[180:183], v[236:239], 0\n"
    "v_accvgpr_mov_b32 a209, a185\n"
    "v_mfma_f32_16x16x32_bf16 v[24:27], a[176:179], v[212:215], v[24:27]\n"
    "ds_write2st64_b32 v38, v52, v53 offset0:16 offset1:18\n"
    "v_mfma_f32_16x16x32_bf16 v[24:27], a[172:175], v[188:191], v[24:27]\n"
    "v_accvgpr_mov_b32 a210, a186\n"
    "s_branch 66f\n"
    "307:\n"
    "s_branch 308f\n"
    "403:\n"
    "v_mfma_f32_16x16x32_bf16 v[32:35], a[144:147], a[68:71], v[32:35]\n"
    "s_sub_u32 s89, s89, 36\n"
    "ds_read_b128 a[88:91], v4\n"
    "v_mfma_f32_16x16x32_bf16 v[32:35], a[140:143], a[44:47], v[32:35]\n"
    "v_add_u32 v7, s78, v7\n"
    "v_mfma_f32_16x16x32_bf16 v[32:35], a[136:139], a[20:23], v[32:35]\n"
    "v_add_u32 v14, 1, v14\n"
    "v_mfma_f32_16x16x32_bf16 v[20:23], a[136:139], a[0:3], 0\n"
    "buffer_store_dwordx4 a[196:199], v10, s[40:43], s9 offen sc1\n"
    "v_mfma_f32_16x16x32_bf16 v[20:23], a[140:143], a[24:27], v[20:23]\n"
    "ds_read_b128 a[92:95], v5\n"
    "v_mfma_f32_16x16x32_bf16 v[20:23], a[144:147], a[48:51], v[20:23]\n"
    "ds_read_b128 a[96:99], v6\n"
    "v_mfma_f32_16x16x32_bf16 v[20:23], a[148:151], a[72:75], v[20:23]\n"
    "v_cvt_pk_bf16_f32 v52, v28, v32\n"
    "v_mfma_f32_16x16x32_bf16 v[20:23], a[152:155], v[64:67], v[20:23]\n"
    "v_cvt_pk_bf16_f32 v53, v29, v33\n"
    "v_mfma_f32_16x16x32_bf16 v[20:23], a[156:159], v[88:91], v[20:23]\n"
    "v_cvt_pk_bf16_f32 v54, v30, v34\n"
    "v_mfma_f32_16x16x32_bf16 v[20:23], a[160:163], v[112:115], v[20:23]\n"
    "v_cvt_pk_bf16_f32 v55, v31, v35\n"
    "v_mfma_f32_16x16x32_bf16 v[20:23], a[164:167], v[136:139], v[20:23]\n"
    "ds_read_b128 a[100:103], v7\n"
    "v_mfma_f32_16x16x32_bf16 v[20:23], a[168:171], v[160:163], v[20:23]\n"
    "s_mov_b64 vcc, s[12:13]\n"
    "v_mfma_f32_16x16x32_bf16 v[20:23], a[172:175], v[184:187], v[20:23]\n"
    "buffer_store_dwordx4 a[200:203], v8, s[40:43], s10 offen sc1\n"
    "v_mfma_f32_16x16x32_bf16 v[20:23], a[176:179], v[208:211], v[20:23]\n"
    "v_mfma_f32_16x16x32_bf16 v[20:23], a[180:183], v[232:235], v[20:23]\n"
    "ds_read_b128 a[104:107], v4 offset:256\n"
    "v_mfma_f32_16x16x32_bf16 v[24:27], a[180:183], v[236:239], 0\n"
    "v_mfma_f32_16x16x32_bf16 v[24:27], a[176:179], v[212:215], v[24:27]\n"
    "ds_write2st64_b32 v38, v52, v53 offset0:16 offset1:18\n"
    "v_mfma_f32_16x16x32_bf16 v[24:27], a[172:175], v[188:191], v[24:27]\n"
    "s_branch 66f\n"
    "308:\n"
    "s_waitcnt vmcnt(12)\n"
    "ds_write_b32 v13, v14\n"
    "ds_read_u16 v250, v18\n"
    "ds_read_u16 v252, v18 offset:8\n"
    "ds_read_u16 v254, v235\n"
    "s_waitcnt vmcnt(11)\n"
    "ds_read2st64_b64 a[188:191], v15 offset0:0 offset1:1\n"
    "s_waitcnt vmcnt(10)\n"
    "ds_read2st64_b64 a[192:195], v15 offset0:2 offset1:3\n"
    "s_waitcnt vmcnt(9)\n"
    "ds_read2st64_b64 a[196:199], v15 offset0:4 offset1:5\n"
    "s_waitcnt lgkmcnt(3)\n"
    "v_lshlrev_b64 v[40:41], v16, v[250:251]\n"
    "v_lshlrev_b64 v[42:43], v16, v[252:253]\n"
    "v_lshlrev_b64 v[44:45], v16, v[254:255]\n"
    "s_waitcnt vmcnt(8) lgkmcnt(2)\n"
    "ds_read2st64_b64 a[200:203], v15 offset0:6 offset1:7\n"
    "s_nop 0\n"
    "v_mfma_f32_4x4x4_16b_bf16 v[20:23], v[40:41], a[188:189], 0 cbsz:2 abid:0\n"
    "ds_read_u16 v250, v18 offset:64\n"
    "s_add_u32 m0, s83, 49152\n"
    "s_add_u32 s84, s60, s62\n"
    "v_mfma_f32_4x4x4_16b_bf16 v[24:27], v[40:41], a[190:191], 0 cbsz:2 abid:1\n"
    "buffer_load_dwordx4 v244, s[44:47], s84 offen lds\n"
    "v_add_u32 v8, v8, v240\n"
    "s_waitcnt vmcnt(8) lgkmcnt(3)\n"
    "ds_read2st64_b32 a[204:205], v38 offset0:16 offset1:17\n"
    "v_mfma_f32_4x4x4_16b_bf16 v[28:31], v[40:41], a[192:193], 0 cbsz:2 abid:2\n"
    "ds_read2st64_b32 a[206:207], v38 offset0:18 offset1:19\n"
    "ds_read_u16 v252, v18 offset:72\n"
    "v_cvt_pk_bf16_f32 v52, v20, v24\n"
    "v_cvt_pk_bf16_f32 v53, v21, v25\n"
    "v_accvgpr_write_b32 a0, v52\n"
    "v_accvgpr_write_b32 a4, v53\n"
    "v_mfma_f32_4x4x4_16b_bf16 v[32:35], v[40:41], a[194:195], 0 cbsz:2 abid:3\n"
    "buffer_load_dwordx4 v245, s[44:47], s84 offen offset:1024 lds\n"
    "v_cvt_pk_bf16_f32 v54, v22, v26\n"
    "v_cvt_pk_bf16_f32 v55, v23, v27\n"
    "v_accvgpr_write_b32 a8, v54\n"
    "v_accvgpr_write_b32 a12, v55\n"
    "v_add_u32 v9, v9, v240\n"
    "s_waitcnt vmcnt(8) lgkmcnt(5)\n"
    "ds_read2st64_b32 a[208:209], v38 offset0:20 offset1:21\n"
    "v_mfma_f32_4x4x4_16b_bf16 v[20:23], v[42:43], a[196:197], 0 cbsz:2 abid:0\n"
    "ds_read2st64_b32 a[210:211], v38 offset0:22 offset1:23\n"
    "ds_read_u16 v254, v235 offset:64\n"
    "v_cvt_pk_bf16_f32 v52, v28, v32\n"
    "v_cvt_pk_bf16_f32 v53, v29, v33\n"
    "v_accvgpr_write_b32 a1, v52\n"
    "v_accvgpr_write_b32 a5, v53\n"
    "v_mfma_f32_4x4x4_16b_bf16 v[24:27], v[42:43], a[198:199], 0 cbsz:2 abid:1\n"
    "buffer_load_dwordx4 v246, s[44:47], s84 offen offset:2048 lds\n"
    "v_cvt_pk_bf16_f32 v54, v30, v34\n"
    "v_cvt_pk_bf16_f32 v55, v31, v35\n"
    "v_accvgpr_write_b32 a9, v54\n"
    "v_accvgpr_write_b32 a13, v55\n"
    "v_add_u32 v10, v10, v241\n"
    "s_waitcnt vmcnt(8) lgkmcnt(6)\n"
    "ds_read2st64_b64 a[188:191], v15 offset0:12 offset1:13\n"
    "v_mfma_f32_4x4x4_16b_bf16 v[28:31], v[42:43], a[200:201], 0 cbsz:2 abid:2\n"
    "v_cvt_pk_bf16_f32 v52, v20, v24\n"
    "v_cvt_pk_bf16_f32 v53, v21, v25\n"
    "v_accvgpr_write_b32 a2, v52\n"
    "v_accvgpr_write_b32 a6, v53\n"
    "v_mfma_f32_4x4x4_16b_bf16 v[32:35], v[42:43], a[202:203], 0 cbsz:2 abid:3\n"
    "buffer_load_dwordx4 v247, s[44:47], s84 offen offset:3072 lds\n"
    "v_cvt_pk_bf16_f32 v54, v22, v26\n"
    "v_cvt_pk_bf16_f32 v55, v23, v27\n"
    "v_accvgpr_write_b32 a10, v54\n"
    "v_accvgpr_write_b32 a14, v55\n"
    "v_lshlrev_b64 v[46:47], v16, v[250:251]\n"
    "s_mov_b32 s12, 0xff00ff\n"
    "s_waitcnt vmcnt(8) lgkmcnt(4)\n"
    "ds_read2st64_b64 a[192:195], v15 offset0:14 offset1:15\n"
    "v_mfma_f32_4x4x4_16b_bf16 v[20:23], v[44:45], a[204:205], 0 cbsz:2 abid:0\n"
    "s_add_u32 m0, s83, 53248\n"
    "v_cvt_pk_bf16_f32 v52, v28, v32\n"
    "v_cvt_pk_bf16_f32 v53, v29, v33\n"
    "v_accvgpr_write_b32 a3, v52\n"
    "v_accvgpr_write_b32 a7, v53\n"
    "v_mfma_f32_4x4x4_16b_bf16 v[24:27], v[44:45], a[206:207], 0 cbsz:2 abid:1\n"
    "buffer_load_dwordx4 v248, s[44:47], s84 offen lds\n"
    "v_cvt_pk_bf16_f32 v54, v30, v34\n"
    "v_cvt_pk_bf16_f32 v55, v31, v35\n"
    "v_accvgpr_write_b32 a11, v54\n"
    "v_accvgpr_write_b32 a15, v55\n"
    "v_lshlrev_b64 v[48:49], v16, v[252:253]\n"
    "s_mov_b32 s13, 0xff00ff\n"
    "s_waitcnt vmcnt(8) lgkmcnt(2)\n"
    "ds_read2st64_b64 a[196:199], v15 offset0:16 offset1:17\n"
    "v_mfma_f32_4x4x4_16b_bf16 v[28:31], v[44:45], a[208:209], 0 cbsz:2 abid:2\n"
    "v_cvt_pk_bf16_f32 v52, v20, v22\n"
    "v_cvt_pk_bf16_f32 v53, v21, v23\n"
    "v_accvgpr_write_b32 a16, v52\n"
    "v_accvgpr_write_b32 a20, v53\n"
    "v_mfma_f32_4x4x4_16b_bf16 v[32:35], v[44:45], a[210:211], 0 cbsz:2 abid:3\n"
    "buffer_load_dwordx4 v249, s[44:47], s84 offen offset:1024 lds\n"
    "v_cvt_pk_bf16_f32 v54, v24, v26\n"
    "v_cvt_pk_bf16_f32 v55, v25, v27\n"
    "v_accvgpr_write_b32 a17, v54\n"
    "v_accvgpr_write_b32 a21, v55\n"
    "v_lshlrev_b64 v[50:51], v16, v[254:255]\n"
    "s_add_u32 s77, s57, 36864\n"
    "s_waitcnt vmcnt(8) lgkmcnt(2)\n"
    "ds_read2st64_b64 a[200:203], v15 offset0:18 offset1:19\n"
    "v_mfma_f32_4x4x4_16b_bf16 v[20:23], v[46:47], a[188:189], 0 cbsz:2 abid:0\n"
    "ds_read_u16 v250, v18 offset:128\n"
    "s_add_u32 m0, s83, 55296\n"
    "s_add_u32 s84, s61, s62\n"
    "v_cvt_pk_bf16_f32 v52, v28, v30\n"
    "v_cvt_pk_bf16_f32 v53, v29, v31\n"
    "v_accvgpr_write_b32 a18, v52\n"
    "v_accvgpr_write_b32 a22, v53\n"
    "v_mfma_f32_4x4x4_16b_bf16 v[24:27], v[46:47], a[190:191], 0 cbsz:2 abid:1\n"
    "buffer_load_dwordx4 v244, s[44:47], s84 offen lds\n"
    "v_cvt_pk_bf16_f32 v54, v32, v34\n"
    "v_cvt_pk_bf16_f32 v55, v33, v35\n"
    "v_accvgpr_write_b32 a19, v54\n"
    "v_accvgpr_write_b32 a23, v55\n"
    "s_movk_i32 s88, 24576\n"
    "s_waitcnt vmcnt(8) lgkmcnt(3)\n"
    "ds_read2st64_b32 a[204:205], v38 offset0:40 offset1:41\n"
    "v_mfma_f32_4x4x4_16b_bf16 v[28:31], v[46:47], a[192:193], 0 cbsz:2 abid:2\n"
    "ds_read2st64_b32 a[206:207], v38 offset0:42 offset1:43\n"
    "ds_read_u16 v252, v18 offset:136\n"
    "v_cvt_pk_bf16_f32 v52, v20, v24\n"
    "v_cvt_pk_bf16_f32 v53, v21, v25\n"
    "v_accvgpr_write_b32 a24, v52\n"
    "v_accvgpr_write_b32 a28, v53\n"
    "v_mfma_f32_4x4x4_16b_bf16 v[32:35], v[46:47], a[194:195], 0 cbsz:2 abid:3\n"
    "buffer_load_dwordx4 v245, s[44:47], s84 offen offset:1024 lds\n"
    "v_cvt_pk_bf16_f32 v54, v22, v26\n"
    "v_cvt_pk_bf16_f32 v55, v23, v27\n"
    "v_accvgpr_write_b32 a32, v54\n"
    "v_accvgpr_write_b32 a36, v55\n"
    "s_movk_i32 s78, 24576\n"
    "s_waitcnt vmcnt(8) lgkmcnt(5)\n"
    "ds_read2st64_b32 a[208:209], v38 offset0:44 offset1:45\n"
    "v_mfma_f32_4x4x4_16b_bf16 v[20:23], v[48:49], a[196:197], 0 cbsz:2 abid:0\n"
    "ds_read2st64_b32 a[210:211], v38 offset0:46 offset1:47\n"
    "ds_read_u16 v254, v235 offset:128\n"
    "v_cvt_pk_bf16_f32 v52, v28, v32\n"
    "v_cvt_pk_bf16_f32 v53, v29, v33\n"
    "v_accvgpr_write_b32 a25, v52\n"
    "v_accvgpr_write_b32 a29, v53\n"
    "v_mfma_f32_4x4x4_16b_bf16 v[24:27], v[48:49], a[198:199], 0 cbsz:2 abid:1\n"
    "buffer_load_dwordx4 v246, s[44:47], s84 offen offset:2048 lds\n"
    "v_cvt_pk_bf16_f32 v54, v30, v34\n"
    "v_cvt_pk_bf16_f32 v55, v31, v35\n"
    "v_accvgpr_write_b32 a33, v54\n"
    "v_accvgpr_write_b32 a37, v55\n"
    "buffer_load_dwordx4 v247, s[44:47], s84 offen offset:3072 lds\n"
    "s_mov_b32 s87, -122880\n"
    "s_waitcnt vmcnt(9) lgkmcnt(6)\n"
    "ds_read2st64_b64 a[188:191], v15 offset0:24 offset1:25\n"
    "v_mfma_f32_4x4x4_16b_bf16 v[28:31], v[48:49], a[200:201], 0 cbsz:2 abid:2\n"
    "s_add_u32 m0, s83, 59392\n"
    "v_cvt_pk_bf16_f32 v52, v20, v24\n"
    "v_cvt_pk_bf16_f32 v53, v21, v25\n"
    "v_accvgpr_write_b32 a26, v52\n"
    "v_accvgpr_write_b32 a30, v53\n"
    "v_mfma_f32_4x4x4_16b_bf16 v[32:35], v[48:49], a[202:203], 0 cbsz:2 abid:3\n"
    "buffer_load_dwordx4 v248, s[44:47], s84 offen lds\n"
    "v_cvt_pk_bf16_f32 v54, v22, v26\n"
    "v_cvt_pk_bf16_f32 v55, v23, v27\n"
    "v_accvgpr_write_b32 a34, v54\n"
    "v_accvgpr_write_b32 a38, v55\n"
    "v_lshlrev_b64 v[40:41], v16, v[250:251]\n"
    "s_add_u32 s80, s57, 147456\n"
    "s_waitcnt vmcnt(9) lgkmcnt(4)\n"
    "ds_read2st64_b64 a[192:195], v15 offset0:26 offset1:27\n"
    "v_mfma_f32_4x4x4_16b_bf16 v[20:23], v[50:51], a[204:205], 0 cbsz:2 abid:0\n"
    "v_cvt_pk_bf16_f32 v52, v28, v32\n"
    "v_cvt_pk_bf16_f32 v53, v29, v33\n"
    "v_accvgpr_write_b32 a27, v52\n"
    "v_accvgpr_write_b32 a31, v53\n"
    "v_mfma_f32_4x4x4_16b_bf16 v[24:27], v[50:51], a[206:207], 0 cbsz:2 abid:1\n"
    "buffer_load_dwordx4 v249, s[44:47], s84 offen offset:1024 lds\n"
    "v_cvt_pk_bf16_f32 v54, v30, v34\n"
    "v_cvt_pk_bf16_f32 v55, v31, v35\n"
    "v_accvgpr_write_b32 a35, v54\n"
    "v_accvgpr_write_b32 a39, v55\n"
    "v_lshlrev_b64 v[42:43], v16, v[252:253]\n"
    "s_mov_b32 s86, 5\n"
    "s_waitcnt vmcnt(9) lgkmcnt(2)\n"
    "ds_read2st64_b64 a[196:199], v15 offset0:28 offset1:29\n"
    "v_mfma_f32_4x4x4_16b_bf16 v[28:31], v[50:51], a[208:209], 0 cbsz:2 abid:2\n"
    "s_add_u32 m0, s83, 61440\n"
    "s_add_u32 s84, s58, s63\n"
    "v_cvt_pk_bf16_f32 v52, v20, v22\n"
    "v_cvt_pk_bf16_f32 v53, v21, v23\n"
    "v_accvgpr_write_b32 a40, v52\n"
    "v_accvgpr_write_b32 a44, v53\n"
    "v_mfma_f32_4x4x4_16b_bf16 v[32:35], v[50:51], a[210:211], 0 cbsz:2 abid:3\n"
    "buffer_load_dwordx4 v244, s[44:47], s84 offen lds\n"
    "v_cvt_pk_bf16_f32 v54, v24, v26\n"
    "v_cvt_pk_bf16_f32 v55, v25, v27\n"
    "v_accvgpr_write_b32 a41, v54\n"
    "v_accvgpr_write_b32 a45, v55\n"
    "v_lshlrev_b64 v[44:45], v16, v[254:255]\n"
    "s_lshr_b32 s7, s2, 1\n"
    "s_waitcnt vmcnt(9) lgkmcnt(2)\n"
    "ds_read2st64_b64 a[200:203], v15 offset0:30 offset1:31\n"
    "v_mfma_f32_4x4x4_16b_bf16 v[20:23], v[40:41], a[188:189], 0 cbsz:2 abid:0\n"
    "ds_read_u16 v250, v18 offset:192\n"
    "v_cvt_pk_bf16_f32 v52, v28, v30\n"
    "v_cvt_pk_bf16_f32 v53, v29, v31\n"
    "v_accvgpr_write_b32 a42, v52\n"
    "v_accvgpr_write_b32 a46, v53\n"
    "v_mfma_f32_4x4x4_16b_bf16 v[24:27], v[40:41], a[190:191], 0 cbsz:2 abid:1\n"
    "buffer_load_dwordx4 v245, s[44:47], s84 offen offset:1024 lds\n"
    "v_cvt_pk_bf16_f32 v54, v32, v34\n"
    "v_cvt_pk_bf16_f32 v55, v33, v35\n"
    "v_accvgpr_write_b32 a43, v54\n"
    "v_accvgpr_write_b32 a47, v55\n"
    "buffer_load_dwordx4 v246, s[44:47], s84 offen offset:2048 lds\n"
    "s_and_b32 s7, s7, 0x7c\n"
    "s_waitcnt vmcnt(10) lgkmcnt(3)\n"
    "ds_read2st64_b32 a[204:205], v38 offset0:64 offset1:65\n"
    "v_mfma_f32_4x4x4_16b_bf16 v[28:31], v[40:41], a[192:193], 0 cbsz:2 abid:2\n"
    "ds_read2st64_b32 a[206:207], v38 offset0:66 offset1:67\n"
    "ds_read_u16 v252, v18 offset:200\n"
    "v_cvt_pk_bf16_f32 v52, v20, v24\n"
    "v_cvt_pk_bf16_f32 v53, v21, v25\n"
    "v_accvgpr_write_b32 a48, v52\n"
    "v_accvgpr_write_b32 a52, v53\n"
    "v_mfma_f32_4x4x4_16b_bf16 v[32:35], v[40:41], a[194:195], 0 cbsz:2 abid:3\n"
    "buffer_load_dwordx4 v247, s[44:47], s84 offen offset:3072 lds\n"
    "v_cvt_pk_bf16_f32 v54, v22, v26\n"
    "v_cvt_pk_bf16_f32 v55, v23, v27\n"
    "v_accvgpr_write_b32 a56, v54\n"
    "v_accvgpr_write_b32 a60, v55\n"
    "s_or_b32 s7, s7, s28\n"
    "s_waitcnt vmcnt(10) lgkmcnt(5)\n"
    "ds_read2st64_b32 a[208:209], v38 offset0:68 offset1:69\n"
    "v_mfma_f32_4x4x4_16b_bf16 v[20:23], v[42:43], a[196:197], 0 cbsz:2 abid:0\n"
    "ds_read2st64_b32 a[210:211], v38 offset0:70 offset1:71\n"
    "ds_read_u16 v254, v235 offset:192\n"
    "s_add_u32 m0, s83, 65536\n"
    "v_cvt_pk_bf16_f32 v52, v28, v32\n"
    "v_cvt_pk_bf16_f32 v53, v29, v33\n"
    "v_accvgpr_write_b32 a49, v52\n"
    "v_accvgpr_write_b32 a53, v53\n"
    "v_mfma_f32_4x4x4_16b_bf16 v[24:27], v[42:43], a[198:199], 0 cbsz:2 abid:1\n"
    "buffer_load_dwordx4 v248, s[44:47], s84 offen lds\n"
    "v_cvt_pk_bf16_f32 v54, v30, v34\n"
    "v_cvt_pk_bf16_f32 v55, v31, v35\n"
    "v_accvgpr_write_b32 a57, v54\n"
    "v_accvgpr_write_b32 a61, v55\n"
    "s_mul_i32 s7, s7, 79\n"
    "s_waitcnt vmcnt(10) lgkmcnt(6)\n"
    "ds_read2st64_b64 a[188:191], v15 offset0:36 offset1:37\n"
    "v_mfma_f32_4x4x4_16b_bf16 v[28:31], v[42:43], a[200:201], 0 cbsz:2 abid:2\n"
    "v_cvt_pk_bf16_f32 v52, v20, v24\n"
    "v_cvt_pk_bf16_f32 v53, v21, v25\n"
    "v_accvgpr_write_b32 a50, v52\n"
    "v_accvgpr_write_b32 a54, v53\n"
    "v_mfma_f32_4x4x4_16b_bf16 v[32:35], v[42:43], a[202:203], 0 cbsz:2 abid:3\n"
    "buffer_load_dwordx4 v249, s[44:47], s84 offen offset:1024 lds\n"
    "v_cvt_pk_bf16_f32 v54, v22, v26\n"
    "v_cvt_pk_bf16_f32 v55, v23, v27\n"
    "v_accvgpr_write_b32 a58, v54\n"
    "v_accvgpr_write_b32 a62, v55\n"
    "v_lshlrev_b64 v[46:47], v16, v[250:251]\n"
    "s_and_b32 s7, s7, 0x7f\n"
    "s_waitcnt vmcnt(10) lgkmcnt(4)\n"
    "ds_read2st64_b64 a[192:195], v15 offset0:38 offset1:39\n"
    "v_mfma_f32_4x4x4_16b_bf16 v[20:23], v[44:45], a[204:205], 0 cbsz:2 abid:0\n"
    "s_add_u32 m0, s83, 36864\n"
    "s_add_u32 s84, s59, s63\n"
    "v_cvt_pk_bf16_f32 v52, v28, v32\n"
    "v_cvt_pk_bf16_f32 v53, v29, v33\n"
    "v_accvgpr_write_b32 a51, v52\n"
    "v_accvgpr_write_b32 a55, v53\n"
    "v_mfma_f32_4x4x4_16b_bf16 v[24:27], v[44:45], a[206:207], 0 cbsz:2 abid:1\n"
    "buffer_load_dwordx4 v244, s[44:47], s84 offen lds\n"
    "v_cvt_pk_bf16_f32 v54, v30, v34\n"
    "v_cvt_pk_bf16_f32 v55, v31, v35\n"
    "v_accvgpr_write_b32 a59, v54\n"
    "v_accvgpr_write_b32 a63, v55\n"
    "buffer_load_dwordx4 v245, s[44:47], s84 offen offset:1024 lds\n"
    "v_lshlrev_b64 v[48:49], v16, v[252:253]\n"
    "s_mul_i32 s7, s7, 50\n"
    "s_waitcnt vmcnt(11) lgkmcnt(2)\n"
    "ds_read2st64_b64 a[196:199], v15 offset0:40 offset1:41\n"
    "v_mfma_f32_4x4x4_16b_bf16 v[28:31], v[44:45], a[208:209], 0 cbsz:2 abid:2\n"
    "v_cvt_pk_bf16_f32 v52, v20, v22\n"
    "v_cvt_pk_bf16_f32 v53, v21, v23\n"
    "v_accvgpr_write_b32 a64, v52\n"
    "v_accvgpr_write_b32 a68, v53\n"
    "v_mfma_f32_4x4x4_16b_bf16 v[32:35], v[44:45], a[210:211], 0 cbsz:2 abid:3\n"
    "buffer_load_dwordx4 v246, s[44:47], s84 offen offset:2048 lds\n"
    "v_cvt_pk_bf16_f32 v54, v24, v26\n"
    "v_cvt_pk_bf16_f32 v55, v25, v27\n"
    "v_accvgpr_write_b32 a65, v54\n"
    "v_accvgpr_write_b32 a69, v55\n"
    "v_lshlrev_b64 v[50:51], v16, v[254:255]\n"
    "s_lshr_b32 s7, s7, 6\n"
    "s_waitcnt vmcnt(11) lgkmcnt(2)\n"
    "ds_read2st64_b64 a[200:203], v15 offset0:42 offset1:43\n"
    "v_mfma_f32_4x4x4_16b_bf16 v[20:23], v[46:47], a[188:189], 0 cbsz:2 abid:0\n"
    "ds_read_u16 v250, v18 offset:256\n"
    "v_cvt_pk_bf16_f32 v52, v28, v30\n"
    "v_cvt_pk_bf16_f32 v53, v29, v31\n"
    "v_accvgpr_write_b32 a66, v52\n"
    "v_accvgpr_write_b32 a70, v53\n"
    "v_mfma_f32_4x4x4_16b_bf16 v[24:27], v[46:47], a[190:191], 0 cbsz:2 abid:1\n"
    "buffer_load_dwordx4 v247, s[44:47], s84 offen offset:3072 lds\n"
    "v_cvt_pk_bf16_f32 v54, v32, v34\n"
    "v_cvt_pk_bf16_f32 v55, v33, v35\n"
    "v_accvgpr_write_b32 a67, v54\n"
    "v_accvgpr_write_b32 a71, v55\n"
    "s_sub_i32 s7, s7, 82\n"
    "s_waitcnt vmcnt(11) lgkmcnt(3)\n"
    "ds_read2st64_b32 a[204:205], v38 offset0:88 offset1:89\n"
    "v_mfma_f32_4x4x4_16b_bf16 v[28:31], v[46:47], a[192:193], 0 cbsz:2 abid:2\n"
    "ds_read2st64_b32 a[206:207], v38 offset0:90 offset1:91\n"
    "ds_read_u16 v252, v18 offset:264\n"
    "s_add_u32 m0, s83, 40960\n"
    "v_cvt_pk_bf16_f32 v52, v20, v24\n"
    "v_cvt_pk_bf16_f32 v53, v21, v25\n"
    "v_accvgpr_write_b32 a72, v52\n"
    "v_accvgpr_write_b32 a76, v53\n"
    "v_mfma_f32_4x4x4_16b_bf16 v[32:35], v[46:47], a[194:195], 0 cbsz:2 abid:3\n"
    "buffer_load_dwordx4 v248, s[44:47], s84 offen lds\n"
    "v_cvt_pk_bf16_f32 v54, v22, v26\n"
    "v_cvt_pk_bf16_f32 v55, v23, v27\n"
    "v_accvgpr_write_b32 a80, v54\n"
    "v_accvgpr_write_b32 a84, v55\n"
    "s_max_i32 s7, s7, 0\n"
    "s_waitcnt vmcnt(11) lgkmcnt(5)\n"
    "ds_read2st64_b32 a[208:209], v38 offset0:92 offset1:93\n"
    "v_mfma_f32_4x4x4_16b_bf16 v[20:23], v[48:49], a[196:197], 0 cbsz:2 abid:0\n"
    "ds_read2st64_b32 a[210:211], v38 offset0:94 offset1:95\n"
    "ds_read_u16 v254, v235 offset:256\n"
    "v_cvt_pk_bf16_f32 v52, v28, v32\n"
    "v_cvt_pk_bf16_f32 v53, v29, v33\n"
    "v_accvgpr_write_b32 a73, v52\n"
    "v_accvgpr_write_b32 a77, v53\n"
    "v_mfma_f32_4x4x4_16b_bf16 v[24:27], v[48:49], a[198:199], 0 cbsz:2 abid:1\n"
    "buffer_load_dwordx4 v249, s[44:47], s84 offen offset:1024 lds\n"
    "s_add_u32 m0, s83, 43008\n"
    "s_add_u32 s84, s60, s63\n"
    "v_cvt_pk_bf16_f32 v54, v30, v34\n"
    "v_cvt_pk_bf16_f32 v55, v31, v35\n"
    "v_accvgpr_write_b32 a81, v54\n"
    "v_accvgpr_write_b32 a85, v55\n"
    "buffer_load_dwordx4 v244, s[44:47], s84 offen lds\n"
    "s_min_i32 s7, s7, 50\n"
    "s_waitcnt vmcnt(12) lgkmcnt(6)\n"
    "ds_read2st64_b64 a[188:191], v15 offset0:48 offset1:49\n"
    "v_mfma_f32_4x4x4_16b_bf16 v[28:31], v[48:49], a[200:201], 0 cbsz:2 abid:2\n"
    "v_cvt_pk_bf16_f32 v52, v20, v24\n"
    "v_cvt_pk_bf16_f32 v53, v21, v25\n"
    "v_accvgpr_write_b32 a74, v52\n"
    "v_accvgpr_write_b32 a78, v53\n"
    "v_mfma_f32_4x4x4_16b_bf16 v[32:35], v[48:49], a[202:203], 0 cbsz:2 abid:3\n"
    "buffer_load_dwordx4 v245, s[44:47], s84 offen offset:1024 lds\n"
    "v_cvt_pk_bf16_f32 v54, v22, v26\n"
    "v_cvt_pk_bf16_f32 v55, v23, v27\n"
    "v_accvgpr_write_b32 a82, v54\n"
    "v_accvgpr_write_b32 a86, v55\n"
    "v_lshlrev_b64 v[40:41], v16, v[250:251]\n"
    "s_bfm_b64 s[34:35], s7, 2\n"
    "s_waitcnt vmcnt(12) lgkmcnt(4)\n"
    "ds_read2st64_b64 a[192:195], v15 offset0:50 offset1:51\n"
    "v_mfma_f32_4x4x4_16b_bf16 v[20:23], v[50:51], a[204:205], 0 cbsz:2 abid:0\n"
    "v_cvt_pk_bf16_f32 v52, v28, v32\n"
    "v_cvt_pk_bf16_f32 v53, v29, v33\n"
    "v_accvgpr_write_b32 a75, v52\n"
    "v_accvgpr_write_b32 a79, v53\n"
    "v_mfma_f32_4x4x4_16b_bf16 v[24:27], v[50:51], a[206:207], 0 cbsz:2 abid:1\n"
    "buffer_load_dwordx4 v246, s[44:47], s84 offen offset:2048 lds\n"
    "v_cvt_pk_bf16_f32 v54, v30, v34\n"
    "v_cvt_pk_bf16_f32 v55, v31, v35\n"
    "v_accvgpr_write_b32 a83, v54\n"
    "v_accvgpr_write_b32 a87, v55\n"
    "v_lshlrev_b64 v[42:43], v16, v[252:253]\n"
    "s_waitcnt vmcnt(12) lgkmcnt(2)\n"
    "ds_read2st64_b64 a[196:199], v15 offset0:52 offset1:53\n"
    "v_mfma_f32_4x4x4_16b_bf16 v[28:31], v[50:51], a[208:209], 0 cbsz:2 abid:2\n"
    "v_cvt_pk_bf16_f32 v56, v20, v22\n"
    "v_cvt_pk_bf16_f32 v60, v21, v23\n"
    "v_mfma_f32_4x4x4_16b_bf16 v[32:35], v[50:51], a[210:211], 0 cbsz:2 abid:3\n"
    "buffer_load_dwordx4 v247, s[44:47], s84 offen offset:3072 lds\n"
    "v_cvt_pk_bf16_f32 v57, v24, v26\n"
    "v_cvt_pk_bf16_f32 v61, v25, v27\n"
    "v_lshlrev_b64 v[44:45], v16, v[254:255]\n"
    "s_waitcnt vmcnt(12) lgkmcnt(2)\n"
    "ds_read2st64_b64 a[200:203], v15 offset0:54 offset1:55\n"
    "v_mfma_f32_4x4x4_16b_bf16 v[20:23], v[40:41], a[188:189], 0 cbsz:2 abid:0\n"
    "ds_read_u16 v250, v18 offset:320\n"
    "s_add_u32 m0, s83, 47104\n"
    "v_cvt_pk_bf16_f32 v58, v28, v30\n"
    "v_cvt_pk_bf16_f32 v62, v29, v31\n"
    "v_mfma_f32_4x4x4_16b_bf16 v[24:27], v[40:41], a[190:191], 0 cbsz:2 abid:1\n"
    "buffer_load_dwordx4 v248, s[44:47], s84 offen lds\n"
    "v_cvt_pk_bf16_f32 v59, v32, v34\n"
    "v_cvt_pk_bf16_f32 v63, v33, v35\n"
    "buffer_load_dwordx4 v249, s[44:47], s84 offen offset:1024 lds\n"
    "s_waitcnt vmcnt(13) lgkmcnt(3)\n"
    "ds_read2st64_b32 a[204:205], v38 offset0:112 offset1:113\n"
    "v_mfma_f32_4x4x4_16b_bf16 v[28:31], v[40:41], a[192:193], 0 cbsz:2 abid:2\n"
    "ds_read2st64_b32 a[206:207], v38 offset0:114 offset1:115\n"
    "ds_read_u16 v252, v18 offset:328\n"
    "s_add_u32 m0, s83, 49152\n"
    "s_add_u32 s84, s61, s63\n"
    "v_cvt_pk_bf16_f32 v64, v20, v24\n"
    "v_cvt_pk_bf16_f32 v68, v21, v25\n"
    "v_mfma_f32_4x4x4_16b_bf16 v[32:35], v[40:41], a[194:195], 0 cbsz:2 abid:3\n"
    "buffer_load_dwordx4 v244, s[44:47], s84 offen lds\n"
    "v_cvt_pk_bf16_f32 v72, v22, v26\n"
    "v_cvt_pk_bf16_f32 v76, v23, v27\n"
    "s_waitcnt vmcnt(13) lgkmcnt(5)\n"
    "ds_read2st64_b32 a[208:209], v38 offset0:116 offset1:117\n"
    "v_mfma_f32_4x4x4_16b_bf16 v[20:23], v[42:43], a[196:197], 0 cbsz:2 abid:0\n"
    "ds_read2st64_b32 a[210:211], v38 offset0:118 offset1:119\n"
    "ds_read_u16 v254, v235 offset:320\n"
    "v_cvt_pk_bf16_f32 v65, v28, v32\n"
    "v_cvt_pk_bf16_f32 v69, v29, v33\n"
    "v_mfma_f32_4x4x4_16b_bf16 v[24:27], v[42:43], a[198:199], 0 cbsz:2 abid:1\n"
    "buffer_load_dwordx4 v245, s[44:47], s84 offen offset:1024 lds\n"
    "v_cvt_pk_bf16_f32 v73, v30, v34\n"
    "v_cvt_pk_bf16_f32 v77, v31, v35\n"
    "s_waitcnt vmcnt(13) lgkmcnt(6)\n"
    "ds_read2st64_b64 a[188:191], v15 offset0:0 offset1:1\n"
    "v_mfma_f32_4x4x4_16b_bf16 v[28:31], v[42:43], a[200:201], 0 cbsz:2 abid:2\n"
    "v_cvt_pk_bf16_f32 v66, v20, v24\n"
    "v_cvt_pk_bf16_f32 v70, v21, v25\n"
    "v_mfma_f32_4x4x4_16b_bf16 v[32:35], v[42:43], a[202:203], 0 cbsz:2 abid:3\n"
    "buffer_load_dwordx4 v246, s[44:47], s84 offen offset:2048 lds\n"
    "v_cvt_pk_bf16_f32 v74, v22, v26\n"
    "v_cvt_pk_bf16_f32 v78, v23, v27\n"
    "v_lshlrev_b64 v[46:47], v16, v[250:251]\n"
    "s_waitcnt vmcnt(13) lgkmcnt(4)\n"
    "ds_read2st64_b64 a[192:195], v15 offset0:2 offset1:3\n"
    "v_mfma_f32_4x4x4_16b_bf16 v[20:23], v[44:45], a[204:205], 0 cbsz:2 abid:0\n"
    "v_cvt_pk_bf16_f32 v67, v28, v32\n"
    "v_cvt_pk_bf16_f32 v71, v29, v33\n"
    "v_mfma_f32_4x4x4_16b_bf16 v[24:27], v[44:45], a[206:207], 0 cbsz:2 abid:1\n"
    "buffer_load_dwordx4 v247, s[44:47], s84 offen offset:3072 lds\n"
    "s_add_u32 m0, s83, 53248\n"
    "v_cvt_pk_bf16_f32 v75, v30, v34\n"
    "v_cvt_pk_bf16_f32 v79, v31, v35\n"
    "buffer_load_dwordx4 v248, s[44:47], s84 offen lds\n"
    "v_lshlrev_b64 v[48:49], v16, v[252:253]\n"
    "s_waitcnt vmcnt(14) lgkmcnt(2)\n"
    "ds_read2st64_b64 a[196:199], v15 offset0:4 offset1:5\n"
    "v_mfma_f32_4x4x4_16b_bf16 v[28:31], v[44:45], a[208:209], 0 cbsz:2 abid:2\n"
    "v_cvt_pk_bf16_f32 v80, v20, v22\n"
    "v_cvt_pk_bf16_f32 v84, v21, v23\n"
    "v_mfma_f32_4x4x4_16b_bf16 v[32:35], v[44:45], a[210:211], 0 cbsz:2 abid:3\n"
    "buffer_load_dwordx4 v249, s[44:47], s84 offen offset:1024 lds\n"
    "v_cvt_pk_bf16_f32 v81, v24, v26\n"
    "v_cvt_pk_bf16_f32 v85, v25, v27\n"
    "v_lshlrev_b64 v[50:51], v16, v[254:255]\n"
    "s_waitcnt vmcnt(14) lgkmcnt(2)\n"
    "ds_read2st64_b64 a[200:203], v15 offset0:6 offset1:7\n"
    "v_mfma_f32_4x4x4_16b_bf16 v[20:23], v[46:47], a[188:189], 0 cbsz:2 abid:0\n"
    "ds_read_u16 v250, v18 offset:384\n"
    "s_memrealtime s[92:93]\n"
    "s_add_u32 m0, s83, 55296\n"
    "s_add_u32 s84, s58, s64\n"
    "v_cvt_pk_bf16_f32 v82, v28, v30\n"
    "v_cvt_pk_bf16_f32 v86, v29, v31\n"
    "v_mfma_f32_4x4x4_16b_bf16 v[24:27], v[46:47], a[190:191], 0 cbsz:2 abid:1\n"
    "buffer_load_dwordx4 v244, s[44:47], s84 offen lds\n"
    "v_cvt_pk_bf16_f32 v83, v32, v34\n"
    "v_cvt_pk_bf16_f32 v87, v33, v35\n"
    "s_waitcnt vmcnt(14) lgkmcnt(3)\n"
    "ds_read2st64_b32 a[204:205], v38 offset0:16 offset1:17\n"
    "v_mfma_f32_4x4x4_16b_bf16 v[28:31], v[46:47], a[192:193], 0 cbsz:2 abid:2\n"
    "ds_read2st64_b32 a[206:207], v38 offset0:18 offset1:19\n"
    "ds_read_u16 v252, v18 offset:392\n"
    "v_cvt_pk_bf16_f32 v88, v20, v24\n"
    "v_cvt_pk_bf16_f32 v92, v21, v25\n"
    "v_mfma_f32_4x4x4_16b_bf16 v[32:35], v[46:47], a[194:195], 0 cbsz:2 abid:3\n"
    "buffer_load_dwordx4 v245, s[44:47], s84 offen offset:1024 lds\n"
    "v_cvt_pk_bf16_f32 v96, v22, v26\n"
    "v_cvt_pk_bf16_f32 v100, v23, v27\n"
    "s_waitcnt vmcnt(14) lgkmcnt(5)\n"
    "ds_read2st64_b32 a[208:209], v38 offset0:20 offset1:21\n"
    "v_mfma_f32_4x4x4_16b_bf16 v[20:23], v[48:49], a[196:197], 0 cbsz:2 abid:0\n"
    "ds_read2st64_b32 a[210:211], v38 offset0:22 offset1:23\n"
    "ds_read_u16 v254, v235 offset:384\n"
    "global_load_dwordx4 v[236:239], v16, s[54:55]\n"
    "v_cvt_pk_bf16_f32 v89, v28, v32\n"
    "v_cvt_pk_bf16_f32 v93, v29, v33\n"
    "v_mfma_f32_4x4x4_16b_bf16 v[24:27], v[48:49], a[198:199], 0 cbsz:2 abid:1\n"
    "buffer_load_dwordx4 v246, s[44:47], s84 offen offset:2048 lds\n"
    "v_cvt_pk_bf16_f32 v97, v30, v34\n"
    "v_cvt_pk_bf16_f32 v101, v31, v35\n"
    "buffer_load_dwordx4 v247, s[44:47], s84 offen offset:3072 lds\n"
    "s_waitcnt vmcnt(16) lgkmcnt(6)\n"
    "ds_read2st64_b64 a[188:191], v15 offset0:12 offset1:13\n"
    "v_mfma_f32_4x4x4_16b_bf16 v[28:31], v[48:49], a[200:201], 0 cbsz:2 abid:2\n"
    "s_add_u32 m0, s83, 59392\n"
    "v_cvt_pk_bf16_f32 v90, v20, v24\n"
    "v_cvt_pk_bf16_f32 v94, v21, v25\n"
    "v_mfma_f32_4x4x4_16b_bf16 v[32:35], v[48:49], a[202:203], 0 cbsz:2 abid:3\n"
    "buffer_load_dwordx4 v248, s[44:47], s84 offen lds\n"
    "v_cvt_pk_bf16_f32 v98, v22, v26\n"
    "v_cvt_pk_bf16_f32 v102, v23, v27\n"
    "v_lshlrev_b64 v[40:41], v16, v[250:251]\n"
    "s_waitcnt vmcnt(16) lgkmcnt(4)\n"
    "ds_read2st64_b64 a[192:195], v15 offset0:14 offset1:15\n"
    "v_mfma_f32_4x4x4_16b_bf16 v[20:23], v[50:51], a[204:205], 0 cbsz:2 abid:0\n"
    "v_cvt_pk_bf16_f32 v91, v28, v32\n"
    "v_cvt_pk_bf16_f32 v95, v29, v33\n"
    "v_mfma_f32_4x4x4_16b_bf16 v[24:27], v[50:51], a[206:207], 0 cbsz:2 abid:1\n"
    "buffer_load_dwordx4 v249, s[44:47], s84 offen offset:1024 lds\n"
    "v_cvt_pk_bf16_f32 v99, v30, v34\n"
    "v_cvt_pk_bf16_f32 v103, v31, v35\n"
    "v_lshlrev_b64 v[42:43], v16, v[252:253]\n"
    "s_waitcnt vmcnt(16) lgkmcnt(2)\n"
    "ds_read2st64_b64 a[196:199], v15 offset0:16 offset1:17\n"
    "v_mfma_f32_4x4x4_16b_bf16 v[28:31], v[50:51], a[208:209], 0 cbsz:2 abid:2\n"
    "s_add_u32 m0, s83, 61440\n"
    "s_add_u32 s84, s59, s64\n"
    "v_cvt_pk_bf16_f32 v104, v20, v22\n"
    "v_cvt_pk_bf16_f32 v108, v21, v23\n"
    "v_mfma_f32_4x4x4_16b_bf16 v[32:35], v[50:51], a[210:211], 0 cbsz:2 abid:3\n"
    "buffer_load_dwordx4 v244, s[44:47], s84 offen lds\n"
    "v_cvt_pk_bf16_f32 v105, v24, v26\n"
    "v_cvt_pk_bf16_f32 v109, v25, v27\n"
    "v_lshlrev_b64 v[44:45], v16, v[254:255]\n"
    "s_waitcnt vmcnt(16) lgkmcnt(2)\n"
    "ds_read2st64_b64 a[200:203], v15 offset0:18 offset1:19\n"
    "v_mfma_f32_4x4x4_16b_bf16 v[20:23], v[40:41], a[188:189], 0 cbsz:2 abid:0\n"
    "ds_read_u16 v250, v18 offset:448\n"
    "v_cvt_pk_bf16_f32 v106, v28, v30\n"
    "v_cvt_pk_bf16_f32 v110, v29, v31\n"
    "v_mfma_f32_4x4x4_16b_bf16 v[24:27], v[40:41], a[190:191], 0 cbsz:2 abid:1\n"
    "buffer_load_dwordx4 v245, s[44:47], s84 offen offset:1024 lds\n"
    "v_cvt_pk_bf16_f32 v107, v32, v34\n"
    "v_cvt_pk_bf16_f32 v111, v33, v35\n"
    "buffer_load_dwordx4 v246, s[44:47], s84 offen offset:2048 lds\n"
    "s_waitcnt vmcnt(17) lgkmcnt(3)\n"
    "ds_read2st64_b32 a[204:205], v38 offset0:40 offset1:41\n"
    "v_mfma_f32_4x4x4_16b_bf16 v[28:31], v[40:41], a[192:193], 0 cbsz:2 abid:2\n"
    "ds_read2st64_b32 a[206:207], v38 offset0:42 offset1:43\n"
    "ds_read_u16 v252, v18 offset:456\n"
    "v_cvt_pk_bf16_f32 v112, v20, v24\n"
    "v_cvt_pk_bf16_f32 v116, v21, v25\n"
    "v_mfma_f32_4x4x4_16b_bf16 v[32:35], v[40:41], a[194:195], 0 cbsz:2 abid:3\n"
    "buffer_load_dwordx4 v247, s[44:47], s84 offen offset:3072 lds\n"
    "v_cvt_pk_bf16_f32 v120, v22, v26\n"
    "v_cvt_pk_bf16_f32 v124, v23, v27\n"
    "s_waitcnt vmcnt(17) lgkmcnt(5)\n"
    "ds_read2st64_b32 a[208:209], v38 offset0:44 offset1:45\n"
    "v_mfma_f32_4x4x4_16b_bf16 v[20:23], v[42:43], a[196:197], 0 cbsz:2 abid:0\n"
    "ds_read2st64_b32 a[210:211], v38 offset0:46 offset1:47\n"
    "ds_read_u16 v254, v235 offset:448\n"
    "s_add_u32 m0, s83, 65536\n"
    "v_cvt_pk_bf16_f32 v113, v28, v32\n"
    "v_cvt_pk_bf16_f32 v117, v29, v33\n"
    "v_mfma_f32_4x4x4_16b_bf16 v[24:27], v[42:43], a[198:199], 0 cbsz:2 abid:1\n"
    "buffer_load_dwordx4 v248, s[44:47], s84 offen lds\n"
    "v_cvt_pk_bf16_f32 v121, v30, v34\n"
    "v_cvt_pk_bf16_f32 v125, v31, v35\n"
    "s_waitcnt vmcnt(17) lgkmcnt(6)\n"
    "ds_read2st64_b64 a[188:191], v15 offset0:24 offset1:25\n"
    "v_mfma_f32_4x4x4_16b_bf16 v[28:31], v[42:43], a[200:201], 0 cbsz:2 abid:2\n"
    "v_cvt_pk_bf16_f32 v114, v20, v24\n"
    "v_cvt_pk_bf16_f32 v118, v21, v25\n"
    "v_mfma_f32_4x4x4_16b_bf16 v[32:35], v[42:43], a[202:203], 0 cbsz:2 abid:3\n"
    "buffer_load_dwordx4 v249, s[44:47], s84 offen offset:1024 lds\n"
    "v_cvt_pk_bf16_f32 v122, v22, v26\n"
    "v_cvt_pk_bf16_f32 v126, v23, v27\n"
    "v_lshlrev_b64 v[46:47], v16, v[250:251]\n"
    "s_waitcnt vmcnt(17) lgkmcnt(4)\n"
    "ds_read2st64_b64 a[192:195], v15 offset0:26 offset1:27\n"
    "v_mfma_f32_4x4x4_16b_bf16 v[20:23], v[44:45], a[204:205], 0 cbsz:2 abid:0\n"
    "s_add_u32 m0, s83, 36864\n"
    "s_add_u32 s84, s60, s64\n"
    "v_cvt_pk_bf16_f32 v115, v28, v32\n"
    "v_cvt_pk_bf16_f32 v119, v29, v33\n"
    "v_mfma_f32_4x4x4_16b_bf16 v[24:27], v[44:45], a[206:207], 0 cbsz:2 abid:1\n"
    "buffer_load_dwordx4 v244, s[44:47], s84 offen lds\n"
    "v_cvt_pk_bf16_f32 v123, v30, v34\n"
    "v_cvt_pk_bf16_f32 v127, v31, v35\n"
    "buffer_load_dwordx4 v245, s[44:47], s84 offen offset:1024 lds\n"
    "v_lshlrev_b64 v[48:49], v16, v[252:253]\n"
    "s_waitcnt vmcnt(18) lgkmcnt(2)\n"
    "ds_read2st64_b64 a[196:199], v15 offset0:28 offset1:29\n"
    "v_mfma_f32_4x4x4_16b_bf16 v[28:31], v[44:45], a[208:209], 0 cbsz:2 abid:2\n"
    "v_cvt_pk_bf16_f32 v128, v20, v22\n"
    "v_cvt_pk_bf16_f32 v132, v21, v23\n"
    "v_mfma_f32_4x4x4_16b_bf16 v[32:35], v[44:45], a[210:211], 0 cbsz:2 abid:3\n"
    "buffer_load_dwordx4 v246, s[44:47], s84 offen offset:2048 lds\n"
    "v_cvt_pk_bf16_f32 v129, v24, v26\n"
    "v_cvt_pk_bf16_f32 v133, v25, v27\n"
    "v_lshlrev_b64 v[50:51], v16, v[254:255]\n"
    "s_waitcnt vmcnt(13)\n"
    "v_and_b32 v242, 15, v237\n"
    "v_and_b32 v243, 1, v239\n"
    "v_or_b32 v237, 15, v237\n"
    "s_waitcnt vmcnt(18) lgkmcnt(2)\n"
    "ds_read2st64_b64 a[200:203], v15 offset0:30 offset1:31\n"
    "v_mfma_f32_4x4x4_16b_bf16 v[20:23], v[46:47], a[188:189], 0 cbsz:2 abid:0\n"
    "ds_read_u16 v250, v18 offset:512\n"
    "v_cvt_pk_bf16_f32 v130, v28, v30\n"
    "v_cvt_pk_bf16_f32 v134, v29, v31\n"
    "v_mfma_f32_4x4x4_16b_bf16 v[24:27], v[46:47], a[190:191], 0 cbsz:2 abid:1\n"
    "buffer_load_dwordx4 v247, s[44:47], s84 offen offset:3072 lds\n"
    "v_cvt_pk_bf16_f32 v131, v32, v34\n"
    "v_cvt_pk_bf16_f32 v135, v33, v35\n"
    "v_or_b32 v239, 15, v239\n"
    "v_cmp_ne_u64 vcc, s[18:19], v[236:237]\n"
    "v_cmp_ne_u64 s[48:49], s[18:19], v[238:239]\n"
    "v_readlane_b32 s9, v242, 0\n"
    "s_waitcnt vmcnt(18) lgkmcnt(3)\n"
    "ds_read2st64_b32 a[204:205], v38 offset0:64 offset1:65\n"
    "v_mfma_f32_4x4x4_16b_bf16 v[28:31], v[46:47], a[192:193], 0 cbsz:2 abid:2\n"
    "ds_read2st64_b32 a[206:207], v38 offset0:66 offset1:67\n"
    "ds_read_u16 v252, v18 offset:520\n"
    "s_add_u32 m0, s83, 40960\n"
    "v_cvt_pk_bf16_f32 v136, v20, v24\n"
    "v_cvt_pk_bf16_f32 v140, v21, v25\n"
    "v_mfma_f32_4x4x4_16b_bf16 v[32:35], v[46:47], a[194:195], 0 cbsz:2 abid:3\n"
    "buffer_load_dwordx4 v248, s[44:47], s84 offen lds\n"
    "v_cvt_pk_bf16_f32 v144, v22, v26\n"
    "v_cvt_pk_bf16_f32 v148, v23, v27\n"
    "v_and_b32 v236, 1, v242\n"
    "s_or_b64 s[48:49], s[48:49], vcc\n"
    "s_cbranch_scc1 75f\n"
    "77:\n"
    "s_waitcnt vmcnt(18) lgkmcnt(5)\n"
    "ds_read2st64_b32 a[208:209], v38 offset0:68 offset1:69\n"
    "v_mfma_f32_4x4x4_16b_bf16 v[20:23], v[48:49], a[196:197], 0 cbsz:2 abid:0\n"
    "ds_read2st64_b32 a[210:211], v38 offset0:70 offset1:71\n"
    "ds_read_u16 v254, v235 offset:512\n"
    "v_cvt_pk_bf16_f32 v137, v28, v32\n"
    "v_cvt_pk_bf16_f32 v141, v29, v33\n"
    "v_mfma_f32_4x4x4_16b_bf16 v[24:27], v[48:49], a[198:199], 0 cbsz:2 abid:1\n"
    "buffer_load_dwordx4 v249, s[44:47], s84 offen offset:1024 lds\n"
    "s_add_u32 m0, s83, 43008\n"
    "s_add_u32 s84, s61, s64\n"
    "v_cvt_pk_bf16_f32 v145, v30, v34\n"
    "v_cvt_pk_bf16_f32 v149, v31, v35\n"
    "buffer_load_dwordx4 v244, s[44:47], s84 offen lds\n"
    "v_cmp_eq_u32 s[24:25], 1, v236\n"
    "s_lshr_b32 s9, s9, 1\n"
    "s_add_u32 s9, s9, s28\n"
    "v_lshrrev_b64 v[240:241], v232, s[24:25]\n"
    "s_waitcnt vmcnt(19) lgkmcnt(6)\n"
    "ds_read2st64_b64 a[188:191], v15 offset0:36 offset1:37\n"
    "v_mfma_f32_4x4x4_16b_bf16 v[28:31], v[48:49], a[200:201], 0 cbsz:2 abid:2\n"
    "v_cvt_pk_bf16_f32 v138, v20, v24\n"
    "v_cvt_pk_bf16_f32 v142, v21, v25\n"
    "v_mfma_f32_4x4x4_16b_bf16 v[32:35], v[48:49], a[202:203], 0 cbsz:2 abid:3\n"
    "buffer_load_dwordx4 v245, s[44:47], s84 offen offset:1024 lds\n"
    "v_cvt_pk_bf16_f32 v146, v22, v26\n"
    "v_cvt_pk_bf16_f32 v150, v23, v27\n"
    "v_lshlrev_b64 v[40:41], v16, v[250:251]\n"
    "v_and_b32 v241, 0xff, v240\n"
    "v_bfe_u32 v240, v240, v233, 4\n"
    "v_bcnt_u32_b32 v241, v241, 8\n"
    "v_bcnt_u32_b32 v240, v240, 0\n"
    "s_waitcnt vmcnt(19) lgkmcnt(4)\n"
    "ds_read2st64_b64 a[192:195], v15 offset0:38 offset1:39\n"
    "v_mfma_f32_4x4x4_16b_bf16 v[20:23], v[50:51], a[204:205], 0 cbsz:2 abid:0\n"
    "v_cvt_pk_bf16_f32 v139, v28, v32\n"
    "v_cvt_pk_bf16_f32 v143, v29, v33\n"
    "v_mfma_f32_4x4x4_16b_bf16 v[24:27], v[50:51], a[206:207], 0 cbsz:2 abid:1\n"
    "buffer_load_dwordx4 v246, s[44:47], s84 offen offset:2048 lds\n"
    "v_cvt_pk_bf16_f32 v147, v30, v34\n"
    "v_cvt_pk_bf16_f32 v151, v31, v35\n"
    "v_lshlrev_b64 v[42:43], v16, v[252:253]\n"
    "v_lshl_add_u32 v240, v240, 1, v241\n"
    "s_lshr_b32 s10, s9, 2\n"
    "v_cmp_lt_u32 vcc, 15, v240\n"
    "v_xor_b32 v234, s10, v234\n"
    "s_waitcnt vmcnt(18) lgkmcnt(2)\n"
    "ds_read2st64_b64 a[196:199], v15 offset0:40 offset1:41\n"
    "v_mfma_f32_4x4x4_16b_bf16 v[28:31], v[50:51], a[208:209], 0 cbsz:2 abid:2\n"
    "v_cvt_pk_bf16_f32 v152, v20, v22\n"
    "v_cvt_pk_bf16_f32 v156, v21, v23\n"
    "v_mfma_f32_4x4x4_16b_bf16 v[32:35], v[50:51], a[210:211], 0 cbsz:2 abid:3\n"
    "buffer_load_dwordx4 v247, s[44:47], s84 offen offset:3072 lds\n"
    "v_cvt_pk_bf16_f32 v153, v24, v26\n"
    "v_cvt_pk_bf16_f32 v157, v25, v27\n"
    "v_lshlrev_b64 v[44:45], v16, v[254:255]\n"
    "s_mov_b64 s[50:51], vcc\n"
    "v_addc_co_u32 v234, vcc, 0, v234, vcc\n"
    "v_and_b32 v234, 1, v234\n"
    "v_mad_u32_u24 v236, v234, s88, v3\n"
    "s_waitcnt vmcnt(18) lgkmcnt(2)\n"
    "ds_read2st64_b64 a[200:203], v15 offset0:42 offset1:43\n"
    "v_mfma_f32_4x4x4_16b_bf16 v[20:23], v[40:41], a[188:189], 0 cbsz:2 abid:0\n"
    "ds_read_u16 v250, v18 offset:576\n"
    "s_add_u32 m0, s83, 47104\n"
    "v_cvt_pk_bf16_f32 v154, v28, v30\n"
    "v_cvt_pk_bf16_f32 v158, v29, v31\n"
    "v_mfma_f32_4x4x4_16b_bf16 v[24:27], v[40:41], a[190:191], 0 cbsz:2 abid:1\n"
    "buffer_load_dwordx4 v248, s[44:47], s84 offen lds\n"
    "v_cvt_pk_bf16_f32 v155, v32, v34\n"
    "v_cvt_pk_bf16_f32 v159, v33, v35\n"
    "v_cmp_eq_u32 s[26:27], 1, v243\n"
    "v_readlane_b32 s82, v236, 0\n"
    "v_readlane_b32 s73, v236, 1\n"
    "s_waitcnt vmcnt(18) lgkmcnt(3)\n"
    "ds_read2st64_b32 a[204:205], v38 offset0:88 offset1:89\n"
    "v_mfma_f32_4x4x4_16b_bf16 v[28:31], v[40:41], a[192:193], 0 cbsz:2 abid:2\n"
    "ds_read2st64_b32 a[206:207], v38 offset0:90 offset1:91\n"
    "ds_read_u16 v252, v18 offset:584\n"
    "v_cvt_pk_bf16_f32 v160, v20, v24\n"
    "v_cvt_pk_bf16_f32 v164, v21, v25\n"
    "v_mfma_f32_4x4x4_16b_bf16 v[32:35], v[40:41], a[194:195], 0 cbsz:2 abid:3\n"
    "buffer_load_dwordx4 v249, s[44:47], s84 offen offset:1024 lds\n"
    "v_cvt_pk_bf16_f32 v168, v22, v26\n"
    "v_cvt_pk_bf16_f32 v172, v23, v27\n"
    "s_waitcnt vmcnt(18) lgkmcnt(5)\n"
    "ds_read2st64_b32 a[208:209], v38 offset0:92 offset1:93\n"
    "v_mfma_f32_4x4x4_16b_bf16 v[20:23], v[42:43], a[196:197], 0 cbsz:2 abid:0\n"
    "ds_read2st64_b32 a[210:211], v38 offset0:94 offset1:95\n"
    "ds_read_u16 v254, v235 offset:576\n"
    "s_barrier\n"
    "s_mov_b32 m0, s57\n"
    "v_cvt_pk_bf16_f32 v161, v28, v32\n"
    "v_cvt_pk_bf16_f32 v165, v29, v33\n"
    "v_mfma_f32_4x4x4_16b_bf16 v[24:27], v[42:43], a[198:199], 0 cbsz:2 abid:1\n"
    "buffer_load_dwordx4 v0, s[36:39], s82 offen nt lds\n"
    "v_cvt_pk_bf16_f32 v169, v30, v34\n"
    "v_cvt_pk_bf16_f32 v173, v31, v35\n"
    "s_waitcnt vmcnt(18) lgkmcnt(6)\n"
    "ds_read2st64_b64 a[188:191], v15 offset0:48 offset1:49\n"
    "v_mfma_f32_4x4x4_16b_bf16 v[28:31], v[42:43], a[200:201], 0 cbsz:2 abid:2\n"
    "v_cvt_pk_bf16_f32 v162, v20, v24\n"
    "v_cvt_pk_bf16_f32 v166, v21, v25\n"
    "v_mfma_f32_4x4x4_16b_bf16 v[32:35], v[42:43], a[202:203], 0 cbsz:2 abid:3\n"
    "buffer_load_dwordx4 v1, s[36:39], s82 offen offset:1024 nt lds\n"
    "v_cvt_pk_bf16_f32 v170, v22, v26\n"
    "v_cvt_pk_bf16_f32 v174, v23, v27\n"
    "v_lshlrev_b64 v[46:47], v16, v[250:251]\n"
    "v_cmp_gt_u32 s[48:49], s96, v232\n"
    "v_lshrrev_b64 v[240:241], v232, s[26:27]\n"
    "v_and_b32 v241, 0xff, v240\n"
    "v_bfe_u32 v240, v240, v233, 4\n"
    "s_waitcnt vmcnt(18) lgkmcnt(4)\n"
    "ds_read2st64_b64 a[192:195], v15 offset0:50 offset1:51\n"
    "v_mfma_f32_4x4x4_16b_bf16 v[20:23], v[44:45], a[204:205], 0 cbsz:2 abid:0\n"
    "v_cvt_pk_bf16_f32 v163, v28, v32\n"
    "v_cvt_pk_bf16_f32 v167, v29, v33\n"
    "v_mfma_f32_4x4x4_16b_bf16 v[24:27], v[44:45], a[206:207], 0 cbsz:2 abid:1\n"
    "buffer_load_dwordx4 v2, s[36:39], s82 offen offset:2048 nt lds\n"
    "v_cvt_pk_bf16_f32 v171, v30, v34\n"
    "v_cvt_pk_bf16_f32 v175, v31, v35\n"
    "v_lshlrev_b64 v[48:49], v16, v[252:253]\n"
    "v_bcnt_u32_b32 v241, v241, 8\n"
    "v_bcnt_u32_b32 v240, v240, 0\n"
    "v_lshl_add_u32 v240, v240, 1, v241\n"
    "v_cmp_lt_u32 s[26:27], 15, v240\n"
    "s_waitcnt vmcnt(18) lgkmcnt(2)\n"
    "ds_read2st64_b64 a[196:199], v15 offset0:52 offset1:53\n"
    "v_mfma_f32_4x4x4_16b_bf16 v[28:31], v[44:45], a[208:209], 0 cbsz:2 abid:2\n"
    "v_cvt_pk_bf16_f32 v176, v20, v22\n"
    "v_cvt_pk_bf16_f32 v180, v21, v23\n"
    "v_mfma_f32_4x4x4_16b_bf16 v[32:35], v[44:45], a[210:211], 0 cbsz:2 abid:3\n"
    "v_cvt_pk_bf16_f32 v177, v24, v26\n"
    "v_cvt_pk_bf16_f32 v181, v25, v27\n"
    "v_lshlrev_b64 v[50:51], v16, v[254:255]\n"
    "s_bfm_b64 s[24:25], s52, 0\n"
    "s_bitcmp1_b32 s9, 0\n"
    "s_cselect_b32 s95, -1, 1\n"
    "s_waitcnt vmcnt(17) lgkmcnt(2)\n"
    "ds_read2st64_b64 a[200:203], v15 offset0:54 offset1:55\n"
    "v_mfma_f32_4x4x4_16b_bf16 v[20:23], v[46:47], a[188:189], 0 cbsz:2 abid:0\n"
    "ds_read_u16 v250, v18 offset:640\n"
    "v_cvt_pk_bf16_f32 v178, v28, v30\n"
    "v_cvt_pk_bf16_f32 v182, v29, v31\n"
    "v_mfma_f32_4x4x4_16b_bf16 v[24:27], v[46:47], a[190:191], 0 cbsz:2 abid:1\n"
    "v_cvt_pk_bf16_f32 v179, v32, v34\n"
    "v_cvt_pk_bf16_f32 v183, v33, v35\n"
    "s_cmp_lt_u32 s52, 12\n"
    "s_cselect_b32 s95, 0, s95\n"
    "s_add_u32 s52, s52, s95\n"
    "s_xor_b64 s[26:27], s[26:27], s[50:51]\n"
    "s_waitcnt vmcnt(16) lgkmcnt(3)\n"
    "ds_read2st64_b32 a[204:205], v38 offset0:112 offset1:113\n"
    "v_mfma_f32_4x4x4_16b_bf16 v[28:31], v[46:47], a[192:193], 0 cbsz:2 abid:2\n"
    "ds_read2st64_b32 a[206:207], v38 offset0:114 offset1:115\n"
    "ds_read_u16 v252, v18 offset:648\n"
    "v_cvt_pk_bf16_f32 v184, v20, v24\n"
    "v_cvt_pk_bf16_f32 v188, v21, v25\n"
    "v_mfma_f32_4x4x4_16b_bf16 v[32:35], v[46:47], a[194:195], 0 cbsz:2 abid:3\n"
    "v_cvt_pk_bf16_f32 v192, v22, v26\n"
    "v_cvt_pk_bf16_f32 v196, v23, v27\n"
    "s_and_b64 s[48:49], s[48:49], s[24:25]\n"
    "s_andn2_b64 s[48:49], s[48:49], s[26:27]\n"
    "s_bcnt1_i32_b64 s10, s[48:49]\n"
    "s_waitcnt vmcnt(15) lgkmcnt(5)\n"
    "ds_read2st64_b32 a[208:209], v38 offset0:116 offset1:117\n"
    "v_mfma_f32_4x4x4_16b_bf16 v[20:23], v[48:49], a[196:197], 0 cbsz:2 abid:0\n"
    "ds_read2st64_b32 a[210:211], v38 offset0:118 offset1:119\n"
    "ds_read_u16 v254, v235 offset:640\n"
    "v_cvt_pk_bf16_f32 v185, v28, v32\n"
    "v_cvt_pk_bf16_f32 v189, v29, v33\n"
    "v_mfma_f32_4x4x4_16b_bf16 v[24:27], v[48:49], a[198:199], 0 cbsz:2 abid:1\n"
    "v_cvt_pk_bf16_f32 v193, v30, v34\n"
    "v_cvt_pk_bf16_f32 v197, v31, v35\n"
    "s_cmp_lt_u32 s10, 4\n"
    "s_cselect_b64 s[34:35], s[34:35], 0\n"
    "s_cselect_b32 s96, 1, 0\n"
    "s_and_b64 vcc, s[34:35], s[26:27]\n"
    "s_waitcnt vmcnt(14) lgkmcnt(6)\n"
    "ds_read2st64_b64 a[188:191], v15 offset0:0 offset1:1\n"
    "v_mfma_f32_4x4x4_16b_bf16 v[28:31], v[48:49], a[200:201], 0 cbsz:2 abid:2\n"
    "v_cvt_pk_bf16_f32 v186, v20, v24\n"
    "v_cvt_pk_bf16_f32 v190, v21, v25\n"
    "v_mfma_f32_4x4x4_16b_bf16 v[32:35], v[48:49], a[202:203], 0 cbsz:2 abid:3\n"
    "v_cvt_pk_bf16_f32 v194, v22, v26\n"
    "v_cvt_pk_bf16_f32 v198, v23, v27\n"
    "v_lshlrev_b64 v[40:41], v16, v[250:251]\n"
    "v_addc_co_u32 v234, vcc, 0, v234, vcc\n"
    "v_cmp_le_u32 vcc, s52, v19\n"
    "v_subrev_u32 v240, 15, v240\n"
    "v_cmp_gt_u32 s[48:49], 3, v240\n"
    "s_waitcnt vmcnt(13) lgkmcnt(4)\n"
    "ds_read2st64_b64 a[192:195], v15 offset0:2 offset1:3\n"
    "v_mfma_f32_4x4x4_16b_bf16 v[20:23], v[50:51], a[204:205], 0 cbsz:2 abid:0\n"
    "v_cvt_pk_bf16_f32 v187, v28, v32\n"
    "v_cvt_pk_bf16_f32 v191, v29, v33\n"
    "v_mfma_f32_4x4x4_16b_bf16 v[24:27], v[50:51], a[206:207], 0 cbsz:2 abid:1\n"
    "v_cvt_pk_bf16_f32 v195, v30, v34\n"
    "v_cvt_pk_bf16_f32 v199, v31, v35\n"
    "v_lshlrev_b64 v[42:43], v16, v[252:253]\n"
    "s_or_b64 s[26:27], s[26:27], s[48:49]\n"
    "v_and_b32 v234, 1, v234\n"
    "v_mov_b32 v236, s33\n"
    "v_mad_u32_u24 v3, v234, s88, v3\n"
    "s_waitcnt vmcnt(12) lgkmcnt(2)\n"
    "ds_read2st64_b64 a[196:199], v15 offset0:4 offset1:5\n"
    "v_mfma_f32_4x4x4_16b_bf16 v[28:31], v[50:51], a[208:209], 0 cbsz:2 abid:2\n"
    "v_cvt_pk_bf16_f32 v200, v20, v22\n"
    "v_cvt_pk_bf16_f32 v204, v21, v23\n"
    "v_mfma_f32_4x4x4_16b_bf16 v[32:35], v[50:51], a[210:211], 0 cbsz:2 abid:3\n"
    "v_cvt_pk_bf16_f32 v201, v24, v26\n"
    "v_cvt_pk_bf16_f32 v205, v25, v27\n"
    "v_lshlrev_b64 v[44:45], v16, v[254:255]\n"
    "v_cndmask_b32 v3, v3, v236, vcc\n"
    "s_nop 0\n"
    "v_readlane_b32 s74, v3, 2\n"
    "v_readlane_b32 s75, v3, 3\n"
    "s_waitcnt vmcnt(11) lgkmcnt(2)\n"
    "ds_read2st64_b64 a[200:203], v15 offset0:6 offset1:7\n"
    "v_mfma_f32_4x4x4_16b_bf16 v[20:23], v[40:41], a[188:189], 0 cbsz:2 abid:0\n"
    "ds_read_u16 v250, v18 offset:704\n"
    "v_cvt_pk_bf16_f32 v202, v28, v30\n"
    "v_cvt_pk_bf16_f32 v206, v29, v31\n"
    "v_mfma_f32_4x4x4_16b_bf16 v[24:27], v[40:41], a[190:191], 0 cbsz:2 abid:1\n"
    "v_cvt_pk_bf16_f32 v203, v32, v34\n"
    "v_cvt_pk_bf16_f32 v207, v33, v35\n"
    "v_readlane_b32 s81, v3, 4\n"
    "s_waitcnt vmcnt(10) lgkmcnt(3)\n"
    "ds_read2st64_b32 a[204:205], v38 offset0:16 offset1:17\n"
    "v_mfma_f32_4x4x4_16b_bf16 v[28:31], v[40:41], a[192:193], 0 cbsz:2 abid:2\n"
    "ds_read2st64_b32 a[206:207], v38 offset0:18 offset1:19\n"
    "ds_read_u16 v252, v18 offset:712\n"
    "v_cvt_pk_bf16_f32 v208, v20, v24\n"
    "v_cvt_pk_bf16_f32 v212, v21, v25\n"
    "v_mfma_f32_4x4x4_16b_bf16 v[32:35], v[40:41], a[194:195], 0 cbsz:2 abid:3\n"
    "v_cvt_pk_bf16_f32 v216, v22, v26\n"
    "v_cvt_pk_bf16_f32 v220, v23, v27\n"
    "s_waitcnt vmcnt(9) lgkmcnt(5)\n"
    "ds_read2st64_b32 a[208:209], v38 offset0:20 offset1:21\n"
    "v_mfma_f32_4x4x4_16b_bf16 v[20:23], v[42:43], a[196:197], 0 cbsz:2 abid:0\n"
    "ds_read2st64_b32 a[210:211], v38 offset0:22 offset1:23\n"
    "ds_read_u16 v254, v235 offset:704\n"
    "v_cvt_pk_bf16_f32 v209, v28, v32\n"
    "v_cvt_pk_bf16_f32 v213, v29, v33\n"
    "v_mfma_f32_4x4x4_16b_bf16 v[24:27], v[42:43], a[198:199], 0 cbsz:2 abid:1\n"
    "v_cvt_pk_bf16_f32 v217, v30, v34\n"
    "v_cvt_pk_bf16_f32 v221, v31, v35\n"
    "s_waitcnt vmcnt(8) lgkmcnt(6)\n"
    "ds_read2st64_b64 a[188:191], v15 offset0:12 offset1:13\n"
    "v_mfma_f32_4x4x4_16b_bf16 v[28:31], v[42:43], a[200:201], 0 cbsz:2 abid:2\n"
    "s_nop 0\n"
    "v_cvt_pk_bf16_f32 v210, v20, v24\n"
    "v_cvt_pk_bf16_f32 v214, v21, v25\n"
    "v_mfma_f32_4x4x4_16b_bf16 v[32:35], v[42:43], a[202:203], 0 cbsz:2 abid:3\n"
    "v_cvt_pk_bf16_f32 v218, v22, v26\n"
    "v_cvt_pk_bf16_f32 v222, v23, v27\n"
    "v_lshlrev_b64 v[46:47], v16, v[250:251]\n"
    "s_waitcnt vmcnt(7) lgkmcnt(4)\n"
    "ds_read2st64_b64 a[192:195], v15 offset0:14 offset1:15\n"
    "v_mfma_f32_4x4x4_16b_bf16 v[20:23], v[44:45], a[204:205], 0 cbsz:2 abid:0\n"
    "s_add_u32 m0, s57, 12288\n"
    "v_cvt_pk_bf16_f32 v211, v28, v32\n"
    "v_cvt_pk_bf16_f32 v215, v29, v33\n"
    "v_mfma_f32_4x4x4_16b_bf16 v[24:27], v[44:45], a[206:207], 0 cbsz:2 abid:1\n"
    "buffer_load_dwordx4 v0, s[36:39], s73 offen nt lds\n"
    "v_cvt_pk_bf16_f32 v219, v30, v34\n"
    "v_cvt_pk_bf16_f32 v223, v31, v35\n"
    "v_lshlrev_b64 v[48:49], v16, v[252:253]\n"
    "s_waitcnt vmcnt(7) lgkmcnt(2)\n"
    "ds_read2st64_b64 a[196:199], v15 offset0:16 offset1:17\n"
    "v_mfma_f32_4x4x4_16b_bf16 v[28:31], v[44:45], a[208:209], 0 cbsz:2 abid:2\n"
    "v_cvt_pk_bf16_f32 v224, v20, v22\n"
    "v_cvt_pk_bf16_f32 v228, v21, v23\n"
    "v_mfma_f32_4x4x4_16b_bf16 v[32:35], v[44:45], a[210:211], 0 cbsz:2 abid:3\n"
    "buffer_load_dwordx4 v1, s[36:39], s73 offen offset:1024 nt lds\n"
    "v_cvt_pk_bf16_f32 v225, v24, v26\n"
    "v_cvt_pk_bf16_f32 v229, v25, v27\n"
    "v_lshlrev_b64 v[50:51], v16, v[254:255]\n"
    "s_waitcnt vmcnt(7) lgkmcnt(2)\n"
    "ds_read2st64_b64 a[200:203], v15 offset0:18 offset1:19\n"
    "v_mfma_f32_4x4x4_16b_bf16 v[20:23], v[46:47], a[188:189], 0 cbsz:2 abid:0\n"
    "v_cvt_pk_bf16_f32 v226, v28, v30\n"
    "v_cvt_pk_bf16_f32 v230, v29, v31\n"
    "v_mfma_f32_4x4x4_16b_bf16 v[24:27], v[46:47], a[190:191], 0 cbsz:2 abid:1\n"
    "buffer_load_dwordx4 v2, s[36:39], s73 offen offset:2048 nt lds\n"
    "v_cvt_pk_bf16_f32 v227, v32, v34\n"
    "v_cvt_pk_bf16_f32 v231, v33, v35\n"
    "s_waitcnt vmcnt(7) lgkmcnt(2)\n"
    "ds_read2st64_b32 a[204:205], v38 offset0:40 offset1:41\n"
    "v_mfma_f32_4x4x4_16b_bf16 v[28:31], v[46:47], a[192:193], 0 cbsz:2 abid:2\n"
    "ds_read2st64_b32 a[206:207], v38 offset0:42 offset1:43\n"
    "v_cvt_pk_bf16_f32 v232, v20, v24\n"
    "v_cvt_pk_bf16_f32 v236, v21, v25\n"
    "v_mfma_f32_4x4x4_16b_bf16 v[32:35], v[46:47], a[194:195], 0 cbsz:2 abid:3\n"
    "v_cvt_pk_bf16_f32 v240, v22, v26\n"
    "v_cvt_pk_bf16_f32 v244, v23, v27\n"
    "s_waitcnt vmcnt(6) lgkmcnt(3)\n"
    "ds_read2st64_b32 a[208:209], v38 offset0:44 offset1:45\n"
    "v_mfma_f32_4x4x4_16b_bf16 v[20:23], v[48:49], a[196:197], 0 cbsz:2 abid:0\n"
    "ds_read2st64_b32 a[210:211], v38 offset0:46 offset1:47\n"
    "v_cvt_pk_bf16_f32 v233, v28, v32\n"
    "v_cvt_pk_bf16_f32 v237, v29, v33\n"
    "v_mfma_f32_4x4x4_16b_bf16 v[24:27], v[48:49], a[198:199], 0 cbsz:2 abid:1\n"
    "v_cvt_pk_bf16_f32 v241, v30, v34\n"
    "v_cvt_pk_bf16_f32 v245, v31, v35\n"
    "s_waitcnt lgkmcnt(4)\n"
    "v_mfma_f32_4x4x4_16b_bf16 v[28:31], v[48:49], a[200:201], 0 cbsz:2 abid:2\n"
    "s_nop 1\n"
    "v_cvt_pk_bf16_f32 v234, v20, v24\n"
    "v_cvt_pk_bf16_f32 v238, v21, v25\n"
    "v_mfma_f32_4x4x4_16b_bf16 v[32:35], v[48:49], a[202:203], 0 cbsz:2 abid:3\n"
    "v_cvt_pk_bf16_f32 v242, v22, v26\n"
    "v_cvt_pk_bf16_f32 v246, v23, v27\n"
    "s_waitcnt lgkmcnt(2)\n"
    "v_mfma_f32_4x4x4_16b_bf16 v[20:23], v[50:51], a[204:205], 0 cbsz:2 abid:0\n"
    "s_nop 1\n"
    "v_cvt_pk_bf16_f32 v235, v28, v32\n"
    "v_cvt_pk_bf16_f32 v239, v29, v33\n"
    "v_mfma_f32_4x4x4_16b_bf16 v[24:27], v[50:51], a[206:207], 0 cbsz:2 abid:1\n"
    "v_cvt_pk_bf16_f32 v243, v30, v34\n"
    "v_cvt_pk_bf16_f32 v247, v31, v35\n"
    "s_waitcnt lgkmcnt(0)\n"
    "v_mfma_f32_4x4x4_16b_bf16 v[28:31], v[50:51], a[208:209], 0 cbsz:2 abid:2\n"
    "s_nop 1\n"
    "v_cvt_pk_bf16_f32 v248, v20, v22\n"
    "v_cvt_pk_bf16_f32 v252, v21, v23\n"
    "v_mfma_f32_4x4x4_16b_bf16 v[32:35], v[50:51], a[210:211], 0 cbsz:2 abid:3\n"
    "v_cvt_pk_bf16_f32 v249, v24, v26\n"
    "v_cvt_pk_bf16_f32 v253, v25, v27\n"
    "s_waitcnt lgkmcnt(0)\n"
    "s_add_u32 s89, s92, 248\n"
    "v_add_u32 v38, s11, v38\n"
    "s_nop 0\n"
    "v_cvt_pk_bf16_f32 v250, v28, v30\n"
    "v_cvt_pk_bf16_f32 v254, v29, v31\n"
    "v_cvt_pk_bf16_f32 v251, v32, v34\n"
    "v_cvt_pk_bf16_f32 v255, v33, v35\n"
    "s_waitcnt vmcnt(3)\n"
    "s_barrier\n"
    "ds_read_b128 a[88:91], v4\n"
    "ds_read_b128 a[92:95], v5\n"
    "ds_read_b128 a[96:99], v6\n"
    "ds_read_b128 a[100:103], v7\n"
    "ds_read_b128 a[104:107], v4 offset:256\n"
    "ds_read_b128 a[108:111], v5 offset:256\n"
    "ds_read_b128 a[112:115], v6 offset:256\n"
    "ds_read_b128 a[116:119], v7 offset:256\n"
    "ds_read_b128 a[120:123], v4 offset:512\n"
    "ds_read_b128 a[124:127], v5 offset:512\n"
    "ds_read_b128 a[128:131], v6 offset:512\n"
    "ds_read_b128 a[132:135], v7 offset:512\n"
    "s_waitcnt lgkmcnt(11)\n"
    "v_mfma_f32_16x16x32_bf16 v[20:23], a[88:91], a[16:19], 0\n"
    "v_add_u32 v14, 1, v14\n"
    "s_sub_i32 s46, s52, 5\n"
    "s_max_i32 s46, s46, 0\n"
    "s_waitcnt lgkmcnt(10)\n"
    "v_mfma_f32_16x16x32_bf16 v[20:23], a[92:95], a[40:43], v[20:23]\n"
    "s_add_u32 m0, s57, 24576\n"
    "s_bfm_b64 s[44:45], s46, 5\n"
    "v_subrev_u32 v43, 5, v19\n"
    "s_waitcnt lgkmcnt(9)\n"
    "v_mfma_f32_16x16x32_bf16 v[20:23], a[96:99], a[64:67], v[20:23]\n"
    "buffer_load_dwordx4 v0, s[36:39], s74 offen nt lds\n"
    "s_andn2_b64 s[50:51], s[44:45], s[26:27]\n"
    "s_waitcnt lgkmcnt(8)\n"
    "v_mfma_f32_16x16x32_bf16 v[20:23], a[100:103], v[56:59], v[20:23]\n"
    "s_and_b64 s[48:49], s[44:45], s[26:27]\n"
    "s_bcnt1_i32_b64 s47, s[50:51]\n"
    "s_sub_u32 s46, s46, s47\n"
    "s_waitcnt lgkmcnt(7)\n"
    "v_mfma_f32_16x16x32_bf16 v[20:23], a[104:107], v[80:83], v[20:23]\n"
    "buffer_load_dwordx4 v1, s[36:39], s74 offen offset:1024 nt lds\n"
    "s_add_u32 s58, s46, 5\n"
    "s_waitcnt lgkmcnt(6)\n"
    "v_mfma_f32_16x16x32_bf16 v[20:23], a[108:111], v[104:107], v[20:23]\n"
    "v_mov_b32 v45, s58\n"
    "v_mbcnt_lo_u32_b32 v47, s48, 5\n"
    "v_mbcnt_lo_u32_b32 v45, s50, v45\n"
    "s_waitcnt lgkmcnt(5)\n"
    "v_mfma_f32_16x16x32_bf16 v[20:23], a[112:115], v[128:131], v[20:23]\n"
    "buffer_load_dwordx4 v2, s[36:39], s74 offen offset:2048 nt lds\n"
    "v_mbcnt_hi_u32_b32 v47, s49, v47\n"
    "s_waitcnt lgkmcnt(4)\n"
    "v_mfma_f32_16x16x32_bf16 v[20:23], a[116:119], v[152:155], v[20:23]\n"
    "v_mbcnt_hi_u32_b32 v45, s51, v45\n"
    "v_cndmask_b32 v47, v19, v47, s[48:49]\n"
    "s_sub_i32 s59, s47, 4\n"
    "s_waitcnt lgkmcnt(3)\n"
    "v_mfma_f32_16x16x32_bf16 v[20:23], a[120:123], v[176:179], v[20:23]\n"
    "v_cndmask_b32 v47, v47, v45, s[50:51]\n"
    "s_max_i32 s59, s59, 0\n"
    "v_lshlrev_b32 v47, 2, v47\n"
    "s_waitcnt lgkmcnt(2)\n"
    "v_mfma_f32_16x16x32_bf16 v[20:23], a[124:127], v[200:203], v[20:23]\n"
    "s_mul_i32 s59, s59, 9\n"
    "s_min_u32 s59, s59, 256\n"
    "ds_permute_b32 v3, v47, v3\n"
    "s_waitcnt lgkmcnt(2)\n"
    "v_mfma_f32_16x16x32_bf16 v[20:23], a[128:131], v[224:227], v[20:23]\n"
    "s_cmp_lg_u32 s96, 0\n"
    "s_cselect_b32 s62, 54, 34\n"
    "s_lshr_b32 s63, s46, 5\n"
    "s_waitcnt lgkmcnt(1)\n"
    "v_mfma_f32_16x16x32_bf16 v[20:23], a[132:135], v[248:251], v[20:23]\n"
    "s_add_u32 s62, s62, s63\n"
    "s_sub_u32 s63, s62, 30\n"
    "v_mul_i32_i24 v40, s59, v43\n"
    "v_mfma_f32_16x16x32_bf16 v[24:27], a[132:135], v[252:255], 0\n"
    "s_mul_i32 s7, s2, 9\n"
    "s_and_b32 s7, s7, 0xff\n"
    "v_add_u32 v40, s7, v40\n"
    "v_mfma_f32_16x16x32_bf16 v[24:27], a[128:131], v[228:231], v[24:27]\n"
    "v_subrev_u32 v42, s46, v43\n"
    "v_add_u32 v41, s59, v40\n"
    "v_ashrrev_i32 v40, 8, v40\n"
    "v_mfma_f32_16x16x32_bf16 v[24:27], a[124:127], v[204:207], v[24:27]\n"
    "v_ashrrev_i32 v41, 8, v41\n"
    "v_min_i32 v40, s47, v40\n"
    "v_min_i32 v41, s47, v41\n"
    "v_mfma_f32_16x16x32_bf16 v[24:27], a[120:123], v[180:183], v[24:27]\n"
    "v_max_i32 v40, v40, v42\n"
    "v_add_u32 v42, 1, v42\n"
    "v_max_i32 v41, v41, v42\n"
    "v_mfma_f32_16x16x32_bf16 v[24:27], a[116:119], v[156:159], v[24:27]\n"
    "v_cmp_gt_i32 s[60:61], v41, v40\n"
    "v_add_u32 v41, s58, v40\n"
    "v_sub_u32 v40, v19, v40\n"
    "v_mfma_f32_16x16x32_bf16 v[24:27], a[112:115], v[132:135], v[24:27]\n"
    "v_cndmask_b32 v40, v40, v41, s[60:61]\n"
    "v_cndmask_b32 v40, v19, v40, s[44:45]\n"
    "v_lshlrev_b32 v40, 2, v40\n"
    "v_mfma_f32_16x16x32_bf16 v[24:27], a[108:111], v[108:111], v[24:27]\n"
    "s_waitcnt lgkmcnt(0)\n"
    "ds_bpermute_b32 v3, v40, v3\n"
    "s_mul_i32 s94, s46, 4\n"
    "v_mfma_f32_16x16x32_bf16 v[24:27], a[104:107], v[84:87], v[24:27]\n"
    "s_lshr_b32 s94, s94, 4\n"
    "s_add_u32 s94, s94, 67\n"
    "v_mfma_f32_16x16x32_bf16 v[24:27], a[100:103], v[60:63], v[24:27]\n"
    "s_cmp_lg_u32 s96, 0\n"
    "s_cselect_b32 s94, 76, s94\n"
    "s_mul_i32 s7, s95, 2\n"
    "v_mfma_f32_16x16x32_bf16 v[24:27], a[96:99], a[68:71], v[24:27]\n"
    "s_waitcnt vmcnt(3)\n"
    "s_sub_u32 s94, s94, s7\n"
    "s_mul_i32 s7, s46, -1\n"
    "v_mfma_f32_16x16x32_bf16 v[24:27], a[92:95], a[44:47], v[24:27]\n"
    "ds_write_b32 v13, v14\n"
    "s_ashr_i32 s7, s7, 4\n"
    "v_mfma_f32_16x16x32_bf16 v[24:27], a[88:91], a[20:23], v[24:27]\n"
    "s_add_u32 m0, s57, 36864\n"
    "s_add_u32 s7, s7, 6\n"
    "v_mfma_f32_16x16x32_bf16 v[28:31], a[88:91], a[0:3], 0\n"
    "buffer_load_dwordx4 v0, s[36:39], s75 offen nt lds\n"
    "v_mfma_f32_16x16x32_bf16 v[28:31], a[92:95], a[24:27], v[28:31]\n"
    "s_cmp_lg_u32 s96, 0\n"
    "s_cselect_b32 s31, 3, s7\n"
    "s_add_u32 s31, s94, s31\n"
    "v_mfma_f32_16x16x32_bf16 v[28:31], a[96:99], a[48:51], v[28:31]\n"
    "buffer_load_dwordx4 v1, s[36:39], s75 offen offset:1024 nt lds\n"
    "s_add_u32 s76, s52, 4\n"
    "v_mfma_f32_16x16x32_bf16 v[28:31], a[100:103], a[72:75], v[28:31]\n"
    "v_cvt_pk_bf16_f32 v52, v20, v24\n"
    "v_subrev_u32 v18, 5, v19\n"
    "s_mov_b32 s30, s94\n"
    "v_mfma_f32_16x16x32_bf16 v[28:31], a[104:107], v[64:67], v[28:31]\n"
    "ds_read_b32 v15, v12\n"
    "s_cmp_lg_u32 s96, 0\n"
    "s_cselect_b32 s7, 8, 12\n"
    "v_mfma_f32_16x16x32_bf16 v[28:31], a[108:111], v[88:91], v[28:31]\n"
    "v_cvt_pk_bf16_f32 v53, v21, v25\n"
    "s_add_u32 s94, s94, s7\n"
    "s_sub_u32 s7, s52, 5\n"
    "v_mfma_f32_16x16x32_bf16 v[28:31], a[112:115], v[112:115], v[28:31]\n"
    "v_cvt_pk_bf16_f32 v54, v22, v26\n"
    "s_andn2_b32 s7, s7, 1\n"
    "v_mfma_f32_16x16x32_bf16 v[28:31], a[116:119], v[136:139], v[28:31]\n"
    "v_cvt_pk_bf16_f32 v55, v23, v27\n"
    "s_waitcnt lgkmcnt(0)\n"
    "v_cmp_lt_u32 vcc, v15, v14\n"
    "v_mfma_f32_16x16x32_bf16 v[28:31], a[120:123], v[160:163], v[28:31]\n"
    "s_cmp_lt_i32 s7, 2\n"
    "s_cselect_b32 s7, 0x1000, s7\n"
    "v_mfma_f32_16x16x32_bf16 v[28:31], a[124:127], v[184:187], v[28:31]\n"
    "s_cbranch_vccnz 81b\n"
    "82:\n"
    "v_mfma_f32_16x16x32_bf16 v[28:31], a[128:131], v[208:211], v[28:31]\n"
    "ds_write2st64_b32 v38, v52, v53 offset0:0 offset1:2\n"
    "v_mfma_f32_16x16x32_bf16 v[28:31], a[132:135], v[232:235], v[28:31]\n"
    "ds_read_b128 a[136:139], v4 offset:12288\n"
    "s_mov_b64 vcc, s[12:13]\n"
    "v_mfma_f32_16x16x32_bf16 v[32:35], a[132:135], v[236:239], 0\n"
    "ds_write2st64_b32 v38, v54, v55 offset0:4 offset1:6\n"
    "v_mfma_f32_16x16x32_bf16 v[32:35], a[128:131], v[212:215], v[32:35]\n"
    "ds_read_b128 a[140:143], v5 offset:12288\n"
    "v_add_u32 v14, 1, v14\n"
    "v_mfma_f32_16x16x32_bf16 v[32:35], a[124:127], v[188:191], v[32:35]\n"
    "buffer_load_dwordx4 v2, s[36:39], s75 offen offset:2048 nt lds\n"
    "v_mfma_f32_16x16x32_bf16 v[32:35], a[120:123], v[164:167], v[32:35]\n"
    "ds_read_b128 a[144:147], v6 offset:12288\n"
    "buffer_load_dword a212, off, s[36:39], s33\n"
    "v_mfma_f32_16x16x32_bf16 v[32:35], a[116:119], v[140:143], v[32:35]\n"
    "buffer_load_dword a212, off, s[36:39], s33\n"
    "buffer_load_dword a212, off, s[36:39], s33\n"
    "v_mfma_f32_16x16x32_bf16 v[32:35], a[112:115], v[116:119], v[32:35]\n"
    "s_cmp_lt_u32 s47, 4\n"
    "s_cselect_b32 s7, 0x1000, s7\n"
    "v_mfma_f32_16x16x32_bf16 v[32:35], a[108:111], v[92:95], v[32:35]\n"
    "ds_read_b128 a[148:151], v7 offset:12288\n"
    "s_cmp_lg_u32 s96, 0\n"
    "s_cselect_b32 s7, 0x1000, s7\n"
    "v_mfma_f32_16x16x32_bf16 v[32:35], a[104:107], v[68:71], v[32:35]\n"
    "s_add_u32 s70, s7, -1\n"
    "v_subrev_u32 v40, s70, v18\n"
    "v_cmp_gt_u32 s[60:61], 2, v40\n"
    "v_mfma_f32_16x16x32_bf16 v[32:35], a[100:103], a[76:79], v[32:35]\n"
    "ds_read_b128 a[152:155], v4 offset:12544\n"
    "s_add_u32 s64, s7, 5\n"
    "s_add_u32 s65, s7, 6\n"
    "v_mfma_f32_16x16x32_bf16 v[32:35], a[96:99], a[52:55], v[32:35]\n"
    "v_cndmask_b32 v18, v18, 63, s[60:61]\n"
    "s_add_u32 s71, s7, 5\n"
    "v_lshlrev_b32 v18, 2, v18\n"
    "v_mfma_f32_16x16x32_bf16 v[32:35], a[92:95], a[28:31], v[32:35]\n"
    "ds_read_b128 a[156:159], v5 offset:12544\n"
    "ds_bpermute_b32 v18, v18, v3\n"
    "v_mfma_f32_16x16x32_bf16 v[32:35], a[88:91], a[4:7], v[32:35]\n"
    "v_readlane_b32 s84, v3, s86\n"
    "v_mfma_f32_16x16x32_bf16 v[20:23], a[88:91], a[8:11], 0\n"
    "ds_read_b128 a[160:163], v6 offset:12544\n"
    "v_mfma_f32_16x16x32_bf16 v[20:23], a[92:95], a[32:35], v[20:23]\n"
    "v_mfma_f32_16x16x32_bf16 v[20:23], a[96:99], a[56:59], v[20:23]\n"
    "ds_read_b128 a[164:167], v7 offset:12544\n"
    "v_mfma_f32_16x16x32_bf16 v[20:23], a[100:103], a[80:83], v[20:23]\n"
    "v_cvt_pk_bf16_f32 v36, v28, v32\n"
    "v_mfma_f32_16x16x32_bf16 v[20:23], a[104:107], v[72:75], v[20:23]\n"
    "ds_read_b128 a[168:171], v4 offset:12800\n"
    "v_mfma_f32_16x16x32_bf16 v[20:23], a[108:111], v[96:99], v[20:23]\n"
    "v_cvt_pk_bf16_f32 v46, v29, v33\n"
    "v_mfma_f32_16x16x32_bf16 v[20:23], a[112:115], v[120:123], v[20:23]\n"
    "ds_read_b128 a[172:175], v5 offset:12800\n"
    "s_add_u32 m0, s57, 49152\n"
    "v_mfma_f32_16x16x32_bf16 v[20:23], a[116:119], v[144:147], v[20:23]\n"
    "buffer_load_dwordx4 v0, s[36:39], s81 offen nt lds\n"
    "v_mfma_f32_16x16x32_bf16 v[20:23], a[120:123], v[168:171], v[20:23]\n"
    "v_cvt_pk_bf16_f32 v37, v30, v34\n"
    "v_mfma_f32_16x16x32_bf16 v[20:23], a[124:127], v[192:195], v[20:23]\n"
    "ds_read_b128 a[176:179], v6 offset:12800\n"
    "v_mfma_f32_16x16x32_bf16 v[20:23], a[128:131], v[216:219], v[20:23]\n"
    "v_cvt_pk_bf16_f32 v50, v31, v35\n"
    "v_mfma_f32_16x16x32_bf16 v[20:23], a[132:135], v[240:243], v[20:23]\n"
    "ds_read_b128 a[180:183], v7 offset:12800\n"
    "v_mfma_f32_16x16x32_bf16 v[24:27], a[132:135], v[244:247], 0\n"
    "buffer_load_dwordx4 v1, s[36:39], s81 offen offset:1024 nt lds\n"
    "v_mfma_f32_16x16x32_bf16 v[24:27], a[128:131], v[220:223], v[24:27]\n"
    "v_cndmask_b32_dpp v44, v46, v36, vcc row_ror:8 row_mask:0xf bank_mask:0xf\n"
    "v_mfma_f32_16x16x32_bf16 v[24:27], a[124:127], v[196:199], v[24:27]\n"
    "buffer_load_dwordx4 v2, s[36:39], s81 offen offset:2048 nt lds\n"
    "v_mfma_f32_16x16x32_bf16 v[24:27], a[120:123], v[172:175], v[24:27]\n"
    "s_memrealtime s[92:93]\n"
    "buffer_load_dword a212, off, s[36:39], s33\n"
    "v_mfma_f32_16x16x32_bf16 v[24:27], a[116:119], v[148:151], v[24:27]\n"
    "v_cndmask_b32_dpp v48, v50, v37, vcc row_ror:8 row_mask:0xf bank_mask:0xf\n"
    "v_mfma_f32_16x16x32_bf16 v[24:27], a[112:115], v[124:127], v[24:27]\n"
    "v_mov_b32_dpp v46, v36 row_ror:8 row_mask:0xf bank_mask:0x3\n"
    "v_mfma_f32_16x16x32_bf16 v[24:27], a[108:111], v[100:103], v[24:27]\n"
    "v_mov_b32_dpp v50, v37 row_ror:8 row_mask:0xf bank_mask:0x3\n"
    "v_mfma_f32_16x16x32_bf16 v[24:27], a[104:107], v[76:79], v[24:27]\n"
    "v_mfma_f32_16x16x32_bf16 v[24:27], a[100:103], a[84:87], v[24:27]\n"
    "s_waitcnt vmcnt(10) lgkmcnt(0)\n"
    "s_sub_u32 s85, s92, s89\n"
    "v_mfma_f32_16x16x32_bf16 v[24:27], a[96:99], a[60:63], v[24:27]\n"
    "s_add_i32 s63, s63, s85\n"
    "s_max_i32 s63, s63, s62\n"
    "s_add_u32 s89, s89, s63\n"
    "v_mfma_f32_16x16x32_bf16 v[24:27], a[92:95], a[36:39], v[24:27]\n"
    "s_mov_b32 s81, s84\n"
    "s_add_u32 m0, s57, 61440\n"
    "v_mfma_f32_16x16x32_bf16 v[24:27], a[88:91], a[12:15], v[24:27]\n"
    "s_branch 52f\n"
    ".p2align 6\n"
    "51:\n"
    "v_mfma_f32_16x16x32_bf16 v[20:23], a[88:91], a[16:19], 0\n"
    "s_add_u32 s86, s86, 1\n"
    "v_mfma_f32_16x16x32_bf16 v[20:23], a[92:95], a[40:43], v[20:23]\n"
    "s_waitcnt vmcnt(13)\n"
    "v_mfma_f32_16x16x32_bf16 v[20:23], a[96:99], a[64:67], v[20:23]\n"
    "ds_write_b32 v13, v14\n"
    "v_mfma_f32_16x16x32_bf16 v[20:23], a[100:103], v[56:59], v[20:23]\n"
    "v_cvt_pk_bf16_f32 v36, v28, v32\n"
    "v_mfma_f32_16x16x32_bf16 v[20:23], a[104:107], v[80:83], v[20:23]\n"
    "v_cvt_pk_bf16_f32 v47, v29, v33\n"
    "v_mfma_f32_16x16x32_bf16 v[20:23], a[108:111], v[104:107], v[20:23]\n"
    "v_cvt_pk_bf16_f32 v37, v30, v34\n"
    "v_mfma_f32_16x16x32_bf16 v[20:23], a[112:115], v[128:131], v[20:23]\n"
    "v_cvt_pk_bf16_f32 v51, v31, v35\n"
    "v_mfma_f32_16x16x32_bf16 v[20:23], a[116:119], v[152:155], v[20:23]\n"
    "v_cndmask_b32_dpp v45, v47, v36, vcc row_ror:8 row_mask:0xf bank_mask:0xf\n"
    "v_mfma_f32_16x16x32_bf16 v[20:23], a[120:123], v[176:179], v[20:23]\n"
    "v_cndmask_b32_dpp v49, v51, v37, vcc row_ror:8 row_mask:0xf bank_mask:0xf\n"
    "v_mfma_f32_16x16x32_bf16 v[20:23], a[124:127], v[200:203], v[20:23]\n"
    "v_mov_b32_dpp v47, v36 row_ror:8 row_mask:0xf bank_mask:0x3\n"
    "v_mfma_f32_16x16x32_bf16 v[20:23], a[128:131], v[224:227], v[20:23]\n"
    "v_mov_b32_dpp v51, v37 row_ror:8 row_mask:0xf bank_mask:0x3\n"
    "v_mfma_f32_16x16x32_bf16 v[20:23], a[132:135], v[248:251], v[20:23]\n"
    "ds_read_b32 v15, v12\n"
    "v_mfma_f32_16x16x32_bf16 v[24:27], a[132:135], v[252:255], 0\n"
    "s_add_u32 s89, s89, s94\n"
    "v_mfma_f32_16x16x32_bf16 v[24:27], a[128:131], v[228:231], v[24:27]\n"
    "s_sub_u32 s94, s94, 6\n"
    "v_mfma_f32_16x16x32_bf16 v[24:27], a[124:127], v[204:207], v[24:27]\n"
    "buffer_store_dwordx4 v[44:47], v8, s[40:43], s82 offen sc1\n"
    "v_mfma_f32_16x16x32_bf16 v[24:27], a[120:123], v[180:183], v[24:27]\n"
    "s_max_u32 s94, s94, s30\n"
    "v_mfma_f32_16x16x32_bf16 v[24:27], a[116:119], v[156:159], v[24:27]\n"
    "s_waitcnt lgkmcnt(0)\n"
    "v_cmp_lt_u32 vcc, v15, v14\n"
    "v_mfma_f32_16x16x32_bf16 v[24:27], a[112:115], v[132:135], v[24:27]\n"
    "v_mfma_f32_16x16x32_bf16 v[24:27], a[108:111], v[108:111], v[24:27]\n"
    "s_cbranch_vccnz 41b\n"
    "42:\n"
    "v_mfma_f32_16x16x32_bf16 v[24:27], a[104:107], v[84:87], v[24:27]\n"
    "ds_read_b128 a[184:187], v39 offset:4096\n"
    "v_mfma_f32_16x16x32_bf16 v[24:27], a[100:103], v[60:63], v[24:27]\n"
    "s_cmp_eq_u32 s86, s64\n"
    "s_cbranch_scc1 45b\n"
    "v_mfma_f32_16x16x32_bf16 v[24:27], a[96:99], a[68:71], v[24:27]\n"
    "ds_read_b128 a[136:139], v4 offset:12288\n"
    "v_mfma_f32_16x16x32_bf16 v[24:27], a[92:95], a[44:47], v[24:27]\n"
    "v_add_u32 v14, 1, v14\n"
    "v_mfma_f32_16x16x32_bf16 v[24:27], a[88:91], a[20:23], v[24:27]\n"
    "s_mov_b64 vcc, s[12:13]\n"
    "v_mfma_f32_16x16x32_bf16 v[28:31], a[88:91], a[0:3], 0\n"
    "buffer_load_dwordx4 v0, s[36:39], s81 offen nt lds\n"
    "v_mfma_f32_16x16x32_bf16 v[28:31], a[92:95], a[24:27], v[28:31]\n"
    "ds_read_b128 a[140:143], v5 offset:12288\n"
    "v_mfma_f32_16x16x32_bf16 v[28:31], a[96:99], a[48:51], v[28:31]\n"
    "ds_read_b128 a[144:147], v6 offset:12288\n"
    "v_mfma_f32_16x16x32_bf16 v[28:31], a[100:103], a[72:75], v[28:31]\n"
    "v_cvt_pk_bf16_f32 v52, v20, v24\n"
    "v_mfma_f32_16x16x32_bf16 v[28:31], a[104:107], v[64:67], v[28:31]\n"
    "v_cvt_pk_bf16_f32 v53, v21, v25\n"
    "v_mfma_f32_16x16x32_bf16 v[28:31], a[108:111], v[88:91], v[28:31]\n"
    "v_cvt_pk_bf16_f32 v54, v22, v26\n"
    "v_mfma_f32_16x16x32_bf16 v[28:31], a[112:115], v[112:115], v[28:31]\n"
    "v_cvt_pk_bf16_f32 v55, v23, v27\n"
    "v_mfma_f32_16x16x32_bf16 v[28:31], a[116:119], v[136:139], v[28:31]\n"
    "ds_read_b128 a[148:151], v7 offset:12288\n"
    "v_mfma_f32_16x16x32_bf16 v[28:31], a[120:123], v[160:163], v[28:31]\n"
    "v_mfma_f32_16x16x32_bf16 v[28:31], a[124:127], v[184:187], v[28:31]\n"
    "buffer_load_dwordx4 v1, s[36:39], s81 offen offset:1024 nt lds\n"
    "v_mfma_f32_16x16x32_bf16 v[28:31], a[128:131], v[208:211], v[28:31]\n"
    "v_mfma_f32_16x16x32_bf16 v[28:31], a[132:135], v[232:235], v[28:31]\n"
    "ds_read_b128 a[152:155], v4 offset:12544\n"
    "v_mfma_f32_16x16x32_bf16 v[32:35], a[132:135], v[236:239], 0\n"
    "v_mfma_f32_16x16x32_bf16 v[32:35], a[128:131], v[212:215], v[32:35]\n"
    "ds_write2st64_b32 v38, v52, v53 offset0:0 offset1:2\n"
    "v_mfma_f32_16x16x32_bf16 v[32:35], a[124:127], v[188:191], v[32:35]\n"
    "46:\n"
    "v_mfma_f32_16x16x32_bf16 v[32:35], a[120:123], v[164:167], v[32:35]\n"
    "ds_read_b128 a[156:159], v5 offset:12544\n"
    "v_mfma_f32_16x16x32_bf16 v[32:35], a[116:119], v[140:143], v[32:35]\n"
    "v_mfma_f32_16x16x32_bf16 v[32:35], a[112:115], v[116:119], v[32:35]\n"
    "v_mfma_f32_16x16x32_bf16 v[32:35], a[108:111], v[92:95], v[32:35]\n"
    "buffer_store_dwordx4 v[48:51], v9, s[40:43], s82 offen sc1\n"
    "v_mfma_f32_16x16x32_bf16 v[32:35], a[104:107], v[68:71], v[32:35]\n"
    "v_mfma_f32_16x16x32_bf16 v[32:35], a[100:103], a[76:79], v[32:35]\n"
    "ds_read_b128 a[160:163], v6 offset:12544\n"
    "v_mfma_f32_16x16x32_bf16 v[32:35], a[96:99], a[52:55], v[32:35]\n"
    "ds_read_b128 a[164:167], v7 offset:12544\n"
    "v_mfma_f32_16x16x32_bf16 v[32:35], a[92:95], a[28:31], v[32:35]\n"
    "ds_read_b128 a[168:171], v4 offset:12800\n"
    "v_mfma_f32_16x16x32_bf16 v[32:35], a[88:91], a[4:7], v[32:35]\n"
    "v_mfma_f32_16x16x32_bf16 v[20:23], a[88:91], a[8:11], 0\n"
    "ds_write2st64_b32 v38, v54, v55 offset0:4 offset1:6\n"
    "v_mfma_f32_16x16x32_bf16 v[20:23], a[92:95], a[32:35], v[20:23]\n"
    "v_mfma_f32_16x16x32_bf16 v[20:23], a[96:99], a[56:59], v[20:23]\n"
    "ds_read_b128 a[172:175], v5 offset:12800\n"
    "v_mfma_f32_16x16x32_bf16 v[20:23], a[100:103], a[80:83], v[20:23]\n"
    "v_cvt_pk_bf16_f32 v36, v28, v32\n"
    "v_mfma_f32_16x16x32_bf16 v[20:23], a[104:107], v[72:75], v[20:23]\n"
    "v_cvt_pk_bf16_f32 v46, v29, v33\n"
    "v_mfma_f32_16x16x32_bf16 v[20:23], a[108:111], v[96:99], v[20:23]\n"
    "ds_read_b128 a[176:179], v6 offset:12800\n"
    "v_mfma_f32_16x16x32_bf16 v[20:23], a[112:115], v[120:123], v[20:23]\n"
    "v_cvt_pk_bf16_f32 v37, v30, v34\n"
    "v_mfma_f32_16x16x32_bf16 v[20:23], a[116:119], v[144:147], v[20:23]\n"
    "v_cvt_pk_bf16_f32 v50, v31, v35\n"
    "v_mfma_f32_16x16x32_bf16 v[20:23], a[120:123], v[168:171], v[20:23]\n"
    "buffer_load_dwordx4 v2, s[36:39], s81 offen offset:2048 nt lds\n"
    "v_mfma_f32_16x16x32_bf16 v[20:23], a[124:127], v[192:195], v[20:23]\n"
    "v_cndmask_b32_dpp v44, v46, v36, vcc row_ror:8 row_mask:0xf bank_mask:0xf\n"
    "v_mfma_f32_16x16x32_bf16 v[20:23], a[128:131], v[216:219], v[20:23]\n"
    "ds_read_b128 a[180:183], v7 offset:12800\n"
    "v_mfma_f32_16x16x32_bf16 v[20:23], a[132:135], v[240:243], v[20:23]\n"
    "v_cndmask_b32_dpp v48, v50, v37, vcc row_ror:8 row_mask:0xf bank_mask:0xf\n"
    "v_mfma_f32_16x16x32_bf16 v[24:27], a[132:135], v[244:247], 0\n"
    "s_memrealtime s[92:93]\n"
    "v_mfma_f32_16x16x32_bf16 v[24:27], a[128:131], v[220:223], v[24:27]\n"
    "v_mov_b32_dpp v46, v36 row_ror:8 row_mask:0xf bank_mask:0x3\n"
    "v_mfma_f32_16x16x32_bf16 v[24:27], a[124:127], v[196:199], v[24:27]\n"
    "v_mov_b32_dpp v50, v37 row_ror:8 row_mask:0xf bank_mask:0x3\n"
    "v_mfma_f32_16x16x32_bf16 v[24:27], a[120:123], v[172:175], v[24:27]\n"
    "v_readlane_b32 s81, v3, s86\n"
    "v_mfma_f32_16x16x32_bf16 v[24:27], a[116:119], v[148:151], v[24:27]\n"
    "s_waitcnt lgkmcnt(14)\n"
    "buffer_store_dwordx4 a[184:187], v10, s[40:43], s82 offen sc1\n"
    "v_mfma_f32_16x16x32_bf16 v[24:27], a[112:115], v[124:127], v[24:27]\n"
    "s_addk_i32 m0, 12288\n"
    "v_mfma_f32_16x16x32_bf16 v[24:27], a[108:111], v[100:103], v[24:27]\n"
    "v_readlane_b32 s82, v18, s86\n"
    "v_mfma_f32_16x16x32_bf16 v[24:27], a[104:107], v[76:79], v[24:27]\n"
    "s_waitcnt lgkmcnt(0)\n"
    "s_sub_u32 s85, s92, s89\n"
    "v_mfma_f32_16x16x32_bf16 v[24:27], a[100:103], a[84:87], v[24:27]\n"
    "s_cbranch_scc1 43b\n"
    "44:\n"
    "v_mfma_f32_16x16x32_bf16 v[24:27], a[96:99], a[60:63], v[24:27]\n"
    "s_cmp_eq_u32 m0, s77\n"
    "s_cselect_b32 s78, s87, s88\n"
    "v_mfma_f32_16x16x32_bf16 v[24:27], a[92:95], a[36:39], v[24:27]\n"
    "s_cmp_lt_u32 s86, s76\n"
    "v_mfma_f32_16x16x32_bf16 v[24:27], a[88:91], a[12:15], v[24:27]\n"
    "s_cbranch_scc0 55f\n"
    "52:\n"
    "v_mfma_f32_16x16x32_bf16 v[28:31], a[136:139], a[16:19], 0\n"
    "s_add_u32 s86, s86, 1\n"
    "v_mfma_f32_16x16x32_bf16 v[28:31], a[140:143], a[40:43], v[28:31]\n"
    "s_waitcnt vmcnt(13)\n"
    "v_mfma_f32_16x16x32_bf16 v[28:31], a[144:147], a[64:67], v[28:31]\n"
    "ds_write_b32 v13, v14\n"
    "v_mfma_f32_16x16x32_bf16 v[28:31], a[148:151], v[56:59], v[28:31]\n"
    "v_cvt_pk_bf16_f32 v36, v20, v24\n"
    "v_mfma_f32_16x16x32_bf16 v[28:31], a[152:155], v[80:83], v[28:31]\n"
    "v_cvt_pk_bf16_f32 v47, v21, v25\n"
    "v_mfma_f32_16x16x32_bf16 v[28:31], a[156:159], v[104:107], v[28:31]\n"
    "v_cvt_pk_bf16_f32 v37, v22, v26\n"
    "v_mfma_f32_16x16x32_bf16 v[28:31], a[160:163], v[128:131], v[28:31]\n"
    "v_cvt_pk_bf16_f32 v51, v23, v27\n"
    "v_mfma_f32_16x16x32_bf16 v[28:31], a[164:167], v[152:155], v[28:31]\n"
    "v_cndmask_b32_dpp v45, v47, v36, vcc row_ror:8 row_mask:0xf bank_mask:0xf\n"
    "v_mfma_f32_16x16x32_bf16 v[28:31], a[168:171], v[176:179], v[28:31]\n"
    "v_cndmask_b32_dpp v49, v51, v37, vcc row_ror:8 row_mask:0xf bank_mask:0xf\n"
    "v_mfma_f32_16x16x32_bf16 v[28:31], a[172:175], v[200:203], v[28:31]\n"
    "v_mov_b32_dpp v47, v36 row_ror:8 row_mask:0xf bank_mask:0x3\n"
    "v_mfma_f32_16x16x32_bf16 v[28:31], a[176:179], v[224:227], v[28:31]\n"
    "v_mov_b32_dpp v51, v37 row_ror:8 row_mask:0xf bank_mask:0x3\n"
    "v_mfma_f32_16x16x32_bf16 v[28:31], a[180:183], v[248:251], v[28:31]\n"
    "ds_read_b32 v15, v12\n"
    "v_mfma_f32_16x16x32_bf16 v[32:35], a[180:183], v[252:255], 0\n"
    "s_add_u32 s89, s89, s94\n"
    "v_mfma_f32_16x16x32_bf16 v[32:35], a[176:179], v[228:231], v[32:35]\n"
    "v_add_u32 v4, s78, v4\n"
    "v_mfma_f32_16x16x32_bf16 v[32:35], a[172:175], v[204:207], v[32:35]\n"
    "buffer_store_dwordx4 v[44:47], v8, s[40:43], s82 offen sc1\n"
    "v_mfma_f32_16x16x32_bf16 v[32:35], a[168:171], v[180:183], v[32:35]\n"
    "v_add_u32 v5, s78, v5\n"
    "v_mfma_f32_16x16x32_bf16 v[32:35], a[164:167], v[156:159], v[32:35]\n"
    "s_waitcnt lgkmcnt(0)\n"
    "v_cmp_lt_u32 vcc, v15, v14\n"
    "v_mfma_f32_16x16x32_bf16 v[32:35], a[160:163], v[132:135], v[32:35]\n"
    "v_add_u32 v6, s78, v6\n"
    "v_mfma_f32_16x16x32_bf16 v[32:35], a[156:159], v[108:111], v[32:35]\n"
    "s_cbranch_vccnz 61b\n"
    "62:\n"
    "v_mfma_f32_16x16x32_bf16 v[32:35], a[152:155], v[84:87], v[32:35]\n"
    "ds_read_b128 a[184:187], v39\n"
    "v_mfma_f32_16x16x32_bf16 v[32:35], a[148:151], v[60:63], v[32:35]\n"
    "s_cmp_eq_u32 s86, s65\n"
    "s_cbranch_scc1 65b\n"
    "v_mfma_f32_16x16x32_bf16 v[32:35], a[144:147], a[68:71], v[32:35]\n"
    "ds_read_b128 a[88:91], v4\n"
    "v_mfma_f32_16x16x32_bf16 v[32:35], a[140:143], a[44:47], v[32:35]\n"
    "v_add_u32 v7, s78, v7\n"
    "v_mfma_f32_16x16x32_bf16 v[32:35], a[136:139], a[20:23], v[32:35]\n"
    "v_add_u32 v14, 1, v14\n"
    "v_mfma_f32_16x16x32_bf16 v[20:23], a[136:139], a[0:3], 0\n"
    "buffer_load_dwordx4 v0, s[36:39], s81 offen nt lds\n"
    "v_mfma_f32_16x16x32_bf16 v[20:23], a[140:143], a[24:27], v[20:23]\n"
    "ds_read_b128 a[92:95], v5\n"
    "v_mfma_f32_16x16x32_bf16 v[20:23], a[144:147], a[48:51], v[20:23]\n"
    "ds_read_b128 a[96:99], v6\n"
    "v_mfma_f32_16x16x32_bf16 v[20:23], a[148:151], a[72:75], v[20:23]\n"
    "v_cvt_pk_bf16_f32 v52, v28, v32\n"
    "v_mfma_f32_16x16x32_bf16 v[20:23], a[152:155], v[64:67], v[20:23]\n"
    "v_cvt_pk_bf16_f32 v53, v29, v33\n"
    "v_mfma_f32_16x16x32_bf16 v[20:23], a[156:159], v[88:91], v[20:23]\n"
    "v_cvt_pk_bf16_f32 v54, v30, v34\n"
    "v_mfma_f32_16x16x32_bf16 v[20:23], a[160:163], v[112:115], v[20:23]\n"
    "v_cvt_pk_bf16_f32 v55, v31, v35\n"
    "v_mfma_f32_16x16x32_bf16 v[20:23], a[164:167], v[136:139], v[20:23]\n"
    "ds_read_b128 a[100:103], v7\n"
    "v_mfma_f32_16x16x32_bf16 v[20:23], a[168:171], v[160:163], v[20:23]\n"
    "s_mov_b64 vcc, s[12:13]\n"
    "v_mfma_f32_16x16x32_bf16 v[20:23], a[172:175], v[184:187], v[20:23]\n"
    "buffer_load_dwordx4 v1, s[36:39], s81 offen offset:1024 nt lds\n"
    "v_mfma_f32_16x16x32_bf16 v[20:23], a[176:179], v[208:211], v[20:23]\n"
    "v_mfma_f32_16x16x32_bf16 v[20:23], a[180:183], v[232:235], v[20:23]\n"
    "ds_read_b128 a[104:107], v4 offset:256\n"
    "v_mfma_f32_16x16x32_bf16 v[24:27], a[180:183], v[236:239], 0\n"
    "v_mfma_f32_16x16x32_bf16 v[24:27], a[176:179], v[212:215], v[24:27]\n"
    "ds_write2st64_b32 v38, v52, v53 offset0:16 offset1:18\n"
    "v_mfma_f32_16x16x32_bf16 v[24:27], a[172:175], v[188:191], v[24:27]\n"
    "66:\n"
    "v_mfma_f32_16x16x32_bf16 v[24:27], a[168:171], v[164:167], v[24:27]\n"
    "ds_read_b128 a[108:111], v5 offset:256\n"
    "v_mfma_f32_16x16x32_bf16 v[24:27], a[164:167], v[140:143], v[24:27]\n"
    "s_cmp_eq_u32 s86, 20\n"
    "v_mfma_f32_16x16x32_bf16 v[24:27], a[160:163], v[116:119], v[24:27]\n"
    "s_cselect_b32 s30, s31, s30\n"
    "v_mfma_f32_16x16x32_bf16 v[24:27], a[156:159], v[92:95], v[24:27]\n"
    "buffer_store_dwordx4 v[48:51], v9, s[40:43], s82 offen sc1\n"
    "v_mfma_f32_16x16x32_bf16 v[24:27], a[152:155], v[68:71], v[24:27]\n"
    "v_mfma_f32_16x16x32_bf16 v[24:27], a[148:151], a[76:79], v[24:27]\n"
    "ds_read_b128 a[112:115], v6 offset:256\n"
    "v_mfma_f32_16x16x32_bf16 v[24:27], a[144:147], a[52:55], v[24:27]\n"
    "ds_read_b128 a[116:119], v7 offset:256\n"
    "v_mfma_f32_16x16x32_bf16 v[24:27], a[140:143], a[28:31], v[24:27]\n"
    "ds_read_b128 a[120:123], v4 offset:512\n"
    "v_mfma_f32_16x16x32_bf16 v[24:27], a[136:139], a[4:7], v[24:27]\n"
    "v_mfma_f32_16x16x32_bf16 v[28:31], a[136:139], a[8:11], 0\n"
    "ds_write2st64_b32 v38, v54, v55 offset0:20 offset1:22\n"
    "v_mfma_f32_16x16x32_bf16 v[28:31], a[140:143], a[32:35], v[28:31]\n"
    "v_mfma_f32_16x16x32_bf16 v[28:31], a[144:147], a[56:59], v[28:31]\n"
    "ds_read_b128 a[124:127], v5 offset:512\n"
    "v_mfma_f32_16x16x32_bf16 v[28:31], a[148:151], a[80:83], v[28:31]\n"
    "v_cvt_pk_bf16_f32 v36, v20, v24\n"
    "v_mfma_f32_16x16x32_bf16 v[28:31], a[152:155], v[72:75], v[28:31]\n"
    "v_cvt_pk_bf16_f32 v46, v21, v25\n"
    "v_mfma_f32_16x16x32_bf16 v[28:31], a[156:159], v[96:99], v[28:31]\n"
    "ds_read_b128 a[128:131], v6 offset:512\n"
    "v_mfma_f32_16x16x32_bf16 v[28:31], a[160:163], v[120:123], v[28:31]\n"
    "v_cvt_pk_bf16_f32 v37, v22, v26\n"
    "v_mfma_f32_16x16x32_bf16 v[28:31], a[164:167], v[144:147], v[28:31]\n"
    "v_cvt_pk_bf16_f32 v50, v23, v27\n"
    "v_mfma_f32_16x16x32_bf16 v[28:31], a[168:171], v[168:171], v[28:31]\n"
    "buffer_load_dwordx4 v2, s[36:39], s81 offen offset:2048 nt lds\n"
    "v_mfma_f32_16x16x32_bf16 v[28:31], a[172:175], v[192:195], v[28:31]\n"
    "v_cndmask_b32_dpp v44, v46, v36, vcc row_ror:8 row_mask:0xf bank_mask:0xf\n"
    "v_mfma_f32_16x16x32_bf16 v[28:31], a[176:179], v[216:219], v[28:31]\n"
    "ds_read_b128 a[132:135], v7 offset:512\n"
    "v_mfma_f32_16x16x32_bf16 v[28:31], a[180:183], v[240:243], v[28:31]\n"
    "v_cndmask_b32_dpp v48, v50, v37, vcc row_ror:8 row_mask:0xf bank_mask:0xf\n"
    "v_mfma_f32_16x16x32_bf16 v[32:35], a[180:183], v[244:247], 0\n"
    "s_memrealtime s[92:93]\n"
    "v_mfma_f32_16x16x32_bf16 v[32:35], a[176:179], v[220:223], v[32:35]\n"
    "v_mov_b32_dpp v46, v36 row_ror:8 row_mask:0xf bank_mask:0x3\n"
    "v_mfma_f32_16x16x32_bf16 v[32:35], a[172:175], v[196:199], v[32:35]\n"
    "v_mov_b32_dpp v50, v37 row_ror:8 row_mask:0xf bank_mask:0x3\n"
    "v_mfma_f32_16x16x32_bf16 v[32:35], a[168:171], v[172:175], v[32:35]\n"
    "v_readlane_b32 s81, v3, s86\n"
    "v_mfma_f32_16x16x32_bf16 v[32:35], a[164:167], v[148:151], v[32:35]\n"
    "s_waitcnt lgkmcnt(14)\n"
    "buffer_store_dwordx4 a[184:187], v10, s[40:43], s82 offen sc1\n"
    "v_mfma_f32_16x16x32_bf16 v[32:35], a[160:163], v[124:127], v[32:35]\n"
    "s_addk_i32 m0, 12288\n"
    "v_mfma_f32_16x16x32_bf16 v[32:35], a[156:159], v[100:103], v[32:35]\n"
    "v_readlane_b32 s82, v18, s86\n"
    "v_mfma_f32_16x16x32_bf16 v[32:35], a[152:155], v[76:79], v[32:35]\n"
    "s_waitcnt lgkmcnt(0)\n"
    "s_sub_u32 s85, s92, s89\n"
    "v_mfma_f32_16x16x32_bf16 v[32:35], a[148:151], a[84:87], v[32:35]\n"
    "s_cbranch_scc1 63b\n"
    "64:\n"
    "v_mfma_f32_16x16x32_bf16 v[32:35], a[144:147], a[60:63], v[32:35]\n"
    "s_cmp_eq_u32 m0, s80\n"
    "s_cselect_b32 m0, s57, m0\n"
    "v_mfma_f32_16x16x32_bf16 v[32:35], a[140:143], a[36:39], v[32:35]\n"
    "s_cmp_lt_u32 s86, s76\n"
    "v_mfma_f32_16x16x32_bf16 v[32:35], a[136:139], a[12:15], v[32:35]\n"
    "s_cbranch_scc1 51b\n"
    "s_barrier\n"
    "ds_read_b128 a[184:187], v39 offset:4096\n"
    "s_nop 4\n"
    "v_cvt_pk_bf16_f32 v36, v28, v32\n"
    "v_cvt_pk_bf16_f32 v47, v29, v33\n"
    "v_cvt_pk_bf16_f32 v37, v30, v34\n"
    "v_cvt_pk_bf16_f32 v51, v31, v35\n"
    "s_branch 56f\n"
    "55:\n"
    "s_barrier\n"
    "ds_read_b128 a[184:187], v39\n"
    "s_nop 4\n"
    "v_cvt_pk_bf16_f32 v36, v20, v24\n"
    "v_cvt_pk_bf16_f32 v47, v21, v25\n"
    "v_cvt_pk_bf16_f32 v37, v22, v26\n"
    "v_cvt_pk_bf16_f32 v51, v23, v27\n"
    "56:\n"
    "v_cndmask_b32_dpp v45, v47, v36, vcc row_ror:8 row_mask:0xf bank_mask:0xf\n"
    "v_mov_b32_dpp v47, v36 row_ror:8 row_mask:0xf bank_mask:0x3\n"
    "v_cndmask_b32_dpp v49, v51, v37, vcc row_ror:8 row_mask:0xf bank_mask:0xf\n"
    "buffer_store_dwordx4 v[44:47], v8, s[40:43], s82 offen sc1\n"
    "v_mov_b32_dpp v51, v37 row_ror:8 row_mask:0xf bank_mask:0x3\n"
    "s_waitcnt lgkmcnt(0)\n"
    "buffer_store_dwordx4 a[184:187], v10, s[40:43], s82 offen sc1\n"
    "buffer_store_dwordx4 v[48:51], v9, s[40:43], s82 offen sc1\n"
    "s_waitcnt vmcnt(0) lgkmcnt(0)\n"
    "s_endpgm\n"
    "75:\n"
    "s_sleep 2\n"
    "global_load_dwordx4 v[236:239], v16, s[54:55] sc1\n"
    "s_waitcnt vmcnt(0)\n"
    "v_and_b32 v242, 15, v237\n"
    "v_and_b32 v243, 1, v239\n"
    "v_or_b32 v237, 15, v237\n"
    "v_or_b32 v239, 15, v239\n"
    "v_cmp_ne_u64 vcc, s[18:19], v[236:237]\n"
    "v_cmp_ne_u64 s[48:49], s[18:19], v[238:239]\n"
    "v_readlane_b32 s9, v242, 0\n"
    "v_and_b32 v236, 1, v242\n"
    "s_or_b64 s[48:49], s[48:49], vcc\n"
    "s_cbranch_scc1 75b\n"
    "s_branch 77b\n"
    "79:\n"
    "s_nop 0\n"
    : "+{s[0:1]}"(dp), "+{s[2:3]}"(ka), "+{s[4:5]}"(did), "+{s6}"(bid), "+{v0}"(tid)
    : [lds] "{s98}"(lds0)
    : "v1","v2","v3","v4","v5","v6","v7","v8","v9","v10","v11","v12","v13","v14","v15","v16","v17","v18","v19","v20","v21","v22","v23","v24","v25","v26","v27","v28","v29","v30","v31","v32","v33","v34","v35","v36","v37","v38","v39","v40","v41","v42","v43","v44","v45","v46","v47","v48","v49","v50","v51","v52","v53","v54","v55","v56","v57","v58","v59","v60","v61","v62","v63","v64","v65","v66","v67","v68","v69","v70","v71","v72","v73","v74","v75","v76","v77","v78","v79","v80","v81","v82","v83","v84","v85","v86","v87","v88","v89","v90","v91","v92","v93","v94","v95","v96","v97","v98","v99","v100","v101","v102","v103","v104","v105","v106","v107","v108","v109","v110","v111","v112","v113","v114","v115","v116","v117","v118","v119","v120","v121","v122","v123","v124","v125","v126","v127","v128","v129","v130","v131","v132","v133","v134","v135","v136","v137","v138","v139","v140","v141","v142","v143","v144","v145","v146","v147","v148","v149","v150","v151","v152","v153","v154","v155","v156","v157","v158","v159","v160","v161","v162","v163","v164","v165","v166","v167","v168","v169","v170","v171","v172","v173","v174","v175","v176","v177","v178","v179","v180","v181","v182","v183","v184","v185","v186","v187","v188","v189","v190","v191","v192","v193","v194","v195","v196","v197","v198","v199","v200","v201","v202","v203","v204","v205","v206","v207","v208","v209","v210","v211","v212","v213","v214","v215","v216","v217","v218","v219","v220","v221","v222","v223","v224","v225","v226","v227","v228","v229","v230","v231","v232","v233","v234","v235","v236","v237","v238","v239","v240","v241","v242","v243","v244","v245","v246","v247","v248","v249","v250","v251","v252","v253","v254","v255","a0","a1","a2","a3","a4","a5","a6","a7","a8","a9","a10","a11","a12","a13","a14","a15","a16","a17","a18","a19","a20","a21","a22","a23","a24","a25","a26","a27","a28","a29","a30","a31","a32","a33","a34","a35","a36","a37","a38","a39","a40","a41","a42","a43","a44","a45","a46","a47","a48","a49","a50","a51","a52","a53","a54","a55","a56","a57","a58","a59","a60","a61","a62","a63","a64","a65","a66","a67","a68","a69","a70","a71","a72","a73","a74","a75","a76","a77","a78","a79","a80","a81","a82","a83","a84","a85","a86","a87","a88","a89","a90","a91","a92","a93","a94","a95","a96","a97","a98","a99","a100","a101","a102","a103","a104","a105","a106","a107","a108","a109","a110","a111","a112","a113","a114","a115","a116","a117","a118","a119","a120","a121","a122","a123","a124","a125","a126","a127","a128","a129","a130","a131","a132","a133","a134","a135","a136","a137","a138","a139","a140","a141","a142","a143","a144","a145","a146","a147","a148","a149","a150","a151","a152","a153","a154","a155","a156","a157","a158","a159","a160","a161","a162","a163","a164","a165","a166","a167","a168","a169","a170","a171","a172","a173","a174","a175","a176","a177","a178","a179","a180","a181","a182","a183","a184","a185","a186","a187","a188","a189","a190","a191","a192","a193","a194","a195","a196","a197","a198","a199","a200","a201","a202","a203","a204","a205","a206","a207","a208","a209","a210","a211","a212","s7","s8","s9","s10","s11","s12","s13","s14","s15","s16","s17","s18","s19","s20","s21","s22","s23","s24","s25","s26","s27","s28","s29","s30","s31","s33","s34","s35","s36","s37","s38","s39","s40","s41","s42","s43","s44","s45","s46","s47","s48","s49","s50","s51","s52","s53","s54","s55","s56","s57","s58","s59","s60","s61","s62","s63","s64","s65","s66","s67","s68","s69","s70","s71","s72","s73","s74","s75","s76","s77","s78","s79","s80","s81","s82","s83","s84","s85","s86","s87","s88","s89","s90","s91","s92","s93","s94","s95","s96","s97","vcc","scc","memory");
}

extern "C" void kernel_launch(void* const* d_in, const int* in_sizes, int n_in,
                              void* d_out, int out_size, void* d_ws, size_t ws_size,
                              hipStream_t stream) {
  Shapes sh;
  for (int j = 0; j < 16; ++j)
    for (int w = 0; w < 4; ++w) {
      const int ty = j >= 11, per = ty ? 10 : 11, n = 16 * (j - 11 * ty);
      auto row = [&](int k) { return ty ? 64 * (k / 10) + 11 + k % 10 : 64 * (k / 22) + 21 * (k / 11 % 2) + k % 11; };
      const int n0 = n + 4 * w, bp = per - n0 % per;
      uint32_t* e = sh.v + 16 * j + 4 * w;
      e[0] = row(n0) * 768 - 512;
      e[1] = (row(n0 + bp) - bp) * 768 - 512;
      e[2] = bp * 768;
      e[3] = (row(n + 8) + 16) * 768 + ty;
      for (int i = 0; i < 4; ++i) sh.rows[16 * j + 4 * w + i] = row(n0 + i) * 768;
    }
  const int grid = 256, maxr = 40;
  const size_t M = (size_t)in_sizes[0] / CH;
  const uint32_t fl = 0x00020000u;
  for (size_t r0 = 0; r0 < M; r0 += (size_t)maxr * grid * TM) {
    const size_t rows = M - r0 < (size_t)maxr * grid * TM ? M - r0 : (size_t)maxr * grid * TM;
    const int nr = (int)((rows + grid * TM - 1) / (grid * TM)), nit = nr < 2 ? 2 : nr;
    const uint32_t nbytes = (uint32_t)(rows * CH * 2);
    const uint32_t np = (nbytes >> 21) & ~1u;
    const uint64_t pages = (np >= 24 ? (np - 14) & ~7u : 64u) | (uint64_t)(np >= 2 ? (np - 2) << 21 : 0u) << 32;
    dwsep_kernel<<<grid, 256, 0, stream>>>((const char*)d_in[0] + r0 * CH * 2, nbytes, fl, (char*)d_out + r0 * CH * 2, nbytes, fl,
                                           (const char*)d_in[2], CH * CH * 2, fl, (const char*)d_in[1], CH * 2, fl,
                                           (uint32_t)nit, (uint32_t)grid * TILE_BYTES, d_ws, pages, sh);
  }
}
```

```cpp
#include <hip/hip_runtime.h>
#include <stdint.h>

#define CH 384
#define TM 16
#define TILE_BYTES (TM * CH * 2)
#define LDS_BYTES (13 * TILE_BYTES + 1024 + 64)

struct Shapes { uint32_t v[256], rows[256]; };
extern "C" __device__ uint64_t dispatch_id_() __asm("llvm.amdgcn.dispatch.id");

__global__ __launch_bounds__(256, 1)
void dwsep_kernel(const char* x, uint32_t xbytes, uint32_t f0, char* out, uint32_t obytes, uint32_t f1,
                  const char* pw, uint32_t pwbytes, uint32_t f2, const char* dw, uint32_t dwbytes, uint32_t f3,
                  uint32_t nit, uint32_t tstride, void* ws, uint64_t pages, Shapes shapes) {
  __shared__ __attribute__((aligned(1024))) char lds[LDS_BYTES];
  uint32_t lds0 = (uint32_t)(uintptr_t)(__attribute__((address_space(3))) char*)lds;
  uint64_t ka = (uint64_t)(uintptr_t)__builtin_amdgcn_kernarg_segment_ptr();
  uint64_t did = dispatch_id_(), dp = (uint64_t)(uintptr_t)__builtin_amdgcn_dispatch_ptr();
  uint32_t bid = blockIdx.x, tid = threadIdx.x;
  asm volatile(
    "s_load_dwordx16 s[36:51], s[2:3], 0x0\n"
    "v_readfirstlane_b32 s8, v0\n"
    "s_load_dwordx4 s[52:55], s[2:3], 0x40\n"
    "s_bfe_u32 s7, s6, 0x40003\n"
    "s_lshr_b32 s9, s8, 2\n"
    "s_lshl_b32 s7, s7, 6\n"
    "s_add_u32 s9, s7, s9\n"
    "s_load_dwordx4 s[20:23], s[2:3], s9 offset:0x58\n"
    "s_load_dwordx2 s[96:97], s[2:3], 0x50\n"
    "s_getpc_b64 s[14:15]\n"
    "70:\n"
    "v_lshlrev_b32 v48, 6, v0\n"
    "s_mov_b32 s13, 79f-70b\n"
    "v_min_u32 v48, s13, v48\n"
    "global_load_dword v49, v48, s[14:15]\n"
    "v_and_b32 v18, 48, v0\n"
    "v_bfe_u32 v55, v0, 3, 1\n"
    "v_bfe_u32 v54, v0, 6, 1\n"
    "v_lshl_add_u32 v53, v55, 2, v18\n"
    "s_mov_b32 s16, s4\n"
    "v_add_u32 v53, s7, v53\n"
    "v_lshl_add_u32 v54, v54, 3, v53\n"
    "global_load_dword v8, v53, s[2:3] offset:1112\n"
    "global_load_dword v9, v53, s[2:3] offset:1120\n"
    "global_load_dword v10, v54, s[2:3] offset:1112\n"
    "s_mov_b32 s2, s6\n"
    "s_mov_b32 s3, %[lds]\n"
    "s_lshr_b32 s4, s8, 6\n"
    "v_and_b32 v53, 24, v0\n"
    "v_bfe_u32 v54, v0, 5, 1\n"
    "s_bfe_u32 s5, s2, 0x20003\n"
    "v_add_u32 v53, v53, v54\n"
    "s_bfe_u32 s6, s2, 0x20005\n"
    "v_mul_u32_u24 v53, 0x300, v53\n"
    "s_cmp_eq_u32 s6, 3\n"
    "v_and_b32 v55, 7, v0\n"
    "s_cselect_b32 s6, 0, s6\n"
    "v_lshl_add_u32 v53, v55, 4, v53\n"
    "s_lshl_b32 s7, s4, 7\n"
    "v_and_b32 v16, 63, v0\n"
    "s_mul_i32 s58, s5, 24576\n"
    "v_add_u32 v244, s7, v53\n"
    "s_mul_i32 s62, s6, 98304\n"
    "v_lshlrev_b32 v16, 4, v16\n"
    "s_lshl_b32 s7, s5, 6\n"
    "s_lshl_b32 s8, s6, 8\n"
    "v_xor_b32 v53, s7, v16\n"
    "s_add_u32 s12, s3, 159744\n"
    "s_mul_i32 s83, s4, 30720\n"
    "v_add_u32 v53, s8, v53\n"
    "s_mov_b32 m0, s12\n"
    "v_subrev_u32 v55, 0x300, v53\n"
    "s_add_u32 s83, s83, s3\n"
    "v_min_u32 v250, v53, v55\n"
    "v_add_u32 v245, 0x200, v244\n"
    "v_add_u32 v246, 0x400, v244\n"
    "v_add_u32 v247, 0x600, v244\n"
    "v_and_b32 v235, 12, v0\n"
    "s_mul_i32 s57, s4, 3072\n"
    "v_bfe_u32 v52, v0, 4, 2\n"
    "s_lshl_b32 s7, s4, 6\n"
    "v_lshl_add_u32 v53, v235, 1, v52\n"
    "v_mul_u32_u24 v53, 0x300, v53\n"
    "s_add_u32 s57, s57, s3\n"
    "v_and_b32 v54, 3, v0\n"
    "s_add_u32 s7, s7, 512\n"
    "v_lshl_add_u32 v53, v54, 4, v53\n"
    "v_lshlrev_b32 v12, 2, v54\n"
    "v_add_u32 v248, s7, v53\n"
    "v_add_u32 v249, 0x800, v248\n"
    "s_lshr_b64 s[18:19], s[0:1], 12\n"
    "s_lshl4_add_u32 s18, s16, s18\n"
    "s_andn2_b32 s17, s18, 15\n"
    "s_bitset1_b32 s17, 31\n"
    "s_mov_b32 s72, s17\n"
    "s_pack_ll_b32_b16 s18, -1, s16\n"
    "s_or_b32 s19, s17, 15\n"
    "s_lshl_b32 s34, s2, 20\n"
    "s_and_b32 s34, s34, 0x7e00000\n"
    "s_mul_i32 s7, s16, 0x9e3779b1\n"
    "s_lshr_b32 s7, s7, 9\n"
    "s_and_b32 s7, s7, 0x1f8f80\n"
    "s_or_b32 s34, s34, s7\n"
    "s_and_b32 s35, s2, 7\n"
    "s_lshl_b32 s7, s4, 1\n"
    "s_sub_u32 s35, s35, s7\n"
    "v_and_b32 v19, 15, v0\n"
    "v_add_u32 v18, s12, v18\n"
    "v_and_b32 v54, 14, v0\n"
    "v_lshrrev_b32 v53, 1, v235\n"
    "v_add_u32 v235, v18, v54\n"
    "v_add_u32 v18, v18, v53\n"
    "s_mov_b32 s9, 0x51403020\n"
    "s_mov_b32 s10, 0x52423121\n"
    "s_mov_b32 s11, 0x53433322\n"
    "s_cmp_eq_u32 s6, 1\n"
    "s_cselect_b32 s9, 0x61204030, s9\n"
    "s_cselect_b32 s10, 0x62521131, s10\n"
    "s_cselect_b32 s11, 0x33534302, s11\n"
    "s_cmp_eq_u32 s6, 2\n"
    "s_cselect_b32 s9, 0x71302040, s9\n"
    "s_cselect_b32 s10, 0x42622111, s10\n"
    "s_cselect_b32 s11, 0x43335312, s11\n"
    "s_xor_b32 s7, s4, s5\n"
    "v_lshlrev_b32 v54, 3, v52\n"
    "s_lshl_b32 s7, s7, 2\n"
    "v_xor_b32 v55, s7, v19\n"
    "v_bfe_u32 v53, s11, v54, 8\n"
    "v_xor_b32 v2, v53, v55\n"
    "v_bfe_u32 v53, s10, v54, 8\n"
    "v_xor_b32 v1, v53, v55\n"
    "v_bfe_u32 v53, s9, v54, 8\n"
    "v_xor_b32 v0, v53, v55\n"
    "s_and_b32 s28, s2, 3\n"
    "s_lshr_b32 s8, s2, 7\n"
    "s_lshl2_add_u32 s7, s8, s28\n"
    "s_waitcnt lgkmcnt(0)\n"
    "s_memrealtime s[0:1]\n"
    "s_barrier\n"
    "buffer_load_dwordx4 v250, s[48:51], 0 offen lds\n"
    "s_cmp_eq_u32 s35, 0\n"
    "s_cbranch_scc0 72f\n"
    "s_bitcmp1_b32 s2, 7\n"
    "s_cselect_b64 s[68:69], s[40:41], s[36:37]\n"
    "s_mov_b64 s[70:71], s[38:39]\n"
    "s_and_b32 s85, s34, 0x3fffff\n"
    "s_add_u32 s85, s85, s97\n"
    "s_min_u32 s34, s34, s85\n"
    "s_buffer_load_dword s73, s[68:71], s34\n"
    "s_buffer_load_dword s72, s[68:71], s34 offset:0x2000\n"
    "72:\n"
    "s_add_u32 m0, s83, 36864\n"
    "s_add_u32 s84, s58, s62\n"
    "s_mul_i32 s56, s7, 393216\n"
    "s_lshr_b32 s29, s2, 2\n"
    "s_mov_b32 s30, s57\n"
    "buffer_load_dwordx4 v244, s[44:47], s84 offen lds\n"
    "s_xor_b32 s7, s5, 1\n"
    "s_mul_i32 s59, s7, 24576\n"
    "s_xor_b32 s7, s5, 2\n"
    "buffer_load_dwordx4 v245, s[44:47], s84 offen offset:1024 lds\n"
    "s_mul_i32 s60, s7, 24576\n"
    "s_xor_b32 s7, s5, 3\n"
    "s_mul_i32 s61, s7, 24576\n"
    "buffer_load_dwordx4 v246, s[44:47], s84 offen offset:2048 lds\n"
    "v_mov_b32 v53, s20\n"
    "v_mov_b32 v54, s21\n"
    "v_cmp_le_i32 vcc, s22, v16\n"
    "s_sub_u32 s22, s22, 0x400\n"
    "s_mov_b32 s33, 0x90000000\n"
    "v_cndmask_b32 v55, v53, v54, vcc\n"
    "buffer_load_dwordx4 v247, s[44:47], s84 offen offset:3072 lds\n"
    "s_add_u32 m0, s83, 40960\n"
    "v_lshl_add_u32 v0, v0, 4, v55\n"
    "v_cmp_le_i32 vcc, s22, v16\n"
    "s_sub_u32 s22, s22, 0x400\n"
    "s_nop 0\n"
    "v_cndmask_b32 v55, v53, v54, vcc\n"
    "v_lshl_add_u32 v1, v1, 4, v55\n"
    "buffer_load_dwordx4 v248, s[44:47], s84 offen lds\n"
    "v_cmp_le_i32 vcc, s22, v16\n"
    "s_sub_u32 s22, s22, 0x400\n"
    "s_nop 0\n"
    "v_cndmask_b32 v55, v53, v54, vcc\n"
    "v_lshl_add_u32 v2, v2, 4, v55\n"
    "s_add_u32 s7, s83, 36864\n"
    "buffer_load_dwordx4 v249, s[44:47], s84 offen offset:1024 lds\n"
    "s_add_u32 m0, s83, 43008\n"
    "s_add_u32 s84, s59, s62\n"
    "v_and_b32 v240, 0x70, v16\n"
    "v_and_b32 v53, 8, v19\n"
    "v_lshl_add_u32 v53, v52, 7, v53\n"
    "buffer_load_dwordx4 v244, s[44:47], s84 offen lds\n"
    "v_lshrrev_b32 v54, 2, v16\n"
    "v_add3_u32 v15, s7, v53, v240\n"
    "v_add_u32 v38, s7, v54\n"
    "buffer_load_dwordx4 v245, s[44:47], s84 offen offset:1024 lds\n"
    "s_cmp_eq_u32 s35, 0\n"
    "s_cbranch_scc0 73f\n"
    "s_getreg_b32 s88, hwreg(HW_REG_XCC_ID, 0, 3)\n"
    "s_lshl_b32 s89, s2, 3\n"
    "s_and_b32 s89, s89, 0x3f0\n"
    "s_lshr_b32 s82, s2, 7\n"
    "s_lshl3_add_u32 s89, s82, s89\n"
    "v_mov_b32 v20, s89\n"
    "s_lshr_b32 s89, s88, 2\n"
    "s_lshl1_add_u32 s88, s88, s89\n"
    "s_or_b32 s88, s88, s17\n"
    "s_bfe_u32 s89, s2, 0x10001\n"
    "s_xor_b32 s88, s88, s89\n"
    "s_waitcnt lgkmcnt(1)\n"
    "s_nop 3\n"
    "s_cmp_lg_u32 s72, s17\n"
    "s_cselect_b32 s85, 1, 0\n"
    "s_xor_b32 s88, s88, s85\n"
    "v_mov_b32 v23, s88\n"
    "73:\n"
    "s_lshr_b32 s8, s4, 1\n"
    "s_lshl_b32 s8, s8, 11\n"
    "s_add_u32 s8, s8, s3\n"
    "buffer_load_dwordx4 v246, s[44:47], s84 offen offset:2048 lds\n"
    "s_add_u32 s8, s8, 147456\n"
    "s_and_b32 s13, s4, 1\n"
    "s_lshl_b32 s14, s13, 8\n"
    "buffer_load_dwordx4 v247, s[44:47], s84 offen offset:3072 lds\n"
    "s_sub_u32 s7, s14, s7\n"
    "s_add_u32 s11, s7, s8\n"
    "s_lshl2_add_u32 s8, s14, s8\n"
    "v_and_b32 v53, 0x30, v16\n"
    "v_lshrrev_b32 v54, 2, v19\n"
    "v_lshl_add_u32 v53, v54, 8, v53\n"
    "v_lshl_add_u32 v53, v52, 6, v53\n"
    "v_add_u32 v39, s8, v53\n"
    "v_mov_b32 v251, 0\n"
    "v_mov_b32 v253, 0\n"
    "v_mov_b32 v255, 0\n"
    "s_add_u32 s63, s62, 98304\n"
    "s_cmp_eq_u32 s63, 294912\n"
    "s_cselect_b32 s63, 0, s63\n"
    "s_add_u32 s64, s63, 98304\n"
    "s_cmp_eq_u32 s64, 294912\n"
    "s_cselect_b32 s64, 0, s64\n"
    "s_add_u32 s7, s3, 160768\n"
    "v_mov_b32 v14, 0\n"
    "v_add_u32 v12, s7, v12\n"
    "s_lshl2_add_u32 s7, s4, s7\n"
    "v_mov_b32 v13, s7\n"
    "v_xor_b32 v53, v52, v19\n"
    "v_mul_u32_u24 v54, 0x300, v19\n"
    "v_lshl_add_u32 v54, v53, 4, v54\n"
    "v_add_u32 v4, s3, v54\n"
    "v_xor_b32 v5, 64, v4\n"
    "v_xor_b32 v6, 128, v4\n"
    "v_xor_b32 v7, 192, v4\n"
    "s_lshl_b32 s7, s4, 7\n"
    "s_lshr_b32 s8, s4, 1\n"
    "s_lshl_b32 s8, s8, 7\n"
    "s_add_u32 s8, s8, 512\n"
    "v_add_u32 v241, s8, v240\n"
    "v_add_u32 v240, s7, v240\n"
    "v_lshrrev_b32 v19, 4, v16\n"
    "s_mul_i32 s7, s52, 5\n"
    "s_lshr_b32 s7, s7, 3\n"
    "s_sub_u32 s8, s52, 1\n"
    "s_bitcmp1_b32 s2, 7\n"
    "s_cselect_b32 s7, s7, 0\n"
    "v_add_u32 v53, s7, v19\n"
    "v_cmp_gt_u32 vcc, s8, v19\n"
    "v_subrev_u32 v54, s8, v53\n"
    "v_min_u32 v53, v53, v54\n"
    "v_cndmask_b32 v53, v19, v53, vcc\n"
    "v_mul_lo_u32 v3, v53, s53\n"
    "v_add_u32 v3, s56, v3\n"
    "s_xor_b32 s29, s29, s23\n"
    "s_mul_i32 s7, s52, s53\n"
    "s_add_u32 s7, s7, s56\n"
    "s_mov_b32 s8, -2752512\n"
    "s_bitcmp1_b32 s2, 0\n"
    "s_cselect_b32 s8, -3538944, s8\n"
    "s_add_u32 s7, s7, s8\n"
    "v_cmp_eq_u32 vcc, s52, v19\n"
    "v_mov_b32 v53, s7\n"
    "s_nop 0\n"
    "v_cndmask_b32 v3, v3, v53, vcc\n"
    "v_add_u32 v53, s23, v3\n"
    "v_lshrrev_b32 v53, 21, v53\n"
    "v_and_b32 v232, 56, v53\n"
    "v_and_b32 v233, 4, v53\n"
    "v_xor_b32 v234, s29, v53\n"
    "s_cmp_eq_u32 s35, 0\n"
    "s_cbranch_scc0 71f\n"
    "s_pack_ll_b32_b16 s88, s0, s16\n"
    "s_mov_b64 exec, 1\n"
    "v_mov_b32 v22, s88\n"
    "global_store_dwordx2 v20, v[22:23], s[54:55] sc1\n"
    "s_mov_b64 exec, -1\n"
    "s_waitcnt lgkmcnt(0)\n"
    "71:\n"
    "s_branch 301f\n"
    "81:\n"
    "ds_read_b32 v15, v12\n"
    "s_waitcnt lgkmcnt(0)\n"
    "v_cmp_lt_u32 vcc, v15, v14\n"
    "s_nop 0\n"
    "s_cbranch_vccnz 81b\n"
    "s_branch 82f\n"
    "301:\n"
    "s_branch 302f\n"
    "41:\n"
    "ds_read_b32 v15, v12\n"
    "s_waitcnt lgkmcnt(0)\n"
    "v_cmp_lt_u32 vcc, v15, v14\n"
    "s_nop 0\n"
    "s_cbranch_vccnz 41b\n"
    "s_branch 42f\n"
    "43:\n"
    "s_memrealtime s[92:93]\n"
    "s_waitcnt lgkmcnt(0)\n"
    "s_sub_u32 s84, s92, s89\n"
    "s_cmp_lt_i32 s84, 0\n"
    "s_cbranch_scc0 44f\n"
    "s_sleep 1\n"
    "s_branch 43b\n"
    "302:\n"
    "s_branch 303f\n"
    "61:\n"
    "ds_read_b32 v15, v12\n"
    "s_waitcnt lgkmcnt(0)\n"
    "v_cmp_lt_u32 vcc, v15, v14\n"
    "s_nop 0\n"
    "s_cbranch_vccnz 61b\n"
    "s_branch 62f\n"
    "63:\n"
    "s_memrealtime s[92:93]\n"
    "s_waitcnt lgkmcnt(0)\n"
    "s_sub_u32 s84, s92, s89\n"
    "s_cmp_lt_i32 s84, 0\n"
    "s_cbranch_scc0 64f\n"
    "s_sleep 1\n"
    "s_branch 63b\n"
    "303:\n"
    "s_branch 304f\n"
    "45:\n"
    "s_sub_u32 s7, s86, s71\n"
    "s_cmp_eq_u32 s7, 2\n"
    "s_cbranch_scc1 402f\n"
    "s_cmp_eq_u32 s7, 4\n"
    "s_cbranch_scc1 404f\n"
    "v_mfma_f32_16x16x32_bf16 v[24:27], a[96:99], a[68:71], v[24:27]\n"
    "s_add_u32 s7, s70, 0\n"
    "s_add_u32 s64, s64, 2\n"
    "ds_read_b128 a[136:139], v4 offset:12288\n"
    "v_mfma_f32_16x16x32_bf16 v[24:27], a[92:95], a[44:47], v[24:27]\n"
    "v_add_u32 v14, 1, v14\n"
    "v_accvgpr_write_b32 a188, v44\n"
    "v_mfma_f32_16x16x32_bf16 v[24:27], a[88:91], a[20:23], v[24:27]\n"
    "s_mov_b64 vcc, s[12:13]\n"
    "v_accvgpr_write_b32 a189, v45\n"
    "v_mfma_f32_16x16x32_bf16 v[28:31], a[88:91], a[0:3], 0\n"
    "buffer_load_dwordx4 v0, s[36:39], s81 offen nt lds\n"
    "v_mfma_f32_16x16x32_bf16 v[28:31], a[92:95], a[24:27], v[28:31]\n"
    "s_waitcnt lgkmcnt(1)\n"
    "ds_read_b128 a[140:143], v5 offset:12288\n"
    "v_mfma_f32_16x16x32_bf16 v[28:31], a[96:99], a[48:51], v[28:31]\n"
    "ds_read_b128 a[144:147], v6 offset:12288\n"
    "v_accvgpr_write_b32 a190, v46\n"
    "v_mfma_f32_16x16x32_bf16 v[28:31], a[100:103], a[72:75], v[28:31]\n"
    "v_cvt_pk_bf16_f32 v52, v20, v24\n"
    "v_accvgpr_write_b32 a191, v47\n"
    "v_mfma_f32_16x16x32_bf16 v[28:31], a[104:107], v[64:67], v[28:31]\n"
    "v_cvt_pk_bf16_f32 v53, v21, v25\n"
    "v_accvgpr_write_b32 a192, v48\n"
    "v_mfma_f32_16x16x32_bf16 v[28:31], a[108:111], v[88:91], v[28:31]\n"
    "v_cvt_pk_bf16_f32 v54, v22, v26\n"
    "v_accvgpr_write_b32 a193, v49\n"
    "v_mfma_f32_16x16x32_bf16 v[28:31], a[112:115], v[112:115], v[28:31]\n"
    "v_cvt_pk_bf16_f32 v55, v23, v27\n"
    "v_accvgpr_write_b32 a194, v50\n"
    "v_mfma_f32_16x16x32_bf16 v[28:31], a[116:119], v[136:139], v[28:31]\n"
    "ds_read_b128 a[148:151], v7 offset:12288\n"
    "v_accvgpr_write_b32 a195, v51\n"
    "v_mfma_f32_16x16x32_bf16 v[28:31], a[120:123], v[160:163], v[28:31]\n"
    "v_accvgpr_mov_b32 a196, a184\n"
    "v_readlane_b32 s9, v3, s7\n"
    "v_mfma_f32_16x16x32_bf16 v[28:31], a[124:127], v[184:187], v[28:31]\n"
    "buffer_load_dwordx4 v1, s[36:39], s81 offen offset:1024 nt lds\n"
    "v_mfma_f32_16x16x32_bf16 v[28:31], a[128:131], v[208:211], v[28:31]\n"
    "v_accvgpr_mov_b32 a197, a185\n"
    "v_mfma_f32_16x16x32_bf16 v[28:31], a[132:135], v[232:235], v[28:31]\n"
    "ds_read_b128 a[152:155], v4 offset:12544\n"
    "v_mfma_f32_16x16x32_bf16 v[32:35], a[132:135], v[236:239], 0\n"
    "v_accvgpr_mov_b32 a198, a186\n"
    "v_mfma_f32_16x16x32_bf16 v[32:35], a[128:131], v[212:215], v[32:35]\n"
    "ds_write2st64_b32 v38, v52, v53 offset0:0 offset1:2\n"
    "v_mfma_f32_16x16x32_bf16 v[32:35], a[124:127], v[188:191], v[32:35]\n"
    "v_accvgpr_mov_b32 a199, a187\n"
    "s_branch 46f\n"
    "304:\n"
    "s_branch 305f\n"
    "402:\n"
    "v_mfma_f32_16x16x32_bf16 v[24:27], a[96:99], a[68:71], v[24:27]\n"
    "s_add_u32 s64, s64, 2\n"
    "ds_read_b128 a[136:139], v4 offset:12288\n"
    "v_mfma_f32_16x16x32_bf16 v[24:27], a[92:95], a[44:47], v[24:27]\n"
    "v_add_u32 v14, 1, v14\n"
    "v_mfma_f32_16x16x32_bf16 v[24:27], a[88:91], a[20:23], v[24:27]\n"
    "s_mov_b64 vcc, s[12:13]\n"
    "v_mfma_f32_16x16x32_bf16 v[28:31], a[88:91], a[0:3], 0\n"
    "buffer_store_dwordx4 a[188:191], v8, s[40:43], s9 offen sc1\n"
    "v_mfma_f32_16x16x32_bf16 v[28:31], a[92:95], a[24:27], v[28:31]\n"
    "ds_read_b128 a[140:143], v5 offset:12288\n"
    "v_mfma_f32_16x16x32_bf16 v[28:31], a[96:99], a[48:51], v[28:31]\n"
    "ds_read_b128 a[144:147], v6 offset:12288\n"
    "v_mfma_f32_16x16x32_bf16 v[28:31], a[100:103], a[72:75], v[28:31]\n"
    "v_cvt_pk_bf16_f32 v52, v20, v24\n"
    "v_mfma_f32_16x16x32_bf16 v[28:31], a[104:107], v[64:67], v[28:31]\n"
    "v_cvt_pk_bf16_f32 v53, v21, v25\n"
    "v_mfma_f32_16x16x32_bf16 v[28:31], a[108:111], v[88:91], v[28:31]\n"
    "v_cvt_pk_bf16_f32 v54, v22, v26\n"
    "v_mfma_f32_16x16x32_bf16 v[28:31], a[112:115], v[112:115], v[28:31]\n"
    "v_cvt_pk_bf16_f32 v55, v23, v27\n"
    "v_mfma_f32_16x16x32_bf16 v[28:31], a[116:119], v[136:139], v[28:31]\n"
    "ds_read_b128 a[148:151], v7 offset:12288\n"
    "v_mfma_f32_16x16x32_bf16 v[28:31], a[120:123], v[160:163], v[28:31]\n"
    "v_mfma_f32_16x16x32_bf16 v[28:31], a[124:127], v[184:187], v[28:31]\n"
    "buffer_store_dwordx4 a[192:195], v9, s[40:43], s9 offen sc1\n"
    "v_mfma_f32_16x16x32_bf16 v[28:31], a[128:131], v[208:211], v[28:31]\n"
    "v_mfma_f32_16x16x32_bf16 v[28:31], a[132:135], v[232:235], v[28:31]\n"
    "ds_read_b128 a[152:155], v4 offset:12544\n"
    "v_mfma_f32_16x16x32_bf16 v[32:35], a[132:135], v[236:239], 0\n"
    "v_mfma_f32_16x16x32_bf16 v[32:35], a[128:131], v[212:215], v[32:35]\n"
    "ds_write2st64_b32 v38, v52, v53 offset0:0 offset1:2\n"
    "v_mfma_f32_16x16x32_bf16 v[32:35], a[124:127], v[188:191], v[32:35]\n"
    "s_branch 46f\n"
    "305:\n"
    "s_branch 306f\n"
    "404:\n"
    "v_mfma_f32_16x16x32_bf16 v[24:27], a[96:99], a[68:71], v[24:27]\n"
    "s_sub_u32 s89, s89, 36\n"
    "ds_read_b128 a[136:139], v4 offset:12288\n"
    "v_mfma_f32_16x16x32_bf16 v[24:27], a[92:95], a[44:47], v[24:27]\n"
    "v_add_u32 v14, 1, v14\n"
    "v_mfma_f32_16x16x32_bf16 v[24:27], a[88:91], a[20:23], v[24:27]\n"
    "s_mov_b64 vcc, s[12:13]\n"
    "v_mfma_f32_16x16x32_bf16 v[28:31], a[88:91], a[0:3], 0\n"
    "buffer_store_dwordx4 a[204:207], v9, s[40:43], s10 offen sc1\n"
    "v_mfma_f32_16x16x32_bf16 v[28:31], a[92:95], a[24:27], v[28:31]\n"
    "ds_read_b128 a[140:143], v5 offset:12288\n"
    "v_mfma_f32_16x16x32_bf16 v[28:31], a[96:99], a[48:51], v[28:31]\n"
    "ds_read_b128 a[144:147], v6 offset:12288\n"
    "v_mfma_f32_16x16x32_bf16 v[28:31], a[100:103], a[72:75], v[28:31]\n"
    "v_cvt_pk_bf16_f32 v52, v20, v24\n"
    "v_mfma_f32_16x16x32_bf16 v[28:31], a[104:107], v[64:67], v[28:31]\n"
    "v_cvt_pk_bf16_f32 v53, v21, v25\n"
    "v_mfma_f32_16x16x32_bf16 v[28:31], a[108:111], v[88:91], v[28:31]\n"
    "v_cvt_pk_bf16_f32 v54, v22, v26\n"
    "v_mfma_f32_16x16x32_bf16 v[28:31], a[112:115], v[112:115], v[28:31]\n"
    "v_cvt_pk_bf16_f32 v55, v23, v27\n"
    "v_mfma_f32_16x16x32_bf16 v[28:31], a[116:119], v[136:139], v[28:31]\n"
    "ds_read_b128 a[148:151], v7 offset:12288\n"
    "v_mfma_f32_16x16x32_bf16 v[28:31], a[120:123], v[160:163], v[28:31]\n"
    "v_mfma_f32_16x16x32_bf16 v[28:31], a[124:127], v[184:187], v[28:31]\n"
    "buffer_store_dwordx4 a[208:211], v10, s[40:43], s10 offen sc1\n"
    "v_mfma_f32_16x16x32_bf16 v[28:31], a[128:131], v[208:211], v[28:31]\n"
    "v_mfma_f32_16x16x32_bf16 v[28:31], a[132:135], v[232:235], v[28:31]\n"
    "ds_read_b128 a[152:155], v4 offset:12544\n"
    "v_mfma_f32_16x16x32_bf16 v[32:35], a[132:135], v[236:239], 0\n"
    "v_mfma_f32_16x16x32_bf16 v[32:35], a[128:131], v[212:215], v[32:35]\n"
    "ds_write2st64_b32 v38, v52, v53 offset0:0 offset1:2\n"
    "v_mfma_f32_16x16x32_bf16 v[32:35], a[124:127], v[188:191], v[32:35]\n"
    "s_branch 46f\n"
    "306:\n"
    "s_branch 307f\n"
    "65:\n"
    "s_sub_u32 s7, s86, s71\n"
    "s_cmp_eq_u32 s7, 3\n"
    "s_cbranch_scc1 403f\n"
    "v_mfma_f32_16x16x32_bf16 v[32:35], a[144:147], a[68:71], v[32:35]\n"
    "s_add_u32 s7, s70, 1\n"
    "s_add_u32 s65, s65, 2\n"
    "ds_read_b128 a[88:91], v4\n"
    "v_mfma_f32_16x16x32_bf16 v[32:35], a[140:143], a[44:47], v[32:35]\n"
    "v_add_u32 v7, s78, v7\n"
    "v_accvgpr_write_b32 a200, v44\n"
    "v_mfma_f32_16x16x32_bf16 v[32:35], a[136:139], a[20:23], v[32:35]\n"
    "v_add_u32 v14, 1, v14\n"
    "v_accvgpr_write_b32 a201, v45\n"
    "v_mfma_f32_16x16x32_bf16 v[20:23], a[136:139], a[0:3], 0\n"
    "buffer_load_dwordx4 v0, s[36:39], s81 offen nt lds\n"
    "v_mfma_f32_16x16x32_bf16 v[20:23], a[140:143], a[24:27], v[20:23]\n"
    "s_waitcnt lgkmcnt(1)\n"
    "ds_read_b128 a[92:95], v5\n"
    "v_mfma_f32_16x16x32_bf16 v[20:23], a[144:147], a[48:51], v[20:23]\n"
    "ds_read_b128 a[96:99], v6\n"
    "v_accvgpr_mov_b32 a211, a187\n"
    "v_mfma_f32_16x16x32_bf16 v[20:23], a[148:151], a[72:75], v[20:23]\n"
    "v_cvt_pk_bf16_f32 v52, v28, v32\n"
    "v_accvgpr_write_b32 a202, v46\n"
    "v_mfma_f32_16x16x32_bf16 v[20:23], a[152:155], v[64:67], v[20:23]\n"
    "v_cvt_pk_bf16_f32 v53, v29, v33\n"
    "v_accvgpr_write_b32 a203, v47\n"
    "v_mfma_f32_16x16x32_bf16 v[20:23], a[156:159], v[88:91], v[20:23]\n"
    "v_cvt_pk_bf16_f32 v54, v30, v34\n"
    "v_accvgpr_write_b32 a204, v48\n"
    "v_mfma_f32_16x16x32_bf16 v[20:23], a[160:163], v[112:115], v[20:23]\n"
    "v_cvt_pk_bf16_f32 v55, v31, v35\n"
    "v_accvgpr_write_b32 a205, v49\n"
    "v_mfma_f32_16x16x32_bf16 v[20:23], a[164:167], v[136:139], v[20:23]\n"
    "ds_read_b128 a[100:103], v7\n"
    "v_accvgpr_write_b32 a206, v50\n"
    "v_mfma_f32_16x16x32_bf16 v[20:23], a[168:171], v[160:163], v[20:23]\n"
    "s_mov_b64 vcc, s[12:13]\n"
    "v_accvgpr_write_b32 a207, v51\n"
    "v_mfma_f32_16x16x32_bf16 v[20:23], a[172:175], v[184:187], v[20:23]\n"
    "buffer_load_dwordx4 v1, s[36:39], s81 offen offset:1024 nt lds\n"
    "v_mfma_f32_16x16x32_bf16 v[20:23], a[176:179], v[208:211], v[20:23]\n"
    "v_accvgpr_mov_b32 a208, a184\n"
    "v_readlane_b32 s10, v3, s7\n"
    "v_mfma_f32_16x16x32_bf16 v[20:23], a[180:183], v[232:235], v[20:23]\n"
    "ds_read_b128 a[104:107], v4 offset:256\n"
    "v_mfma_f32_16x16x32_bf16 v[24:27], a[180:183], v[236:239], 0\n"
    "v_accvgpr_mov_b32 a209, a185\n"
    "v_mfma_f32_16x16x32_bf16 v[24:27], a[176:179], v[212:215], v[24:27]\n"
    "ds_write2st64_b32 v38, v52, v53 offset0:16 offset1:18\n"
    "v_mfma_f32_16x16x32_bf16 v[24:27], a[172:175], v[188:191], v[24:27]\n"
    "v_accvgpr_mov_b32 a210, a186\n"
    "s_branch 66f\n"
    "307:\n"
    "s_branch 308f\n"
    "403:\n"
    "v_mfma_f32_16x16x32_bf16 v[32:35], a[144:147], a[68:71], v[32:35]\n"
    "s_sub_u32 s89, s89, 36\n"
    "ds_read_b128 a[88:91], v4\n"
    "v_mfma_f32_16x16x32_bf16 v[32:35], a[140:143], a[44:47], v[32:35]\n"
    "v_add_u32 v7, s78, v7\n"
    "v_mfma_f32_16x16x32_bf16 v[32:35], a[136:139], a[20:23], v[32:35]\n"
    "v_add_u32 v14, 1, v14\n"
    "v_mfma_f32_16x16x32_bf16 v[20:23], a[136:139], a[0:3], 0\n"
    "buffer_store_dwordx4 a[196:199], v10, s[40:43], s9 offen sc1\n"
    "v_mfma_f32_16x16x32_bf16 v[20:23], a[140:143], a[24:27], v[20:23]\n"
    "ds_read_b128 a[92:95], v5\n"
    "v_mfma_f32_16x16x32_bf16 v[20:23], a[144:147], a[48:51], v[20:23]\n"
    "ds_read_b128 a[96:99], v6\n"
    "v_mfma_f32_16x16x32_bf16 v[20:23], a[148:151], a[72:75], v[20:23]\n"
    "v_cvt_pk_bf16_f32 v52, v28, v32\n"
    "v_mfma_f32_16x16x32_bf16 v[20:23], a[152:155], v[64:67], v[20:23]\n"
    "v_cvt_pk_bf16_f32 v53, v29, v33\n"
    "v_mfma_f32_16x16x32_bf16 v[20:23], a[156:159], v[88:91], v[20:23]\n"
    "v_cvt_pk_bf16_f32 v54, v30, v34\n"
    "v_mfma_f32_16x16x32_bf16 v[20:23], a[160:163], v[112:115], v[20:23]\n"
    "v_cvt_pk_bf16_f32 v55, v31, v35\n"
    "v_mfma_f32_16x16x32_bf16 v[20:23], a[164:167], v[136:139], v[20:23]\n"
    "ds_read_b128 a[100:103], v7\n"
    "v_mfma_f32_16x16x32_bf16 v[20:23], a[168:171], v[160:163], v[20:23]\n"
    "s_mov_b64 vcc, s[12:13]\n"
    "v_mfma_f32_16x16x32_bf16 v[20:23], a[172:175], v[184:187], v[20:23]\n"
    "buffer_store_dwordx4 a[200:203], v8, s[40:43], s10 offen sc1\n"
    "v_mfma_f32_16x16x32_bf16 v[20:23], a[176:179], v[208:211], v[20:23]\n"
    "v_mfma_f32_16x16x32_bf16 v[20:23], a[180:183], v[232:235], v[20:23]\n"
    "ds_read_b128 a[104:107], v4 offset:256\n"
    "v_mfma_f32_16x16x32_bf16 v[24:27], a[180:183], v[236:239], 0\n"
    "v_mfma_f32_16x16x32_bf16 v[24:27], a[176:179], v[212:215], v[24:27]\n"
    "ds_write2st64_b32 v38, v52, v53 offset0:16 offset1:18\n"
    "v_mfma_f32_16x16x32_bf16 v[24:27], a[172:175], v[188:191], v[24:27]\n"
    "s_branch 66f\n"
    "308:\n"
    "s_waitcnt vmcnt(10)\n"
    "ds_write_b32 v13, v14\n"
    "ds_read_u16 v250, v18\n"
    "ds_read_u16 v252, v18 offset:8\n"
    "ds_read_u16 v254, v235\n"
    "s_waitcnt vmcnt(9)\n"
    "ds_read2st64_b64 a[188:191], v15 offset0:0 offset1:1\n"
    "s_waitcnt vmcnt(8)\n"
    "ds_read2st64_b64 a[192:195], v15 offset0:2 offset1:3\n"
    "s_waitcnt vmcnt(7)\n"
    "ds_read2st64_b64 a[196:199], v15 offset0:4 offset1:5\n"
    "s_waitcnt lgkmcnt(3)\n"
    "v_lshlrev_b64 v[40:41], v16, v[250:251]\n"
    "v_lshlrev_b64 v[42:43], v16, v[252:253]\n"
    "v_lshlrev_b64 v[44:45], v16, v[254:255]\n"
    "s_waitcnt vmcnt(6) lgkmcnt(2)\n"
    "ds_read2st64_b64 a[200:203], v15 offset0:6 offset1:7\n"
    "s_nop 0\n"
    "v_mfma_f32_4x4x4_16b_bf16 v[20:23], v[40:41], a[188:189], 0 cbsz:2 abid:0\n"
    "ds_read_u16 v250, v18 offset:64\n"
    "s_add_u32 m0, s83, 47104\n"
    "v_mfma_f32_4x4x4_16b_bf16 v[24:27], v[40:41], a[190:191], 0 cbsz:2 abid:1\n"
    "buffer_load_dwordx4 v248, s[44:47], s84 offen lds\n"
    "v_add_u32 v8, v8, v240\n"
    "s_waitcnt vmcnt(6) lgkmcnt(3)\n"
    "ds_read2st64_b32 a[204:205], v38 offset0:16 offset1:17\n"
    "v_mfma_f32_4x4x4_16b_bf16 v[28:31], v[40:41], a[192:193], 0 cbsz:2 abid:2\n"
    "ds_read2st64_b32 a[206:207], v38 offset0:18 offset1:19\n"
    "ds_read_u16 v252, v18 offset:72\n"
    "v_cvt_pk_bf16_f32 v52, v20, v24\n"
    "v_cvt_pk_bf16_f32 v53, v21, v25\n"
    "v_accvgpr_write_b32 a0, v52\n"
    "v_accvgpr_write_b32 a4, v53\n"
    "v_mfma_f32_4x4x4_16b_bf16 v[32:35], v[40:41], a[194:195], 0 cbsz:2 abid:3\n"
    "buffer_load_dwordx4 v249, s[44:47], s84 offen offset:1024 lds\n"
    "v_cvt_pk_bf16_f32 v54, v22, v26\n"
    "v_cvt_pk_bf16_f32 v55, v23, v27\n"
    "v_accvgpr_write_b32 a8, v54\n"
    "v_accvgpr_write_b32 a12, v55\n"
    "v_add_u32 v9, v9, v240\n"
    "s_waitcnt vmcnt(6) lgkmcnt(5)\n"
    "ds_read2st64_b32 a[208:209], v38 offset0:20 offset1:21\n"
    "v_mfma_f32_4x4x4_16b_bf16 v[20:23], v[42:43], a[196:197], 0 cbsz:2 abid:0\n"
    "ds_read2st64_b32 a[210:211], v38 offset0:22 offset1:23\n"
    "ds_read_u16 v254, v235 offset:64\n"
    "s_add_u32 m0, s83, 49152\n"
    "s_add_u32 s84, s60, s62\n"
    "v_cvt_pk_bf16_f32 v52, v28, v32\n"
    "v_cvt_pk_bf16_f32 v53, v29, v33\n"
    "v_accvgpr_write_b32 a1, v52\n"
    "v_accvgpr_write_b32 a5, v53\n"
    "v_mfma_f32_4x4x4_16b_bf16 v[24:27], v[42:43], a[198:199], 0 cbsz:2 abid:1\n"
    "buffer_load_dwordx4 v244, s[44:47], s84 offen lds\n"
    "v_cvt_pk_bf16_f32 v54, v30, v34\n"
    "v_cvt_pk_bf16_f32 v55, v31, v35\n"
    "v_accvgpr_write_b32 a9, v54\n"
    "v_accvgpr_write_b32 a13, v55\n"
    "v_add_u32 v10, v10, v241\n"
    "s_waitcnt vmcnt(6) lgkmcnt(6)\n"
    "ds_read2st64_b64 a[188:191], v15 offset0:12 offset1:13\n"
    "v_mfma_f32_4x4x4_16b_bf16 v[28:31], v[42:43], a[200:201], 0 cbsz:2 abid:2\n"
    "v_cvt_pk_bf16_f32 v52, v20, v24\n"
    "v_cvt_pk_bf16_f32 v53, v21, v25\n"
    "v_accvgpr_write_b32 a2, v52\n"
    "v_accvgpr_write_b32 a6, v53\n"
    "v_mfma_f32_4x4x4_16b_bf16 v[32:35], v[42:43], a[202:203], 0 cbsz:2 abid:3\n"
    "buffer_load_dwordx4 v245, s[44:47], s84 offen offset:1024 lds\n"
    "v_cvt_pk_bf16_f32 v54, v22, v26\n"
    "v_cvt_pk_bf16_f32 v55, v23, v27\n"
    "v_accvgpr_write_b32 a10, v54\n"
    "v_accvgpr_write_b32 a14, v55\n"
    "v_lshlrev_b64 v[46:47], v16, v[250:251]\n"
    "s_mov_b32 s12, 0xff00ff\n"
    "s_waitcnt vmcnt(6) lgkmcnt(4)\n"
    "ds_read2st64_b64 a[192:195], v15 offset0:14 offset1:15\n"
    "v_mfma_f32_4x4x4_16b_bf16 v[20:23], v[44:45], a[204:205], 0 cbsz:2 abid:0\n"
    "v_cvt_pk_bf16_f32 v52, v28, v32\n"
    "v_cvt_pk_bf16_f32 v53, v29, v33\n"
    "v_accvgpr_write_b32 a3, v52\n"
    "v_accvgpr_write_b32 a7, v53\n"
    "v_mfma_f32_4x4x4_16b_bf16 v[24:27], v[44:45], a[206:207], 0 cbsz:2 abid:1\n"
    "buffer_load_dwordx4 v246, s[44:47], s84 offen offset:2048 lds\n"
    "v_cvt_pk_bf16_f32 v54, v30, v34\n"
    "v_cvt_pk_bf16_f32 v55, v31, v35\n"
    "v_accvgpr_write_b32 a11, v54\n"
    "v_accvgpr_write_b32 a15, v55\n"
    "buffer_load_dwordx4 v247, s[44:47], s84 offen offset:3072 lds\n"
    "v_lshlrev_b64 v[48:49], v16, v[252:253]\n"
    "s_mov_b32 s13, 0xff00ff\n"
    "s_waitcnt vmcnt(7) lgkmcnt(2)\n"
    "ds_read2st64_b64 a[196:199], v15 offset0:16 offset1:17\n"
    "v_mfma_f32_4x4x4_16b_bf16 v[28:31], v[44:45], a[208:209], 0 cbsz:2 abid:2\n"
    "s_add_u32 m0, s83, 53248\n"
    "v_cvt_pk_bf16_f32 v52, v20, v22\n"
    "v_cvt_pk_bf16_f32 v53, v21, v23\n"
    "v_accvgpr_write_b32 a16, v52\n"
    "v_accvgpr_write_b32 a20, v53\n"
    "v_mfma_f32_4x4x4_16b_bf16 v[32:35], v[44:45], a[210:211], 0 cbsz:2 abid:3\n"
    "buffer_load_dwordx4 v248, s[44:47], s84 offen lds\n"
    "v_cvt_pk_bf16_f32 v54, v24, v26\n"
    "v_cvt_pk_bf16_f32 v55, v25, v27\n"
    "v_accvgpr_write_b32 a17, v54\n"
    "v_accvgpr_write_b32 a21, v55\n"
    "v_lshlrev_b64 v[50:51], v16, v[254:255]\n"
    "s_add_u32 s77, s57, 36864\n"
    "s_waitcnt vmcnt(7) lgkmcnt(2)\n"
    "ds_read2st64_b64 a[200:203], v15 offset0:18 offset1:19\n"
    "v_mfma_f32_4x4x4_16b_bf16 v[20:23], v[46:47], a[188:189], 0 cbsz:2 abid:0\n"
    "ds_read_u16 v250, v18 offset:128\n"
    "v_cvt_pk_bf16_f32 v52, v28, v30\n"
    "v_cvt_pk_bf16_f32 v53, v29, v31\n"
    "v_accvgpr_write_b32 a18, v52\n"
    "v_accvgpr_write_b32 a22, v53\n"
    "v_mfma_f32_4x4x4_16b_bf16 v[24:27], v[46:47], a[190:191], 0 cbsz:2 abid:1\n"
    "buffer_load_dwordx4 v249, s[44:47], s84 offen offset:1024 lds\n"
    "v_cvt_pk_bf16_f32 v54, v32, v34\n"
    "v_cvt_pk_bf16_f32 v55, v33, v35\n"
    "v_accvgpr_write_b32 a19, v54\n"
    "v_accvgpr_write_b32 a23, v55\n"
    "s_movk_i32 s88, 24576\n"
    "s_waitcnt vmcnt(7) lgkmcnt(3)\n"
    "ds_read2st64_b32 a[204:205], v38 offset0:40 offset1:41\n"
    "v_mfma_f32_4x4x4_16b_bf16 v[28:31], v[46:47], a[192:193], 0 cbsz:2 abid:2\n"
    "ds_read2st64_b32 a[206:207], v38 offset0:42 offset1:43\n"
    "ds_read_u16 v252, v18 offset:136\n"
    "s_add_u32 m0, s83, 55296\n"
    "s_add_u32 s84, s61, s62\n"
    "v_cvt_pk_bf16_f32 v52, v20, v24\n"
    "v_cvt_pk_bf16_f32 v53, v21, v25\n"
    "v_accvgpr_write_b32 a24, v52\n"
    "v_accvgpr_write_b32 a28, v53\n"
    "v_mfma_f32_4x4x4_16b_bf16 v[32:35], v[46:47], a[194:195], 0 cbsz:2 abid:3\n"
    "buffer_load_dwordx4 v244, s[44:47], s84 offen lds\n"
    "v_cvt_pk_bf16_f32 v54, v22, v26\n"
    "v_cvt_pk_bf16_f32 v55, v23, v27\n"
    "v_accvgpr_write_b32 a32, v54\n"
    "v_accvgpr_write_b32 a36, v55\n"
    "buffer_load_dwordx4 v245, s[44:47], s84 offen offset:1024 lds\n"
    "s_movk_i32 s78, 24576\n"
    "s_waitcnt vmcnt(8) lgkmcnt(5)\n"
    "ds_read2st64_b32 a[208:209], v38 offset0:44 offset1:45\n"
    "v_mfma_f32_4x4x4_16b_bf16 v[20:23], v[48:49], a[196:197], 0 cbsz:2 abid:0\n"
    "ds_read2st64_b32 a[210:211], v38 offset0:46 offset1:47\n"
    "ds_read_u16 v254, v235 offset:128\n"
    "v_cvt_pk_bf16_f32 v52, v28, v32\n"
    "v_cvt_pk_bf16_f32 v53, v29, v33\n"
    "v_accvgpr_write_b32 a25, v52\n"
    "v_accvgpr_write_b32 a29, v53\n"
    "v_mfma_f32_4x4x4_16b_bf16 v[24:27], v[48:49], a[198:199], 0 cbsz:2 abid:1\n"
    "buffer_load_dwordx4 v246, s[44:47], s84 offen offset:2048 lds\n"
    "v_cvt_pk_bf16_f32 v54, v30, v34\n"
    "v_cvt_pk_bf16_f32 v55, v31, v35\n"
    "v_accvgpr_write_b32 a33, v54\n"
    "v_accvgpr_write_b32 a37, v55\n"
    "s_mov_b32 s87, -122880\n"
    "s_waitcnt vmcnt(8) lgkmcnt(6)\n"
    "ds_read2st64_b64 a[188:191], v15 offset0:24 offset1:25\n"
    "v_mfma_f32_4x4x4_16b_bf16 v[28:31], v[48:49], a[200:201], 0 cbsz:2 abid:2\n"
    "v_cvt_pk_bf16_f32 v52, v20, v24\n"
    "v_cvt_pk_bf16_f32 v53, v21, v25\n"
    "v_accvgpr_write_b32 a26, v52\n"
    "v_accvgpr_write_b32 a30, v53\n"
    "v_mfma_f32_4x4x4_16b_bf16 v[32:35], v[48:49], a[202:203], 0 cbsz:2 abid:3\n"
    "buffer_load_dwordx4 v247, s[44:47], s84 offen offset:3072 lds\n"
    "v_cvt_pk_bf16_f32 v54, v22, v26\n"
    "v_cvt_pk_bf16_f32 v55, v23, v27\n"
    "v_accvgpr_write_b32 a34, v54\n"
    "v_accvgpr_write_b32 a38, v55\n"
    "v_lshlrev_b64 v[40:41], v16, v[250:251]\n"
    "s_add_u32 s80, s57, 147456\n"
    "s_waitcnt vmcnt(8) lgkmcnt(4)\n"
    "ds_read2st64_b64 a[192:195], v15 offset0:26 offset1:27\n"
    "v_mfma_f32_4x4x4_16b_bf16 v[20:23], v[50:51], a[204:205], 0 cbsz:2 abid:0\n"
    "s_add_u32 m0, s83, 59392\n"
    "v_cvt_pk_bf16_f32 v52, v28, v32\n"
    "v_cvt_pk_bf16_f32 v53, v29, v33\n"
    "v_accvgpr_write_b32 a27, v52\n"
    "v_accvgpr_write_b32 a31, v53\n"
    "v_mfma_f32_4x4x4_16b_bf16 v[24:27], v[50:51], a[206:207], 0 cbsz:2 abid:1\n"
    "buffer_load_dwordx4 v248, s[44:47], s84 offen lds\n"
    "v_cvt_pk_bf16_f32 v54, v30, v34\n"
    "v_cvt_pk_bf16_f32 v55, v31, v35\n"
    "v_accvgpr_write_b32 a35, v54\n"
    "v_accvgpr_write_b32 a39, v55\n"
    "v_lshlrev_b64 v[42:43], v16, v[252:253]\n"
    "s_mov_b32 s86, 5\n"
    "s_waitcnt vmcnt(8) lgkmcnt(2)\n"
    "ds_read2st64_b64 a[196:199], v15 offset0:28 offset1:29\n"
    "v_mfma_f32_4x4x4_16b_bf16 v[28:31], v[50:51], a[208:209], 0 cbsz:2 abid:2\n"
    "v_cvt_pk_bf16_f32 v52, v20, v22\n"
    "v_cvt_pk_bf16_f32 v53, v21, v23\n"
    "v_accvgpr_write_b32 a40, v52\n"
    "v_accvgpr_write_b32 a44, v53\n"
    "v_mfma_f32_4x4x4_16b_bf16 v[32:35], v[50:51], a[210:211], 0 cbsz:2 abid:3\n"
    "buffer_load_dwordx4 v249, s[44:47], s84 offen offset:1024 lds\n"
    "s_add_u32 m0, s83, 61440\n"
    "s_add_u32 s84, s58, s63\n"
    "v_cvt_pk_bf16_f32 v54, v24, v26\n"
    "v_cvt_pk_bf16_f32 v55, v25, v27\n"
    "v_accvgpr_write_b32 a41, v54\n"
    "v_accvgpr_write_b32 a45, v55\n"
    "buffer_load_dwordx4 v244, s[44:47], s84 offen lds\n"
    "v_lshlrev_b64 v[44:45], v16, v[254:255]\n"
    "s_lshr_b32 s7, s2, 1\n"
    "s_waitcnt vmcnt(9) lgkmcnt(2)\n"
    "s_barrier\n"
    "ds_read2st64_b64 a[200:203], v15 offset0:30 offset1:31\n"
    "v_mfma_f32_4x4x4_16b_bf16 v[20:23], v[40:41], a[188:189], 0 cbsz:2 abid:0\n"
    "ds_read_u16 v250, v18 offset:192\n"
    "v_cvt_pk_bf16_f32 v52, v28, v30\n"
    "v_cvt_pk_bf16_f32 v53, v29, v31\n"
    "v_accvgpr_write_b32 a42, v52\n"
    "v_accvgpr_write_b32 a46, v53\n"
    "v_mfma_f32_4x4x4_16b_bf16 v[24:27], v[40:41], a[190:191], 0 cbsz:2 abid:1\n"
    "buffer_load_dwordx4 v245, s[44:47], s84 offen offset:1024 lds\n"
    "v_cvt_pk_bf16_f32 v54, v32, v34\n"
    "v_cvt_pk_bf16_f32 v55, v33, v35\n"
    "v_accvgpr_write_b32 a43, v54\n"
    "v_accvgpr_write_b32 a47, v55\n"
    "s_and_b32 s7, s7, 0x7c\n"
    "s_waitcnt vmcnt(9) lgkmcnt(3)\n"
    "ds_read2st64_b32 a[204:205], v38 offset0:64 offset1:65\n"
    "v_mfma_f32_4x4x4_16b_bf16 v[28:31], v[40:41], a[192:193], 0 cbsz:2 abid:2\n"
    "ds_read2st64_b32 a[206:207], v38 offset0:66 offset1:67\n"
    "ds_read_u16 v252, v18 offset:200\n"
    "v_cvt_pk_bf16_f32 v52, v20, v24\n"
    "v_cvt_pk_bf16_f32 v53, v21, v25\n"
    "v_accvgpr_write_b32 a48, v52\n"
    "v_accvgpr_write_b32 a52, v53\n"
    "v_mfma_f32_4x4x4_16b_bf16 v[32:35], v[40:41], a[194:195], 0 cbsz:2 abid:3\n"
    "buffer_load_dwordx4 v246, s[44:47], s84 offen offset:2048 lds\n"
    "v_cvt_pk_bf16_f32 v54, v22, v26\n"
    "v_cvt_pk_bf16_f32 v55, v23, v27\n"
    "v_accvgpr_write_b32 a56, v54\n"
    "v_accvgpr_write_b32 a60, v55\n"
    "s_or_b32 s7, s7, s28\n"
    "s_waitcnt vmcnt(9) lgkmcnt(5)\n"
    "ds_read2st64_b32 a[208:209], v38 offset0:68 offset1:69\n"
    "v_mfma_f32_4x4x4_16b_bf16 v[20:23], v[42:43], a[196:197], 0 cbsz:2 abid:0\n"
    "ds_read2st64_b32 a[210:211], v38 offset0:70 offset1:71\n"
    "ds_read_u16 v254, v235 offset:192\n"
    "v_cvt_pk_bf16_f32 v52, v28, v32\n"
    "v_cvt_pk_bf16_f32 v53, v29, v33\n"
    "v_accvgpr_write_b32 a49, v52\n"
    "v_accvgpr_write_b32 a53, v53\n"
    "v_mfma_f32_4x4x4_16b_bf16 v[24:27], v[42:43], a[198:199], 0 cbsz:2 abid:1\n"
    "buffer_load_dwordx4 v247, s[44:47], s84 offen offset:3072 lds\n"
    "v_cvt_pk_bf16_f32 v54, v30, v34\n"
    "v_cvt_pk_bf16_f32 v55, v31, v35\n"
    "v_accvgpr_write_b32 a57, v54\n"
    "v_accvgpr_write_b32 a61, v55\n"
    "s_mul_i32 s7, s7, 79\n"
    "s_waitcnt vmcnt(9) lgkmcnt(6)\n"
    "ds_read2st64_b64 a[188:191], v15 offset0:36 offset1:37\n"
    "v_mfma_f32_4x4x4_16b_bf16 v[28:31], v[42:43], a[200:201], 0 cbsz:2 abid:2\n"
    "s_add_u32 m0, s83, 65536\n"
    "v_cvt_pk_bf16_f32 v52, v20, v24\n"
    "v_cvt_pk_bf16_f32 v53, v21, v25\n"
    "v_accvgpr_write_b32 a50, v52\n"
    "v_accvgpr_write_b32 a54, v53\n"
    "v_mfma_f32_4x4x4_16b_bf16 v[32:35], v[42:43], a[202:203], 0 cbsz:2 abid:3\n"
    "buffer_load_dwordx4 v248, s[44:47], s84 offen lds\n"
    "v_cvt_pk_bf16_f32 v54, v22, v26\n"
    "v_cvt_pk_bf16_f32 v55, v23, v27\n"
    "v_accvgpr_write_b32 a58, v54\n"
    "v_accvgpr_write_b32 a62, v55\n"
    "buffer_load_dwordx4 v249, s[44:47], s84 offen offset:1024 lds\n"
    "v_lshlrev_b64 v[46:47], v16, v[250:251]\n"
    "s_and_b32 s7, s7, 0x7f\n"
    "s_waitcnt vmcnt(10) lgkmcnt(4)\n"
    "ds_read2st64_b64 a[192:195], v15 offset0:38 offset1:39\n"
    "v_mfma_f32_4x4x4_16b_bf16 v[20:23], v[44:45], a[204:205], 0 cbsz:2 abid:0\n"
    "s_add_u32 m0, s83, 36864\n"
    "s_add_u32 s84, s59, s63\n"
    "v_cvt_pk_bf16_f32 v52, v28, v32\n"
    "v_cvt_pk_bf16_f32 v53, v29, v33\n"
    "v_accvgpr_write_b32 a51, v52\n"
    "v_accvgpr_write_b32 a55, v53\n"
    "v_mfma_f32_4x4x4_16b_bf16 v[24:27], v[44:45], a[206:207], 0 cbsz:2 abid:1\n"
    "buffer_load_dwordx4 v244, s[44:47], s84 offen lds\n"
    "v_cvt_pk_bf16_f32 v54, v30, v34\n"
    "v_cvt_pk_bf16_f32 v55, v31, v35\n"
    "v_accvgpr_write_b32 a59, v54\n"
    "v_accvgpr_write_b32 a63, v55\n"
    "v_lshlrev_b64 v[48:49], v16, v[252:253]\n"
    "s_mul_i32 s7, s7, 50\n"
    "s_waitcnt vmcnt(10) lgkmcnt(2)\n"
    "ds_read2st64_b64 a[196:199], v15 offset0:40 offset1:41\n"
    "v_mfma_f32_4x4x4_16b_bf16 v[28:31], v[44:45], a[208:209], 0 cbsz:2 abid:2\n"
    "v_cvt_pk_bf16_f32 v52, v20, v22\n"
    "v_cvt_pk_bf16_f32 v53, v21, v23\n"
    "v_accvgpr_write_b32 a64, v52\n"
    "v_accvgpr_write_b32 a68, v53\n"
    "v_mfma_f32_4x4x4_16b_bf16 v[32:35], v[44:45], a[210:211], 0 cbsz:2 abid:3\n"
    "buffer_load_dwordx4 v245, s[44:47], s84 offen offset:1024 lds\n"
    "v_cvt_pk_bf16_f32 v54, v24, v26\n"
    "v_cvt_pk_bf16_f32 v55, v25, v27\n"
    "v_accvgpr_write_b32 a65, v54\n"
    "v_accvgpr_write_b32 a69, v55\n"
    "v_lshlrev_b64 v[50:51], v16, v[254:255]\n"
    "s_lshr_b32 s7, s7, 6\n"
    "s_waitcnt vmcnt(10) lgkmcnt(2)\n"
    "ds_read2st64_b64 a[200:203], v15 offset0:42 offset1:43\n"
    "v_mfma_f32_4x4x4_16b_bf16 v[20:23], v[46:47], a[188:189], 0 cbsz:2 abid:0\n"
    "ds_read_u16 v250, v18 offset:256\n"
    "v_cvt_pk_bf16_f32 v52, v28, v30\n"
    "v_cvt_pk_bf16_f32 v53, v29, v31\n"
    "v_accvgpr_write_b32 a66, v52\n"
    "v_accvgpr_write_b32 a70, v53\n"
    "v_mfma_f32_4x4x4_16b_bf16 v[24:27], v[46:47], a[190:191], 0 cbsz:2 abid:1\n"
    "buffer_load_dwordx4 v246, s[44:47], s84 offen offset:2048 lds\n"
    "v_cvt_pk_bf16_f32 v54, v32, v34\n"
    "v_cvt_pk_bf16_f32 v55, v33, v35\n"
    "v_accvgpr_write_b32 a67, v54\n"
    "v_accvgpr_write_b32 a71, v55\n"
    "s_sub_i32 s7, s7, 82\n"
    "s_waitcnt vmcnt(10) lgkmcnt(3)\n"
    "ds_read2st64_b32 a[204:205], v38 offset0:88 offset1:89\n"
    "v_mfma_f32_4x4x4_16b_bf16 v[28:31], v[46:47], a[192:193], 0 cbsz:2 abid:2\n"
    "ds_read2st64_b32 a[206:207], v38 offset0:90 offset1:91\n"
    "ds_read_u16 v252, v18 offset:264\n"
    "v_cvt_pk_bf16_f32 v52, v20, v24\n"
    "v_cvt_pk_bf16_f32 v53, v21, v25\n"
    "v_accvgpr_write_b32 a72, v52\n"
    "v_accvgpr_write_b32 a76, v53\n"
    "v_mfma_f32_4x4x4_16b_bf16 v[32:35], v[46:47], a[194:195], 0 cbsz:2 abid:3\n"
    "buffer_load_dwordx4 v247, s[44:47], s84 offen offset:3072 lds\n"
    "s_add_u32 m0, s83, 40960\n"
    "v_cvt_pk_bf16_f32 v54, v22, v26\n"
    "v_cvt_pk_bf16_f32 v55, v23, v27\n"
    "v_accvgpr_write_b32 a80, v54\n"
    "v_accvgpr_write_b32 a84, v55\n"
    "buffer_load_dwordx4 v248, s[44:47], s84 offen lds\n"
    "s_max_i32 s7, s7, 0\n"
    "s_waitcnt vmcnt(11) lgkmcnt(5)\n"
    "ds_read2st64_b32 a[208:209], v38 offset0:92 offset1:93\n"
    "v_mfma_f32_4x4x4_16b_bf16 v[20:23], v[48:49], a[196:197], 0 cbsz:2 abid:0\n"
    "ds_read2st64_b32 a[210:211], v38 offset0:94 offset1:95\n"
    "ds_read_u16 v254, v235 offset:256\n"
    "v_cvt_pk_bf16_f32 v52, v28, v32\n"
    "v_cvt_pk_bf16_f32 v53, v29, v33\n"
    "v_accvgpr_write_b32 a73, v52\n"
    "v_accvgpr_write_b32 a77, v53\n"
    "v_mfma_f32_4x4x4_16b_bf16 v[24:27], v[48:49], a[198:199], 0 cbsz:2 abid:1\n"
    "buffer_load_dwordx4 v249, s[44:47], s84 offen offset:1024 lds\n"
    "v_cvt_pk_bf16_f32 v54, v30, v34\n"
    "v_cvt_pk_bf16_f32 v55, v31, v35\n"
    "v_accvgpr_write_b32 a81, v54\n"
    "v_accvgpr_write_b32 a85, v55\n"
    "s_min_i32 s7, s7, 50\n"
    "s_waitcnt vmcnt(11) lgkmcnt(6)\n"
    "ds_read2st64_b64 a[188:191], v15 offset0:48 offset1:49\n"
    "v_mfma_f32_4x4x4_16b_bf16 v[28:31], v[48:49], a[200:201], 0 cbsz:2 abid:2\n"
    "s_add_u32 m0, s83, 43008\n"
    "s_add_u32 s84, s60, s63\n"
    "v_cvt_pk_bf16_f32 v52, v20, v24\n"
    "v_cvt_pk_bf16_f32 v53, v21, v25\n"
    "v_accvgpr_write_b32 a74, v52\n"
    "v_accvgpr_write_b32 a78, v53\n"
    "v_mfma_f32_4x4x4_16b_bf16 v[32:35], v[48:49], a[202:203], 0 cbsz:2 abid:3\n"
    "buffer_load_dwordx4 v244, s[44:47], s84 offen lds\n"
    "v_cvt_pk_bf16_f32 v54, v22, v26\n"
    "v_cvt_pk_bf16_f32 v55, v23, v27\n"
    "v_accvgpr_write_b32 a82, v54\n"
    "v_accvgpr_write_b32 a86, v55\n"
    "v_lshlrev_b64 v[40:41], v16, v[250:251]\n"
    "s_bfm_b64 s[34:35], s7, 2\n"
    "s_waitcnt vmcnt(11) lgkmcnt(4)\n"
    "ds_read2st64_b64 a[192:195], v15 offset0:50 offset1:51\n"
    "v_mfma_f32_4x4x4_16b_bf16 v[20:23], v[50:51], a[204:205], 0 cbsz:2 abid:0\n"
    "v_cvt_pk_bf16_f32 v52, v28, v32\n"
    "v_cvt_pk_bf16_f32 v53, v29, v33\n"
    "v_accvgpr_write_b32 a75, v52\n"
    "v_accvgpr_write_b32 a79, v53\n"
    "v_mfma_f32_4x4x4_16b_bf16 v[24:27], v[50:51], a[206:207], 0 cbsz:2 abid:1\n"
    "buffer_load_dwordx4 v245, s[44:47], s84 offen offset:1024 lds\n"
    "v_cvt_pk_bf16_f32 v54, v30, v34\n"
    "v_cvt_pk_bf16_f32 v55, v31, v35\n"
    "v_accvgpr_write_b32 a83, v54\n"
    "v_accvgpr_write_b32 a87, v55\n"
    "v_lshlrev_b64 v[42:43], v16, v[252:253]\n"
    "s_waitcnt vmcnt(11) lgkmcnt(2)\n"
    "ds_read2st64_b64 a[196:199], v15 offset0:52 offset1:53\n"
    "v_mfma_f32_4x4x4_16b_bf16 v[28:31], v[50:51], a[208:209], 0 cbsz:2 abid:2\n"
    "v_cvt_pk_bf16_f32 v56, v20, v22\n"
    "v_cvt_pk_bf16_f32 v60, v21, v23\n"
    "v_mfma_f32_4x4x4_16b_bf16 v[32:35], v[50:51], a[210:211], 0 cbsz:2 abid:3\n"
    "buffer_load_dwordx4 v246, s[44:47], s84 offen offset:2048 lds\n"
    "v_cvt_pk_bf16_f32 v57, v24, v26\n"
    "v_cvt_pk_bf16_f32 v61, v25, v27\n"
    "buffer_load_dwordx4 v247, s[44:47], s84 offen offset:3072 lds\n"
    "v_lshlrev_b64 v[44:45], v16, v[254:255]\n"
    "s_waitcnt vmcnt(12) lgkmcnt(2)\n"
    "ds_read2st64_b64 a[200:203], v15 offset0:54 offset1:55\n"
    "v_mfma_f32_4x4x4_16b_bf16 v[20:23], v[40:41], a[188:189], 0 cbsz:2 abid:0\n"
    "ds_read_u16 v250, v18 offset:320\n"
    "s_add_u32 m0, s83, 47104\n"
    "v_cvt_pk_bf16_f32 v58, v28, v30\n"
    "v_cvt_pk_bf16_f32 v62, v29, v31\n"
    "v_mfma_f32_4x4x4_16b_bf16 v[24:27], v[40:41], a[190:191], 0 cbsz:2 abid:1\n"
    "buffer_load_dwordx4 v248, s[44:47], s84 offen lds\n"
    "v_cvt_pk_bf16_f32 v59, v32, v34\n"
    "v_cvt_pk_bf16_f32 v63, v33, v35\n"
    "s_waitcnt vmcnt(12) lgkmcnt(3)\n"
    "ds_read2st64_b32 a[204:205], v38 offset0:112 offset1:113\n"
    "v_mfma_f32_4x4x4_16b_bf16 v[28:31], v[40:41], a[192:193], 0 cbsz:2 abid:2\n"
    "ds_read2st64_b32 a[206:207], v38 offset0:114 offset1:115\n"
    "ds_read_u16 v252, v18 offset:328\n"
    "v_cvt_pk_bf16_f32 v64, v20, v24\n"
    "v_cvt_pk_bf16_f32 v68, v21, v25\n"
    "v_mfma_f32_4x4x4_16b_bf16 v[32:35], v[40:41], a[194:195], 0 cbsz:2 abid:3\n"
    "buffer_load_dwordx4 v249, s[44:47], s84 offen offset:1024 lds\n"
    "v_cvt_pk_bf16_f32 v72, v22, v26\n"
    "v_cvt_pk_bf16_f32 v76, v23, v27\n"
    "s_waitcnt vmcnt(12) lgkmcnt(5)\n"
    "ds_read2st64_b32 a[208:209], v38 offset0:116 offset1:117\n"
    "v_mfma_f32_4x4x4_16b_bf16 v[20:23], v[42:43], a[196:197], 0 cbsz:2 abid:0\n"
    "ds_read2st64_b32 a[210:211], v38 offset0:118 offset1:119\n"
    "ds_read_u16 v254, v235 offset:320\n"
    "s_add_u32 m0, s83, 49152\n"
    "s_add_u32 s84, s61, s63\n"
    "v_cvt_pk_bf16_f32 v65, v28, v32\n"
    "v_cvt_pk_bf16_f32 v69, v29, v33\n"
    "v_mfma_f32_4x4x4_16b_bf16 v[24:27], v[42:43], a[198:199], 0 cbsz:2 abid:1\n"
    "buffer_load_dwordx4 v244, s[44:47], s84 offen lds\n"
    "v_cvt_pk_bf16_f32 v73, v30, v34\n"
    "v_cvt_pk_bf16_f32 v77, v31, v35\n"
    "buffer_load_dwordx4 v245, s[44:47], s84 offen offset:1024 lds\n"
    "s_waitcnt vmcnt(13) lgkmcnt(6)\n"
    "ds_read2st64_b64 a[188:191], v15 offset0:0 offset1:1\n"
    "v_mfma_f32_4x4x4_16b_bf16 v[28:31], v[42:43], a[200:201], 0 cbsz:2 abid:2\n"
    "v_cvt_pk_bf16_f32 v66, v20, v24\n"
    "v_cvt_pk_bf16_f32 v70, v21, v25\n"
    "v_mfma_f32_4x4x4_16b_bf16 v[32:35], v[42:43], a[202:203], 0 cbsz:2 abid:3\n"
    "buffer_load_dwordx4 v246, s[44:47], s84 offen offset:2048 lds\n"
    "v_cvt_pk_bf16_f32 v74, v22, v26\n"
    "v_cvt_pk_bf16_f32 v78, v23, v27\n"
    "v_lshlrev_b64 v[46:47], v16, v[250:251]\n"
    "s_waitcnt vmcnt(13) lgkmcnt(4)\n"
    "ds_read2st64_b64 a[192:195], v15 offset0:2 offset1:3\n"
    "v_mfma_f32_4x4x4_16b_bf16 v[20:23], v[44:45], a[204:205], 0 cbsz:2 abid:0\n"
    "v_cvt_pk_bf16_f32 v67, v28, v32\n"
    "v_cvt_pk_bf16_f32 v71, v29, v33\n"
    "v_mfma_f32_4x4x4_16b_bf16 v[24:27], v[44:45], a[206:207], 0 cbsz:2 abid:1\n"
    "buffer_load_dwordx4 v247, s[44:47], s84 offen offset:3072 lds\n"
    "v_cvt_pk_bf16_f32 v75, v30, v34\n"
    "v_cvt_pk_bf16_f32 v79, v31, v35\n"
    "v_lshlrev_b64 v[48:49], v16, v[252:253]\n"
    "s_waitcnt vmcnt(13) lgkmcnt(2)\n"
    "ds_read2st64_b64 a[196:199], v15 offset0:4 offset1:5\n"
    "v_mfma_f32_4x4x4_16b_bf16 v[28:31], v[44:45], a[208:209], 0 cbsz:2 abid:2\n"
    "s_add_u32 m0, s83, 53248\n"
    "v_cvt_pk_bf16_f32 v80, v20, v22\n"
    "v_cvt_pk_bf16_f32 v84, v21, v23\n"
    "v_mfma_f32_4x4x4_16b_bf16 v[32:35], v[44:45], a[210:211], 0 cbsz:2 abid:3\n"
    "buffer_load_dwordx4 v248, s[44:47], s84 offen lds\n"
    "v_cvt_pk_bf16_f32 v81, v24, v26\n"
    "v_cvt_pk_bf16_f32 v85, v25, v27\n"
    "v_lshlrev_b64 v[50:51], v16, v[254:255]\n"
    "s_waitcnt vmcnt(13) lgkmcnt(2)\n"
    "ds_read2st64_b64 a[200:203], v15 offset0:6 offset1:7\n"
    "v_mfma_f32_4x4x4_16b_bf16 v[20:23], v[46:47], a[188:189], 0 cbsz:2 abid:0\n"
    "ds_read_u16 v250, v18 offset:384\n"
    "s_memrealtime s[92:93]\n"
    "v_cvt_pk_bf16_f32 v82, v28, v30\n"
    "v_cvt_pk_bf16_f32 v86, v29, v31\n"
    "v_mfma_f32_4x4x4_16b_bf16 v[24:27], v[46:47], a[190:191], 0 cbsz:2 abid:1\n"
    "buffer_load_dwordx4 v249, s[44:47], s84 offen offset:1024 lds\n"
    "s_add_u32 m0, s83, 55296\n"
    "s_add_u32 s84, s58, s64\n"
    "v_cvt_pk_bf16_f32 v83, v32, v34\n"
    "v_cvt_pk_bf16_f32 v87, v33, v35\n"
    "buffer_load_dwordx4 v244, s[44:47], s84 offen lds\n"
    "s_waitcnt vmcnt(14) lgkmcnt(3)\n"
    "ds_read2st64_b32 a[204:205], v38 offset0:16 offset1:17\n"
    "v_mfma_f32_4x4x4_16b_bf16 v[28:31], v[46:47], a[192:193], 0 cbsz:2 abid:2\n"
    "ds_read2st64_b32 a[206:207], v38 offset0:18 offset1:19\n"
    "ds_read_u16 v252, v18 offset:392\n"
    "v_cvt_pk_bf16_f32 v88, v20, v24\n"
    "v_cvt_pk_bf16_f32 v92, v21, v25\n"
    "v_mfma_f32_4x4x4_16b_bf16 v[32:35], v[46:47], a[194:195], 0 cbsz:2 abid:3\n"
    "buffer_load_dwordx4 v245, s[44:47], s84 offen offset:1024 lds\n"
    "v_cvt_pk_bf16_f32 v96, v22, v26\n"
    "v_cvt_pk_bf16_f32 v100, v23, v27\n"
    "s_waitcnt vmcnt(14) lgkmcnt(5)\n"
    "ds_read2st64_b32 a[208:209], v38 offset0:20 offset1:21\n"
    "v_mfma_f32_4x4x4_16b_bf16 v[20:23], v[48:49], a[196:197], 0 cbsz:2 abid:0\n"
    "ds_read2st64_b32 a[210:211], v38 offset0:22 offset1:23\n"
    "ds_read_u16 v254, v235 offset:384\n"
    "global_load_dwordx4 v[236:239], v16, s[54:55]\n"
    "v_cvt_pk_bf16_f32 v89, v28, v32\n"
    "v_cvt_pk_bf16_f32 v93, v29, v33\n"
    "v_mfma_f32_4x4x4_16b_bf16 v[24:27], v[48:49], a[198:199], 0 cbsz:2 abid:1\n"
    "buffer_load_dwordx4 v246, s[44:47], s84 offen offset:2048 lds\n"
    "v_cvt_pk_bf16_f32 v97, v30, v34\n"
    "v_cvt_pk_bf16_f32 v101, v31, v35\n"
    "s_waitcnt vmcnt(15) lgkmcnt(6)\n"
    "ds_read2st64_b64 a[188:191], v15 offset0:12 offset1:13\n"
    "v_mfma_f32_4x4x4_16b_bf16 v[28:31], v[48:49], a[200:201], 0 cbsz:2 abid:2\n"
    "v_cvt_pk_bf16_f32 v90, v20, v24\n"
    "v_cvt_pk_bf16_f32 v94, v21, v25\n"
    "v_mfma_f32_4x4x4_16b_bf16 v[32:35], v[48:49], a[202:203], 0 cbsz:2 abid:3\n"
    "buffer_load_dwordx4 v247, s[44:47], s84 offen offset:3072 lds\n"
    "v_cvt_pk_bf16_f32 v98, v22, v26\n"
    "v_cvt_pk_bf16_f32 v102, v23, v27\n"
    "v_lshlrev_b64 v[40:41], v16, v[250:251]\n"
    "s_waitcnt vmcnt(15) lgkmcnt(4)\n"
    "ds_read2st64_b64 a[192:195], v15 offset0:14 offset1:15\n"
    "v_mfma_f32_4x4x4_16b_bf16 v[20:23], v[50:51], a[204:205], 0 cbsz:2 abid:0\n"
    "s_add_u32 m0, s83, 59392\n"
    "v_cvt_pk_bf16_f32 v91, v28, v32\n"
    "v_cvt_pk_bf16_f32 v95, v29, v33\n"
    "v_mfma_f32_4x4x4_16b_bf16 v[24:27], v[50:51], a[206:207], 0 cbsz:2 abid:1\n"
    "buffer_load_dwordx4 v248, s[44:47], s84 offen lds\n"
    "v_cvt_pk_bf16_f32 v99, v30, v34\n"
    "v_cvt_pk_bf16_f32 v103, v31, v35\n"
    "buffer_load_dwordx4 v249, s[44:47], s84 offen offset:1024 lds\n"
    "v_lshlrev_b64 v[42:43], v16, v[252:253]\n"
    "s_waitcnt vmcnt(16) lgkmcnt(2)\n"
    "ds_read2st64_b64 a[196:199], v15 offset0:16 offset1:17\n"
    "v_mfma_f32_4x4x4_16b_bf16 v[28:31], v[50:51], a[208:209], 0 cbsz:2 abid:2\n"
    "s_add_u32 m0, s83, 61440\n"
    "s_add_u32 s84, s59, s64\n"
    "v_cvt_pk_bf16_f32 v104, v20, v22\n"
    "v_cvt_pk_bf16_f32 v108, v21, v23\n"
    "v_mfma_f32_4x4x4_16b_bf16 v[32:35], v[50:51], a[210:211], 0 cbsz:2 abid:3\n"
    "buffer_load_dwordx4 v244, s[44:47], s84 offen lds\n"
    "v_cvt_pk_bf16_f32 v105, v24, v26\n"
    "v_cvt_pk_bf16_f32 v109, v25, v27\n"
    "v_lshlrev_b64 v[44:45], v16, v[254:255]\n"
    "s_waitcnt vmcnt(16) lgkmcnt(2)\n"
    "ds_read2st64_b64 a[200:203], v15 offset0:18 offset1:19\n"
    "v_mfma_f32_4x4x4_16b_bf16 v[20:23], v[40:41], a[188:189], 0 cbsz:2 abid:0\n"
    "ds_read_u16 v250, v18 offset:448\n"
    "v_cvt_pk_bf16_f32 v106, v28, v30\n"
    "v_cvt_pk_bf16_f32 v110, v29, v31\n"
    "v_mfma_f32_4x4x4_16b_bf16 v[24:27], v[40:41], a[190:191], 0 cbsz:2 abid:1\n"
    "buffer_load_dwordx4 v245, s[44:47], s84 offen offset:1024 lds\n"
    "v_cvt_pk_bf16_f32 v107, v32, v34\n"
    "v_cvt_pk_bf16_f32 v111, v33, v35\n"
    "s_waitcnt vmcnt(16) lgkmcnt(3)\n"
    "ds_read2st64_b32 a[204:205], v38 offset0:40 offset1:41\n"
    "v_mfma_f32_4x4x4_16b_bf16 v[28:31], v[40:41], a[192:193], 0 cbsz:2 abid:2\n"
    "ds_read2st64_b32 a[206:207], v38 offset0:42 offset1:43\n"
    "ds_read_u16 v252, v18 offset:456\n"
    "v_cvt_pk_bf16_f32 v112, v20, v24\n"
    "v_cvt_pk_bf16_f32 v116, v21, v25\n"
    "v_mfma_f32_4x4x4_16b_bf16 v[32:35], v[40:41], a[194:195], 0 cbsz:2 abid:3\n"
    "buffer_load_dwordx4 v246, s[44:47], s84 offen offset:2048 lds\n"
    "v_cvt_pk_bf16_f32 v120, v22, v26\n"
    "v_cvt_pk_bf16_f32 v124, v23, v27\n"
    "s_waitcnt vmcnt(16) lgkmcnt(5)\n"
    "ds_read2st64_b32 a[208:209], v38 offset0:44 offset1:45\n"
    "v_mfma_f32_4x4x4_16b_bf16 v[20:23], v[42:43], a[196:197], 0 cbsz:2 abid:0\n"
    "ds_read2st64_b32 a[210:211], v38 offset0:46 offset1:47\n"
    "ds_read_u16 v254, v235 offset:448\n"
    "v_cvt_pk_bf16_f32 v113, v28, v32\n"
    "v_cvt_pk_bf16_f32 v117, v29, v33\n"
    "v_mfma_f32_4x4x4_16b_bf16 v[24:27], v[42:43], a[198:199], 0 cbsz:2 abid:1\n"
    "buffer_load_dwordx4 v247, s[44:47], s84 offen offset:3072 lds\n"
    "s_add_u32 m0, s83, 65536\n"
    "v_cvt_pk_bf16_f32 v121, v30, v34\n"
    "v_cvt_pk_bf16_f32 v125, v31, v35\n"
    "buffer_load_dwordx4 v248, s[44:47], s84 offen lds\n"
    "s_waitcnt vmcnt(17) lgkmcnt(6)\n"
    "ds_read2st64_b64 a[188:191], v15 offset0:24 offset1:25\n"
    "v_mfma_f32_4x4x4_16b_bf16 v[28:31], v[42:43], a[200:201], 0 cbsz:2 abid:2\n"
    "v_cvt_pk_bf16_f32 v114, v20, v24\n"
    "v_cvt_pk_bf16_f32 v118, v21, v25\n"
    "v_mfma_f32_4x4x4_16b_bf16 v[32:35], v[42:43], a[202:203], 0 cbsz:2 abid:3\n"
    "buffer_load_dwordx4 v249, s[44:47], s84 offen offset:1024 lds\n"
    "v_cvt_pk_bf16_f32 v122, v22, v26\n"
    "v_cvt_pk_bf16_f32 v126, v23, v27\n"
    "v_lshlrev_b64 v[46:47], v16, v[250:251]\n"
    "s_waitcnt vmcnt(17) lgkmcnt(4)\n"
    "ds_read2st64_b64 a[192:195], v15 offset0:26 offset1:27\n"
    "v_mfma_f32_4x4x4_16b_bf16 v[20:23], v[44:45], a[204:205], 0 cbsz:2 abid:0\n"
    "s_add_u32 m0, s83, 36864\n"
    "s_add_u32 s84, s60, s64\n"
    "v_cvt_pk_bf16_f32 v115, v28, v32\n"
    "v_cvt_pk_bf16_f32 v119, v29, v33\n"
    "v_mfma_f32_4x4x4_16b_bf16 v[24:27], v[44:45], a[206:207], 0 cbsz:2 abid:1\n"
    "buffer_load_dwordx4 v244, s[44:47], s84 offen lds\n"
    "v_cvt_pk_bf16_f32 v123, v30, v34\n"
    "v_cvt_pk_bf16_f32 v127, v31, v35\n"
    "v_lshlrev_b64 v[48:49], v16, v[252:253]\n"
    "s_waitcnt vmcnt(17) lgkmcnt(2)\n"
    "ds_read2st64_b64 a[196:199], v15 offset0:28 offset1:29\n"
    "v_mfma_f32_4x4x4_16b_bf16 v[28:31], v[44:45], a[208:209], 0 cbsz:2 abid:2\n"
    "v_cvt_pk_bf16_f32 v128, v20, v22\n"
    "v_cvt_pk_bf16_f32 v132, v21, v23\n"
    "v_mfma_f32_4x4x4_16b_bf16 v[32:35], v[44:45], a[210:211], 0 cbsz:2 abid:3\n"
    "buffer_load_dwordx4 v245, s[44:47], s84 offen offset:1024 lds\n"
    "v_cvt_pk_bf16_f32 v129, v24, v26\n"
    "v_cvt_pk_bf16_f32 v133, v25, v27\n"
    "v_lshlrev_b64 v[50:51], v16, v[254:255]\n"
    "s_waitcnt vmcnt(12)\n"
    "v_sub_u32 v11, s0, v236\n"
    "v_or_b32 v236, 0xffff, v236\n"
    "v_or_b32 v238, 0xffff, v238\n"
    "s_waitcnt vmcnt(17) lgkmcnt(2)\n"
    "ds_read2st64_b64 a[200:203], v15 offset0:30 offset1:31\n"
    "v_mfma_f32_4x4x4_16b_bf16 v[20:23], v[46:47], a[188:189], 0 cbsz:2 abid:0\n"
    "ds_read_u16 v250, v18 offset:512\n"
    "v_cvt_pk_bf16_f32 v130, v28, v30\n"
    "v_cvt_pk_bf16_f32 v134, v29, v31\n"
    "v_mfma_f32_4x4x4_16b_bf16 v[24:27], v[46:47], a[190:191], 0 cbsz:2 abid:1\n"
    "buffer_load_dwordx4 v246, s[44:47], s84 offen offset:2048 lds\n"
    "v_cvt_pk_bf16_f32 v131, v32, v34\n"
    "v_cvt_pk_bf16_f32 v135, v33, v35\n"
    "buffer_load_dwordx4 v247, s[44:47], s84 offen offset:3072 lds\n"
    "v_and_b32 v242, 15, v237\n"
    "v_and_b32 v243, 1, v239\n"
    "v_or_b32 v237, 15, v237\n"
    "v_or_b32 v239, 15, v239\n"
    "s_waitcnt vmcnt(18) lgkmcnt(3)\n"
    "ds_read2st64_b32 a[204:205], v38 offset0:64 offset1:65\n"
    "v_mfma_f32_4x4x4_16b_bf16 v[28:31], v[46:47], a[192:193], 0 cbsz:2 abid:2\n"
    "ds_read2st64_b32 a[206:207], v38 offset0:66 offset1:67\n"
    "ds_read_u16 v252, v18 offset:520\n"
    "s_add_u32 m0, s83, 40960\n"
    "v_cvt_pk_bf16_f32 v136, v20, v24\n"
    "v_cvt_pk_bf16_f32 v140, v21, v25\n"
    "v_mfma_f32_4x4x4_16b_bf16 v[32:35], v[46:47], a[194:195], 0 cbsz:2 abid:3\n"
    "buffer_load_dwordx4 v248, s[44:47], s84 offen lds\n"
    "v_cvt_pk_bf16_f32 v144, v22, v26\n"
    "v_cvt_pk_bf16_f32 v148, v23, v27\n"
    "v_cmp_ne_u64 vcc, s[18:19], v[236:237]\n"
    "v_cmp_ne_u64 s[48:49], s[18:19], v[238:239]\n"
    "v_readlane_b32 s9, v242, 0\n"
    "v_and_b32 v236, 1, v242\n"
    "s_waitcnt vmcnt(18) lgkmcnt(5)\n"
    "ds_read2st64_b32 a[208:209], v38 offset0:68 offset1:69\n"
    "v_mfma_f32_4x4x4_16b_bf16 v[20:23], v[48:49], a[196:197], 0 cbsz:2 abid:0\n"
    "ds_read2st64_b32 a[210:211], v38 offset0:70 offset1:71\n"
    "ds_read_u16 v254, v235 offset:512\n"
    "v_cvt_pk_bf16_f32 v137, v28, v32\n"
    "v_cvt_pk_bf16_f32 v141, v29, v33\n"
    "v_mfma_f32_4x4x4_16b_bf16 v[24:27], v[48:49], a[198:199], 0 cbsz:2 abid:1\n"
    "buffer_load_dwordx4 v249, s[44:47], s84 offen offset:1024 lds\n"
    "v_cvt_pk_bf16_f32 v145, v30, v34\n"
    "v_cvt_pk_bf16_f32 v149, v31, v35\n"
    "s_or_b64 s[48:49], s[48:49], vcc\n"
    "s_cbranch_scc1 75f\n"
    "77:\n"
    "v_cmp_eq_u32 s[24:25], 1, v236\n"
    "s_waitcnt vmcnt(18) lgkmcnt(6)\n"
    "ds_read2st64_b64 a[188:191], v15 offset0:36 offset1:37\n"
    "v_mfma_f32_4x4x4_16b_bf16 v[28:31], v[48:49], a[200:201], 0 cbsz:2 abid:2\n"
    "s_add_u32 m0, s83, 43008\n"
    "s_add_u32 s84, s61, s64\n"
    "v_cvt_pk_bf16_f32 v138, v20, v24\n"
    "v_cvt_pk_bf16_f32 v142, v21, v25\n"
    "v_mfma_f32_4x4x4_16b_bf16 v[32:35], v[48:49], a[202:203], 0 cbsz:2 abid:3\n"
    "buffer_load_dwordx4 v244, s[44:47], s84 offen lds\n"
    "v_cvt_pk_bf16_f32 v146, v22, v26\n"
    "v_cvt_pk_bf16_f32 v150, v23, v27\n"
    "buffer_load_dwordx4 v245, s[44:47], s84 offen offset:1024 lds\n"
    "v_lshlrev_b64 v[40:41], v16, v[250:251]\n"
    "s_lshr_b32 s9, s9, 1\n"
    "s_add_u32 s9, s9, s28\n"
    "v_lshrrev_b64 v[240:241], v232, s[24:25]\n"
    "v_and_b32 v241, 0xff, v240\n"
    "s_waitcnt vmcnt(19) lgkmcnt(4)\n"
    "ds_read2st64_b64 a[192:195], v15 offset0:38 offset1:39\n"
    "v_mfma_f32_4x4x4_16b_bf16 v[20:23], v[50:51], a[204:205], 0 cbsz:2 abid:0\n"
    "v_cvt_pk_bf16_f32 v139, v28, v32\n"
    "v_cvt_pk_bf16_f32 v143, v29, v33\n"
    "v_mfma_f32_4x4x4_16b_bf16 v[24:27], v[50:51], a[206:207], 0 cbsz:2 abid:1\n"
    "buffer_load_dwordx4 v246, s[44:47], s84 offen offset:2048 lds\n"
    "v_cvt_pk_bf16_f32 v147, v30, v34\n"
    "v_cvt_pk_bf16_f32 v151, v31, v35\n"
    "v_lshlrev_b64 v[42:43], v16, v[252:253]\n"
    "v_bfe_u32 v240, v240, v233, 4\n"
    "v_bcnt_u32_b32 v241, v241, 8\n"
    "v_bcnt_u32_b32 v240, v240, 0\n"
    "v_lshl_add_u32 v240, v240, 1, v241\n"
    "s_waitcnt vmcnt(18) lgkmcnt(2)\n"
    "ds_read2st64_b64 a[196:199], v15 offset0:40 offset1:41\n"
    "v_mfma_f32_4x4x4_16b_bf16 v[28:31], v[50:51], a[208:209], 0 cbsz:2 abid:2\n"
    "v_cvt_pk_bf16_f32 v152, v20, v22\n"
    "v_cvt_pk_bf16_f32 v156, v21, v23\n"
    "v_mfma_f32_4x4x4_16b_bf16 v[32:35], v[50:51], a[210:211], 0 cbsz:2 abid:3\n"
    "buffer_load_dwordx4 v247, s[44:47], s84 offen offset:3072 lds\n"
    "v_cvt_pk_bf16_f32 v153, v24, v26\n"
    "v_cvt_pk_bf16_f32 v157, v25, v27\n"
    "v_lshlrev_b64 v[44:45], v16, v[254:255]\n"
    "s_lshr_b32 s10, s9, 2\n"
    "v_cmp_lt_u32 vcc, 15, v240\n"
    "v_xor_b32 v234, s10, v234\n"
    "s_mov_b64 s[50:51], vcc\n"
    "s_waitcnt vmcnt(18) lgkmcnt(2)\n"
    "ds_read2st64_b64 a[200:203], v15 offset0:42 offset1:43\n"
    "v_mfma_f32_4x4x4_16b_bf16 v[20:23], v[40:41], a[188:189], 0 cbsz:2 abid:0\n"
    "ds_read_u16 v250, v18 offset:576\n"
    "s_add_u32 m0, s83, 47104\n"
    "v_cvt_pk_bf16_f32 v154, v28, v30\n"
    "v_cvt_pk_bf16_f32 v158, v29, v31\n"
    "v_mfma_f32_4x4x4_16b_bf16 v[24:27], v[40:41], a[190:191], 0 cbsz:2 abid:1\n"
    "buffer_load_dwordx4 v248, s[44:47], s84 offen lds\n"
    "v_cvt_pk_bf16_f32 v155, v32, v34\n"
    "v_cvt_pk_bf16_f32 v159, v33, v35\n"
    "v_addc_co_u32 v234, vcc, 0, v234, vcc\n"
    "v_and_b32 v234, 1, v234\n"
    "v_mad_u32_u24 v236, v234, s88, v3\n"
    "v_cmp_eq_u32 s[26:27], 1, v243\n"
    "s_waitcnt vmcnt(18) lgkmcnt(3)\n"
    "ds_read2st64_b32 a[204:205], v38 offset0:88 offset1:89\n"
    "v_mfma_f32_4x4x4_16b_bf16 v[28:31], v[40:41], a[192:193], 0 cbsz:2 abid:2\n"
    "ds_read2st64_b32 a[206:207], v38 offset0:90 offset1:91\n"
    "ds_read_u16 v252, v18 offset:584\n"
    "v_cvt_pk_bf16_f32 v160, v20, v24\n"
    "v_cvt_pk_bf16_f32 v164, v21, v25\n"
    "v_mfma_f32_4x4x4_16b_bf16 v[32:35], v[40:41], a[194:195], 0 cbsz:2 abid:3\n"
    "buffer_load_dwordx4 v249, s[44:47], s84 offen offset:1024 lds\n"
    "v_cvt_pk_bf16_f32 v168, v22, v26\n"
    "v_cvt_pk_bf16_f32 v172, v23, v27\n"
    "v_readlane_b32 s82, v236, 0\n"
    "v_readlane_b32 s73, v236, 1\n"
    "s_waitcnt vmcnt(18) lgkmcnt(5)\n"
    "ds_read2st64_b32 a[208:209], v38 offset0:92 offset1:93\n"
    "v_mfma_f32_4x4x4_16b_bf16 v[20:23], v[42:43], a[196:197], 0 cbsz:2 abid:0\n"
    "ds_read2st64_b32 a[210:211], v38 offset0:94 offset1:95\n"
    "ds_read_u16 v254, v235 offset:576\n"
    "s_barrier\n"
    "s_mov_b32 m0, s57\n"
    "v_cvt_pk_bf16_f32 v161, v28, v32\n"
    "v_cvt_pk_bf16_f32 v165, v29, v33\n"
    "v_mfma_f32_4x4x4_16b_bf16 v[24:27], v[42:43], a[198:199], 0 cbsz:2 abid:1\n"
    "buffer_load_dwordx4 v0, s[36:39], s82 offen nt lds\n"
    "v_cvt_pk_bf16_f32 v169, v30, v34\n"
    "v_cvt_pk_bf16_f32 v173, v31, v35\n"
    "s_waitcnt vmcnt(18) lgkmcnt(6)\n"
    "ds_read2st64_b64 a[188:191], v15 offset0:48 offset1:49\n"
    "v_mfma_f32_4x4x4_16b_bf16 v[28:31], v[42:43], a[200:201], 0 cbsz:2 abid:2\n"
    "v_cvt_pk_bf16_f32 v162, v20, v24\n"
    "v_cvt_pk_bf16_f32 v166, v21, v25\n"
    "v_mfma_f32_4x4x4_16b_bf16 v[32:35], v[42:43], a[202:203], 0 cbsz:2 abid:3\n"
    "buffer_load_dwordx4 v1, s[36:39], s82 offen offset:1024 nt lds\n"
    "v_cvt_pk_bf16_f32 v170, v22, v26\n"
    "v_cvt_pk_bf16_f32 v174, v23, v27\n"
    "v_lshlrev_b64 v[46:47], v16, v[250:251]\n"
    "v_cmp_gt_u32 s[48:49], s96, v232\n"
    "v_lshrrev_b64 v[240:241], v232, s[26:27]\n"
    "v_and_b32 v241, 0xff, v240\n"
    "v_bfe_u32 v240, v240, v233, 4\n"
    "s_waitcnt vmcnt(18) lgkmcnt(4)\n"
    "ds_read2st64_b64 a[192:195], v15 offset0:50 offset1:51\n"
    "v_mfma_f32_4x4x4_16b_bf16 v[20:23], v[44:45], a[204:205], 0 cbsz:2 abid:0\n"
    "v_cvt_pk_bf16_f32 v163, v28, v32\n"
    "v_cvt_pk_bf16_f32 v167, v29, v33\n"
    "v_mfma_f32_4x4x4_16b_bf16 v[24:27], v[44:45], a[206:207], 0 cbsz:2 abid:1\n"
    "buffer_load_dwordx4 v2, s[36:39], s82 offen offset:2048 nt lds\n"
    "v_cvt_pk_bf16_f32 v171, v30, v34\n"
    "v_cvt_pk_bf16_f32 v175, v31, v35\n"
    "v_lshlrev_b64 v[48:49], v16, v[252:253]\n"
    "v_bcnt_u32_b32 v241, v241, 8\n"
    "v_bcnt_u32_b32 v240, v240, 0\n"
    "v_lshl_add_u32 v240, v240, 1, v241\n"
    "v_cmp_lt_u32 s[26:27], 15, v240\n"
    "s_waitcnt vmcnt(18) lgkmcnt(2)\n"
    "ds_read2st64_b64 a[196:199], v15 offset0:52 offset1:53\n"
    "v_mfma_f32_4x4x4_16b_bf16 v[28:31], v[44:45], a[208:209], 0 cbsz:2 abid:2\n"
    "v_cvt_pk_bf16_f32 v176, v20, v22\n"
    "v_cvt_pk_bf16_f32 v180, v21, v23\n"
    "v_mfma_f32_4x4x4_16b_bf16 v[32:35], v[44:45], a[210:211], 0 cbsz:2 abid:3\n"
    "v_cvt_pk_bf16_f32 v177, v24, v26\n"
    "v_cvt_pk_bf16_f32 v181, v25, v27\n"
    "v_lshlrev_b64 v[50:51], v16, v[254:255]\n"
    "s_bfm_b64 s[24:25], s52, 0\n"
    "s_bitcmp1_b32 s9, 0\n"
    "s_cselect_b32 s95, -1, 1\n"
    "s_waitcnt vmcnt(17) lgkmcnt(2)\n"
    "ds_read2st64_b64 a[200:203], v15 offset0:54 offset1:55\n"
    "v_mfma_f32_4x4x4_16b_bf16 v[20:23], v[46:47], a[188:189], 0 cbsz:2 abid:0\n"
    "ds_read_u16 v250, v18 offset:640\n"
    "v_cvt_pk_bf16_f32 v178, v28, v30\n"
    "v_cvt_pk_bf16_f32 v182, v29, v31\n"
    "v_mfma_f32_4x4x4_16b_bf16 v[24:27], v[46:47], a[190:191], 0 cbsz:2 abid:1\n"
    "v_cvt_pk_bf16_f32 v179, v32, v34\n"
    "v_cvt_pk_bf16_f32 v183, v33, v35\n"
    "s_cmp_lt_u32 s52, 12\n"
    "s_cselect_b32 s95, 0, s95\n"
    "s_add_u32 s52, s52, s95\n"
    "s_xor_b64 s[26:27], s[26:27], s[50:51]\n"
    "s_waitcnt vmcnt(16) lgkmcnt(3)\n"
    "ds_read2st64_b32 a[204:205], v38 offset0:112 offset1:113\n"
    "v_mfma_f32_4x4x4_16b_bf16 v[28:31], v[46:47], a[192:193], 0 cbsz:2 abid:2\n"
    "ds_read2st64_b32 a[206:207], v38 offset0:114 offset1:115\n"
    "ds_read_u16 v252, v18 offset:648\n"
    "v_cvt_pk_bf16_f32 v184, v20, v24\n"
    "v_cvt_pk_bf16_f32 v188, v21, v25\n"
    "v_mfma_f32_4x4x4_16b_bf16 v[32:35], v[46:47], a[194:195], 0 cbsz:2 abid:3\n"
    "v_cvt_pk_bf16_f32 v192, v22, v26\n"
    "v_cvt_pk_bf16_f32 v196, v23, v27\n"
    "s_and_b64 s[48:49], s[48:49], s[24:25]\n"
    "s_and_b64 s[24:25], s[24:25], s[26:27]\n"
    "s_andn2_b64 s[48:49], s[48:49], s[26:27]\n"
    "s_bcnt1_i32_b64 s10, s[48:49]\n"
    "s_waitcnt vmcnt(15) lgkmcnt(5)\n"
    "ds_read2st64_b32 a[208:209], v38 offset0:116 offset1:117\n"
    "v_mfma_f32_4x4x4_16b_bf16 v[20:23], v[48:49], a[196:197], 0 cbsz:2 abid:0\n"
    "ds_read2st64_b32 a[210:211], v38 offset0:118 offset1:119\n"
    "ds_read_u16 v254, v235 offset:640\n"
    "v_cvt_pk_bf16_f32 v185, v28, v32\n"
    "v_cvt_pk_bf16_f32 v189, v29, v33\n"
    "v_mfma_f32_4x4x4_16b_bf16 v[24:27], v[48:49], a[198:199], 0 cbsz:2 abid:1\n"
    "v_cvt_pk_bf16_f32 v193, v30, v34\n"
    "v_cvt_pk_bf16_f32 v197, v31, v35\n"
    "s_cmp_lt_u32 s10, 4\n"
    "s_cselect_b64 s[34:35], s[34:35], 0\n"
    "s_cselect_b32 s96, 1, 0\n"
    "s_and_b64 vcc, s[34:35], s[26:27]\n"
    "s_waitcnt vmcnt(14) lgkmcnt(6)\n"
    "ds_read2st64_b64 a[188:191], v15 offset0:0 offset1:1\n"
    "v_mfma_f32_4x4x4_16b_bf16 v[28:31], v[48:49], a[200:201], 0 cbsz:2 abid:2\n"
    "v_cvt_pk_bf16_f32 v186, v20, v24\n"
    "v_cvt_pk_bf16_f32 v190, v21, v25\n"
    "v_mfma_f32_4x4x4_16b_bf16 v[32:35], v[48:49], a[202:203], 0 cbsz:2 abid:3\n"
    "v_cvt_pk_bf16_f32 v194, v22, v26\n"
    "v_cvt_pk_bf16_f32 v198, v23, v27\n"
    "v_lshlrev_b64 v[40:41], v16, v[250:251]\n"
    "v_addc_co_u32 v234, vcc, 0, v234, vcc\n"
    "v_cmp_le_u32 vcc, s52, v19\n"
    "v_subrev_u32 v240, 15, v240\n"
    "v_cmp_gt_u32 s[48:49], 3, v240\n"
    "s_waitcnt vmcnt(13) lgkmcnt(4)\n"
    "ds_read2st64_b64 a[192:195], v15 offset0:2 offset1:3\n"
    "v_mfma_f32_4x4x4_16b_bf16 v[20:23], v[50:51], a[204:205], 0 cbsz:2 abid:0\n"
    "v_cvt_pk_bf16_f32 v187, v28, v32\n"
    "v_cvt_pk_bf16_f32 v191, v29, v33\n"
    "v_mfma_f32_4x4x4_16b_bf16 v[24:27], v[50:51], a[206:207], 0 cbsz:2 abid:1\n"
    "v_cvt_pk_bf16_f32 v195, v30, v34\n"
    "v_cvt_pk_bf16_f32 v199, v31, v35\n"
    "v_lshlrev_b64 v[42:43], v16, v[252:253]\n"
    "s_or_b64 s[26:27], s[26:27], s[48:49]\n"
    "v_and_b32 v234, 1, v234\n"
    "v_mov_b32 v236, s33\n"
    "v_mad_u32_u24 v3, v234, s88, v3\n"
    "s_waitcnt vmcnt(12) lgkmcnt(2)\n"
    "ds_read2st64_b64 a[196:199], v15 offset0:4 offset1:5\n"
    "v_mfma_f32_4x4x4_16b_bf16 v[28:31], v[50:51], a[208:209], 0 cbsz:2 abid:2\n"
    "v_cvt_pk_bf16_f32 v200, v20, v22\n"
    "v_cvt_pk_bf16_f32 v204, v21, v23\n"
    "v_mfma_f32_4x4x4_16b_bf16 v[32:35], v[50:51], a[210:211], 0 cbsz:2 abid:3\n"
    "v_cvt_pk_bf16_f32 v201, v24, v26\n"
    "v_cvt_pk_bf16_f32 v205, v25, v27\n"
    "v_lshlrev_b64 v[44:45], v16, v[254:255]\n"
    "v_cndmask_b32 v3, v3, v236, vcc\n"
    "s_nop 0\n"
    "v_readlane_b32 s74, v3, 2\n"
    "v_readlane_b32 s75, v3, 3\n"
    "s_waitcnt vmcnt(11) lgkmcnt(2)\n"
    "ds_read2st64_b64 a[200:203], v15 offset0:6 offset1:7\n"
    "v_mfma_f32_4x4x4_16b_bf16 v[20:23], v[40:41], a[188:189], 0 cbsz:2 abid:0\n"
    "ds_read_u16 v250, v18 offset:704\n"
    "v_cvt_pk_bf16_f32 v202, v28, v30\n"
    "v_cvt_pk_bf16_f32 v206, v29, v31\n"
    "v_mfma_f32_4x4x4_16b_bf16 v[24:27], v[40:41], a[190:191], 0 cbsz:2 abid:1\n"
    "v_cvt_pk_bf16_f32 v203, v32, v34\n"
    "v_cvt_pk_bf16_f32 v207, v33, v35\n"
    "v_readlane_b32 s81, v3, 4\n"
    "s_waitcnt vmcnt(10) lgkmcnt(3)\n"
    "ds_read2st64_b32 a[204:205], v38 offset0:16 offset1:17\n"
    "v_mfma_f32_4x4x4_16b_bf16 v[28:31], v[40:41], a[192:193], 0 cbsz:2 abid:2\n"
    "ds_read2st64_b32 a[206:207], v38 offset0:18 offset1:19\n"
    "ds_read_u16 v252, v18 offset:712\n"
    "v_cvt_pk_bf16_f32 v208, v20, v24\n"
    "v_cvt_pk_bf16_f32 v212, v21, v25\n"
    "v_mfma_f32_4x4x4_16b_bf16 v[32:35], v[40:41], a[194:195], 0 cbsz:2 abid:3\n"
    "v_cvt_pk_bf16_f32 v216, v22, v26\n"
    "v_cvt_pk_bf16_f32 v220, v23, v27\n"
    "s_waitcnt vmcnt(9) lgkmcnt(5)\n"
    "ds_read2st64_b32 a[208:209], v38 offset0:20 offset1:21\n"
    "v_mfma_f32_4x4x4_16b_bf16 v[20:23], v[42:43], a[196:197], 0 cbsz:2 abid:0\n"
    "ds_read2st64_b32 a[210:211], v38 offset0:22 offset1:23\n"
    "ds_read_u16 v254, v235 offset:704\n"
    "v_cvt_pk_bf16_f32 v209, v28, v32\n"
    "v_cvt_pk_bf16_f32 v213, v29, v33\n"
    "v_mfma_f32_4x4x4_16b_bf16 v[24:27], v[42:43], a[198:199], 0 cbsz:2 abid:1\n"
    "v_cvt_pk_bf16_f32 v217, v30, v34\n"
    "v_cvt_pk_bf16_f32 v221, v31, v35\n"
    "s_waitcnt vmcnt(8) lgkmcnt(6)\n"
    "ds_read2st64_b64 a[188:191], v15 offset0:12 offset1:13\n"
    "v_mfma_f32_4x4x4_16b_bf16 v[28:31], v[42:43], a[200:201], 0 cbsz:2 abid:2\n"
    "s_nop 0\n"
    "v_cvt_pk_bf16_f32 v210, v20, v24\n"
    "v_cvt_pk_bf16_f32 v214, v21, v25\n"
    "v_mfma_f32_4x4x4_16b_bf16 v[32:35], v[42:43], a[202:203], 0 cbsz:2 abid:3\n"
    "v_cvt_pk_bf16_f32 v218, v22, v26\n"
    "v_cvt_pk_bf16_f32 v222, v23, v27\n"
    "v_lshlrev_b64 v[46:47], v16, v[250:251]\n"
    "s_waitcnt vmcnt(7) lgkmcnt(4)\n"
    "ds_read2st64_b64 a[192:195], v15 offset0:14 offset1:15\n"
    "v_mfma_f32_4x4x4_16b_bf16 v[20:23], v[44:45], a[204:205], 0 cbsz:2 abid:0\n"
    "s_add_u32 m0, s57, 12288\n"
    "v_cvt_pk_bf16_f32 v211, v28, v32\n"
    "v_cvt_pk_bf16_f32 v215, v29, v33\n"
    "v_mfma_f32_4x4x4_16b_bf16 v[24:27], v[44:45], a[206:207], 0 cbsz:2 abid:1\n"
    "buffer_load_dwordx4 v0, s[36:39], s73 offen nt lds\n"
    "v_cvt_pk_bf16_f32 v219, v30, v34\n"
    "v_cvt_pk_bf16_f32 v223, v31, v35\n"
    "v_lshlrev_b64 v[48:49], v16, v[252:253]\n"
    "s_waitcnt vmcnt(7) lgkmcnt(2)\n"
    "ds_read2st64_b64 a[196:199], v15 offset0:16 offset1:17\n"
    "v_mfma_f32_4x4x4_16b_bf16 v[28:31], v[44:45], a[208:209], 0 cbsz:2 abid:2\n"
    "v_cvt_pk_bf16_f32 v224, v20, v22\n"
    "v_cvt_pk_bf16_f32 v228, v21, v23\n"
    "v_mfma_f32_4x4x4_16b_bf16 v[32:35], v[44:45], a[210:211], 0 cbsz:2 abid:3\n"
    "buffer_load_dwordx4 v1, s[36:39], s73 offen offset:1024 nt lds\n"
    "v_cvt_pk_bf16_f32 v225, v24, v26\n"
    "v_cvt_pk_bf16_f32 v229, v25, v27\n"
    "v_lshlrev_b64 v[50:51], v16, v[254:255]\n"
    "s_waitcnt vmcnt(7) lgkmcnt(2)\n"
    "ds_read2st64_b64 a[200:203], v15 offset0:18 offset1:19\n"
    "v_mfma_f32_4x4x4_16b_bf16 v[20:23], v[46:47], a[188:189], 0 cbsz:2 abid:0\n"
    "v_cvt_pk_bf16_f32 v226, v28, v30\n"
    "v_cvt_pk_bf16_f32 v230, v29, v31\n"
    "v_mfma_f32_4x4x4_16b_bf16 v[24:27], v[46:47], a[190:191], 0 cbsz:2 abid:1\n"
    "buffer_load_dwordx4 v2, s[36:39], s73 offen offset:2048 nt lds\n"
    "v_cvt_pk_bf16_f32 v227, v32, v34\n"
    "v_cvt_pk_bf16_f32 v231, v33, v35\n"
    "s_waitcnt vmcnt(7) lgkmcnt(2)\n"
    "ds_read2st64_b32 a[204:205], v38 offset0:40 offset1:41\n"
    "v_mfma_f32_4x4x4_16b_bf16 v[28:31], v[46:47], a[192:193], 0 cbsz:2 abid:2\n"
    "ds_read2st64_b32 a[206:207], v38 offset0:42 offset1:43\n"
    "v_cvt_pk_bf16_f32 v232, v20, v24\n"
    "v_cvt_pk_bf16_f32 v236, v21, v25\n"
    "v_mfma_f32_4x4x4_16b_bf16 v[32:35], v[46:47], a[194:195], 0 cbsz:2 abid:3\n"
    "v_cvt_pk_bf16_f32 v240, v22, v26\n"
    "v_cvt_pk_bf16_f32 v244, v23, v27\n"
    "s_waitcnt vmcnt(6) lgkmcnt(3)\n"
    "ds_read2st64_b32 a[208:209], v38 offset0:44 offset1:45\n"
    "v_mfma_f32_4x4x4_16b_bf16 v[20:23], v[48:49], a[196:197], 0 cbsz:2 abid:0\n"
    "ds_read2st64_b32 a[210:211], v38 offset0:46 offset1:47\n"
    "v_cvt_pk_bf16_f32 v233, v28, v32\n"
    "v_cvt_pk_bf16_f32 v237, v29, v33\n"
    "v_mfma_f32_4x4x4_16b_bf16 v[24:27], v[48:49], a[198:199], 0 cbsz:2 abid:1\n"
    "v_cvt_pk_bf16_f32 v241, v30, v34\n"
    "v_cvt_pk_bf16_f32 v245, v31, v35\n"
    "s_waitcnt lgkmcnt(4)\n"
    "v_mfma_f32_4x4x4_16b_bf16 v[28:31], v[48:49], a[200:201], 0 cbsz:2 abid:2\n"
    "s_nop 1\n"
    "v_cvt_pk_bf16_f32 v234, v20, v24\n"
    "v_cvt_pk_bf16_f32 v238, v21, v25\n"
    "v_mfma_f32_4x4x4_16b_bf16 v[32:35], v[48:49], a[202:203], 0 cbsz:2 abid:3\n"
    "v_cvt_pk_bf16_f32 v242, v22, v26\n"
    "v_cvt_pk_bf16_f32 v246, v23, v27\n"
    "s_waitcnt lgkmcnt(2)\n"
    "v_mfma_f32_4x4x4_16b_bf16 v[20:23], v[50:51], a[204:205], 0 cbsz:2 abid:0\n"
    "s_nop 1\n"
    "v_cvt_pk_bf16_f32 v235, v28, v32\n"
    "v_cvt_pk_bf16_f32 v239, v29, v33\n"
    "v_mfma_f32_4x4x4_16b_bf16 v[24:27], v[50:51], a[206:207], 0 cbsz:2 abid:1\n"
    "v_cvt_pk_bf16_f32 v243, v30, v34\n"
    "v_cvt_pk_bf16_f32 v247, v31, v35\n"
    "s_waitcnt lgkmcnt(0)\n"
    "v_mfma_f32_4x4x4_16b_bf16 v[28:31], v[50:51], a[208:209], 0 cbsz:2 abid:2\n"
    "s_nop 1\n"
    "v_cvt_pk_bf16_f32 v248, v20, v22\n"
    "v_cvt_pk_bf16_f32 v252, v21, v23\n"
    "v_mfma_f32_4x4x4_16b_bf16 v[32:35], v[50:51], a[210:211], 0 cbsz:2 abid:3\n"
    "v_cvt_pk_bf16_f32 v249, v24, v26\n"
    "v_cvt_pk_bf16_f32 v253, v25, v27\n"
    "s_waitcnt lgkmcnt(0)\n"
    "s_add_u32 s89, s92, 248\n"
    "v_add_u32 v38, s11, v38\n"
    "s_nop 0\n"
    "v_cvt_pk_bf16_f32 v250, v28, v30\n"
    "v_cvt_pk_bf16_f32 v254, v29, v31\n"
    "v_cvt_pk_bf16_f32 v251, v32, v34\n"
    "v_cvt_pk_bf16_f32 v255, v33, v35\n"
    "s_waitcnt vmcnt(3)\n"
    "s_barrier\n"
    "ds_read_b128 a[88:91], v4\n"
    "ds_read_b128 a[92:95], v5\n"
    "ds_read_b128 a[96:99], v6\n"
    "ds_read_b128 a[100:103], v7\n"
    "ds_read_b128 a[104:107], v4 offset:256\n"
    "ds_read_b128 a[108:111], v5 offset:256\n"
    "ds_read_b128 a[112:115], v6 offset:256\n"
    "ds_read_b128 a[116:119], v7 offset:256\n"
    "ds_read_b128 a[120:123], v4 offset:512\n"
    "ds_read_b128 a[124:127], v5 offset:512\n"
    "ds_read_b128 a[128:131], v6 offset:512\n"
    "ds_read_b128 a[132:135], v7 offset:512\n"
    "s_waitcnt lgkmcnt(11)\n"
    "v_mfma_f32_16x16x32_bf16 v[20:23], a[88:91], a[16:19], 0\n"
    "v_add_u32 v14, 1, v14\n"
    "s_sub_i32 s46, s52, 5\n"
    "s_max_i32 s46, s46, 0\n"
    "s_waitcnt lgkmcnt(10)\n"
    "v_mfma_f32_16x16x32_bf16 v[20:23], a[92:95], a[40:43], v[20:23]\n"
    "s_add_u32 m0, s57, 24576\n"
    "s_bfm_b64 s[44:45], s46, 5\n"
    "v_subrev_u32 v43, 5, v19\n"
    "s_waitcnt lgkmcnt(9)\n"
    "v_mfma_f32_16x16x32_bf16 v[20:23], a[96:99], a[64:67], v[20:23]\n"
    "buffer_load_dwordx4 v0, s[36:39], s74 offen nt lds\n"
    "s_andn2_b64 s[50:51], s[44:45], s[26:27]\n"
    "s_waitcnt lgkmcnt(8)\n"
    "v_mfma_f32_16x16x32_bf16 v[20:23], a[100:103], v[56:59], v[20:23]\n"
    "s_and_b64 s[48:49], s[44:45], s[26:27]\n"
    "s_bcnt1_i32_b64 s47, s[50:51]\n"
    "s_sub_u32 s46, s46, s47\n"
    "s_waitcnt lgkmcnt(7)\n"
    "v_mfma_f32_16x16x32_bf16 v[20:23], a[104:107], v[80:83], v[20:23]\n"
    "buffer_load_dwordx4 v1, s[36:39], s74 offen offset:1024 nt lds\n"
    "s_add_u32 s58, s46, 5\n"
    "s_waitcnt lgkmcnt(6)\n"
    "v_mfma_f32_16x16x32_bf16 v[20:23], a[108:111], v[104:107], v[20:23]\n"
    "v_mov_b32 v45, s58\n"
    "v_mbcnt_lo_u32_b32 v47, s48, 5\n"
    "v_mbcnt_lo_u32_b32 v45, s50, v45\n"
    "s_waitcnt lgkmcnt(5)\n"
    "v_mfma_f32_16x16x32_bf16 v[20:23], a[112:115], v[128:131], v[20:23]\n"
    "buffer_load_dwordx4 v2, s[36:39], s74 offen offset:2048 nt lds\n"
    "v_mbcnt_hi_u32_b32 v47, s49, v47\n"
    "s_waitcnt lgkmcnt(4)\n"
    "v_mfma_f32_16x16x32_bf16 v[20:23], a[116:119], v[152:155], v[20:23]\n"
    "v_mbcnt_hi_u32_b32 v45, s51, v45\n"
    "v_cndmask_b32 v47, v19, v47, s[48:49]\n"
    "s_sub_i32 s59, s47, 4\n"
    "s_waitcnt lgkmcnt(3)\n"
    "v_mfma_f32_16x16x32_bf16 v[20:23], a[120:123], v[176:179], v[20:23]\n"
    "v_cndmask_b32 v47, v47, v45, s[50:51]\n"
    "s_max_i32 s59, s59, 0\n"
    "v_lshlrev_b32 v47, 2, v47\n"
    "s_waitcnt lgkmcnt(2)\n"
    "v_mfma_f32_16x16x32_bf16 v[20:23], a[124:127], v[200:203], v[20:23]\n"
    "s_mul_i32 s59, s59, 9\n"
    "s_min_u32 s59, s59, 256\n"
    "ds_permute_b32 v3, v47, v3\n"
    "s_waitcnt lgkmcnt(2)\n"
    "v_mfma_f32_16x16x32_bf16 v[20:23], a[128:131], v[224:227], v[20:23]\n"
    "s_bcnt1_i32_b64 s73, s[24:25]\n"
    "s_mul_i32 s62, s73, 16\n"
    "s_lshr_b32 s62, s62, 5\n"
    "s_waitcnt lgkmcnt(1)\n"
    "v_mfma_f32_16x16x32_bf16 v[20:23], a[132:135], v[248:251], v[20:23]\n"
    "s_add_u32 s62, s62, 32\n"
    "s_sub_u32 s63, s62, 30\n"
    "v_bfe_i32 v11, v11, 0, 16\n"
    "v_mfma_f32_16x16x32_bf16 v[24:27], a[132:135], v[252:255], 0\n"
    "s_nop 0\n"
    "v_readlane_b32 s7, v11, 0\n"
    "v_readlane_b32 s8, v11, 1\n"
    "v_mfma_f32_16x16x32_bf16 v[24:27], a[128:131], v[228:231], v[24:27]\n"
    "v_readlane_b32 s60, v11, 2\n"
    "v_readlane_b32 s61, v11, 3\n"
    "s_max_i32 s7, s7, s8\n"
    "v_mfma_f32_16x16x32_bf16 v[24:27], a[124:127], v[204:207], v[24:27]\n"
    "s_max_i32 s8, s60, s61\n"
    "s_max_i32 s7, s7, s8\n"
    "s_mul_i32 s7, s7, 3\n"
    "v_mfma_f32_16x16x32_bf16 v[24:27], a[120:123], v[180:183], v[24:27]\n"
    "s_add_i32 s7, s7, 32\n"
    "s_ashr_i32 s7, s7, 6\n"
    "s_max_i32 s7, s7, 0\n"
    "v_mfma_f32_16x16x32_bf16 v[24:27], a[116:119], v[156:159], v[24:27]\n"
    "s_min_i32 s72, s7, 4\n"
    "v_mul_i32_i24 v40, s59, v43\n"
    "s_mul_i32 s7, s2, 9\n"
    "v_mfma_f32_16x16x32_bf16 v[24:27], a[112:115], v[132:135], v[24:27]\n"
    "s_and_b32 s7, s7, 0xff\n"
    "v_add_u32 v40, s7, v40\n"
    "v_subrev_u32 v42, s46, v43\n"
    "v_mfma_f32_16x16x32_bf16 v[24:27], a[108:111], v[108:111], v[24:27]\n"
    "v_add_u32 v41, s59, v40\n"
    "v_ashrrev_i32 v40, 8, v40\n"
    "v_ashrrev_i32 v41, 8, v41\n"
    "v_mfma_f32_16x16x32_bf16 v[24:27], a[104:107], v[84:87], v[24:27]\n"
    "v_min_i32 v40, s47, v40\n"
    "v_min_i32 v41, s47, v41\n"
    "v_max_i32 v40, v40, v42\n"
    "v_mfma_f32_16x16x32_bf16 v[24:27], a[100:103], v[60:63], v[24:27]\n"
    "v_add_u32 v42, 1, v42\n"
    "v_max_i32 v41, v41, v42\n"
    "v_cmp_gt_i32 s[60:61], v41, v40\n"
    "v_mfma_f32_16x16x32_bf16 v[24:27], a[96:99], a[68:71], v[24:27]\n"
    "s_waitcnt vmcnt(3)\n"
    "v_add_u32 v41, s58, v40\n"
    "v_sub_u32 v40, v19, v40\n"
    "v_mfma_f32_16x16x32_bf16 v[24:27], a[92:95], a[44:47], v[24:27]\n"
    "ds_write_b32 v13, v14\n"
    "v_cndmask_b32 v40, v40, v41, s[60:61]\n"
    "v_mfma_f32_16x16x32_bf16 v[24:27], a[88:91], a[20:23], v[24:27]\n"
    "s_add_u32 m0, s57, 36864\n"
    "v_cndmask_b32 v40, v19, v40, s[44:45]\n"
    "v_lshlrev_b32 v40, 2, v40\n"
    "v_mfma_f32_16x16x32_bf16 v[28:31], a[88:91], a[0:3], 0\n"
    "buffer_load_dwordx4 v0, s[36:39], s75 offen nt lds\n"
    "v_mfma_f32_16x16x32_bf16 v[28:31], a[92:95], a[24:27], v[28:31]\n"
    "s_waitcnt lgkmcnt(1)\n"
    "ds_bpermute_b32 v3, v40, v3\n"
    "s_mul_i32 s94, s73, 8\n"
    "v_mfma_f32_16x16x32_bf16 v[28:31], a[96:99], a[48:51], v[28:31]\n"
    "buffer_load_dwordx4 v1, s[36:39], s75 offen offset:1024 nt lds\n"
    "s_mul_i32 s31, s73, 6\n"
    "v_mfma_f32_16x16x32_bf16 v[28:31], a[100:103], a[72:75], v[28:31]\n"
    "v_cvt_pk_bf16_f32 v52, v20, v24\n"
    "s_lshr_b32 s94, s94, 5\n"
    "s_lshr_b32 s31, s31, 5\n"
    "v_mfma_f32_16x16x32_bf16 v[28:31], a[104:107], v[64:67], v[28:31]\n"
    "ds_read_b32 v15, v12\n"
    "s_mul_i32 s7, s95, -2\n"
    "s_add_u32 s7, s7, 67\n"
    "v_mfma_f32_16x16x32_bf16 v[28:31], a[108:111], v[88:91], v[28:31]\n"
    "v_cvt_pk_bf16_f32 v53, v21, v25\n"
    "s_add_u32 s94, s94, s7\n"
    "s_add_u32 s31, s31, s7\n"
    "v_mfma_f32_16x16x32_bf16 v[28:31], a[112:115], v[112:115], v[28:31]\n"
    "v_cvt_pk_bf16_f32 v54, v22, v26\n"
    "s_sub_u32 s7, 6, s72\n"
    "s_add_u32 s31, s31, s7\n"
    "v_mfma_f32_16x16x32_bf16 v[28:31], a[116:119], v[136:139], v[28:31]\n"
    "v_cvt_pk_bf16_f32 v55, v23, v27\n"
    "s_waitcnt lgkmcnt(0)\n"
    "v_cmp_lt_u32 vcc, v15, v14\n"
    "v_mfma_f32_16x16x32_bf16 v[28:31], a[120:123], v[160:163], v[28:31]\n"
    "s_add_u32 s76, s52, 4\n"
    "v_subrev_u32 v18, 5, v19\n"
    "s_mov_b32 s30, s94\n"
    "v_mfma_f32_16x16x32_bf16 v[28:31], a[124:127], v[184:187], v[28:31]\n"
    "s_cbranch_vccnz 81b\n"
    "82:\n"
    "s_add_u32 s94, s94, 12\n"
    "v_mfma_f32_16x16x32_bf16 v[28:31], a[128:131], v[208:211], v[28:31]\n"
    "ds_write2st64_b32 v38, v52, v53 offset0:0 offset1:2\n"
    "s_sub_u32 s7, s52, 5\n"
    "v_mfma_f32_16x16x32_bf16 v[28:31], a[132:135], v[232:235], v[28:31]\n"
    "ds_read_b128 a[136:139], v4 offset:12288\n"
    "s_mov_b64 vcc, s[12:13]\n"
    "s_andn2_b32 s7, s7, 1\n"
    "v_mfma_f32_16x16x32_bf16 v[32:35], a[132:135], v[236:239], 0\n"
    "ds_write2st64_b32 v38, v54, v55 offset0:4 offset1:6\n"
    "v_mfma_f32_16x16x32_bf16 v[32:35], a[128:131], v[212:215], v[32:35]\n"
    "ds_read_b128 a[140:143], v5 offset:12288\n"
    "v_add_u32 v14, 1, v14\n"
    "v_mfma_f32_16x16x32_bf16 v[32:35], a[124:127], v[188:191], v[32:35]\n"
    "buffer_load_dwordx4 v2, s[36:39], s75 offen offset:2048 nt lds\n"
    "v_mfma_f32_16x16x32_bf16 v[32:35], a[120:123], v[164:167], v[32:35]\n"
    "ds_read_b128 a[144:147], v6 offset:12288\n"
    "buffer_load_dword a212, off, s[36:39], s33\n"
    "v_mfma_f32_16x16x32_bf16 v[32:35], a[116:119], v[140:143], v[32:35]\n"
    "buffer_load_dword a212, off, s[36:39], s33\n"
    "buffer_load_dword a212, off, s[36:39], s33\n"
    "v_mfma_f32_16x16x32_bf16 v[32:35], a[112:115], v[116:119], v[32:35]\n"
    "s_cmp_lt_i32 s7, 2\n"
    "s_cselect_b32 s7, 0x1000, s7\n"
    "v_mfma_f32_16x16x32_bf16 v[32:35], a[108:111], v[92:95], v[32:35]\n"
    "ds_read_b128 a[148:151], v7 offset:12288\n"
    "s_cmp_lt_u32 s47, 4\n"
    "s_cselect_b32 s7, 0x1000, s7\n"
    "v_mfma_f32_16x16x32_bf16 v[32:35], a[104:107], v[68:71], v[32:35]\n"
    "s_cmp_lg_u32 s96, 0\n"
    "s_cselect_b32 s7, 0x1000, s7\n"
    "s_add_u32 s70, s7, -1\n"
    "v_mfma_f32_16x16x32_bf16 v[32:35], a[100:103], a[76:79], v[32:35]\n"
    "ds_read_b128 a[152:155], v4 offset:12544\n"
    "v_subrev_u32 v40, s70, v18\n"
    "v_cmp_gt_u32 s[60:61], 2, v40\n"
    "v_mfma_f32_16x16x32_bf16 v[32:35], a[96:99], a[52:55], v[32:35]\n"
    "s_add_u32 s64, s7, 5\n"
    "s_add_u32 s65, s7, 6\n"
    "v_cndmask_b32 v18, v18, 63, s[60:61]\n"
    "v_mfma_f32_16x16x32_bf16 v[32:35], a[92:95], a[28:31], v[32:35]\n"
    "ds_read_b128 a[156:159], v5 offset:12544\n"
    "s_add_u32 s71, s7, 5\n"
    "v_lshlrev_b32 v18, 2, v18\n"
    "v_mfma_f32_16x16x32_bf16 v[32:35], a[88:91], a[4:7], v[32:35]\n"
    "ds_bpermute_b32 v18, v18, v3\n"
    "v_readlane_b32 s84, v3, s86\n"
    "v_mfma_f32_16x16x32_bf16 v[20:23], a[88:91], a[8:11], 0\n"
    "ds_read_b128 a[160:163], v6 offset:12544\n"
    "v_mfma_f32_16x16x32_bf16 v[20:23], a[92:95], a[32:35], v[20:23]\n"
    "v_mfma_f32_16x16x32_bf16 v[20:23], a[96:99], a[56:59], v[20:23]\n"
    "ds_read_b128 a[164:167], v7 offset:12544\n"
    "v_mfma_f32_16x16x32_bf16 v[20:23], a[100:103], a[80:83], v[20:23]\n"
    "v_cvt_pk_bf16_f32 v36, v28, v32\n"
    "v_mfma_f32_16x16x32_bf16 v[20:23], a[104:107], v[72:75], v[20:23]\n"
    "ds_read_b128 a[168:171], v4 offset:12800\n"
    "v_mfma_f32_16x16x32_bf16 v[20:23], a[108:111], v[96:99], v[20:23]\n"
    "v_cvt_pk_bf16_f32 v46, v29, v33\n"
    "v_mfma_f32_16x16x32_bf16 v[20:23], a[112:115], v[120:123], v[20:23]\n"
    "ds_read_b128 a[172:175], v5 offset:12800\n"
    "s_add_u32 m0, s57, 49152\n"
    "v_mfma_f32_16x16x32_bf16 v[20:23], a[116:119], v[144:147], v[20:23]\n"
    "buffer_load_dwordx4 v0, s[36:39], s81 offen nt lds\n"
    "v_mfma_f32_16x16x32_bf16 v[20:23], a[120:123], v[168:171], v[20:23]\n"
    "v_cvt_pk_bf16_f32 v37, v30, v34\n"
    "v_mfma_f32_16x16x32_bf16 v[20:23], a[124:127], v[192:195], v[20:23]\n"
    "ds_read_b128 a[176:179], v6 offset:12800\n"
    "v_mfma_f32_16x16x32_bf16 v[20:23], a[128:131], v[216:219], v[20:23]\n"
    "v_cvt_pk_bf16_f32 v50, v31, v35\n"
    "v_mfma_f32_16x16x32_bf16 v[20:23], a[132:135], v[240:243], v[20:23]\n"
    "ds_read_b128 a[180:183], v7 offset:12800\n"
    "v_mfma_f32_16x16x32_bf16 v[24:27], a[132:135], v[244:247], 0\n"
    "buffer_load_dwordx4 v1, s[36:39], s81 offen offset:1024 nt lds\n"
    "v_mfma_f32_16x16x32_bf16 v[24:27], a[128:131], v[220:223], v[24:27]\n"
    "v_cndmask_b32_dpp v44, v46, v36, vcc row_ror:8 row_mask:0xf bank_mask:0xf\n"
    "v_mfma_f32_16x16x32_bf16 v[24:27], a[124:127], v[196:199], v[24:27]\n"
    "buffer_load_dwordx4 v2, s[36:39], s81 offen offset:2048 nt lds\n"
    "v_mfma_f32_16x16x32_bf16 v[24:27], a[120:123], v[172:175], v[24:27]\n"
    "s_memrealtime s[92:93]\n"
    "buffer_load_dword a212, off, s[36:39], s33\n"
    "v_mfma_f32_16x16x32_bf16 v[24:27], a[116:119], v[148:151], v[24:27]\n"
    "v_cndmask_b32_dpp v48, v50, v37, vcc row_ror:8 row_mask:0xf bank_mask:0xf\n"
    "v_mfma_f32_16x16x32_bf16 v[24:27], a[112:115], v[124:127], v[24:27]\n"
    "v_mov_b32_dpp v46, v36 row_ror:8 row_mask:0xf bank_mask:0x3\n"
    "v_mfma_f32_16x16x32_bf16 v[24:27], a[108:111], v[100:103], v[24:27]\n"
    "v_mov_b32_dpp v50, v37 row_ror:8 row_mask:0xf bank_mask:0x3\n"
    "v_mfma_f32_16x16x32_bf16 v[24:27], a[104:107], v[76:79], v[24:27]\n"
    "v_mfma_f32_16x16x32_bf16 v[24:27], a[100:103], a[84:87], v[24:27]\n"
    "s_waitcnt vmcnt(10) lgkmcnt(0)\n"
    "s_sub_u32 s85, s92, s89\n"
    "v_mfma_f32_16x16x32_bf16 v[24:27], a[96:99], a[60:63], v[24:27]\n"
    "s_add_i32 s63, s63, s85\n"
    "s_max_i32 s63, s63, s62\n"
    "s_add_u32 s89, s89, s63\n"
    "v_mfma_f32_16x16x32_bf16 v[24:27], a[92:95], a[36:39], v[24:27]\n"
    "s_mov_b32 s81, s84\n"
    "s_add_u32 m0, s57, 61440\n"
    "v_mfma_f32_16x16x32_bf16 v[24:27], a[88:91], a[12:15], v[24:27]\n"
    "s_branch 52f\n"
    ".p2align 6\n"
    "51:\n"
    "v_mfma_f32_16x16x32_bf16 v[20:23], a[88:91], a[16:19], 0\n"
    "s_add_u32 s86, s86, 1\n"
    "v_mfma_f32_16x16x32_bf16 v[20:23], a[92:95], a[40:43], v[20:23]\n"
    "s_waitcnt vmcnt(13)\n"
    "v_mfma_f32_16x16x32_bf16 v[20:23], a[96:99], a[64:67], v[20:23]\n"
    "ds_write_b32 v13, v14\n"
    "v_mfma_f32_16x16x32_bf16 v[20:23], a[100:103], v[56:59], v[20:23]\n"
    "v_cvt_pk_bf16_f32 v36, v28, v32\n"
    "v_mfma_f32_16x16x32_bf16 v[20:23], a[104:107], v[80:83], v[20:23]\n"
    "v_cvt_pk_bf16_f32 v47, v29, v33\n"
    "v_mfma_f32_16x16x32_bf16 v[20:23], a[108:111], v[104:107], v[20:23]\n"
    "v_cvt_pk_bf16_f32 v37, v30, v34\n"
    "v_mfma_f32_16x16x32_bf16 v[20:23], a[112:115], v[128:131], v[20:23]\n"
    "v_cvt_pk_bf16_f32 v51, v31, v35\n"
    "v_mfma_f32_16x16x32_bf16 v[20:23], a[116:119], v[152:155], v[20:23]\n"
    "v_cndmask_b32_dpp v45, v47, v36, vcc row_ror:8 row_mask:0xf bank_mask:0xf\n"
    "v_mfma_f32_16x16x32_bf16 v[20:23], a[120:123], v[176:179], v[20:23]\n"
    "v_cndmask_b32_dpp v49, v51, v37, vcc row_ror:8 row_mask:0xf bank_mask:0xf\n"
    "v_mfma_f32_16x16x32_bf16 v[20:23], a[124:127], v[200:203], v[20:23]\n"
    "v_mov_b32_dpp v47, v36 row_ror:8 row_mask:0xf bank_mask:0x3\n"
    "v_mfma_f32_16x16x32_bf16 v[20:23], a[128:131], v[224:227], v[20:23]\n"
    "v_mov_b32_dpp v51, v37 row_ror:8 row_mask:0xf bank_mask:0x3\n"
    "v_mfma_f32_16x16x32_bf16 v[20:23], a[132:135], v[248:251], v[20:23]\n"
    "ds_read_b32 v15, v12\n"
    "v_mfma_f32_16x16x32_bf16 v[24:27], a[132:135], v[252:255], 0\n"
    "s_add_u32 s89, s89, s94\n"
    "v_mfma_f32_16x16x32_bf16 v[24:27], a[128:131], v[228:231], v[24:27]\n"
    "s_sub_u32 s94, s94, 6\n"
    "v_mfma_f32_16x16x32_bf16 v[24:27], a[124:127], v[204:207], v[24:27]\n"
    "buffer_store_dwordx4 v[44:47], v8, s[40:43], s82 offen sc1\n"
    "v_mfma_f32_16x16x32_bf16 v[24:27], a[120:123], v[180:183], v[24:27]\n"
    "s_max_u32 s94, s94, s30\n"
    "v_mfma_f32_16x16x32_bf16 v[24:27], a[116:119], v[156:159], v[24:27]\n"
    "s_waitcnt lgkmcnt(0)\n"
    "v_cmp_lt_u32 vcc, v15, v14\n"
    "v_mfma_f32_16x16x32_bf16 v[24:27], a[112:115], v[132:135], v[24:27]\n"
    "v_mfma_f32_16x16x32_bf16 v[24:27], a[108:111], v[108:111], v[24:27]\n"
    "s_cbranch_vccnz 41b\n"
    "42:\n"
    "v_mfma_f32_16x16x32_bf16 v[24:27], a[104:107], v[84:87], v[24:27]\n"
    "ds_read_b128 a[184:187], v39 offset:4096\n"
    "v_mfma_f32_16x16x32_bf16 v[24:27], a[100:103], v[60:63], v[24:27]\n"
    "s_cmp_eq_u32 s86, s64\n"
    "s_cbranch_scc1 45b\n"
    "v_mfma_f32_16x16x32_bf16 v[24:27], a[96:99], a[68:71], v[24:27]\n"
    "ds_read_b128 a[136:139], v4 offset:12288\n"
    "v_mfma_f32_16x16x32_bf16 v[24:27], a[92:95], a[44:47], v[24:27]\n"
    "v_add_u32 v14, 1, v14\n"
    "v_mfma_f32_16x16x32_bf16 v[24:27], a[88:91], a[20:23], v[24:27]\n"
    "s_mov_b64 vcc, s[12:13]\n"
    "v_mfma_f32_16x16x32_bf16 v[28:31], a[88:91], a[0:3], 0\n"
    "buffer_load_dwordx4 v0, s[36:39], s81 offen nt lds\n"
    "v_mfma_f32_16x16x32_bf16 v[28:31], a[92:95], a[24:27], v[28:31]\n"
    "ds_read_b128 a[140:143], v5 offset:12288\n"
    "v_mfma_f32_16x16x32_bf16 v[28:31], a[96:99], a[48:51], v[28:31]\n"
    "ds_read_b128 a[144:147], v6 offset:12288\n"
    "v_mfma_f32_16x16x32_bf16 v[28:31], a[100:103], a[72:75], v[28:31]\n"
    "v_cvt_pk_bf16_f32 v52, v20, v24\n"
    "v_mfma_f32_16x16x32_bf16 v[28:31], a[104:107], v[64:67], v[28:31]\n"
    "v_cvt_pk_bf16_f32 v53, v21, v25\n"
    "v_mfma_f32_16x16x32_bf16 v[28:31], a[108:111], v[88:91], v[28:31]\n"
    "v_cvt_pk_bf16_f32 v54, v22, v26\n"
    "v_mfma_f32_16x16x32_bf16 v[28:31], a[112:115], v[112:115], v[28:31]\n"
    "v_cvt_pk_bf16_f32 v55, v23, v27\n"
    "v_mfma_f32_16x16x32_bf16 v[28:31], a[116:119], v[136:139], v[28:31]\n"
    "ds_read_b128 a[148:151], v7 offset:12288\n"
    "v_mfma_f32_16x16x32_bf16 v[28:31], a[120:123], v[160:163], v[28:31]\n"
    "v_mfma_f32_16x16x32_bf16 v[28:31], a[124:127], v[184:187], v[28:31]\n"
    "buffer_load_dwordx4 v1, s[36:39], s81 offen offset:1024 nt lds\n"
    "v_mfma_f32_16x16x32_bf16 v[28:31], a[128:131], v[208:211], v[28:31]\n"
    "v_mfma_f32_16x16x32_bf16 v[28:31], a[132:135], v[232:235], v[28:31]\n"
    "ds_read_b128 a[152:155], v4 offset:12544\n"
    "v_mfma_f32_16x16x32_bf16 v[32:35], a[132:135], v[236:239], 0\n"
    "v_mfma_f32_16x16x32_bf16 v[32:35], a[128:131], v[212:215], v[32:35]\n"
    "ds_write2st64_b32 v38, v52, v53 offset0:0 offset1:2\n"
    "v_mfma_f32_16x16x32_bf16 v[32:35], a[124:127], v[188:191], v[32:35]\n"
    "46:\n"
    "v_mfma_f32_16x16x32_bf16 v[32:35], a[120:123], v[164:167], v[32:35]\n"
    "ds_read_b128 a[156:159], v5 offset:12544\n"
    "v_mfma_f32_16x16x32_bf16 v[32:35], a[116:119], v[140:143], v[32:35]\n"
    "v_mfma_f32_16x16x32_bf16 v[32:35], a[112:115], v[116:119], v[32:35]\n"
    "v_mfma_f32_16x16x32_bf16 v[32:35], a[108:111], v[92:95], v[32:35]\n"
    "buffer_store_dwordx4 v[48:51], v9, s[40:43], s82 offen sc1\n"
    "v_mfma_f32_16x16x32_bf16 v[32:35], a[104:107], v[68:71], v[32:35]\n"
    "v_mfma_f32_16x16x32_bf16 v[32:35], a[100:103], a[76:79], v[32:35]\n"
    "ds_read_b128 a[160:163], v6 offset:12544\n"
    "v_mfma_f32_16x16x32_bf16 v[32:35], a[96:99], a[52:55], v[32:35]\n"
    "ds_read_b128 a[164:167], v7 offset:12544\n"
    "v_mfma_f32_16x16x32_bf16 v[32:35], a[92:95], a[28:31], v[32:35]\n"
    "ds_read_b128 a[168:171], v4 offset:12800\n"
    "v_mfma_f32_16x16x32_bf16 v[32:35], a[88:91], a[4:7], v[32:35]\n"
    "v_mfma_f32_16x16x32_bf16 v[20:23], a[88:91], a[8:11], 0\n"
    "ds_write2st64_b32 v38, v54, v55 offset0:4 offset1:6\n"
    "v_mfma_f32_16x16x32_bf16 v[20:23], a[92:95], a[32:35], v[20:23]\n"
    "v_mfma_f32_16x16x32_bf16 v[20:23], a[96:99], a[56:59], v[20:23]\n"
    "ds_read_b128 a[172:175], v5 offset:12800\n"
    "v_mfma_f32_16x16x32_bf16 v[20:23], a[100:103], a[80:83], v[20:23]\n"
    "v_cvt_pk_bf16_f32 v36, v28, v32\n"
    "v_mfma_f32_16x16x32_bf16 v[20:23], a[104:107], v[72:75], v[20:23]\n"
    "v_cvt_pk_bf16_f32 v46, v29, v33\n"
    "v_mfma_f32_16x16x32_bf16 v[20:23], a[108:111], v[96:99], v[20:23]\n"
    "ds_read_b128 a[176:179], v6 offset:12800\n"
    "v_mfma_f32_16x16x32_bf16 v[20:23], a[112:115], v[120:123], v[20:23]\n"
    "v_cvt_pk_bf16_f32 v37, v30, v34\n"
    "v_mfma_f32_16x16x32_bf16 v[20:23], a[116:119], v[144:147], v[20:23]\n"
    "v_cvt_pk_bf16_f32 v50, v31, v35\n"
    "v_mfma_f32_16x16x32_bf16 v[20:23], a[120:123], v[168:171], v[20:23]\n"
    "buffer_load_dwordx4 v2, s[36:39], s81 offen offset:2048 nt lds\n"
    "v_mfma_f32_16x16x32_bf16 v[20:23], a[124:127], v[192:195], v[20:23]\n"
    "v_cndmask_b32_dpp v44, v46, v36, vcc row_ror:8 row_mask:0xf bank_mask:0xf\n"
    "v_mfma_f32_16x16x32_bf16 v[20:23], a[128:131], v[216:219], v[20:23]\n"
    "ds_read_b128 a[180:183], v7 offset:12800\n"
    "v_mfma_f32_16x16x32_bf16 v[20:23], a[132:135], v[240:243], v[20:23]\n"
    "v_cndmask_b32_dpp v48, v50, v37, vcc row_ror:8 row_mask:0xf bank_mask:0xf\n"
    "v_mfma_f32_16x16x32_bf16 v[24:27], a[132:135], v[244:247], 0\n"
    "s_memrealtime s[92:93]\n"
    "v_mfma_f32_16x16x32_bf16 v[24:27], a[128:131], v[220:223], v[24:27]\n"
    "v_mov_b32_dpp v46, v36 row_ror:8 row_mask:0xf bank_mask:0x3\n"
    "v_mfma_f32_16x16x32_bf16 v[24:27], a[124:127], v[196:199], v[24:27]\n"
    "v_mov_b32_dpp v50, v37 row_ror:8 row_mask:0xf bank_mask:0x3\n"
    "v_mfma_f32_16x16x32_bf16 v[24:27], a[120:123], v[172:175], v[24:27]\n"
    "v_readlane_b32 s81, v3, s86\n"
    "v_mfma_f32_16x16x32_bf16 v[24:27], a[116:119], v[148:151], v[24:27]\n"
    "s_waitcnt lgkmcnt(14)\n"
    "buffer_store_dwordx4 a[184:187], v10, s[40:43], s82 offen sc1\n"
    "v_mfma_f32_16x16x32_bf16 v[24:27], a[112:115], v[124:127], v[24:27]\n"
    "s_addk_i32 m0, 12288\n"
    "v_mfma_f32_16x16x32_bf16 v[24:27], a[108:111], v[100:103], v[24:27]\n"
    "v_readlane_b32 s82, v18, s86\n"
    "v_mfma_f32_16x16x32_bf16 v[24:27], a[104:107], v[76:79], v[24:27]\n"
    "s_waitcnt lgkmcnt(0)\n"
    "s_sub_u32 s85, s92, s89\n"
    "v_mfma_f32_16x16x32_bf16 v[24:27], a[100:103], a[84:87], v[24:27]\n"
    "s_cbranch_scc1 43b\n"
    "44:\n"
    "v_mfma_f32_16x16x32_bf16 v[24:27], a[96:99], a[60:63], v[24:27]\n"
    "s_cmp_eq_u32 m0, s77\n"
    "s_cselect_b32 s78, s87, s88\n"
    "v_mfma_f32_16x16x32_bf16 v[24:27], a[92:95], a[36:39], v[24:27]\n"
    "s_cmp_lt_u32 s86, s76\n"
    "v_mfma_f32_16x16x32_bf16 v[24:27], a[88:91], a[12:15], v[24:27]\n"
    "s_cbranch_scc0 55f\n"
    "52:\n"
    "v_mfma_f32_16x16x32_bf16 v[28:31], a[136:139], a[16:19], 0\n"
    "s_add_u32 s86, s86, 1\n"
    "v_mfma_f32_16x16x32_bf16 v[28:31], a[140:143], a[40:43], v[28:31]\n"
    "s_waitcnt vmcnt(13)\n"
    "v_mfma_f32_16x16x32_bf16 v[28:31], a[144:147], a[64:67], v[28:31]\n"
    "ds_write_b32 v13, v14\n"
    "v_mfma_f32_16x16x32_bf16 v[28:31], a[148:151], v[56:59], v[28:31]\n"
    "v_cvt_pk_bf16_f32 v36, v20, v24\n"
    "v_mfma_f32_16x16x32_bf16 v[28:31], a[152:155], v[80:83], v[28:31]\n"
    "v_cvt_pk_bf16_f32 v47, v21, v25\n"
    "v_mfma_f32_16x16x32_bf16 v[28:31], a[156:159], v[104:107], v[28:31]\n"
    "v_cvt_pk_bf16_f32 v37, v22, v26\n"
    "v_mfma_f32_16x16x32_bf16 v[28:31], a[160:163], v[128:131], v[28:31]\n"
    "v_cvt_pk_bf16_f32 v51, v23, v27\n"
    "v_mfma_f32_16x16x32_bf16 v[28:31], a[164:167], v[152:155], v[28:31]\n"
    "v_cndmask_b32_dpp v45, v47, v36, vcc row_ror:8 row_mask:0xf bank_mask:0xf\n"
    "v_mfma_f32_16x16x32_bf16 v[28:31], a[168:171], v[176:179], v[28:31]\n"
    "v_cndmask_b32_dpp v49, v51, v37, vcc row_ror:8 row_mask:0xf bank_mask:0xf\n"
    "v_mfma_f32_16x16x32_bf16 v[28:31], a[172:175], v[200:203], v[28:31]\n"
    "v_mov_b32_dpp v47, v36 row_ror:8 row_mask:0xf bank_mask:0x3\n"
    "v_mfma_f32_16x16x32_bf16 v[28:31], a[176:179], v[224:227], v[28:31]\n"
    "v_mov_b32_dpp v51, v37 row_ror:8 row_mask:0xf bank_mask:0x3\n"
    "v_mfma_f32_16x16x32_bf16 v[28:31], a[180:183], v[248:251], v[28:31]\n"
    "ds_read_b32 v15, v12\n"
    "v_mfma_f32_16x16x32_bf16 v[32:35], a[180:183], v[252:255], 0\n"
    "s_add_u32 s89, s89, s94\n"
    "v_mfma_f32_16x16x32_bf16 v[32:35], a[176:179], v[228:231], v[32:35]\n"
    "v_add_u32 v4, s78, v4\n"
    "v_mfma_f32_16x16x32_bf16 v[32:35], a[172:175], v[204:207], v[32:35]\n"
    "buffer_store_dwordx4 v[44:47], v8, s[40:43], s82 offen sc1\n"
    "v_mfma_f32_16x16x32_bf16 v[32:35], a[168:171], v[180:183], v[32:35]\n"
    "v_add_u32 v5, s78, v5\n"
    "v_mfma_f32_16x16x32_bf16 v[32:35], a[164:167], v[156:159], v[32:35]\n"
    "s_waitcnt lgkmcnt(0)\n"
    "v_cmp_lt_u32 vcc, v15, v14\n"
    "v_mfma_f32_16x16x32_bf16 v[32:35], a[160:163], v[132:135], v[32:35]\n"
    "v_add_u32 v6, s78, v6\n"
    "v_mfma_f32_16x16x32_bf16 v[32:35], a[156:159], v[108:111], v[32:35]\n"
    "s_cbranch_vccnz 61b\n"
    "62:\n"
    "v_mfma_f32_16x16x32_bf16 v[32:35], a[152:155], v[84:87], v[32:35]\n"
    "ds_read_b128 a[184:187], v39\n"
    "v_mfma_f32_16x16x32_bf16 v[32:35], a[148:151], v[60:63], v[32:35]\n"
    "s_cmp_eq_u32 s86, s65\n"
    "s_cbranch_scc1 65b\n"
    "v_mfma_f32_16x16x32_bf16 v[32:35], a[144:147], a[68:71], v[32:35]\n"
    "ds_read_b128 a[88:91], v4\n"
    "v_mfma_f32_16x16x32_bf16 v[32:35], a[140:143], a[44:47], v[32:35]\n"
    "v_add_u32 v7, s78, v7\n"
    "v_mfma_f32_16x16x32_bf16 v[32:35], a[136:139], a[20:23], v[32:35]\n"
    "v_add_u32 v14, 1, v14\n"
    "v_mfma_f32_16x16x32_bf16 v[20:23], a[136:139], a[0:3], 0\n"
    "buffer_load_dwordx4 v0, s[36:39], s81 offen nt lds\n"
    "v_mfma_f32_16x16x32_bf16 v[20:23], a[140:143], a[24:27], v[20:23]\n"
    "ds_read_b128 a[92:95], v5\n"
    "v_mfma_f32_16x16x32_bf16 v[20:23], a[144:147], a[48:51], v[20:23]\n"
    "ds_read_b128 a[96:99], v6\n"
    "v_mfma_f32_16x16x32_bf16 v[20:23], a[148:151], a[72:75], v[20:23]\n"
    "v_cvt_pk_bf16_f32 v52, v28, v32\n"
    "v_mfma_f32_16x16x32_bf16 v[20:23], a[152:155], v[64:67], v[20:23]\n"
    "v_cvt_pk_bf16_f32 v53, v29, v33\n"
    "v_mfma_f32_16x16x32_bf16 v[20:23], a[156:159], v[88:91], v[20:23]\n"
    "v_cvt_pk_bf16_f32 v54, v30, v34\n"
    "v_mfma_f32_16x16x32_bf16 v[20:23], a[160:163], v[112:115], v[20:23]\n"
    "v_cvt_pk_bf16_f32 v55, v31, v35\n"
    "v_mfma_f32_16x16x32_bf16 v[20:23], a[164:167], v[136:139], v[20:23]\n"
    "ds_read_b128 a[100:103], v7\n"
    "v_mfma_f32_16x16x32_bf16 v[20:23], a[168:171], v[160:163], v[20:23]\n"
    "s_mov_b64 vcc, s[12:13]\n"
    "v_mfma_f32_16x16x32_bf16 v[20:23], a[172:175], v[184:187], v[20:23]\n"
    "buffer_load_dwordx4 v1, s[36:39], s81 offen offset:1024 nt lds\n"
    "v_mfma_f32_16x16x32_bf16 v[20:23], a[176:179], v[208:211], v[20:23]\n"
    "v_mfma_f32_16x16x32_bf16 v[20:23], a[180:183], v[232:235], v[20:23]\n"
    "ds_read_b128 a[104:107], v4 offset:256\n"
    "v_mfma_f32_16x16x32_bf16 v[24:27], a[180:183], v[236:239], 0\n"
    "v_mfma_f32_16x16x32_bf16 v[24:27], a[176:179], v[212:215], v[24:27]\n"
    "ds_write2st64_b32 v38, v52, v53 offset0:16 offset1:18\n"
    "v_mfma_f32_16x16x32_bf16 v[24:27], a[172:175], v[188:191], v[24:27]\n"
    "66:\n"
    "v_mfma_f32_16x16x32_bf16 v[24:27], a[168:171], v[164:167], v[24:27]\n"
    "ds_read_b128 a[108:111], v5 offset:256\n"
    "v_mfma_f32_16x16x32_bf16 v[24:27], a[164:167], v[140:143], v[24:27]\n"
    "s_cmp_eq_u32 s86, 20\n"
    "v_mfma_f32_16x16x32_bf16 v[24:27], a[160:163], v[116:119], v[24:27]\n"
    "s_cselect_b32 s30, s31, s30\n"
    "v_mfma_f32_16x16x32_bf16 v[24:27], a[156:159], v[92:95], v[24:27]\n"
    "buffer_store_dwordx4 v[48:51], v9, s[40:43], s82 offen sc1\n"
    "v_mfma_f32_16x16x32_bf16 v[24:27], a[152:155], v[68:71], v[24:27]\n"
    "v_mfma_f32_16x16x32_bf16 v[24:27], a[148:151], a[76:79], v[24:27]\n"
    "ds_read_b128 a[112:115], v6 offset:256\n"
    "v_mfma_f32_16x16x32_bf16 v[24:27], a[144:147], a[52:55], v[24:27]\n"
    "ds_read_b128 a[116:119], v7 offset:256\n"
    "v_mfma_f32_16x16x32_bf16 v[24:27], a[140:143], a[28:31], v[24:27]\n"
    "ds_read_b128 a[120:123], v4 offset:512\n"
    "v_mfma_f32_16x16x32_bf16 v[24:27], a[136:139], a[4:7], v[24:27]\n"
    "v_mfma_f32_16x16x32_bf16 v[28:31], a[136:139], a[8:11], 0\n"
    "ds_write2st64_b32 v38, v54, v55 offset0:20 offset1:22\n"
    "v_mfma_f32_16x16x32_bf16 v[28:31], a[140:143], a[32:35], v[28:31]\n"
    "v_mfma_f32_16x16x32_bf16 v[28:31], a[144:147], a[56:59], v[28:31]\n"
    "ds_read_b128 a[124:127], v5 offset:512\n"
    "v_mfma_f32_16x16x32_bf16 v[28:31], a[148:151], a[80:83], v[28:31]\n"
    "v_cvt_pk_bf16_f32 v36, v20, v24\n"
    "v_mfma_f32_16x16x32_bf16 v[28:31], a[152:155], v[72:75], v[28:31]\n"
    "v_cvt_pk_bf16_f32 v46, v21, v25\n"
    "v_mfma_f32_16x16x32_bf16 v[28:31], a[156:159], v[96:99], v[28:31]\n"
    "ds_read_b128 a[128:131], v6 offset:512\n"
    "v_mfma_f32_16x16x32_bf16 v[28:31], a[160:163], v[120:123], v[28:31]\n"
    "v_cvt_pk_bf16_f32 v37, v22, v26\n"
    "v_mfma_f32_16x16x32_bf16 v[28:31], a[164:167], v[144:147], v[28:31]\n"
    "v_cvt_pk_bf16_f32 v50, v23, v27\n"
    "v_mfma_f32_16x16x32_bf16 v[28:31], a[168:171], v[168:171], v[28:31]\n"
    "buffer_load_dwordx4 v2, s[36:39], s81 offen offset:2048 nt lds\n"
    "v_mfma_f32_16x16x32_bf16 v[28:31], a[172:175], v[192:195], v[28:31]\n"
    "v_cndmask_b32_dpp v44, v46, v36, vcc row_ror:8 row_mask:0xf bank_mask:0xf\n"
    "v_mfma_f32_16x16x32_bf16 v[28:31], a[176:179], v[216:219], v[28:31]\n"
    "ds_read_b128 a[132:135], v7 offset:512\n"
    "v_mfma_f32_16x16x32_bf16 v[28:31], a[180:183], v[240:243], v[28:31]\n"
    "v_cndmask_b32_dpp v48, v50, v37, vcc row_ror:8 row_mask:0xf bank_mask:0xf\n"
    "v_mfma_f32_16x16x32_bf16 v[32:35], a[180:183], v[244:247], 0\n"
    "s_memrealtime s[92:93]\n"
    "v_mfma_f32_16x16x32_bf16 v[32:35], a[176:179], v[220:223], v[32:35]\n"
    "v_mov_b32_dpp v46, v36 row_ror:8 row_mask:0xf bank_mask:0x3\n"
    "v_mfma_f32_16x16x32_bf16 v[32:35], a[172:175], v[196:199], v[32:35]\n"
    "v_mov_b32_dpp v50, v37 row_ror:8 row_mask:0xf bank_mask:0x3\n"
    "v_mfma_f32_16x16x32_bf16 v[32:35], a[168:171], v[172:175], v[32:35]\n"
    "v_readlane_b32 s81, v3, s86\n"
    "v_mfma_f32_16x16x32_bf16 v[32:35], a[164:167], v[148:151], v[32:35]\n"
    "s_waitcnt lgkmcnt(14)\n"
    "buffer_store_dwordx4 a[184:187], v10, s[40:43], s82 offen sc1\n"
    "v_mfma_f32_16x16x32_bf16 v[32:35], a[160:163], v[124:127], v[32:35]\n"
    "s_addk_i32 m0, 12288\n"
    "v_mfma_f32_16x16x32_bf16 v[32:35], a[156:159], v[100:103], v[32:35]\n"
    "v_readlane_b32 s82, v18, s86\n"
    "v_mfma_f32_16x16x32_bf16 v[32:35], a[152:155], v[76:79], v[32:35]\n"
    "s_waitcnt lgkmcnt(0)\n"
    "s_sub_u32 s85, s92, s89\n"
    "v_mfma_f32_16x16x32_bf16 v[32:35], a[148:151], a[84:87], v[32:35]\n"
    "s_cbranch_scc1 63b\n"
    "64:\n"
    "v_mfma_f32_16x16x32_bf16 v[32:35], a[144:147], a[60:63], v[32:35]\n"
    "s_cmp_eq_u32 m0, s80\n"
    "s_cselect_b32 m0, s57, m0\n"
    "v_mfma_f32_16x16x32_bf16 v[32:35], a[140:143], a[36:39], v[32:35]\n"
    "s_cmp_lt_u32 s86, s76\n"
    "v_mfma_f32_16x16x32_bf16 v[32:35], a[136:139], a[12:15], v[32:35]\n"
    "s_cbranch_scc1 51b\n"
    "s_barrier\n"
    "ds_read_b128 a[184:187], v39 offset:4096\n"
    "s_nop 4\n"
    "v_cvt_pk_bf16_f32 v36, v28, v32\n"
    "v_cvt_pk_bf16_f32 v47, v29, v33\n"
    "v_cvt_pk_bf16_f32 v37, v30, v34\n"
    "v_cvt_pk_bf16_f32 v51, v31, v35\n"
    "s_branch 56f\n"
    "55:\n"
    "s_barrier\n"
    "ds_read_b128 a[184:187], v39\n"
    "s_nop 4\n"
    "v_cvt_pk_bf16_f32 v36, v20, v24\n"
    "v_cvt_pk_bf16_f32 v47, v21, v25\n"
    "v_cvt_pk_bf16_f32 v37, v22, v26\n"
    "v_cvt_pk_bf16_f32 v51, v23, v27\n"
    "56:\n"
    "v_cndmask_b32_dpp v45, v47, v36, vcc row_ror:8 row_mask:0xf bank_mask:0xf\n"
    "v_mov_b32_dpp v47, v36 row_ror:8 row_mask:0xf bank_mask:0x3\n"
    "v_cndmask_b32_dpp v49, v51, v37, vcc row_ror:8 row_mask:0xf bank_mask:0xf\n"
    "buffer_store_dwordx4 v[44:47], v8, s[40:43], s82 offen sc1\n"
    "v_mov_b32_dpp v51, v37 row_ror:8 row_mask:0xf bank_mask:0x3\n"
    "s_waitcnt lgkmcnt(0)\n"
    "buffer_store_dwordx4 a[184:187], v10, s[40:43], s82 offen sc1\n"
    "buffer_store_dwordx4 v[48:51], v9, s[40:43], s82 offen sc1\n"
    "s_waitcnt vmcnt(0) lgkmcnt(0)\n"
    "s_endpgm\n"
    "75:\n"
    "s_sleep 2\n"
    "global_load_dwordx4 v[236:239], v16, s[54:55] sc1\n"
    "s_waitcnt vmcnt(0)\n"
    "v_sub_u32 v11, s0, v236\n"
    "v_or_b32 v236, 0xffff, v236\n"
    "v_or_b32 v238, 0xffff, v238\n"
    "v_and_b32 v242, 15, v237\n"
    "v_and_b32 v243, 1, v239\n"
    "v_or_b32 v237, 15, v237\n"
    "v_or_b32 v239, 15, v239\n"
    "v_cmp_ne_u64 vcc, s[18:19], v[236:237]\n"
    "v_cmp_ne_u64 s[48:49], s[18:19], v[238:239]\n"
    "v_readlane_b32 s9, v242, 0\n"
    "v_and_b32 v236, 1, v242\n"
    "s_or_b64 s[48:49], s[48:49], vcc\n"
    "s_cbranch_scc1 75b\n"
    "s_branch 77b\n"
    "79:\n"
    "s_nop 0\n"
    : "+{s[0:1]}"(dp), "+{s[2:3]}"(ka), "+{s[4:5]}"(did), "+{s6}"(bid), "+{v0}"(tid)
    : [lds] "{s98}"(lds0)
    : "v1","v2","v3","v4","v5","v6","v7","v8","v9","v10","v11","v12","v13","v14","v15","v16","v17","v18","v19","v20","v21","v22","v23","v24","v25","v26","v27","v28","v29","v30","v31","v32","v33","v34","v35","v36","v37","v38","v39","v40","v41","v42","v43","v44","v45","v46","v47","v48","v49","v50","v51","v52","v53","v54","v55","v56","v57","v58","v59","v60","v61","v62","v63","v64","v65","v66","v67","v68","v69","v70","v71","v72","v73","v74","v75","v76","v77","v78","v79","v80","v81","v82","v83","v84","v85","v86","v87","v88","v89","v90","v91","v92","v93","v94","v95","v96","v97","v98","v99","v100","v101","v102","v103","v104","v105","v106","v107","v108","v109","v110","v111","v112","v113","v114","v115","v116","v117","v118","v119","v120","v121","v122","v123","v124","v125","v126","v127","v128","v129","v130","v131","v132","v133","v134","v135","v136","v137","v138","v139","v140","v141","v142","v143","v144","v145","v146","v147","v148","v149","v150","v151","v152","v153","v154","v155","v156","v157","v158","v159","v160","v161","v162","v163","v164","v165","v166","v167","v168","v169","v170","v171","v172","v173","v174","v175","v176","v177","v178","v179","v180","v181","v182","v183","v184","v185","v186","v187","v188","v189","v190","v191","v192","v193","v194","v195","v196","v197","v198","v199","v200","v201","v202","v203","v204","v205","v206","v207","v208","v209","v210","v211","v212","v213","v214","v215","v216","v217","v218","v219","v220","v221","v222","v223","v224","v225","v226","v227","v228","v229","v230","v231","v232","v233","v234","v235","v236","v237","v238","v239","v240","v241","v242","v243","v244","v245","v246","v247","v248","v249","v250","v251","v252","v253","v254","v255","a0","a1","a2","a3","a4","a5","a6","a7","a8","a9","a10","a11","a12","a13","a14","a15","a16","a17","a18","a19","a20","a21","a22","a23","a24","a25","a26","a27","a28","a29","a30","a31","a32","a33","a34","a35","a36","a37","a38","a39","a40","a41","a42","a43","a44","a45","a46","a47","a48","a49","a50","a51","a52","a53","a54","a55","a56","a57","a58","a59","a60","a61","a62","a63","a64","a65","a66","a67","a68","a69","a70","a71","a72","a73","a74","a75","a76","a77","a78","a79","a80","a81","a82","a83","a84","a85","a86","a87","a88","a89","a90","a91","a92","a93","a94","a95","a96","a97","a98","a99","a100","a101","a102","a103","a104","a105","a106","a107","a108","a109","a110","a111","a112","a113","a114","a115","a116","a117","a118","a119","a120","a121","a122","a123","a124","a125","a126","a127","a128","a129","a130","a131","a132","a133","a134","a135","a136","a137","a138","a139","a140","a141","a142","a143","a144","a145","a146","a147","a148","a149","a150","a151","a152","a153","a154","a155","a156","a157","a158","a159","a160","a161","a162","a163","a164","a165","a166","a167","a168","a169","a170","a171","a172","a173","a174","a175","a176","a177","a178","a179","a180","a181","a182","a183","a184","a185","a186","a187","a188","a189","a190","a191","a192","a193","a194","a195","a196","a197","a198","a199","a200","a201","a202","a203","a204","a205","a206","a207","a208","a209","a210","a211","a212","s7","s8","s9","s10","s11","s12","s13","s14","s15","s16","s17","s18","s19","s20","s21","s22","s23","s24","s25","s26","s27","s28","s29","s30","s31","s33","s34","s35","s36","s37","s38","s39","s40","s41","s42","s43","s44","s45","s46","s47","s48","s49","s50","s51","s52","s53","s54","s55","s56","s57","s58","s59","s60","s61","s62","s63","s64","s65","s66","s67","s68","s69","s70","s71","s72","s73","s74","s75","s76","s77","s78","s79","s80","s81","s82","s83","s84","s85","s86","s87","s88","s89","s90","s91","s92","s93","s94","s95","s96","s97","vcc","scc","memory");
}

extern "C" void kernel_launch(void* const* d_in, const int* in_sizes, int n_in,
                              void* d_out, int out_size, void* d_ws, size_t ws_size,
                              hipStream_t stream) {
  Shapes sh;
  for (int j = 0; j < 16; ++j)
    for (int w = 0; w < 4; ++w) {
      const int ty = j >= 11, per = ty ? 10 : 11, n = 16 * (j - 11 * ty);
      auto row = [&](int k) { return ty ? 64 * (k / 10) + 11 + k % 10 : 64 * (k / 22) + 21 * (k / 11 % 2) + k % 11; };
      const int n0 = n + 4 * w, bp = per - n0 % per;
      uint32_t* e = sh.v + 16 * j + 4 * w;
      e[0] = row(n0) * 768 - 512;
      e[1] = (row(n0 + bp) - bp) * 768 - 512;
      e[2] = bp * 768;
      e[3] = (row(n + 8) + 16) * 768 + ty;
      for (int i = 0; i < 4; ++i) sh.rows[16 * j + 4 * w + i] = row(n0 + i) * 768;
    }
  const int grid = 256, maxr = 40;
  const size_t M = (size_t)in_sizes[0] / CH;
  const uint32_t fl = 0x00020000u;
  for (size_t r0 = 0; r0 < M; r0 += (size_t)maxr * grid * TM) {
    const size_t rows = M - r0 < (size_t)maxr * grid * TM ? M - r0 : (size_t)maxr * grid * TM;
    const int nr = (int)((rows + grid * TM - 1) / (grid * TM)), nit = nr < 2 ? 2 : nr;
    const uint32_t nbytes = (uint32_t)(rows * CH * 2);
    const uint32_t np = (nbytes >> 21) & ~1u;
    const uint64_t pages = (np >= 24 ? (np - 14) & ~7u : 64u) | (uint64_t)(np >= 2 ? (np - 2) << 21 : 0u) << 32;
    dwsep_kernel<<<grid, 256, 0, stream>>>((const char*)d_in[0] + r0 * CH * 2, nbytes, fl, (char*)d_out + r0 * CH * 2, nbytes, fl,
                                           (const char*)d_in[2], CH * CH * 2, fl, (const char*)d_in[1], CH * 2, fl,
                                           (uint32_t)nit, (uint32_t)grid * TILE_BYTES, d_ws, pages, sh);
  }
}
```

```cpp
#include <hip/hip_runtime.h>
#include <stdint.h>

#define CH 384
#define TM 16
#define TILE_BYTES (TM * CH * 2)
#define LDS_BYTES (13 * TILE_BYTES + 1024 + 64)

struct Shapes { uint32_t v[256], rows[256]; };
extern "C" __device__ uint64_t dispatch_id_() __asm("llvm.amdgcn.dispatch.id");

__global__ __launch_bounds__(256, 1)
void dwsep_kernel(const char* x, uint32_t xbytes, uint32_t f0, char* out, uint32_t obytes, uint32_t f1,
                  const char* pw, uint32_t pwbytes, uint32_t f2, const char* dw, uint32_t dwbytes, uint32_t f3,
                  uint32_t nit, uint32_t tstride, void* ws, uint64_t pages, Shapes shapes) {
  __shared__ __attribute__((aligned(1024))) char lds[LDS_BYTES];
  uint32_t lds0 = (uint32_t)(uintptr_t)(__attribute__((address_space(3))) char*)lds;
  uint64_t ka = (uint64_t)(uintptr_t)__builtin_amdgcn_kernarg_segment_ptr();
  uint64_t did = dispatch_id_(), dp = (uint64_t)(uintptr_t)__builtin_amdgcn_dispatch_ptr();
  uint32_t bid = blockIdx.x, tid = threadIdx.x;
  asm volatile(
    "s_load_dwordx16 s[36:51], s[2:3], 0x0\n"
    "v_readfirstlane_b32 s8, v0\n"
    "s_load_dwordx4 s[52:55], s[2:3], 0x40\n"
    "s_bfe_u32 s7, s6, 0x40003\n"
    "s_lshr_b32 s9, s8, 2\n"
    "s_lshl_b32 s7, s7, 6\n"
    "s_add_u32 s9, s7, s9\n"
    "s_load_dwordx4 s[20:23], s[2:3], s9 offset:0x58\n"
    "s_load_dwordx2 s[96:97], s[2:3], 0x50\n"
    "s_getpc_b64 s[14:15]\n"
    "70:\n"
    "v_lshlrev_b32 v48, 6, v0\n"
    "s_mov_b32 s13, 79f-70b\n"
    "v_min_u32 v48, s13, v48\n"
    "global_load_dword v49, v48, s[14:15]\n"
    "v_and_b32 v18, 48, v0\n"
    "v_bfe_u32 v55, v0, 3, 1\n"
    "v_bfe_u32 v54, v0, 6, 1\n"
    "v_lshl_add_u32 v53, v55, 2, v18\n"
    "s_mov_b32 s16, s4\n"
    "v_add_u32 v53, s7, v53\n"
    "v_lshl_add_u32 v54, v54, 3, v53\n"
    "global_load_dword v8, v53, s[2:3] offset:1112\n"
    "global_load_dword v9, v53, s[2:3] offset:1120\n"
    "global_load_dword v10, v54, s[2:3] offset:1112\n"
    "s_mov_b32 s2, s6\n"
    "s_mov_b32 s3, %[lds]\n"
    "s_lshr_b32 s4, s8, 6\n"
    "v_and_b32 v53, 24, v0\n"
    "v_bfe_u32 v54, v0, 5, 1\n"
    "s_bfe_u32 s5, s2, 0x20003\n"
    "v_add_u32 v53, v53, v54\n"
    "s_bfe_u32 s6, s2, 0x20005\n"
    "v_mul_u32_u24 v53, 0x300, v53\n"
    "s_cmp_eq_u32 s6, 3\n"
    "v_and_b32 v55, 7, v0\n"
    "s_cselect_b32 s6, 0, s6\n"
    "v_lshl_add_u32 v53, v55, 4, v53\n"
    "s_lshl_b32 s7, s4, 7\n"
    "v_and_b32 v16, 63, v0\n"
    "s_mul_i32 s58, s5, 24576\n"
    "v_add_u32 v244, s7, v53\n"
    "s_mul_i32 s62, s6, 98304\n"
    "v_lshlrev_b32 v16, 4, v16\n"
    "s_lshl_b32 s7, s5, 6\n"
    "s_lshl_b32 s8, s6, 8\n"
    "v_xor_b32 v53, s7, v16\n"
    "s_add_u32 s12, s3, 159744\n"
    "s_mul_i32 s83, s4, 30720\n"
    "v_add_u32 v53, s8, v53\n"
    "s_mov_b32 m0, s12\n"
    "v_subrev_u32 v55, 0x300, v53\n"
    "s_add_u32 s83, s83, s3\n"
    "v_min_u32 v250, v53, v55\n"
    "v_add_u32 v245, 0x200, v244\n"
    "v_add_u32 v246, 0x400, v244\n"
    "v_add_u32 v247, 0x600, v244\n"
    "v_and_b32 v235, 12, v0\n"
    "s_mul_i32 s57, s4, 3072\n"
    "v_bfe_u32 v52, v0, 4, 2\n"
    "s_lshl_b32 s7, s4, 6\n"
    "v_lshl_add_u32 v53, v235, 1, v52\n"
    "v_mul_u32_u24 v53, 0x300, v53\n"
    "s_add_u32 s57, s57, s3\n"
    "v_and_b32 v54, 3, v0\n"
    "s_add_u32 s7, s7, 512\n"
    "v_lshl_add_u32 v53, v54, 4, v53\n"
    "v_lshlrev_b32 v12, 2, v54\n"
    "v_add_u32 v248, s7, v53\n"
    "v_add_u32 v249, 0x800, v248\n"
    "s_lshr_b64 s[18:19], s[0:1], 12\n"
    "s_lshl4_add_u32 s18, s16, s18\n"
    "s_andn2_b32 s17, s18, 15\n"
    "s_bitset1_b32 s17, 31\n"
    "s_mov_b32 s72, s17\n"
    "s_pack_ll_b32_b16 s18, -1, s16\n"
    "s_or_b32 s19, s17, 15\n"
    "s_lshl_b32 s34, s2, 20\n"
    "s_and_b32 s34, s34, 0x7e00000\n"
    "s_lshl_b32 s7, s16, 8\n"
    "s_add_u32 s7, s7, s2\n"
    "s_mul_i32 s7, s7, 0x9e3779b1\n"
    "s_lshr_b32 s7, s7, 9\n"
    "s_and_b32 s7, s7, 2088832\n"
    "s_or_b32 s34, s34, s7\n"
    "s_and_b32 s35, s2, 7\n"
    "s_lshl_b32 s7, s4, 1\n"
    "s_sub_u32 s35, s35, s7\n"
    "v_and_b32 v19, 15, v0\n"
    "v_add_u32 v18, s12, v18\n"
    "v_and_b32 v54, 14, v0\n"
    "v_lshrrev_b32 v53, 1, v235\n"
    "v_add_u32 v235, v18, v54\n"
    "v_add_u32 v18, v18, v53\n"
    "s_mov_b32 s9, 0x51403020\n"
    "s_mov_b32 s10, 0x52423121\n"
    "s_mov_b32 s11, 0x53433322\n"
    "s_cmp_eq_u32 s6, 1\n"
    "s_cselect_b32 s9, 0x61204030, s9\n"
    "s_cselect_b32 s10, 0x62521131, s10\n"
    "s_cselect_b32 s11, 0x33534302, s11\n"
    "s_cmp_eq_u32 s6, 2\n"
    "s_cselect_b32 s9, 0x71302040, s9\n"
    "s_cselect_b32 s10, 0x42622111, s10\n"
    "s_cselect_b32 s11, 0x43335312, s11\n"
    "s_xor_b32 s7, s4, s5\n"
    "v_lshlrev_b32 v54, 3, v52\n"
    "s_lshl_b32 s7, s7, 2\n"
    "v_xor_b32 v55, s7, v19\n"
    "v_bfe_u32 v53, s11, v54, 8\n"
    "v_xor_b32 v2, v53, v55\n"
    "v_bfe_u32 v53, s10, v54, 8\n"
    "v_xor_b32 v1, v53, v55\n"
    "v_bfe_u32 v53, s9, v54, 8\n"
    "v_xor_b32 v0, v53, v55\n"
    "s_and_b32 s28, s2, 3\n"
    "s_waitcnt lgkmcnt(0)\n"
    "s_memrealtime s[0:1]\n"
    "s_barrier\n"
    "buffer_load_dwordx4 v250, s[48:51], 0 offen lds\n"
    "s_cmp_eq_u32 s35, 0\n"
    "s_cbranch_scc0 72f\n"
    "s_bitcmp1_b32 s2, 7\n"
    "s_cselect_b64 s[68:69], s[40:41], s[36:37]\n"
    "s_mov_b64 s[70:71], s[38:39]\n"
    "s_and_b32 s85, s34, 0x3fffff\n"
    "s_add_u32 s85, s85, s97\n"
    "s_min_u32 s34, s34, s85\n"
    "s_buffer_load_dword s73, s[68:71], s34\n"
    "s_buffer_load_dword s72, s[68:71], s34 offset:0x2000\n"
    "72:\n"
    "s_add_u32 m0, s83, 36864\n"
    "s_add_u32 s84, s58, s62\n"
    "s_lshr_b32 s8, s2, 7\n"
    "s_lshl2_add_u32 s7, s8, s28\n"
    "s_mul_i32 s56, s7, 393216\n"
    "buffer_load_dwordx4 v244, s[44:47], s84 offen lds\n"
    "s_lshr_b32 s29, s2, 2\n"
    "s_mov_b32 s30, s57\n"
    "s_xor_b32 s7, s5, 1\n"
    "buffer_load_dwordx4 v245, s[44:47], s84 offen offset:1024 lds\n"
    "s_mul_i32 s59, s7, 24576\n"
    "s_xor_b32 s7, s5, 2\n"
    "s_mul_i32 s60, s7, 24576\n"
    "buffer_load_dwordx4 v246, s[44:47], s84 offen offset:2048 lds\n"
    "s_xor_b32 s7, s5, 3\n"
    "s_mul_i32 s61, s7, 24576\n"
    "v_mov_b32 v53, s20\n"
    "buffer_load_dwordx4 v247, s[44:47], s84 offen offset:3072 lds\n"
    "s_add_u32 m0, s83, 40960\n"
    "v_mov_b32 v54, s21\n"
    "v_cmp_le_i32 vcc, s22, v16\n"
    "s_sub_u32 s22, s22, 0x400\n"
    "s_mov_b32 s33, 0x90000000\n"
    "v_cndmask_b32 v55, v53, v54, vcc\n"
    "v_lshl_add_u32 v0, v0, 4, v55\n"
    "buffer_load_dwordx4 v248, s[44:47], s84 offen lds\n"
    "v_cmp_le_i32 vcc, s22, v16\n"
    "s_sub_u32 s22, s22, 0x400\n"
    "s_nop 0\n"
    "v_cndmask_b32 v55, v53, v54, vcc\n"
    "v_lshl_add_u32 v1, v1, 4, v55\n"
    "v_cmp_le_i32 vcc, s22, v16\n"
    "s_sub_u32 s22, s22, 0x400\n"
    "s_nop 0\n"
    "v_cndmask_b32 v55, v53, v54, vcc\n"
    "buffer_load_dwordx4 v249, s[44:47], s84 offen offset:1024 lds\n"
    "s_add_u32 m0, s83, 43008\n"
    "s_add_u32 s84, s59, s62\n"
    "v_lshl_add_u32 v2, v2, 4, v55\n"
    "s_add_u32 s7, s83, 36864\n"
    "v_and_b32 v240, 0x70, v16\n"
    "buffer_load_dwordx4 v244, s[44:47], s84 offen lds\n"
    "v_and_b32 v53, 8, v19\n"
    "v_lshl_add_u32 v53, v52, 7, v53\n"
    "v_lshrrev_b32 v54, 2, v16\n"
    "buffer_load_dwordx4 v245, s[44:47], s84 offen offset:1024 lds\n"
    "s_cmp_eq_u32 s35, 0\n"
    "s_cbranch_scc0 73f\n"
    "s_getreg_b32 s88, hwreg(HW_REG_XCC_ID, 0, 3)\n"
    "s_lshl_b32 s89, s2, 3\n"
    "s_and_b32 s89, s89, 0x3f0\n"
    "s_lshr_b32 s82, s2, 7\n"
    "s_lshl3_add_u32 s89, s82, s89\n"
    "v_mov_b32 v20, s89\n"
    "s_lshr_b32 s89, s88, 2\n"
    "s_lshl1_add_u32 s88, s88, s89\n"
    "s_or_b32 s88, s88, s17\n"
    "s_bfe_u32 s89, s2, 0x10001\n"
    "s_xor_b32 s88, s88, s89\n"
    "s_waitcnt lgkmcnt(1)\n"
    "s_nop 3\n"
    "s_cmp_lg_u32 s72, s17\n"
    "s_cselect_b32 s85, 1, 0\n"
    "s_xor_b32 s88, s88, s85\n"
    "v_mov_b32 v23, s88\n"
    "73:\n"
    "v_add3_u32 v15, s7, v53, v240\n"
    "v_add_u32 v38, s7, v54\n"
    "s_lshr_b32 s8, s4, 1\n"
    "buffer_load_dwordx4 v246, s[44:47], s84 offen offset:2048 lds\n"
    "s_lshl_b32 s8, s8, 11\n"
    "s_add_u32 s8, s8, s3\n"
    "s_add_u32 s8, s8, 147456\n"
    "buffer_load_dwordx4 v247, s[44:47], s84 offen offset:3072 lds\n"
    "s_and_b32 s13, s4, 1\n"
    "s_lshl_b32 s14, s13, 8\n"
    "s_sub_u32 s7, s14, s7\n"
    "s_add_u32 s11, s7, s8\n"
    "s_lshl2_add_u32 s8, s14, s8\n"
    "v_and_b32 v53, 0x30, v16\n"
    "v_lshrrev_b32 v54, 2, v19\n"
    "v_lshl_add_u32 v53, v54, 8, v53\n"
    "v_lshl_add_u32 v53, v52, 6, v53\n"
    "v_add_u32 v39, s8, v53\n"
    "v_mov_b32 v251, 0\n"
    "v_mov_b32 v253, 0\n"
    "v_mov_b32 v255, 0\n"
    "s_add_u32 s63, s62, 98304\n"
    "s_cmp_eq_u32 s63, 294912\n"
    "s_cselect_b32 s63, 0, s63\n"
    "s_add_u32 s64, s63, 98304\n"
    "s_cmp_eq_u32 s64, 294912\n"
    "s_cselect_b32 s64, 0, s64\n"
    "s_add_u32 s7, s3, 160768\n"
    "v_mov_b32 v14, 0\n"
    "v_add_u32 v12, s7, v12\n"
    "s_lshl2_add_u32 s7, s4, s7\n"
    "v_mov_b32 v13, s7\n"
    "v_xor_b32 v53, v52, v19\n"
    "v_mul_u32_u24 v54, 0x300, v19\n"
    "v_lshl_add_u32 v54, v53, 4, v54\n"
    "v_add_u32 v4, s3, v54\n"
    "v_xor_b32 v5, 64, v4\n"
    "v_xor_b32 v6, 128, v4\n"
    "v_xor_b32 v7, 192, v4\n"
    "s_lshl_b32 s7, s4, 7\n"
    "s_lshr_b32 s8, s4, 1\n"
    "s_lshl_b32 s8, s8, 7\n"
    "s_add_u32 s8, s8, 512\n"
    "v_add_u32 v241, s8, v240\n"
    "v_add_u32 v240, s7, v240\n"
    "v_lshrrev_b32 v19, 4, v16\n"
    "s_mul_i32 s7, s52, 5\n"
    "s_lshr_b32 s7, s7, 3\n"
    "s_sub_u32 s8, s52, 1\n"
    "s_bitcmp1_b32 s2, 7\n"
    "s_cselect_b32 s7, s7, 0\n"
    "v_add_u32 v53, s7, v19\n"
    "v_cmp_gt_u32 vcc, s8, v19\n"
    "v_subrev_u32 v54, s8, v53\n"
    "v_min_u32 v53, v53, v54\n"
    "v_cndmask_b32 v53, v19, v53, vcc\n"
    "v_mul_lo_u32 v3, v53, s53\n"
    "v_add_u32 v3, s56, v3\n"
    "s_xor_b32 s29, s29, s23\n"
    "s_mul_i32 s7, s52, s53\n"
    "s_add_u32 s7, s7, s56\n"
    "s_mov_b32 s8, -2752512\n"
    "s_bitcmp1_b32 s2, 0\n"
    "s_cselect_b32 s8, -3538944, s8\n"
    "s_add_u32 s7, s7, s8\n"
    "v_cmp_eq_u32 vcc, s52, v19\n"
    "v_mov_b32 v53, s7\n"
    "s_nop 0\n"
    "v_cndmask_b32 v3, v3, v53, vcc\n"
    "v_add_u32 v53, s23, v3\n"
    "v_lshrrev_b32 v53, 21, v53\n"
    "v_and_b32 v232, 56, v53\n"
    "v_and_b32 v233, 4, v53\n"
    "v_xor_b32 v234, s29, v53\n"
    "s_cmp_eq_u32 s35, 0\n"
    "s_cbranch_scc0 71f\n"
    "s_pack_ll_b32_b16 s88, s0, s16\n"
    "s_mov_b64 exec, 1\n"
    "v_mov_b32 v22, s88\n"
    "global_store_dwordx2 v20, v[22:23], s[54:55] sc1\n"
    "s_mov_b64 exec, -1\n"
    "s_waitcnt lgkmcnt(0)\n"
    "71:\n"
    "s_branch 301f\n"
    "81:\n"
    "ds_read_b32 v15, v12\n"
    "s_waitcnt lgkmcnt(0)\n"
    "v_cmp_lt_u32 vcc, v15, v14\n"
    "s_nop 0\n"
    "s_cbranch_vccnz 81b\n"
    "s_branch 82f\n"
    "301:\n"
    "s_branch 302f\n"
    "41:\n"
    "ds_read_b32 v15, v12\n"
    "s_waitcnt lgkmcnt(0)\n"
    "v_cmp_lt_u32 vcc, v15, v14\n"
    "s_nop 0\n"
    "s_cbranch_vccnz 41b\n"
    "s_branch 42f\n"
    "43:\n"
    "s_memrealtime s[92:93]\n"
    "s_waitcnt lgkmcnt(0)\n"
    "s_sub_u32 s84, s92, s89\n"
    "s_cmp_lt_i32 s84, 0\n"
    "s_cbranch_scc0 44f\n"
    "s_sleep 1\n"
    "s_branch 43b\n"
    "302:\n"
    "s_branch 303f\n"
    "61:\n"
    "ds_read_b32 v15, v12\n"
    "s_waitcnt lgkmcnt(0)\n"
    "v_cmp_lt_u32 vcc, v15, v14\n"
    "s_nop 0\n"
    "s_cbranch_vccnz 61b\n"
    "s_branch 62f\n"
    "63:\n"
    "s_memrealtime s[92:93]\n"
    "s_waitcnt lgkmcnt(0)\n"
    "s_sub_u32 s84, s92, s89\n"
    "s_cmp_lt_i32 s84, 0\n"
    "s_cbranch_scc0 64f\n"
    "s_sleep 1\n"
    "s_branch 63b\n"
    "303:\n"
    "s_branch 304f\n"
    "45:\n"
    "s_sub_u32 s7, s86, s71\n"
    "s_cmp_eq_u32 s7, 2\n"
    "s_cbranch_scc1 402f\n"
    "s_cmp_eq_u32 s7, 4\n"
    "s_cbranch_scc1 404f\n"
    "v_mfma_f32_16x16x32_bf16 v[24:27], a[96:99], a[68:71], v[24:27]\n"
    "s_add_u32 s7, s70, 0\n"
    "s_add_u32 s64, s64, 2\n"
    "ds_read_b128 a[136:139], v4 offset:12288\n"
    "v_mfma_f32_16x16x32_bf16 v[24:27], a[92:95], a[44:47], v[24:27]\n"
    "v_add_u32 v14, 1, v14\n"
    "v_accvgpr_write_b32 a188, v44\n"
    "v_mfma_f32_16x16x32_bf16 v[24:27], a[88:91], a[20:23], v[24:27]\n"
    "s_mov_b64 vcc, s[12:13]\n"
    "v_accvgpr_write_b32 a189, v45\n"
    "v_mfma_f32_16x16x32_bf16 v[28:31], a[88:91], a[0:3], 0\n"
    "buffer_load_dwordx4 v0, s[36:39], s81 offen nt lds\n"
    "v_mfma_f32_16x16x32_bf16 v[28:31], a[92:95], a[24:27], v[28:31]\n"
    "s_waitcnt lgkmcnt(1)\n"
    "ds_read_b128 a[140:143], v5 offset:12288\n"
    "v_mfma_f32_16x16x32_bf16 v[28:31], a[96:99], a[48:51], v[28:31]\n"
    "ds_read_b128 a[144:147], v6 offset:12288\n"
    "v_accvgpr_write_b32 a190, v46\n"
    "v_mfma_f32_16x16x32_bf16 v[28:31], a[100:103], a[72:75], v[28:31]\n"
    "v_cvt_pk_bf16_f32 v52, v20, v24\n"
    "v_accvgpr_write_b32 a191, v47\n"
    "v_mfma_f32_16x16x32_bf16 v[28:31], a[104:107], v[64:67], v[28:31]\n"
    "v_cvt_pk_bf16_f32 v53, v21, v25\n"
    "v_accvgpr_write_b32 a192, v48\n"
    "v_mfma_f32_16x16x32_bf16 v[28:31], a[108:111], v[88:91], v[28:31]\n"
    "v_cvt_pk_bf16_f32 v54, v22, v26\n"
    "v_accvgpr_write_b32 a193, v49\n"
    "v_mfma_f32_16x16x32_bf16 v[28:31], a[112:115], v[112:115], v[28:31]\n"
    "v_cvt_pk_bf16_f32 v55, v23, v27\n"
    "v_accvgpr_write_b32 a194, v50\n"
    "v_mfma_f32_16x16x32_bf16 v[28:31], a[116:119], v[136:139], v[28:31]\n"
    "ds_read_b128 a[148:151], v7 offset:12288\n"
    "v_accvgpr_write_b32 a195, v51\n"
    "v_mfma_f32_16x16x32_bf16 v[28:31], a[120:123], v[160:163], v[28:31]\n"
    "v_accvgpr_mov_b32 a196, a184\n"
    "v_readlane_b32 s9, v3, s7\n"
    "v_mfma_f32_16x16x32_bf16 v[28:31], a[124:127], v[184:187], v[28:31]\n"
    "buffer_load_dwordx4 v1, s[36:39], s81 offen offset:1024 nt lds\n"
    "v_mfma_f32_16x16x32_bf16 v[28:31], a[128:131], v[208:211], v[28:31]\n"
    "v_accvgpr_mov_b32 a197, a185\n"
    "v_mfma_f32_16x16x32_bf16 v[28:31], a[132:135], v[232:235], v[28:31]\n"
    "ds_read_b128 a[152:155], v4 offset:12544\n"
    "v_mfma_f32_16x16x32_bf16 v[32:35], a[132:135], v[236:239], 0\n"
    "v_accvgpr_mov_b32 a198, a186\n"
    "v_mfma_f32_16x16x32_bf16 v[32:35], a[128:131], v[212:215], v[32:35]\n"
    "ds_write2st64_b32 v38, v52, v53 offset0:0 offset1:2\n"
    "v_mfma_f32_16x16x32_bf16 v[32:35], a[124:127], v[188:191], v[32:35]\n"
    "v_accvgpr_mov_b32 a199, a187\n"
    "s_branch 46f\n"
    "304:\n"
    "s_branch 305f\n"
    "402:\n"
    "v_mfma_f32_16x16x32_bf16 v[24:27], a[96:99], a[68:71], v[24:27]\n"
    "s_add_u32 s64, s64, 2\n"
    "ds_read_b128 a[136:139], v4 offset:12288\n"
    "v_mfma_f32_16x16x32_bf16 v[24:27], a[92:95], a[44:47], v[24:27]\n"
    "v_add_u32 v14, 1, v14\n"
    "v_mfma_f32_16x16x32_bf16 v[24:27], a[88:91], a[20:23], v[24:27]\n"
    "s_mov_b64 vcc, s[12:13]\n"
    "v_mfma_f32_16x16x32_bf16 v[28:31], a[88:91], a[0:3], 0\n"
    "buffer_store_dwordx4 a[188:191], v8, s[40:43], s9 offen sc1\n"
    "v_mfma_f32_16x16x32_bf16 v[28:31], a[92:95], a[24:27], v[28:31]\n"
    "ds_read_b128 a[140:143], v5 offset:12288\n"
    "v_mfma_f32_16x16x32_bf16 v[28:31], a[96:99], a[48:51], v[28:31]\n"
    "ds_read_b128 a[144:147], v6 offset:12288\n"
    "v_mfma_f32_16x16x32_bf16 v[28:31], a[100:103], a[72:75], v[28:31]\n"
    "v_cvt_pk_bf16_f32 v52, v20, v24\n"
    "v_mfma_f32_16x16x32_bf16 v[28:31], a[104:107], v[64:67], v[28:31]\n"
    "v_cvt_pk_bf16_f32 v53, v21, v25\n"
    "v_mfma_f32_16x16x32_bf16 v[28:31], a[108:111], v[88:91], v[28:31]\n"
    "v_cvt_pk_bf16_f32 v54, v22, v26\n"
    "v_mfma_f32_16x16x32_bf16 v[28:31], a[112:115], v[112:115], v[28:31]\n"
    "v_cvt_pk_bf16_f32 v55, v23, v27\n"
    "v_mfma_f32_16x16x32_bf16 v[28:31], a[116:119], v[136:139], v[28:31]\n"
    "ds_read_b128 a[148:151], v7 offset:12288\n"
    "v_mfma_f32_16x16x32_bf16 v[28:31], a[120:123], v[160:163], v[28:31]\n"
    "v_mfma_f32_16x16x32_bf16 v[28:31], a[124:127], v[184:187], v[28:31]\n"
    "buffer_store_dwordx4 a[192:195], v9, s[40:43], s9 offen sc1\n"
    "v_mfma_f32_16x16x32_bf16 v[28:31], a[128:131], v[208:211], v[28:31]\n"
    "v_mfma_f32_16x16x32_bf16 v[28:31], a[132:135], v[232:235], v[28:31]\n"
    "ds_read_b128 a[152:155], v4 offset:12544\n"
    "v_mfma_f32_16x16x32_bf16 v[32:35], a[132:135], v[236:239], 0\n"
    "v_mfma_f32_16x16x32_bf16 v[32:35], a[128:131], v[212:215], v[32:35]\n"
    "ds_write2st64_b32 v38, v52, v53 offset0:0 offset1:2\n"
    "v_mfma_f32_16x16x32_bf16 v[32:35], a[124:127], v[188:191], v[32:35]\n"
    "s_branch 46f\n"
    "305:\n"
    "s_branch 306f\n"
    "404:\n"
    "v_mfma_f32_16x16x32_bf16 v[24:27], a[96:99], a[68:71], v[24:27]\n"
    "s_sub_u32 s89, s89, 36\n"
    "ds_read_b128 a[136:139], v4 offset:12288\n"
    "v_mfma_f32_16x16x32_bf16 v[24:27], a[92:95], a[44:47], v[24:27]\n"
    "v_add_u32 v14, 1, v14\n"
    "v_mfma_f32_16x16x32_bf16 v[24:27], a[88:91], a[20:23], v[24:27]\n"
    "s_mov_b64 vcc, s[12:13]\n"
    "v_mfma_f32_16x16x32_bf16 v[28:31], a[88:91], a[0:3], 0\n"
    "buffer_store_dwordx4 a[204:207], v9, s[40:43], s10 offen sc1\n"
    "v_mfma_f32_16x16x32_bf16 v[28:31], a[92:95], a[24:27], v[28:31]\n"
    "ds_read_b128 a[140:143], v5 offset:12288\n"
    "v_mfma_f32_16x16x32_bf16 v[28:31], a[96:99], a[48:51], v[28:31]\n"
    "ds_read_b128 a[144:147], v6 offset:12288\n"
    "v_mfma_f32_16x16x32_bf16 v[28:31], a[100:103], a[72:75], v[28:31]\n"
    "v_cvt_pk_bf16_f32 v52, v20, v24\n"
    "v_mfma_f32_16x16x32_bf16 v[28:31], a[104:107], v[64:67], v[28:31]\n"
    "v_cvt_pk_bf16_f32 v53, v21, v25\n"
    "v_mfma_f32_16x16x32_bf16 v[28:31], a[108:111], v[88:91], v[28:31]\n"
    "v_cvt_pk_bf16_f32 v54, v22, v26\n"
    "v_mfma_f32_16x16x32_bf16 v[28:31], a[112:115], v[112:115], v[28:31]\n"
    "v_cvt_pk_bf16_f32 v55, v23, v27\n"
    "v_mfma_f32_16x16x32_bf16 v[28:31], a[116:119], v[136:139], v[28:31]\n"
    "ds_read_b128 a[148:151], v7 offset:12288\n"
    "v_mfma_f32_16x16x32_bf16 v[28:31], a[120:123], v[160:163], v[28:31]\n"
    "v_mfma_f32_16x16x32_bf16 v[28:31], a[124:127], v[184:187], v[28:31]\n"
    "buffer_store_dwordx4 a[208:211], v10, s[40:43], s10 offen sc1\n"
    "v_mfma_f32_16x16x32_bf16 v[28:31], a[128:131], v[208:211], v[28:31]\n"
    "v_mfma_f32_16x16x32_bf16 v[28:31], a[132:135], v[232:235], v[28:31]\n"
    "ds_read_b128 a[152:155], v4 offset:12544\n"
    "v_mfma_f32_16x16x32_bf16 v[32:35], a[132:135], v[236:239], 0\n"
    "v_mfma_f32_16x16x32_bf16 v[32:35], a[128:131], v[212:215], v[32:35]\n"
    "ds_write2st64_b32 v38, v52, v53 offset0:0 offset1:2\n"
    "v_mfma_f32_16x16x32_bf16 v[32:35], a[124:127], v[188:191], v[32:35]\n"
    "s_branch 46f\n"
    "306:\n"
    "s_branch 307f\n"
    "65:\n"
    "s_sub_u32 s7, s86, s71\n"
    "s_cmp_eq_u32 s7, 3\n"
    "s_cbranch_scc1 403f\n"
    "v_mfma_f32_16x16x32_bf16 v[32:35], a[144:147], a[68:71], v[32:35]\n"
    "s_add_u32 s7, s70, 1\n"
    "s_add_u32 s65, s65, 2\n"
    "ds_read_b128 a[88:91], v4\n"
    "v_mfma_f32_16x16x32_bf16 v[32:35], a[140:143], a[44:47], v[32:35]\n"
    "v_add_u32 v7, s78, v7\n"
    "v_accvgpr_write_b32 a200, v44\n"
    "v_mfma_f32_16x16x32_bf16 v[32:35], a[136:139], a[20:23], v[32:35]\n"
    "v_add_u32 v14, 1, v14\n"
    "v_accvgpr_write_b32 a201, v45\n"
    "v_mfma_f32_16x16x32_bf16 v[20:23], a[136:139], a[0:3], 0\n"
    "buffer_load_dwordx4 v0, s[36:39], s81 offen nt lds\n"
    "v_mfma_f32_16x16x32_bf16 v[20:23], a[140:143], a[24:27], v[20:23]\n"
    "s_waitcnt lgkmcnt(1)\n"
    "ds_read_b128 a[92:95], v5\n"
    "v_mfma_f32_16x16x32_bf16 v[20:23], a[144:147], a[48:51], v[20:23]\n"
    "ds_read_b128 a[96:99], v6\n"
    "v_accvgpr_mov_b32 a211, a187\n"
    "v_mfma_f32_16x16x32_bf16 v[20:23], a[148:151], a[72:75], v[20:23]\n"
    "v_cvt_pk_bf16_f32 v52, v28, v32\n"
    "v_accvgpr_write_b32 a202, v46\n"
    "v_mfma_f32_16x16x32_bf16 v[20:23], a[152:155], v[64:67], v[20:23]\n"
    "v_cvt_pk_bf16_f32 v53, v29, v33\n"
    "v_accvgpr_write_b32 a203, v47\n"
    "v_mfma_f32_16x16x32_bf16 v[20:23], a[156:159], v[88:91], v[20:23]\n"
    "v_cvt_pk_bf16_f32 v54, v30, v34\n"
    "v_accvgpr_write_b32 a204, v48\n"
    "v_mfma_f32_16x16x32_bf16 v[20:23], a[160:163], v[112:115], v[20:23]\n"
    "v_cvt_pk_bf16_f32 v55, v31, v35\n"
    "v_accvgpr_write_b32 a205, v49\n"
    "v_mfma_f32_16x16x32_bf16 v[20:23], a[164:167], v[136:139], v[20:23]\n"
    "ds_read_b128 a[100:103], v7\n"
    "v_accvgpr_write_b32 a206, v50\n"
    "v_mfma_f32_16x16x32_bf16 v[20:23], a[168:171], v[160:163], v[20:23]\n"
    "s_mov_b64 vcc, s[12:13]\n"
    "v_accvgpr_write_b32 a207, v51\n"
    "v_mfma_f32_16x16x32_bf16 v[20:23], a[172:175], v[184:187], v[20:23]\n"
    "buffer_load_dwordx4 v1, s[36:39], s81 offen offset:1024 nt lds\n"
    "v_mfma_f32_16x16x32_bf16 v[20:23], a[176:179], v[208:211], v[20:23]\n"
    "v_accvgpr_mov_b32 a208, a184\n"
    "v_readlane_b32 s10, v3, s7\n"
    "v_mfma_f32_16x16x32_bf16 v[20:23], a[180:183], v[232:235], v[20:23]\n"
    "ds_read_b128 a[104:107], v4 offset:256\n"
    "v_mfma_f32_16x16x32_bf16 v[24:27], a[180:183], v[236:239], 0\n"
    "v_accvgpr_mov_b32 a209, a185\n"
    "v_mfma_f32_16x16x32_bf16 v[24:27], a[176:179], v[212:215], v[24:27]\n"
    "ds_write2st64_b32 v38, v52, v53 offset0:16 offset1:18\n"
    "v_mfma_f32_16x16x32_bf16 v[24:27], a[172:175], v[188:191], v[24:27]\n"
    "v_accvgpr_mov_b32 a210, a186\n"
    "s_branch 66f\n"
    "307:\n"
    "s_branch 308f\n"
    "403:\n"
    "v_mfma_f32_16x16x32_bf16 v[32:35], a[144:147], a[68:71], v[32:35]\n"
    "s_sub_u32 s89, s89, 36\n"
    "ds_read_b128 a[88:91], v4\n"
    "v_mfma_f32_16x16x32_bf16 v[32:35], a[140:143], a[44:47], v[32:35]\n"
    "v_add_u32 v7, s78, v7\n"
    "v_mfma_f32_16x16x32_bf16 v[32:35], a[136:139], a[20:23], v[32:35]\n"
    "v_add_u32 v14, 1, v14\n"
    "v_mfma_f32_16x16x32_bf16 v[20:23], a[136:139], a[0:3], 0\n"
    "buffer_store_dwordx4 a[196:199], v10, s[40:43], s9 offen sc1\n"
    "v_mfma_f32_16x16x32_bf16 v[20:23], a[140:143], a[24:27], v[20:23]\n"
    "ds_read_b128 a[92:95], v5\n"
    "v_mfma_f32_16x16x32_bf16 v[20:23], a[144:147], a[48:51], v[20:23]\n"
    "ds_read_b128 a[96:99], v6\n"
    "v_mfma_f32_16x16x32_bf16 v[20:23], a[148:151], a[72:75], v[20:23]\n"
    "v_cvt_pk_bf16_f32 v52, v28, v32\n"
    "v_mfma_f32_16x16x32_bf16 v[20:23], a[152:155], v[64:67], v[20:23]\n"
    "v_cvt_pk_bf16_f32 v53, v29, v33\n"
    "v_mfma_f32_16x16x32_bf16 v[20:23], a[156:159], v[88:91], v[20:23]\n"
    "v_cvt_pk_bf16_f32 v54, v30, v34\n"
    "v_mfma_f32_16x16x32_bf16 v[20:23], a[160:163], v[112:115], v[20:23]\n"
    "v_cvt_pk_bf16_f32 v55, v31, v35\n"
    "v_mfma_f32_16x16x32_bf16 v[20:23], a[164:167], v[136:139], v[20:23]\n"
    "ds_read_b128 a[100:103], v7\n"
    "v_mfma_f32_16x16x32_bf16 v[20:23], a[168:171], v[160:163], v[20:23]\n"
    "s_mov_b64 vcc, s[12:13]\n"
    "v_mfma_f32_16x16x32_bf16 v[20:23], a[172:175], v[184:187], v[20:23]\n"
    "buffer_store_dwordx4 a[200:203], v8, s[40:43], s10 offen sc1\n"
    "v_mfma_f32_16x16x32_bf16 v[20:23], a[176:179], v[208:211], v[20:23]\n"
    "v_mfma_f32_16x16x32_bf16 v[20:23], a[180:183], v[232:235], v[20:23]\n"
    "ds_read_b128 a[104:107], v4 offset:256\n"
    "v_mfma_f32_16x16x32_bf16 v[24:27], a[180:183], v[236:239], 0\n"
    "v_mfma_f32_16x16x32_bf16 v[24:27], a[176:179], v[212:215], v[24:27]\n"
    "ds_write2st64_b32 v38, v52, v53 offset0:16 offset1:18\n"
    "v_mfma_f32_16x16x32_bf16 v[24:27], a[172:175], v[188:191], v[24:27]\n"
    "s_branch 66f\n"
    "308:\n"
    "s_waitcnt vmcnt(10)\n"
    "ds_write_b32 v13, v14\n"
    "ds_read_u16 v250, v18\n"
    "ds_read_u16 v252, v18 offset:8\n"
    "ds_read_u16 v254, v235\n"
    "s_waitcnt vmcnt(9)\n"
    "ds_read2st64_b64 a[188:191], v15 offset0:0 offset1:1\n"
    "s_waitcnt vmcnt(8)\n"
    "ds_read2st64_b64 a[192:195], v15 offset0:2 offset1:3\n"
    "s_waitcnt vmcnt(7)\n"
    "ds_read2st64_b64 a[196:199], v15 offset0:4 offset1:5\n"
    "s_waitcnt lgkmcnt(3)\n"
    "v_lshlrev_b64 v[40:41], v16, v[250:251]\n"
    "v_lshlrev_b64 v[42:43], v16, v[252:253]\n"
    "v_lshlrev_b64 v[44:45], v16, v[254:255]\n"
    "s_waitcnt vmcnt(6) lgkmcnt(2)\n"
    "ds_read2st64_b64 a[200:203], v15 offset0:6 offset1:7\n"
    "s_nop 0\n"
    "v_mfma_f32_4x4x4_16b_bf16 v[20:23], v[40:41], a[188:189], 0 cbsz:2 abid:0\n"
    "ds_read_u16 v250, v18 offset:64\n"
    "s_add_u32 m0, s83, 47104\n"
    "v_mfma_f32_4x4x4_16b_bf16 v[24:27], v[40:41], a[190:191], 0 cbsz:2 abid:1\n"
    "buffer_load_dwordx4 v248, s[44:47], s84 offen lds\n"
    "v_add_u32 v8, v8, v240\n"
    "s_waitcnt vmcnt(6) lgkmcnt(3)\n"
    "ds_read2st64_b32 a[204:205], v38 offset0:16 offset1:17\n"
    "v_mfma_f32_4x4x4_16b_bf16 v[28:31], v[40:41], a[192:193], 0 cbsz:2 abid:2\n"
    "ds_read2st64_b32 a[206:207], v38 offset0:18 offset1:19\n"
    "ds_read_u16 v252, v18 offset:72\n"
    "v_cvt_pk_bf16_f32 v52, v20, v24\n"
    "v_cvt_pk_bf16_f32 v53, v21, v25\n"
    "v_accvgpr_write_b32 a0, v52\n"
    "v_accvgpr_write_b32 a4, v53\n"
    "v_mfma_f32_4x4x4_16b_bf16 v[32:35], v[40:41], a[194:195], 0 cbsz:2 abid:3\n"
    "buffer_load_dwordx4 v249, s[44:47], s84 offen offset:1024 lds\n"
    "v_cvt_pk_bf16_f32 v54, v22, v26\n"
    "v_cvt_pk_bf16_f32 v55, v23, v27\n"
    "v_accvgpr_write_b32 a8, v54\n"
    "v_accvgpr_write_b32 a12, v55\n"
    "v_add_u32 v9, v9, v240\n"
    "s_waitcnt vmcnt(6) lgkmcnt(5)\n"
    "ds_read2st64_b32 a[208:209], v38 offset0:20 offset1:21\n"
    "v_mfma_f32_4x4x4_16b_bf16 v[20:23], v[42:43], a[196:197], 0 cbsz:2 abid:0\n"
    "ds_read2st64_b32 a[210:211], v38 offset0:22 offset1:23\n"
    "ds_read_u16 v254, v235 offset:64\n"
    "s_add_u32 m0, s83, 49152\n"
    "s_add_u32 s84, s60, s62\n"
    "v_cvt_pk_bf16_f32 v52, v28, v32\n"
    "v_cvt_pk_bf16_f32 v53, v29, v33\n"
    "v_accvgpr_write_b32 a1, v52\n"
    "v_accvgpr_write_b32 a5, v53\n"
    "v_mfma_f32_4x4x4_16b_bf16 v[24:27], v[42:43], a[198:199], 0 cbsz:2 abid:1\n"
    "buffer_load_dwordx4 v244, s[44:47], s84 offen lds\n"
    "v_cvt_pk_bf16_f32 v54, v30, v34\n"
    "v_cvt_pk_bf16_f32 v55, v31, v35\n"
    "v_accvgpr_write_b32 a9, v54\n"
    "v_accvgpr_write_b32 a13, v55\n"
    "v_add_u32 v10, v10, v241\n"
    "s_waitcnt vmcnt(6) lgkmcnt(6)\n"
    "ds_read2st64_b64 a[188:191], v15 offset0:12 offset1:13\n"
    "v_mfma_f32_4x4x4_16b_bf16 v[28:31], v[42:43], a[200:201], 0 cbsz:2 abid:2\n"
    "v_cvt_pk_bf16_f32 v52, v20, v24\n"
    "v_cvt_pk_bf16_f32 v53, v21, v25\n"
    "v_accvgpr_write_b32 a2, v52\n"
    "v_accvgpr_write_b32 a6, v53\n"
    "v_mfma_f32_4x4x4_16b_bf16 v[32:35], v[42:43], a[202:203], 0 cbsz:2 abid:3\n"
    "buffer_load_dwordx4 v245, s[44:47], s84 offen offset:1024 lds\n"
    "v_cvt_pk_bf16_f32 v54, v22, v26\n"
    "v_cvt_pk_bf16_f32 v55, v23, v27\n"
    "v_accvgpr_write_b32 a10, v54\n"
    "v_accvgpr_write_b32 a14, v55\n"
    "v_lshlrev_b64 v[46:47], v16, v[250:251]\n"
    "s_mov_b32 s12, 0xff00ff\n"
    "s_waitcnt vmcnt(6) lgkmcnt(4)\n"
    "ds_read2st64_b64 a[192:195], v15 offset0:14 offset1:15\n"
    "v_mfma_f32_4x4x4_16b_bf16 v[20:23], v[44:45], a[204:205], 0 cbsz:2 abid:0\n"
    "v_cvt_pk_bf16_f32 v52, v28, v32\n"
    "v_cvt_pk_bf16_f32 v53, v29, v33\n"
    "v_accvgpr_write_b32 a3, v52\n"
    "v_accvgpr_write_b32 a7, v53\n"
    "v_mfma_f32_4x4x4_16b_bf16 v[24:27], v[44:45], a[206:207], 0 cbsz:2 abid:1\n"
    "buffer_load_dwordx4 v246, s[44:47], s84 offen offset:2048 lds\n"
    "v_cvt_pk_bf16_f32 v54, v30, v34\n"
    "v_cvt_pk_bf16_f32 v55, v31, v35\n"
    "v_accvgpr_write_b32 a11, v54\n"
    "v_accvgpr_write_b32 a15, v55\n"
    "buffer_load_dwordx4 v247, s[44:47], s84 offen offset:3072 lds\n"
    "v_lshlrev_b64 v[48:49], v16, v[252:253]\n"
    "s_mov_b32 s13, 0xff00ff\n"
    "s_waitcnt vmcnt(7) lgkmcnt(2)\n"
    "ds_read2st64_b64 a[196:199], v15 offset0:16 offset1:17\n"
    "v_mfma_f32_4x4x4_16b_bf16 v[28:31], v[44:45], a[208:209], 0 cbsz:2 abid:2\n"
    "s_add_u32 m0, s83, 53248\n"
    "v_cvt_pk_bf16_f32 v52, v20, v22\n"
    "v_cvt_pk_bf16_f32 v53, v21, v23\n"
    "v_accvgpr_write_b32 a16, v52\n"
    "v_accvgpr_write_b32 a20, v53\n"
    "v_mfma_f32_4x4x4_16b_bf16 v[32:35], v[44:45], a[210:211], 0 cbsz:2 abid:3\n"
    "buffer_load_dwordx4 v248, s[44:47], s84 offen lds\n"
    "v_cvt_pk_bf16_f32 v54, v24, v26\n"
    "v_cvt_pk_bf16_f32 v55, v25, v27\n"
    "v_accvgpr_write_b32 a17, v54\n"
    "v_accvgpr_write_b32 a21, v55\n"
    "v_lshlrev_b64 v[50:51], v16, v[254:255]\n"
    "s_add_u32 s77, s57, 36864\n"
    "s_waitcnt vmcnt(7) lgkmcnt(2)\n"
    "ds_read2st64_b64 a[200:203], v15 offset0:18 offset1:19\n"
    "v_mfma_f32_4x4x4_16b_bf16 v[20:23], v[46:47], a[188:189], 0 cbsz:2 abid:0\n"
    "ds_read_u16 v250, v18 offset:128\n"
    "v_cvt_pk_bf16_f32 v52, v28, v30\n"
    "v_cvt_pk_bf16_f32 v53, v29, v31\n"
    "v_accvgpr_write_b32 a18, v52\n"
    "v_accvgpr_write_b32 a22, v53\n"
    "v_mfma_f32_4x4x4_16b_bf16 v[24:27], v[46:47], a[190:191], 0 cbsz:2 abid:1\n"
    "buffer_load_dwordx4 v249, s[44:47], s84 offen offset:1024 lds\n"
    "v_cvt_pk_bf16_f32 v54, v32, v34\n"
    "v_cvt_pk_bf16_f32 v55, v33, v35\n"
    "v_accvgpr_write_b32 a19, v54\n"
    "v_accvgpr_write_b32 a23, v55\n"
    "s_movk_i32 s88, 24576\n"
    "s_waitcnt vmcnt(7) lgkmcnt(3)\n"
    "ds_read2st64_b32 a[204:205], v38 offset0:40 offset1:41\n"
    "v_mfma_f32_4x4x4_16b_bf16 v[28:31], v[46:47], a[192:193], 0 cbsz:2 abid:2\n"
    "ds_read2st64_b32 a[206:207], v38 offset0:42 offset1:43\n"
    "ds_read_u16 v252, v18 offset:136\n"
    "s_add_u32 m0, s83, 55296\n"
    "s_add_u32 s84, s61, s62\n"
    "v_cvt_pk_bf16_f32 v52, v20, v24\n"
    "v_cvt_pk_bf16_f32 v53, v21, v25\n"
    "v_accvgpr_write_b32 a24, v52\n"
    "v_accvgpr_write_b32 a28, v53\n"
    "v_mfma_f32_4x4x4_16b_bf16 v[32:35], v[46:47], a[194:195], 0 cbsz:2 abid:3\n"
    "buffer_load_dwordx4 v244, s[44:47], s84 offen lds\n"
    "v_cvt_pk_bf16_f32 v54, v22, v26\n"
    "v_cvt_pk_bf16_f32 v55, v23, v27\n"
    "v_accvgpr_write_b32 a32, v54\n"
    "v_accvgpr_write_b32 a36, v55\n"
    "buffer_load_dwordx4 v245, s[44:47], s84 offen offset:1024 lds\n"
    "s_movk_i32 s78, 24576\n"
    "s_waitcnt vmcnt(8) lgkmcnt(5)\n"
    "ds_read2st64_b32 a[208:209], v38 offset0:44 offset1:45\n"
    "v_mfma_f32_4x4x4_16b_bf16 v[20:23], v[48:49], a[196:197], 0 cbsz:2 abid:0\n"
    "ds_read2st64_b32 a[210:211], v38 offset0:46 offset1:47\n"
    "ds_read_u16 v254, v235 offset:128\n"
    "v_cvt_pk_bf16_f32 v52, v28, v32\n"
    "v_cvt_pk_bf16_f32 v53, v29, v33\n"
    "v_accvgpr_write_b32 a25, v52\n"
    "v_accvgpr_write_b32 a29, v53\n"
    "v_mfma_f32_4x4x4_16b_bf16 v[24:27], v[48:49], a[198:199], 0 cbsz:2 abid:1\n"
    "buffer_load_dwordx4 v246, s[44:47], s84 offen offset:2048 lds\n"
    "v_cvt_pk_bf16_f32 v54, v30, v34\n"
    "v_cvt_pk_bf16_f32 v55, v31, v35\n"
    "v_accvgpr_write_b32 a33, v54\n"
    "v_accvgpr_write_b32 a37, v55\n"
    "s_mov_b32 s87, -122880\n"
    "s_waitcnt vmcnt(8) lgkmcnt(6)\n"
    "ds_read2st64_b64 a[188:191], v15 offset0:24 offset1:25\n"
    "v_mfma_f32_4x4x4_16b_bf16 v[28:31], v[48:49], a[200:201], 0 cbsz:2 abid:2\n"
    "v_cvt_pk_bf16_f32 v52, v20, v24\n"
    "v_cvt_pk_bf16_f32 v53, v21, v25\n"
    "v_accvgpr_write_b32 a26, v52\n"
    "v_accvgpr_write_b32 a30, v53\n"
    "v_mfma_f32_4x4x4_16b_bf16 v[32:35], v[48:49], a[202:203], 0 cbsz:2 abid:3\n"
    "buffer_load_dwordx4 v247, s[44:47], s84 offen offset:3072 lds\n"
    "v_cvt_pk_bf16_f32 v54, v22, v26\n"
    "v_cvt_pk_bf16_f32 v55, v23, v27\n"
    "v_accvgpr_write_b32 a34, v54\n"
    "v_accvgpr_write_b32 a38, v55\n"
    "v_lshlrev_b64 v[40:41], v16, v[250:251]\n"
    "s_add_u32 s80, s57, 147456\n"
    "s_waitcnt vmcnt(8) lgkmcnt(4)\n"
    "ds_read2st64_b64 a[192:195], v15 offset0:26 offset1:27\n"
    "v_mfma_f32_4x4x4_16b_bf16 v[20:23], v[50:51], a[204:205], 0 cbsz:2 abid:0\n"
    "s_add_u32 m0, s83, 59392\n"
    "v_cvt_pk_bf16_f32 v52, v28, v32\n"
    "v_cvt_pk_bf16_f32 v53, v29, v33\n"
    "v_accvgpr_write_b32 a27, v52\n"
    "v_accvgpr_write_b32 a31, v53\n"
    "v_mfma_f32_4x4x4_16b_bf16 v[24:27], v[50:51], a[206:207], 0 cbsz:2 abid:1\n"
    "buffer_load_dwordx4 v248, s[44:47], s84 offen lds\n"
    "v_cvt_pk_bf16_f32 v54, v30, v34\n"
    "v_cvt_pk_bf16_f32 v55, v31, v35\n"
    "v_accvgpr_write_b32 a35, v54\n"
    "v_accvgpr_write_b32 a39, v55\n"
    "v_lshlrev_b64 v[42:43], v16, v[252:253]\n"
    "s_mov_b32 s86, 5\n"
    "s_waitcnt vmcnt(8) lgkmcnt(2)\n"
    "ds_read2st64_b64 a[196:199], v15 offset0:28 offset1:29\n"
    "v_mfma_f32_4x4x4_16b_bf16 v[28:31], v[50:51], a[208:209], 0 cbsz:2 abid:2\n"
    "v_cvt_pk_bf16_f32 v52, v20, v22\n"
    "v_cvt_pk_bf16_f32 v53, v21, v23\n"
    "v_accvgpr_write_b32 a40, v52\n"
    "v_accvgpr_write_b32 a44, v53\n"
    "v_mfma_f32_4x4x4_16b_bf16 v[32:35], v[50:51], a[210:211], 0 cbsz:2 abid:3\n"
    "buffer_load_dwordx4 v249, s[44:47], s84 offen offset:1024 lds\n"
    "s_add_u32 m0, s83, 61440\n"
    "s_add_u32 s84, s58, s63\n"
    "v_cvt_pk_bf16_f32 v54, v24, v26\n"
    "v_cvt_pk_bf16_f32 v55, v25, v27\n"
    "v_accvgpr_write_b32 a41, v54\n"
    "v_accvgpr_write_b32 a45, v55\n"
    "buffer_load_dwordx4 v244, s[44:47], s84 offen lds\n"
    "v_lshlrev_b64 v[44:45], v16, v[254:255]\n"
    "s_lshr_b32 s7, s2, 1\n"
    "s_waitcnt vmcnt(9) lgkmcnt(2)\n"
    "s_barrier\n"
    "ds_read2st64_b64 a[200:203], v15 offset0:30 offset1:31\n"
    "v_mfma_f32_4x4x4_16b_bf16 v[20:23], v[40:41], a[188:189], 0 cbsz:2 abid:0\n"
    "ds_read_u16 v250, v18 offset:192\n"
    "v_cvt_pk_bf16_f32 v52, v28, v30\n"
    "v_cvt_pk_bf16_f32 v53, v29, v31\n"
    "v_accvgpr_write_b32 a42, v52\n"
    "v_accvgpr_write_b32 a46, v53\n"
    "v_mfma_f32_4x4x4_16b_bf16 v[24:27], v[40:41], a[190:191], 0 cbsz:2 abid:1\n"
    "buffer_load_dwordx4 v245, s[44:47], s84 offen offset:1024 lds\n"
    "v_cvt_pk_bf16_f32 v54, v32, v34\n"
    "v_cvt_pk_bf16_f32 v55, v33, v35\n"
    "v_accvgpr_write_b32 a43, v54\n"
    "v_accvgpr_write_b32 a47, v55\n"
    "s_and_b32 s7, s7, 0x7c\n"
    "s_waitcnt vmcnt(9) lgkmcnt(3)\n"
    "ds_read2st64_b32 a[204:205], v38 offset0:64 offset1:65\n"
    "v_mfma_f32_4x4x4_16b_bf16 v[28:31], v[40:41], a[192:193], 0 cbsz:2 abid:2\n"
    "ds_read2st64_b32 a[206:207], v38 offset0:66 offset1:67\n"
    "ds_read_u16 v252, v18 offset:200\n"
    "v_cvt_pk_bf16_f32 v52, v20, v24\n"
    "v_cvt_pk_bf16_f32 v53, v21, v25\n"
    "v_accvgpr_write_b32 a48, v52\n"
    "v_accvgpr_write_b32 a52, v53\n"
    "v_mfma_f32_4x4x4_16b_bf16 v[32:35], v[40:41], a[194:195], 0 cbsz:2 abid:3\n"
    "buffer_load_dwordx4 v246, s[44:47], s84 offen offset:2048 lds\n"
    "v_cvt_pk_bf16_f32 v54, v22, v26\n"
    "v_cvt_pk_bf16_f32 v55, v23, v27\n"
    "v_accvgpr_write_b32 a56, v54\n"
    "v_accvgpr_write_b32 a60, v55\n"
    "s_or_b32 s7, s7, s28\n"
    "s_waitcnt vmcnt(9) lgkmcnt(5)\n"
    "ds_read2st64_b32 a[208:209], v38 offset0:68 offset1:69\n"
    "v_mfma_f32_4x4x4_16b_bf16 v[20:23], v[42:43], a[196:197], 0 cbsz:2 abid:0\n"
    "ds_read2st64_b32 a[210:211], v38 offset0:70 offset1:71\n"
    "ds_read_u16 v254, v235 offset:192\n"
    "v_cvt_pk_bf16_f32 v52, v28, v32\n"
    "v_cvt_pk_bf16_f32 v53, v29, v33\n"
    "v_accvgpr_write_b32 a49, v52\n"
    "v_accvgpr_write_b32 a53, v53\n"
    "v_mfma_f32_4x4x4_16b_bf16 v[24:27], v[42:43], a[198:199], 0 cbsz:2 abid:1\n"
    "buffer_load_dwordx4 v247, s[44:47], s84 offen offset:3072 lds\n"
    "v_cvt_pk_bf16_f32 v54, v30, v34\n"
    "v_cvt_pk_bf16_f32 v55, v31, v35\n"
    "v_accvgpr_write_b32 a57, v54\n"
    "v_accvgpr_write_b32 a61, v55\n"
    "s_mul_i32 s7, s7, 79\n"
    "s_waitcnt vmcnt(9) lgkmcnt(6)\n"
    "ds_read2st64_b64 a[188:191], v15 offset0:36 offset1:37\n"
    "v_mfma_f32_4x4x4_16b_bf16 v[28:31], v[42:43], a[200:201], 0 cbsz:2 abid:2\n"
    "s_add_u32 m0, s83, 65536\n"
    "v_cvt_pk_bf16_f32 v52, v20, v24\n"
    "v_cvt_pk_bf16_f32 v53, v21, v25\n"
    "v_accvgpr_write_b32 a50, v52\n"
    "v_accvgpr_write_b32 a54, v53\n"
    "v_mfma_f32_4x4x4_16b_bf16 v[32:35], v[42:43], a[202:203], 0 cbsz:2 abid:3\n"
    "buffer_load_dwordx4 v248, s[44:47], s84 offen lds\n"
    "v_cvt_pk_bf16_f32 v54, v22, v26\n"
    "v_cvt_pk_bf16_f32 v55, v23, v27\n"
    "v_accvgpr_write_b32 a58, v54\n"
    "v_accvgpr_write_b32 a62, v55\n"
    "buffer_load_dwordx4 v249, s[44:47], s84 offen offset:1024 lds\n"
    "v_lshlrev_b64 v[46:47], v16, v[250:251]\n"
    "s_and_b32 s7, s7, 0x7f\n"
    "s_waitcnt vmcnt(10) lgkmcnt(4)\n"
    "ds_read2st64_b64 a[192:195], v15 offset0:38 offset1:39\n"
    "v_mfma_f32_4x4x4_16b_bf16 v[20:23], v[44:45], a[204:205], 0 cbsz:2 abid:0\n"
    "s_add_u32 m0, s83, 36864\n"
    "s_add_u32 s84, s59, s63\n"
    "v_cvt_pk_bf16_f32 v52, v28, v32\n"
    "v_cvt_pk_bf16_f32 v53, v29, v33\n"
    "v_accvgpr_write_b32 a51, v52\n"
    "v_accvgpr_write_b32 a55, v53\n"
    "v_mfma_f32_4x4x4_16b_bf16 v[24:27], v[44:45], a[206:207], 0 cbsz:2 abid:1\n"
    "buffer_load_dwordx4 v244, s[44:47], s84 offen lds\n"
    "v_cvt_pk_bf16_f32 v54, v30, v34\n"
    "v_cvt_pk_bf16_f32 v55, v31, v35\n"
    "v_accvgpr_write_b32 a59, v54\n"
    "v_accvgpr_write_b32 a63, v55\n"
    "v_lshlrev_b64 v[48:49], v16, v[252:253]\n"
    "s_mul_i32 s7, s7, 50\n"
    "s_waitcnt vmcnt(10) lgkmcnt(2)\n"
    "ds_read2st64_b64 a[196:199], v15 offset0:40 offset1:41\n"
    "v_mfma_f32_4x4x4_16b_bf16 v[28:31], v[44:45], a[208:209], 0 cbsz:2 abid:2\n"
    "v_cvt_pk_bf16_f32 v52, v20, v22\n"
    "v_cvt_pk_bf16_f32 v53, v21, v23\n"
    "v_accvgpr_write_b32 a64, v52\n"
    "v_accvgpr_write_b32 a68, v53\n"
    "v_mfma_f32_4x4x4_16b_bf16 v[32:35], v[44:45], a[210:211], 0 cbsz:2 abid:3\n"
    "buffer_load_dwordx4 v245, s[44:47], s84 offen offset:1024 lds\n"
    "v_cvt_pk_bf16_f32 v54, v24, v26\n"
    "v_cvt_pk_bf16_f32 v55, v25, v27\n"
    "v_accvgpr_write_b32 a65, v54\n"
    "v_accvgpr_write_b32 a69, v55\n"
    "v_lshlrev_b64 v[50:51], v16, v[254:255]\n"
    "s_lshr_b32 s7, s7, 6\n"
    "s_waitcnt vmcnt(10) lgkmcnt(2)\n"
    "ds_read2st64_b64 a[200:203], v15 offset0:42 offset1:43\n"
    "v_mfma_f32_4x4x4_16b_bf16 v[20:23], v[46:47], a[188:189], 0 cbsz:2 abid:0\n"
    "ds_read_u16 v250, v18 offset:256\n"
    "v_cvt_pk_bf16_f32 v52, v28, v30\n"
    "v_cvt_pk_bf16_f32 v53, v29, v31\n"
    "v_accvgpr_write_b32 a66, v52\n"
    "v_accvgpr_write_b32 a70, v53\n"
    "v_mfma_f32_4x4x4_16b_bf16 v[24:27], v[46:47], a[190:191], 0 cbsz:2 abid:1\n"
    "buffer_load_dwordx4 v246, s[44:47], s84 offen offset:2048 lds\n"
    "v_cvt_pk_bf16_f32 v54, v32, v34\n"
    "v_cvt_pk_bf16_f32 v55, v33, v35\n"
    "v_accvgpr_write_b32 a67, v54\n"
    "v_accvgpr_write_b32 a71, v55\n"
    "s_sub_i32 s7, s7, 82\n"
    "s_waitcnt vmcnt(10) lgkmcnt(3)\n"
    "ds_read2st64_b32 a[204:205], v38 offset0:88 offset1:89\n"
    "v_mfma_f32_4x4x4_16b_bf16 v[28:31], v[46:47], a[192:193], 0 cbsz:2 abid:2\n"
    "ds_read2st64_b32 a[206:207], v38 offset0:90 offset1:91\n"
    "ds_read_u16 v252, v18 offset:264\n"
    "v_cvt_pk_bf16_f32 v52, v20, v24\n"
    "v_cvt_pk_bf16_f32 v53, v21, v25\n"
    "v_accvgpr_write_b32 a72, v52\n"
    "v_accvgpr_write_b32 a76, v53\n"
    "v_mfma_f32_4x4x4_16b_bf16 v[32:35], v[46:47], a[194:195], 0 cbsz:2 abid:3\n"
    "buffer_load_dwordx4 v247, s[44:47], s84 offen offset:3072 lds\n"
    "s_add_u32 m0, s83, 40960\n"
    "v_cvt_pk_bf16_f32 v54, v22, v26\n"
    "v_cvt_pk_bf16_f32 v55, v23, v27\n"
    "v_accvgpr_write_b32 a80, v54\n"
    "v_accvgpr_write_b32 a84, v55\n"
    "buffer_load_dwordx4 v248, s[44:47], s84 offen lds\n"
    "s_max_i32 s7, s7, 0\n"
    "s_waitcnt vmcnt(11) lgkmcnt(5)\n"
    "ds_read2st64_b32 a[208:209], v38 offset0:92 offset1:93\n"
    "v_mfma_f32_4x4x4_16b_bf16 v[20:23], v[48:49], a[196:197], 0 cbsz:2 abid:0\n"
    "ds_read2st64_b32 a[210:211], v38 offset0:94 offset1:95\n"
    "ds_read_u16 v254, v235 offset:256\n"
    "v_cvt_pk_bf16_f32 v52, v28, v32\n"
    "v_cvt_pk_bf16_f32 v53, v29, v33\n"
    "v_accvgpr_write_b32 a73, v52\n"
    "v_accvgpr_write_b32 a77, v53\n"
    "v_mfma_f32_4x4x4_16b_bf16 v[24:27], v[48:49], a[198:199], 0 cbsz:2 abid:1\n"
    "buffer_load_dwordx4 v249, s[44:47], s84 offen offset:1024 lds\n"
    "v_cvt_pk_bf16_f32 v54, v30, v34\n"
    "v_cvt_pk_bf16_f32 v55, v31, v35\n"
    "v_accvgpr_write_b32 a81, v54\n"
    "v_accvgpr_write_b32 a85, v55\n"
    "s_min_i32 s7, s7, 50\n"
    "s_waitcnt vmcnt(11) lgkmcnt(6)\n"
    "ds_read2st64_b64 a[188:191], v15 offset0:48 offset1:49\n"
    "v_mfma_f32_4x4x4_16b_bf16 v[28:31], v[48:49], a[200:201], 0 cbsz:2 abid:2\n"
    "s_add_u32 m0, s83, 43008\n"
    "s_add_u32 s84, s60, s63\n"
    "v_cvt_pk_bf16_f32 v52, v20, v24\n"
    "v_cvt_pk_bf16_f32 v53, v21, v25\n"
    "v_accvgpr_write_b32 a74, v52\n"
    "v_accvgpr_write_b32 a78, v53\n"
    "v_mfma_f32_4x4x4_16b_bf16 v[32:35], v[48:49], a[202:203], 0 cbsz:2 abid:3\n"
    "buffer_load_dwordx4 v244, s[44:47], s84 offen lds\n"
    "v_cvt_pk_bf16_f32 v54, v22, v26\n"
    "v_cvt_pk_bf16_f32 v55, v23, v27\n"
    "v_accvgpr_write_b32 a82, v54\n"
    "v_accvgpr_write_b32 a86, v55\n"
    "v_lshlrev_b64 v[40:41], v16, v[250:251]\n"
    "s_bfm_b64 s[34:35], s7, 2\n"
    "s_waitcnt vmcnt(11) lgkmcnt(4)\n"
    "ds_read2st64_b64 a[192:195], v15 offset0:50 offset1:51\n"
    "v_mfma_f32_4x4x4_16b_bf16 v[20:23], v[50:51], a[204:205], 0 cbsz:2 abid:0\n"
    "v_cvt_pk_bf16_f32 v52, v28, v32\n"
    "v_cvt_pk_bf16_f32 v53, v29, v33\n"
    "v_accvgpr_write_b32 a75, v52\n"
    "v_accvgpr_write_b32 a79, v53\n"
    "v_mfma_f32_4x4x4_16b_bf16 v[24:27], v[50:51], a[206:207], 0 cbsz:2 abid:1\n"
    "buffer_load_dwordx4 v245, s[44:47], s84 offen offset:1024 lds\n"
    "v_cvt_pk_bf16_f32 v54, v30, v34\n"
    "v_cvt_pk_bf16_f32 v55, v31, v35\n"
    "v_accvgpr_write_b32 a83, v54\n"
    "v_accvgpr_write_b32 a87, v55\n"
    "v_lshlrev_b64 v[42:43], v16, v[252:253]\n"
    "s_waitcnt vmcnt(11) lgkmcnt(2)\n"
    "ds_read2st64_b64 a[196:199], v15 offset0:52 offset1:53\n"
    "v_mfma_f32_4x4x4_16b_bf16 v[28:31], v[50:51], a[208:209], 0 cbsz:2 abid:2\n"
    "v_cvt_pk_bf16_f32 v56, v20, v22\n"
    "v_cvt_pk_bf16_f32 v60, v21, v23\n"
    "v_mfma_f32_4x4x4_16b_bf16 v[32:35], v[50:51], a[210:211], 0 cbsz:2 abid:3\n"
    "buffer_load_dwordx4 v246, s[44:47], s84 offen offset:2048 lds\n"
    "v_cvt_pk_bf16_f32 v57, v24, v26\n"
    "v_cvt_pk_bf16_f32 v61, v25, v27\n"
    "buffer_load_dwordx4 v247, s[44:47], s84 offen offset:3072 lds\n"
    "v_lshlrev_b64 v[44:45], v16, v[254:255]\n"
    "s_waitcnt vmcnt(12) lgkmcnt(2)\n"
    "ds_read2st64_b64 a[200:203], v15 offset0:54 offset1:55\n"
    "v_mfma_f32_4x4x4_16b_bf16 v[20:23], v[40:41], a[188:189], 0 cbsz:2 abid:0\n"
    "ds_read_u16 v250, v18 offset:320\n"
    "s_add_u32 m0, s83, 47104\n"
    "v_cvt_pk_bf16_f32 v58, v28, v30\n"
    "v_cvt_pk_bf16_f32 v62, v29, v31\n"
    "v_mfma_f32_4x4x4_16b_bf16 v[24:27], v[40:41], a[190:191], 0 cbsz:2 abid:1\n"
    "buffer_load_dwordx4 v248, s[44:47], s84 offen lds\n"
    "v_cvt_pk_bf16_f32 v59, v32, v34\n"
    "v_cvt_pk_bf16_f32 v63, v33, v35\n"
    "s_waitcnt vmcnt(12) lgkmcnt(3)\n"
    "ds_read2st64_b32 a[204:205], v38 offset0:112 offset1:113\n"
    "v_mfma_f32_4x4x4_16b_bf16 v[28:31], v[40:41], a[192:193], 0 cbsz:2 abid:2\n"
    "ds_read2st64_b32 a[206:207], v38 offset0:114 offset1:115\n"
    "ds_read_u16 v252, v18 offset:328\n"
    "v_cvt_pk_bf16_f32 v64, v20, v24\n"
    "v_cvt_pk_bf16_f32 v68, v21, v25\n"
    "v_mfma_f32_4x4x4_16b_bf16 v[32:35], v[40:41], a[194:195], 0 cbsz:2 abid:3\n"
    "buffer_load_dwordx4 v249, s[44:47], s84 offen offset:1024 lds\n"
    "v_cvt_pk_bf16_f32 v72, v22, v26\n"
    "v_cvt_pk_bf16_f32 v76, v23, v27\n"
    "s_waitcnt vmcnt(12) lgkmcnt(5)\n"
    "ds_read2st64_b32 a[208:209], v38 offset0:116 offset1:117\n"
    "v_mfma_f32_4x4x4_16b_bf16 v[20:23], v[42:43], a[196:197], 0 cbsz:2 abid:0\n"
    "ds_read2st64_b32 a[210:211], v38 offset0:118 offset1:119\n"
    "ds_read_u16 v254, v235 offset:320\n"
    "s_add_u32 m0, s83, 49152\n"
    "s_add_u32 s84, s61, s63\n"
    "v_cvt_pk_bf16_f32 v65, v28, v32\n"
    "v_cvt_pk_bf16_f32 v69, v29, v33\n"
    "v_mfma_f32_4x4x4_16b_bf16 v[24:27], v[42:43], a[198:199], 0 cbsz:2 abid:1\n"
    "buffer_load_dwordx4 v244, s[44:47], s84 offen lds\n"
    "v_cvt_pk_bf16_f32 v73, v30, v34\n"
    "v_cvt_pk_bf16_f32 v77, v31, v35\n"
    "buffer_load_dwordx4 v245, s[44:47], s84 offen offset:1024 lds\n"
    "s_waitcnt vmcnt(13) lgkmcnt(6)\n"
    "ds_read2st64_b64 a[188:191], v15 offset0:0 offset1:1\n"
    "v_mfma_f32_4x4x4_16b_bf16 v[28:31], v[42:43], a[200:201], 0 cbsz:2 abid:2\n"
    "v_cvt_pk_bf16_f32 v66, v20, v24\n"
    "v_cvt_pk_bf16_f32 v70, v21, v25\n"
    "v_mfma_f32_4x4x4_16b_bf16 v[32:35], v[42:43], a[202:203], 0 cbsz:2 abid:3\n"
    "buffer_load_dwordx4 v246, s[44:47], s84 offen offset:2048 lds\n"
    "v_cvt_pk_bf16_f32 v74, v22, v26\n"
    "v_cvt_pk_bf16_f32 v78, v23, v27\n"
    "v_lshlrev_b64 v[46:47], v16, v[250:251]\n"
    "s_waitcnt vmcnt(13) lgkmcnt(4)\n"
    "ds_read2st64_b64 a[192:195], v15 offset0:2 offset1:3\n"
    "v_mfma_f32_4x4x4_16b_bf16 v[20:23], v[44:45], a[204:205], 0 cbsz:2 abid:0\n"
    "v_cvt_pk_bf16_f32 v67, v28, v32\n"
    "v_cvt_pk_bf16_f32 v71, v29, v33\n"
    "v_mfma_f32_4x4x4_16b_bf16 v[24:27], v[44:45], a[206:207], 0 cbsz:2 abid:1\n"
    "buffer_load_dwordx4 v247, s[44:47], s84 offen offset:3072 lds\n"
    "v_cvt_pk_bf16_f32 v75, v30, v34\n"
    "v_cvt_pk_bf16_f32 v79, v31, v35\n"
    "v_lshlrev_b64 v[48:49], v16, v[252:253]\n"
    "s_waitcnt vmcnt(13) lgkmcnt(2)\n"
    "ds_read2st64_b64 a[196:199], v15 offset0:4 offset1:5\n"
    "v_mfma_f32_4x4x4_16b_bf16 v[28:31], v[44:45], a[208:209], 0 cbsz:2 abid:2\n"
    "s_add_u32 m0, s83, 53248\n"
    "v_cvt_pk_bf16_f32 v80, v20, v22\n"
    "v_cvt_pk_bf16_f32 v84, v21, v23\n"
    "v_mfma_f32_4x4x4_16b_bf16 v[32:35], v[44:45], a[210:211], 0 cbsz:2 abid:3\n"
    "buffer_load_dwordx4 v248, s[44:47], s84 offen lds\n"
    "v_cvt_pk_bf16_f32 v81, v24, v26\n"
    "v_cvt_pk_bf16_f32 v85, v25, v27\n"
    "v_lshlrev_b64 v[50:51], v16, v[254:255]\n"
    "s_waitcnt vmcnt(13) lgkmcnt(2)\n"
    "ds_read2st64_b64 a[200:203], v15 offset0:6 offset1:7\n"
    "v_mfma_f32_4x4x4_16b_bf16 v[20:23], v[46:47], a[188:189], 0 cbsz:2 abid:0\n"
    "ds_read_u16 v250, v18 offset:384\n"
    "s_memrealtime s[92:93]\n"
    "v_cvt_pk_bf16_f32 v82, v28, v30\n"
    "v_cvt_pk_bf16_f32 v86, v29, v31\n"
    "v_mfma_f32_4x4x4_16b_bf16 v[24:27], v[46:47], a[190:191], 0 cbsz:2 abid:1\n"
    "buffer_load_dwordx4 v249, s[44:47], s84 offen offset:1024 lds\n"
    "s_add_u32 m0, s83, 55296\n"
    "s_add_u32 s84, s58, s64\n"
    "v_cvt_pk_bf16_f32 v83, v32, v34\n"
    "v_cvt_pk_bf16_f32 v87, v33, v35\n"
    "buffer_load_dwordx4 v244, s[44:47], s84 offen lds\n"
    "s_waitcnt vmcnt(14) lgkmcnt(3)\n"
    "ds_read2st64_b32 a[204:205], v38 offset0:16 offset1:17\n"
    "v_mfma_f32_4x4x4_16b_bf16 v[28:31], v[46:47], a[192:193], 0 cbsz:2 abid:2\n"
    "ds_read2st64_b32 a[206:207], v38 offset0:18 offset1:19\n"
    "ds_read_u16 v252, v18 offset:392\n"
    "v_cvt_pk_bf16_f32 v88, v20, v24\n"
    "v_cvt_pk_bf16_f32 v92, v21, v25\n"
    "v_mfma_f32_4x4x4_16b_bf16 v[32:35], v[46:47], a[194:195], 0 cbsz:2 abid:3\n"
    "buffer_load_dwordx4 v245, s[44:47], s84 offen offset:1024 lds\n"
    "v_cvt_pk_bf16_f32 v96, v22, v26\n"
    "v_cvt_pk_bf16_f32 v100, v23, v27\n"
    "s_waitcnt vmcnt(14) lgkmcnt(5)\n"
    "ds_read2st64_b32 a[208:209], v38 offset0:20 offset1:21\n"
    "v_mfma_f32_4x4x4_16b_bf16 v[20:23], v[48:49], a[196:197], 0 cbsz:2 abid:0\n"
    "ds_read2st64_b32 a[210:211], v38 offset0:22 offset1:23\n"
    "ds_read_u16 v254, v235 offset:384\n"
    "global_load_dwordx4 v[236:239], v16, s[54:55]\n"
    "v_cvt_pk_bf16_f32 v89, v28, v32\n"
    "v_cvt_pk_bf16_f32 v93, v29, v33\n"
    "v_mfma_f32_4x4x4_16b_bf16 v[24:27], v[48:49], a[198:199], 0 cbsz:2 abid:1\n"
    "buffer_load_dwordx4 v246, s[44:47], s84 offen offset:2048 lds\n"
    "v_cvt_pk_bf16_f32 v97, v30, v34\n"
    "v_cvt_pk_bf16_f32 v101, v31, v35\n"
    "s_waitcnt vmcnt(15) lgkmcnt(6)\n"
    "ds_read2st64_b64 a[188:191], v15 offset0:12 offset1:13\n"
    "v_mfma_f32_4x4x4_16b_bf16 v[28:31], v[48:49], a[200:201], 0 cbsz:2 abid:2\n"
    "v_cvt_pk_bf16_f32 v90, v20, v24\n"
    "v_cvt_pk_bf16_f32 v94, v21, v25\n"
    "v_mfma_f32_4x4x4_16b_bf16 v[32:35], v[48:49], a[202:203], 0 cbsz:2 abid:3\n"
    "buffer_load_dwordx4 v247, s[44:47], s84 offen offset:3072 lds\n"
    "v_cvt_pk_bf16_f32 v98, v22, v26\n"
    "v_cvt_pk_bf16_f32 v102, v23, v27\n"
    "v_lshlrev_b64 v[40:41], v16, v[250:251]\n"
    "s_waitcnt vmcnt(15) lgkmcnt(4)\n"
    "ds_read2st64_b64 a[192:195], v15 offset0:14 offset1:15\n"
    "v_mfma_f32_4x4x4_16b_bf16 v[20:23], v[50:51], a[204:205], 0 cbsz:2 abid:0\n"
    "s_add_u32 m0, s83, 59392\n"
    "v_cvt_pk_bf16_f32 v91, v28, v32\n"
    "v_cvt_pk_bf16_f32 v95, v29, v33\n"
    "v_mfma_f32_4x4x4_16b_bf16 v[24:27], v[50:51], a[206:207], 0 cbsz:2 abid:1\n"
    "buffer_load_dwordx4 v248, s[44:47], s84 offen lds\n"
    "v_cvt_pk_bf16_f32 v99, v30, v34\n"
    "v_cvt_pk_bf16_f32 v103, v31, v35\n"
    "buffer_load_dwordx4 v249, s[44:47], s84 offen offset:1024 lds\n"
    "v_lshlrev_b64 v[42:43], v16, v[252:253]\n"
    "s_waitcnt vmcnt(16) lgkmcnt(2)\n"
    "ds_read2st64_b64 a[196:199], v15 offset0:16 offset1:17\n"
    "v_mfma_f32_4x4x4_16b_bf16 v[28:31], v[50:51], a[208:209], 0 cbsz:2 abid:2\n"
    "s_add_u32 m0, s83, 61440\n"
    "s_add_u32 s84, s59, s64\n"
    "v_cvt_pk_bf16_f32 v104, v20, v22\n"
    "v_cvt_pk_bf16_f32 v108, v21, v23\n"
    "v_mfma_f32_4x4x4_16b_bf16 v[32:35], v[50:51], a[210:211], 0 cbsz:2 abid:3\n"
    "buffer_load_dwordx4 v244, s[44:47], s84 offen lds\n"
    "v_cvt_pk_bf16_f32 v105, v24, v26\n"
    "v_cvt_pk_bf16_f32 v109, v25, v27\n"
    "v_lshlrev_b64 v[44:45], v16, v[254:255]\n"
    "s_waitcnt vmcnt(16) lgkmcnt(2)\n"
    "ds_read2st64_b64 a[200:203], v15 offset0:18 offset1:19\n"
    "v_mfma_f32_4x4x4_16b_bf16 v[20:23], v[40:41], a[188:189], 0 cbsz:2 abid:0\n"
    "ds_read_u16 v250, v18 offset:448\n"
    "v_cvt_pk_bf16_f32 v106, v28, v30\n"
    "v_cvt_pk_bf16_f32 v110, v29, v31\n"
    "v_mfma_f32_4x4x4_16b_bf16 v[24:27], v[40:41], a[190:191], 0 cbsz:2 abid:1\n"
    "buffer_load_dwordx4 v245, s[44:47], s84 offen offset:1024 lds\n"
    "v_cvt_pk_bf16_f32 v107, v32, v34\n"
    "v_cvt_pk_bf16_f32 v111, v33, v35\n"
    "s_waitcnt vmcnt(16) lgkmcnt(3)\n"
    "ds_read2st64_b32 a[204:205], v38 offset0:40 offset1:41\n"
    "v_mfma_f32_4x4x4_16b_bf16 v[28:31], v[40:41], a[192:193], 0 cbsz:2 abid:2\n"
    "ds_read2st64_b32 a[206:207], v38 offset0:42 offset1:43\n"
    "ds_read_u16 v252, v18 offset:456\n"
    "v_cvt_pk_bf16_f32 v112, v20, v24\n"
    "v_cvt_pk_bf16_f32 v116, v21, v25\n"
    "v_mfma_f32_4x4x4_16b_bf16 v[32:35], v[40:41], a[194:195], 0 cbsz:2 abid:3\n"
    "buffer_load_dwordx4 v246, s[44:47], s84 offen offset:2048 lds\n"
    "v_cvt_pk_bf16_f32 v120, v22, v26\n"
    "v_cvt_pk_bf16_f32 v124, v23, v27\n"
    "s_waitcnt vmcnt(16) lgkmcnt(5)\n"
    "ds_read2st64_b32 a[208:209], v38 offset0:44 offset1:45\n"
    "v_mfma_f32_4x4x4_16b_bf16 v[20:23], v[42:43], a[196:197], 0 cbsz:2 abid:0\n"
    "ds_read2st64_b32 a[210:211], v38 offset0:46 offset1:47\n"
    "ds_read_u16 v254, v235 offset:448\n"
    "v_cvt_pk_bf16_f32 v113, v28, v32\n"
    "v_cvt_pk_bf16_f32 v117, v29, v33\n"
    "v_mfma_f32_4x4x4_16b_bf16 v[24:27], v[42:43], a[198:199], 0 cbsz:2 abid:1\n"
    "buffer_load_dwordx4 v247, s[44:47], s84 offen offset:3072 lds\n"
    "s_add_u32 m0, s83, 65536\n"
    "v_cvt_pk_bf16_f32 v121, v30, v34\n"
    "v_cvt_pk_bf16_f32 v125, v31, v35\n"
    "buffer_load_dwordx4 v248, s[44:47], s84 offen lds\n"
    "s_waitcnt vmcnt(17) lgkmcnt(6)\n"
    "ds_read2st64_b64 a[188:191], v15 offset0:24 offset1:25\n"
    "v_mfma_f32_4x4x4_16b_bf16 v[28:31], v[42:43], a[200:201], 0 cbsz:2 abid:2\n"
    "v_cvt_pk_bf16_f32 v114, v20, v24\n"
    "v_cvt_pk_bf16_f32 v118, v21, v25\n"
    "v_mfma_f32_4x4x4_16b_bf16 v[32:35], v[42:43], a[202:203], 0 cbsz:2 abid:3\n"
    "buffer_load_dwordx4 v249, s[44:47], s84 offen offset:1024 lds\n"
    "v_cvt_pk_bf16_f32 v122, v22, v26\n"
    "v_cvt_pk_bf16_f32 v126, v23, v27\n"
    "v_lshlrev_b64 v[46:47], v16, v[250:251]\n"
    "s_waitcnt vmcnt(17) lgkmcnt(4)\n"
    "ds_read2st64_b64 a[192:195], v15 offset0:26 offset1:27\n"
    "v_mfma_f32_4x4x4_16b_bf16 v[20:23], v[44:45], a[204:205], 0 cbsz:2 abid:0\n"
    "s_add_u32 m0, s83, 36864\n"
    "s_add_u32 s84, s60, s64\n"
    "v_cvt_pk_bf16_f32 v115, v28, v32\n"
    "v_cvt_pk_bf16_f32 v119, v29, v33\n"
    "v_mfma_f32_4x4x4_16b_bf16 v[24:27], v[44:45], a[206:207], 0 cbsz:2 abid:1\n"
    "buffer_load_dwordx4 v244, s[44:47], s84 offen lds\n"
    "v_cvt_pk_bf16_f32 v123, v30, v34\n"
    "v_cvt_pk_bf16_f32 v127, v31, v35\n"
    "v_lshlrev_b64 v[48:49], v16, v[252:253]\n"
    "s_waitcnt vmcnt(17) lgkmcnt(2)\n"
    "ds_read2st64_b64 a[196:199], v15 offset0:28 offset1:29\n"
    "v_mfma_f32_4x4x4_16b_bf16 v[28:31], v[44:45], a[208:209], 0 cbsz:2 abid:2\n"
    "v_cvt_pk_bf16_f32 v128, v20, v22\n"
    "v_cvt_pk_bf16_f32 v132, v21, v23\n"
    "v_mfma_f32_4x4x4_16b_bf16 v[32:35], v[44:45], a[210:211], 0 cbsz:2 abid:3\n"
    "buffer_load_dwordx4 v245, s[44:47], s84 offen offset:1024 lds\n"
    "v_cvt_pk_bf16_f32 v129, v24, v26\n"
    "v_cvt_pk_bf16_f32 v133, v25, v27\n"
    "v_lshlrev_b64 v[50:51], v16, v[254:255]\n"
    "s_waitcnt vmcnt(12)\n"
    "v_sub_u32 v11, s0, v236\n"
    "v_or_b32 v236, 0xffff, v236\n"
    "v_or_b32 v238, 0xffff, v238\n"
    "s_waitcnt vmcnt(17) lgkmcnt(2)\n"
    "ds_read2st64_b64 a[200:203], v15 offset0:30 offset1:31\n"
    "v_mfma_f32_4x4x4_16b_bf16 v[20:23], v[46:47], a[188:189], 0 cbsz:2 abid:0\n"
    "ds_read_u16 v250, v18 offset:512\n"
    "v_cvt_pk_bf16_f32 v130, v28, v30\n"
    "v_cvt_pk_bf16_f32 v134, v29, v31\n"
    "v_mfma_f32_4x4x4_16b_bf16 v[24:27], v[46:47], a[190:191], 0 cbsz:2 abid:1\n"
    "buffer_load_dwordx4 v246, s[44:47], s84 offen offset:2048 lds\n"
    "v_cvt_pk_bf16_f32 v131, v32, v34\n"
    "v_cvt_pk_bf16_f32 v135, v33, v35\n"
    "buffer_load_dwordx4 v247, s[44:47], s84 offen offset:3072 lds\n"
    "v_and_b32 v242, 15, v237\n"
    "v_and_b32 v243, 1, v239\n"
    "v_or_b32 v237, 15, v237\n"
    "v_or_b32 v239, 15, v239\n"
    "s_waitcnt vmcnt(18) lgkmcnt(3)\n"
    "ds_read2st64_b32 a[204:205], v38 offset0:64 offset1:65\n"
    "v_mfma_f32_4x4x4_16b_bf16 v[28:31], v[46:47], a[192:193], 0 cbsz:2 abid:2\n"
    "ds_read2st64_b32 a[206:207], v38 offset0:66 offset1:67\n"
    "ds_read_u16 v252, v18 offset:520\n"
    "s_add_u32 m0, s83, 40960\n"
    "v_cvt_pk_bf16_f32 v136, v20, v24\n"
    "v_cvt_pk_bf16_f32 v140, v21, v25\n"
    "v_mfma_f32_4x4x4_16b_bf16 v[32:35], v[46:47], a[194:195], 0 cbsz:2 abid:3\n"
    "buffer_load_dwordx4 v248, s[44:47], s84 offen lds\n"
    "v_cvt_pk_bf16_f32 v144, v22, v26\n"
    "v_cvt_pk_bf16_f32 v148, v23, v27\n"
    "v_cmp_ne_u64 vcc, s[18:19], v[236:237]\n"
    "v_cmp_ne_u64 s[48:49], s[18:19], v[238:239]\n"
    "v_readlane_b32 s9, v242, 0\n"
    "v_and_b32 v236, 1, v242\n"
    "s_waitcnt vmcnt(18) lgkmcnt(5)\n"
    "ds_read2st64_b32 a[208:209], v38 offset0:68 offset1:69\n"
    "v_mfma_f32_4x4x4_16b_bf16 v[20:23], v[48:49], a[196:197], 0 cbsz:2 abid:0\n"
    "ds_read2st64_b32 a[210:211], v38 offset0:70 offset1:71\n"
    "ds_read_u16 v254, v235 offset:512\n"
    "v_cvt_pk_bf16_f32 v137, v28, v32\n"
    "v_cvt_pk_bf16_f32 v141, v29, v33\n"
    "v_mfma_f32_4x4x4_16b_bf16 v[24:27], v[48:49], a[198:199], 0 cbsz:2 abid:1\n"
    "buffer_load_dwordx4 v249, s[44:47], s84 offen offset:1024 lds\n"
    "v_cvt_pk_bf16_f32 v145, v30, v34\n"
    "v_cvt_pk_bf16_f32 v149, v31, v35\n"
    "s_or_b64 s[48:49], s[48:49], vcc\n"
    "s_cbranch_scc1 75f\n"
    "77:\n"
    "v_cmp_eq_u32 s[24:25], 1, v236\n"
    "s_waitcnt vmcnt(18) lgkmcnt(6)\n"
    "ds_read2st64_b64 a[188:191], v15 offset0:36 offset1:37\n"
    "v_mfma_f32_4x4x4_16b_bf16 v[28:31], v[48:49], a[200:201], 0 cbsz:2 abid:2\n"
    "s_add_u32 m0, s83, 43008\n"
    "s_add_u32 s84, s61, s64\n"
    "v_cvt_pk_bf16_f32 v138, v20, v24\n"
    "v_cvt_pk_bf16_f32 v142, v21, v25\n"
    "v_mfma_f32_4x4x4_16b_bf16 v[32:35], v[48:49], a[202:203], 0 cbsz:2 abid:3\n"
    "buffer_load_dwordx4 v244, s[44:47], s84 offen lds\n"
    "v_cvt_pk_bf16_f32 v146, v22, v26\n"
    "v_cvt_pk_bf16_f32 v150, v23, v27\n"
    "buffer_load_dwordx4 v245, s[44:47], s84 offen offset:1024 lds\n"
    "v_lshlrev_b64 v[40:41], v16, v[250:251]\n"
    "s_lshr_b32 s9, s9, 1\n"
    "s_add_u32 s9, s9, s28\n"
    "v_lshrrev_b64 v[240:241], v232, s[24:25]\n"
    "v_and_b32 v241, 0xff, v240\n"
    "s_waitcnt vmcnt(19) lgkmcnt(4)\n"
    "ds_read2st64_b64 a[192:195], v15 offset0:38 offset1:39\n"
    "v_mfma_f32_4x4x4_16b_bf16 v[20:23], v[50:51], a[204:205], 0 cbsz:2 abid:0\n"
    "v_cvt_pk_bf16_f32 v139, v28, v32\n"
    "v_cvt_pk_bf16_f32 v143, v29, v33\n"
    "v_mfma_f32_4x4x4_16b_bf16 v[24:27], v[50:51], a[206:207], 0 cbsz:2 abid:1\n"
    "buffer_load_dwordx4 v246, s[44:47], s84 offen offset:2048 lds\n"
    "v_cvt_pk_bf16_f32 v147, v30, v34\n"
    "v_cvt_pk_bf16_f32 v151, v31, v35\n"
    "v_lshlrev_b64 v[42:43], v16, v[252:253]\n"
    "v_bfe_u32 v240, v240, v233, 4\n"
    "v_bcnt_u32_b32 v241, v241, 8\n"
    "v_bcnt_u32_b32 v240, v240, 0\n"
    "v_lshl_add_u32 v240, v240, 1, v241\n"
    "s_waitcnt vmcnt(18) lgkmcnt(2)\n"
    "ds_read2st64_b64 a[196:199], v15 offset0:40 offset1:41\n"
    "v_mfma_f32_4x4x4_16b_bf16 v[28:31], v[50:51], a[208:209], 0 cbsz:2 abid:2\n"
    "v_cvt_pk_bf16_f32 v152, v20, v22\n"
    "v_cvt_pk_bf16_f32 v156, v21, v23\n"
    "v_mfma_f32_4x4x4_16b_bf16 v[32:35], v[50:51], a[210:211], 0 cbsz:2 abid:3\n"
    "buffer_load_dwordx4 v247, s[44:47], s84 offen offset:3072 lds\n"
    "v_cvt_pk_bf16_f32 v153, v24, v26\n"
    "v_cvt_pk_bf16_f32 v157, v25, v27\n"
    "v_lshlrev_b64 v[44:45], v16, v[254:255]\n"
    "s_lshr_b32 s10, s9, 2\n"
    "v_cmp_lt_u32 vcc, 15, v240\n"
    "v_xor_b32 v234, s10, v234\n"
    "s_mov_b64 s[50:51], vcc\n"
    "s_waitcnt vmcnt(18) lgkmcnt(2)\n"
    "ds_read2st64_b64 a[200:203], v15 offset0:42 offset1:43\n"
    "v_mfma_f32_4x4x4_16b_bf16 v[20:23], v[40:41], a[188:189], 0 cbsz:2 abid:0\n"
    "ds_read_u16 v250, v18 offset:576\n"
    "s_add_u32 m0, s83, 47104\n"
    "v_cvt_pk_bf16_f32 v154, v28, v30\n"
    "v_cvt_pk_bf16_f32 v158, v29, v31\n"
    "v_mfma_f32_4x4x4_16b_bf16 v[24:27], v[40:41], a[190:191], 0 cbsz:2 abid:1\n"
    "buffer_load_dwordx4 v248, s[44:47], s84 offen lds\n"
    "v_cvt_pk_bf16_f32 v155, v32, v34\n"
    "v_cvt_pk_bf16_f32 v159, v33, v35\n"
    "v_addc_co_u32 v234, vcc, 0, v234, vcc\n"
    "v_and_b32 v234, 1, v234\n"
    "v_mad_u32_u24 v236, v234, s88, v3\n"
    "v_cmp_eq_u32 s[26:27], 1, v243\n"
    "s_waitcnt vmcnt(18) lgkmcnt(3)\n"
    "ds_read2st64_b32 a[204:205], v38 offset0:88 offset1:89\n"
    "v_mfma_f32_4x4x4_16b_bf16 v[28:31], v[40:41], a[192:193], 0 cbsz:2 abid:2\n"
    "ds_read2st64_b32 a[206:207], v38 offset0:90 offset1:91\n"
    "ds_read_u16 v252, v18 offset:584\n"
    "v_cvt_pk_bf16_f32 v160, v20, v24\n"
    "v_cvt_pk_bf16_f32 v164, v21, v25\n"
    "v_mfma_f32_4x4x4_16b_bf16 v[32:35], v[40:41], a[194:195], 0 cbsz:2 abid:3\n"
    "buffer_load_dwordx4 v249, s[44:47], s84 offen offset:1024 lds\n"
    "v_cvt_pk_bf16_f32 v168, v22, v26\n"
    "v_cvt_pk_bf16_f32 v172, v23, v27\n"
    "v_readlane_b32 s82, v236, 0\n"
    "v_readlane_b32 s73, v236, 1\n"
    "s_waitcnt vmcnt(18) lgkmcnt(5)\n"
    "ds_read2st64_b32 a[208:209], v38 offset0:92 offset1:93\n"
    "v_mfma_f32_4x4x4_16b_bf16 v[20:23], v[42:43], a[196:197], 0 cbsz:2 abid:0\n"
    "ds_read2st64_b32 a[210:211], v38 offset0:94 offset1:95\n"
    "ds_read_u16 v254, v235 offset:576\n"
    "s_barrier\n"
    "s_mov_b32 m0, s57\n"
    "v_cvt_pk_bf16_f32 v161, v28, v32\n"
    "v_cvt_pk_bf16_f32 v165, v29, v33\n"
    "v_mfma_f32_4x4x4_16b_bf16 v[24:27], v[42:43], a[198:199], 0 cbsz:2 abid:1\n"
    "buffer_load_dwordx4 v0, s[36:39], s82 offen nt lds\n"
    "v_cvt_pk_bf16_f32 v169, v30, v34\n"
    "v_cvt_pk_bf16_f32 v173, v31, v35\n"
    "s_waitcnt vmcnt(18) lgkmcnt(6)\n"
    "ds_read2st64_b64 a[188:191], v15 offset0:48 offset1:49\n"
    "v_mfma_f32_4x4x4_16b_bf16 v[28:31], v[42:43], a[200:201], 0 cbsz:2 abid:2\n"
    "v_cvt_pk_bf16_f32 v162, v20, v24\n"
    "v_cvt_pk_bf16_f32 v166, v21, v25\n"
    "v_mfma_f32_4x4x4_16b_bf16 v[32:35], v[42:43], a[202:203], 0 cbsz:2 abid:3\n"
    "buffer_load_dwordx4 v1, s[36:39], s82 offen offset:1024 nt lds\n"
    "v_cvt_pk_bf16_f32 v170, v22, v26\n"
    "v_cvt_pk_bf16_f32 v174, v23, v27\n"
    "v_lshlrev_b64 v[46:47], v16, v[250:251]\n"
    "v_cmp_gt_u32 s[48:49], s96, v232\n"
    "v_lshrrev_b64 v[240:241], v232, s[26:27]\n"
    "v_and_b32 v241, 0xff, v240\n"
    "v_bfe_u32 v240, v240, v233, 4\n"
    "s_waitcnt vmcnt(18) lgkmcnt(4)\n"
    "ds_read2st64_b64 a[192:195], v15 offset0:50 offset1:51\n"
    "v_mfma_f32_4x4x4_16b_bf16 v[20:23], v[44:45], a[204:205], 0 cbsz:2 abid:0\n"
    "v_cvt_pk_bf16_f32 v163, v28, v32\n"
    "v_cvt_pk_bf16_f32 v167, v29, v33\n"
    "v_mfma_f32_4x4x4_16b_bf16 v[24:27], v[44:45], a[206:207], 0 cbsz:2 abid:1\n"
    "buffer_load_dwordx4 v2, s[36:39], s82 offen offset:2048 nt lds\n"
    "v_cvt_pk_bf16_f32 v171, v30, v34\n"
    "v_cvt_pk_bf16_f32 v175, v31, v35\n"
    "v_lshlrev_b64 v[48:49], v16, v[252:253]\n"
    "v_bcnt_u32_b32 v241, v241, 8\n"
    "v_bcnt_u32_b32 v240, v240, 0\n"
    "v_lshl_add_u32 v240, v240, 1, v241\n"
    "v_cmp_lt_u32 s[26:27], 15, v240\n"
    "s_waitcnt vmcnt(18) lgkmcnt(2)\n"
    "ds_read2st64_b64 a[196:199], v15 offset0:52 offset1:53\n"
    "v_mfma_f32_4x4x4_16b_bf16 v[28:31], v[44:45], a[208:209], 0 cbsz:2 abid:2\n"
    "v_cvt_pk_bf16_f32 v176, v20, v22\n"
    "v_cvt_pk_bf16_f32 v180, v21, v23\n"
    "v_mfma_f32_4x4x4_16b_bf16 v[32:35], v[44:45], a[210:211], 0 cbsz:2 abid:3\n"
    "v_cvt_pk_bf16_f32 v177, v24, v26\n"
    "v_cvt_pk_bf16_f32 v181, v25, v27\n"
    "v_lshlrev_b64 v[50:51], v16, v[254:255]\n"
    "s_bfm_b64 s[24:25], s52, 0\n"
    "s_bitcmp1_b32 s9, 0\n"
    "s_cselect_b32 s95, -1, 1\n"
    "s_waitcnt vmcnt(17) lgkmcnt(2)\n"
    "ds_read2st64_b64 a[200:203], v15 offset0:54 offset1:55\n"
    "v_mfma_f32_4x4x4_16b_bf16 v[20:23], v[46:47], a[188:189], 0 cbsz:2 abid:0\n"
    "ds_read_u16 v250, v18 offset:640\n"
    "v_cvt_pk_bf16_f32 v178, v28, v30\n"
    "v_cvt_pk_bf16_f32 v182, v29, v31\n"
    "v_mfma_f32_4x4x4_16b_bf16 v[24:27], v[46:47], a[190:191], 0 cbsz:2 abid:1\n"
    "v_cvt_pk_bf16_f32 v179, v32, v34\n"
    "v_cvt_pk_bf16_f32 v183, v33, v35\n"
    "s_cmp_lt_u32 s52, 12\n"
    "s_cselect_b32 s95, 0, s95\n"
    "s_add_u32 s52, s52, s95\n"
    "s_xor_b64 s[26:27], s[26:27], s[50:51]\n"
    "s_waitcnt vmcnt(16) lgkmcnt(3)\n"
    "ds_read2st64_b32 a[204:205], v38 offset0:112 offset1:113\n"
    "v_mfma_f32_4x4x4_16b_bf16 v[28:31], v[46:47], a[192:193], 0 cbsz:2 abid:2\n"
    "ds_read2st64_b32 a[206:207], v38 offset0:114 offset1:115\n"
    "ds_read_u16 v252, v18 offset:648\n"
    "v_cvt_pk_bf16_f32 v184, v20, v24\n"
    "v_cvt_pk_bf16_f32 v188, v21, v25\n"
    "v_mfma_f32_4x4x4_16b_bf16 v[32:35], v[46:47], a[194:195], 0 cbsz:2 abid:3\n"
    "v_cvt_pk_bf16_f32 v192, v22, v26\n"
    "v_cvt_pk_bf16_f32 v196, v23, v27\n"
    "s_and_b64 s[48:49], s[48:49], s[24:25]\n"
    "s_and_b64 s[24:25], s[24:25], s[26:27]\n"
    "s_andn2_b64 s[48:49], s[48:49], s[26:27]\n"
    "s_bcnt1_i32_b64 s10, s[48:49]\n"
    "s_waitcnt vmcnt(15) lgkmcnt(5)\n"
    "ds_read2st64_b32 a[208:209], v38 offset0:116 offset1:117\n"
    "v_mfma_f32_4x4x4_16b_bf16 v[20:23], v[48:49], a[196:197], 0 cbsz:2 abid:0\n"
    "ds_read2st64_b32 a[210:211], v38 offset0:118 offset1:119\n"
    "ds_read_u16 v254, v235 offset:640\n"
    "v_cvt_pk_bf16_f32 v185, v28, v32\n"
    "v_cvt_pk_bf16_f32 v189, v29, v33\n"
    "v_mfma_f32_4x4x4_16b_bf16 v[24:27], v[48:49], a[198:199], 0 cbsz:2 abid:1\n"
    "v_cvt_pk_bf16_f32 v193, v30, v34\n"
    "v_cvt_pk_bf16_f32 v197, v31, v35\n"
    "s_cmp_lt_u32 s10, 4\n"
    "s_cselect_b64 s[34:35], s[34:35], 0\n"
    "s_cselect_b32 s96, 1, 0\n"
    "s_and_b64 vcc, s[34:35], s[26:27]\n"
    "s_waitcnt vmcnt(14) lgkmcnt(6)\n"
    "ds_read2st64_b64 a[188:191], v15 offset0:0 offset1:1\n"
    "v_mfma_f32_4x4x4_16b_bf16 v[28:31], v[48:49], a[200:201], 0 cbsz:2 abid:2\n"
    "v_cvt_pk_bf16_f32 v186, v20, v24\n"
    "v_cvt_pk_bf16_f32 v190, v21, v25\n"
    "v_mfma_f32_4x4x4_16b_bf16 v[32:35], v[48:49], a[202:203], 0 cbsz:2 abid:3\n"
    "v_cvt_pk_bf16_f32 v194, v22, v26\n"
    "v_cvt_pk_bf16_f32 v198, v23, v27\n"
    "v_lshlrev_b64 v[40:41], v16, v[250:251]\n"
    "v_addc_co_u32 v234, vcc, 0, v234, vcc\n"
    "v_cmp_le_u32 vcc, s52, v19\n"
    "v_subrev_u32 v240, 15, v240\n"
    "v_cmp_gt_u32 s[48:49], 3, v240\n"
    "s_waitcnt vmcnt(13) lgkmcnt(4)\n"
    "ds_read2st64_b64 a[192:195], v15 offset0:2 offset1:3\n"
    "v_mfma_f32_4x4x4_16b_bf16 v[20:23], v[50:51], a[204:205], 0 cbsz:2 abid:0\n"
    "v_cvt_pk_bf16_f32 v187, v28, v32\n"
    "v_cvt_pk_bf16_f32 v191, v29, v33\n"
    "v_mfma_f32_4x4x4_16b_bf16 v[24:27], v[50:51], a[206:207], 0 cbsz:2 abid:1\n"
    "v_cvt_pk_bf16_f32 v195, v30, v34\n"
    "v_cvt_pk_bf16_f32 v199, v31, v35\n"
    "v_lshlrev_b64 v[42:43], v16, v[252:253]\n"
    "s_or_b64 s[26:27], s[26:27], s[48:49]\n"
    "v_and_b32 v234, 1, v234\n"
    "v_mov_b32 v236, s33\n"
    "v_mad_u32_u24 v3, v234, s88, v3\n"
    "s_waitcnt vmcnt(12) lgkmcnt(2)\n"
    "ds_read2st64_b64 a[196:199], v15 offset0:4 offset1:5\n"
    "v_mfma_f32_4x4x4_16b_bf16 v[28:31], v[50:51], a[208:209], 0 cbsz:2 abid:2\n"
    "v_cvt_pk_bf16_f32 v200, v20, v22\n"
    "v_cvt_pk_bf16_f32 v204, v21, v23\n"
    "v_mfma_f32_4x4x4_16b_bf16 v[32:35], v[50:51], a[210:211], 0 cbsz:2 abid:3\n"
    "v_cvt_pk_bf16_f32 v201, v24, v26\n"
    "v_cvt_pk_bf16_f32 v205, v25, v27\n"
    "v_lshlrev_b64 v[44:45], v16, v[254:255]\n"
    "v_cndmask_b32 v3, v3, v236, vcc\n"
    "s_nop 0\n"
    "v_readlane_b32 s74, v3, 2\n"
    "v_readlane_b32 s75, v3, 3\n"
    "s_waitcnt vmcnt(11) lgkmcnt(2)\n"
    "ds_read2st64_b64 a[200:203], v15 offset0:6 offset1:7\n"
    "v_mfma_f32_4x4x4_16b_bf16 v[20:23], v[40:41], a[188:189], 0 cbsz:2 abid:0\n"
    "ds_read_u16 v250, v18 offset:704\n"
    "v_cvt_pk_bf16_f32 v202, v28, v30\n"
    "v_cvt_pk_bf16_f32 v206, v29, v31\n"
    "v_mfma_f32_4x4x4_16b_bf16 v[24:27], v[40:41], a[190:191], 0 cbsz:2 abid:1\n"
    "v_cvt_pk_bf16_f32 v203, v32, v34\n"
    "v_cvt_pk_bf16_f32 v207, v33, v35\n"
    "v_readlane_b32 s81, v3, 4\n"
    "s_waitcnt vmcnt(10) lgkmcnt(3)\n"
    "ds_read2st64_b32 a[204:205], v38 offset0:16 offset1:17\n"
    "v_mfma_f32_4x4x4_16b_bf16 v[28:31], v[40:41], a[192:193], 0 cbsz:2 abid:2\n"
    "ds_read2st64_b32 a[206:207], v38 offset0:18 offset1:19\n"
    "ds_read_u16 v252, v18 offset:712\n"
    "v_cvt_pk_bf16_f32 v208, v20, v24\n"
    "v_cvt_pk_bf16_f32 v212, v21, v25\n"
    "v_mfma_f32_4x4x4_16b_bf16 v[32:35], v[40:41], a[194:195], 0 cbsz:2 abid:3\n"
    "v_cvt_pk_bf16_f32 v216, v22, v26\n"
    "v_cvt_pk_bf16_f32 v220, v23, v27\n"
    "s_waitcnt vmcnt(9) lgkmcnt(5)\n"
    "ds_read2st64_b32 a[208:209], v38 offset0:20 offset1:21\n"
    "v_mfma_f32_4x4x4_16b_bf16 v[20:23], v[42:43], a[196:197], 0 cbsz:2 abid:0\n"
    "ds_read2st64_b32 a[210:211], v38 offset0:22 offset1:23\n"
    "ds_read_u16 v254, v235 offset:704\n"
    "v_cvt_pk_bf16_f32 v209, v28, v32\n"
    "v_cvt_pk_bf16_f32 v213, v29, v33\n"
    "v_mfma_f32_4x4x4_16b_bf16 v[24:27], v[42:43], a[198:199], 0 cbsz:2 abid:1\n"
    "v_cvt_pk_bf16_f32 v217, v30, v34\n"
    "v_cvt_pk_bf16_f32 v221, v31, v35\n"
    "s_waitcnt vmcnt(8) lgkmcnt(6)\n"
    "ds_read2st64_b64 a[188:191], v15 offset0:12 offset1:13\n"
    "v_mfma_f32_4x4x4_16b_bf16 v[28:31], v[42:43], a[200:201], 0 cbsz:2 abid:2\n"
    "s_nop 0\n"
    "v_cvt_pk_bf16_f32 v210, v20, v24\n"
    "v_cvt_pk_bf16_f32 v214, v21, v25\n"
    "v_mfma_f32_4x4x4_16b_bf16 v[32:35], v[42:43], a[202:203], 0 cbsz:2 abid:3\n"
    "v_cvt_pk_bf16_f32 v218, v22, v26\n"
    "v_cvt_pk_bf16_f32 v222, v23, v27\n"
    "v_lshlrev_b64 v[46:47], v16, v[250:251]\n"
    "s_waitcnt vmcnt(7) lgkmcnt(4)\n"
    "ds_read2st64_b64 a[192:195], v15 offset0:14 offset1:15\n"
    "v_mfma_f32_4x4x4_16b_bf16 v[20:23], v[44:45], a[204:205], 0 cbsz:2 abid:0\n"
    "s_add_u32 m0, s57, 12288\n"
    "v_cvt_pk_bf16_f32 v211, v28, v32\n"
    "v_cvt_pk_bf16_f32 v215, v29, v33\n"
    "v_mfma_f32_4x4x4_16b_bf16 v[24:27], v[44:45], a[206:207], 0 cbsz:2 abid:1\n"
    "buffer_load_dwordx4 v0, s[36:39], s73 offen nt lds\n"
    "v_cvt_pk_bf16_f32 v219, v30, v34\n"
    "v_cvt_pk_bf16_f32 v223, v31, v35\n"
    "v_lshlrev_b64 v[48:49], v16, v[252:253]\n"
    "s_waitcnt vmcnt(7) lgkmcnt(2)\n"
    "ds_read2st64_b64 a[196:199], v15 offset0:16 offset1:17\n"
    "v_mfma_f32_4x4x4_16b_bf16 v[28:31], v[44:45], a[208:209], 0 cbsz:2 abid:2\n"
    "v_cvt_pk_bf16_f32 v224, v20, v22\n"
    "v_cvt_pk_bf16_f32 v228, v21, v23\n"
    "v_mfma_f32_4x4x4_16b_bf16 v[32:35], v[44:45], a[210:211], 0 cbsz:2 abid:3\n"
    "buffer_load_dwordx4 v1, s[36:39], s73 offen offset:1024 nt lds\n"
    "v_cvt_pk_bf16_f32 v225, v24, v26\n"
    "v_cvt_pk_bf16_f32 v229, v25, v27\n"
    "v_lshlrev_b64 v[50:51], v16, v[254:255]\n"
    "s_waitcnt vmcnt(7) lgkmcnt(2)\n"
    "ds_read2st64_b64 a[200:203], v15 offset0:18 offset1:19\n"
    "v_mfma_f32_4x4x4_16b_bf16 v[20:23], v[46:47], a[188:189], 0 cbsz:2 abid:0\n"
    "v_cvt_pk_bf16_f32 v226, v28, v30\n"
    "v_cvt_pk_bf16_f32 v230, v29, v31\n"
    "v_mfma_f32_4x4x4_16b_bf16 v[24:27], v[46:47], a[190:191], 0 cbsz:2 abid:1\n"
    "buffer_load_dwordx4 v2, s[36:39], s73 offen offset:2048 nt lds\n"
    "v_cvt_pk_bf16_f32 v227, v32, v34\n"
    "v_cvt_pk_bf16_f32 v231, v33, v35\n"
    "s_waitcnt vmcnt(7) lgkmcnt(2)\n"
    "ds_read2st64_b32 a[204:205], v38 offset0:40 offset1:41\n"
    "v_mfma_f32_4x4x4_16b_bf16 v[28:31], v[46:47], a[192:193], 0 cbsz:2 abid:2\n"
    "ds_read2st64_b32 a[206:207], v38 offset0:42 offset1:43\n"
    "v_cvt_pk_bf16_f32 v232, v20, v24\n"
    "v_cvt_pk_bf16_f32 v236, v21, v25\n"
    "v_mfma_f32_4x4x4_16b_bf16 v[32:35], v[46:47], a[194:195], 0 cbsz:2 abid:3\n"
    "v_cvt_pk_bf16_f32 v240, v22, v26\n"
    "v_cvt_pk_bf16_f32 v244, v23, v27\n"
    "s_waitcnt vmcnt(6) lgkmcnt(3)\n"
    "ds_read2st64_b32 a[208:209], v38 offset0:44 offset1:45\n"
    "v_mfma_f32_4x4x4_16b_bf16 v[20:23], v[48:49], a[196:197], 0 cbsz:2 abid:0\n"
    "ds_read2st64_b32 a[210:211], v38 offset0:46 offset1:47\n"
    "v_cvt_pk_bf16_f32 v233, v28, v32\n"
    "v_cvt_pk_bf16_f32 v237, v29, v33\n"
    "v_mfma_f32_4x4x4_16b_bf16 v[24:27], v[48:49], a[198:199], 0 cbsz:2 abid:1\n"
    "v_cvt_pk_bf16_f32 v241, v30, v34\n"
    "v_cvt_pk_bf16_f32 v245, v31, v35\n"
    "s_waitcnt lgkmcnt(4)\n"
    "v_mfma_f32_4x4x4_16b_bf16 v[28:31], v[48:49], a[200:201], 0 cbsz:2 abid:2\n"
    "s_nop 1\n"
    "v_cvt_pk_bf16_f32 v234, v20, v24\n"
    "v_cvt_pk_bf16_f32 v238, v21, v25\n"
    "v_mfma_f32_4x4x4_16b_bf16 v[32:35], v[48:49], a[202:203], 0 cbsz:2 abid:3\n"
    "v_cvt_pk_bf16_f32 v242, v22, v26\n"
    "v_cvt_pk_bf16_f32 v246, v23, v27\n"
    "s_waitcnt lgkmcnt(2)\n"
    "v_mfma_f32_4x4x4_16b_bf16 v[20:23], v[50:51], a[204:205], 0 cbsz:2 abid:0\n"
    "s_nop 1\n"
    "v_cvt_pk_bf16_f32 v235, v28, v32\n"
    "v_cvt_pk_bf16_f32 v239, v29, v33\n"
    "v_mfma_f32_4x4x4_16b_bf16 v[24:27], v[50:51], a[206:207], 0 cbsz:2 abid:1\n"
    "v_cvt_pk_bf16_f32 v243, v30, v34\n"
    "v_cvt_pk_bf16_f32 v247, v31, v35\n"
    "s_waitcnt lgkmcnt(0)\n"
    "v_mfma_f32_4x4x4_16b_bf16 v[28:31], v[50:51], a[208:209], 0 cbsz:2 abid:2\n"
    "s_nop 1\n"
    "v_cvt_pk_bf16_f32 v248, v20, v22\n"
    "v_cvt_pk_bf16_f32 v252, v21, v23\n"
    "v_mfma_f32_4x4x4_16b_bf16 v[32:35], v[50:51], a[210:211], 0 cbsz:2 abid:3\n"
    "v_cvt_pk_bf16_f32 v249, v24, v26\n"
    "v_cvt_pk_bf16_f32 v253, v25, v27\n"
    "s_waitcnt lgkmcnt(0)\n"
    "s_add_u32 s89, s92, 248\n"
    "v_add_u32 v38, s11, v38\n"
    "s_nop 0\n"
    "v_cvt_pk_bf16_f32 v250, v28, v30\n"
    "v_cvt_pk_bf16_f32 v254, v29, v31\n"
    "v_cvt_pk_bf16_f32 v251, v32, v34\n"
    "v_cvt_pk_bf16_f32 v255, v33, v35\n"
    "s_waitcnt vmcnt(3)\n"
    "s_barrier\n"
    "ds_read_b128 a[88:91], v4\n"
    "ds_read_b128 a[92:95], v5\n"
    "ds_read_b128 a[96:99], v6\n"
    "ds_read_b128 a[100:103], v7\n"
    "ds_read_b128 a[104:107], v4 offset:256\n"
    "ds_read_b128 a[108:111], v5 offset:256\n"
    "ds_read_b128 a[112:115], v6 offset:256\n"
    "ds_read_b128 a[116:119], v7 offset:256\n"
    "ds_read_b128 a[120:123], v4 offset:512\n"
    "ds_read_b128 a[124:127], v5 offset:512\n"
    "ds_read_b128 a[128:131], v6 offset:512\n"
    "ds_read_b128 a[132:135], v7 offset:512\n"
    "s_waitcnt lgkmcnt(11)\n"
    "v_mfma_f32_16x16x32_bf16 v[20:23], a[88:91], a[16:19], 0\n"
    "v_add_u32 v14, 1, v14\n"
    "s_sub_i32 s46, s52, 5\n"
    "s_max_i32 s46, s46, 0\n"
    "s_waitcnt lgkmcnt(10)\n"
    "v_mfma_f32_16x16x32_bf16 v[20:23], a[92:95], a[40:43], v[20:23]\n"
    "s_add_u32 m0, s57, 24576\n"
    "s_bfm_b64 s[44:45], s46, 5\n"
    "v_subrev_u32 v43, 5, v19\n"
    "s_waitcnt lgkmcnt(9)\n"
    "v_mfma_f32_16x16x32_bf16 v[20:23], a[96:99], a[64:67], v[20:23]\n"
    "buffer_load_dwordx4 v0, s[36:39], s74 offen nt lds\n"
    "s_andn2_b64 s[50:51], s[44:45], s[26:27]\n"
    "s_waitcnt lgkmcnt(8)\n"
    "v_mfma_f32_16x16x32_bf16 v[20:23], a[100:103], v[56:59], v[20:23]\n"
    "s_and_b64 s[48:49], s[44:45], s[26:27]\n"
    "s_bcnt1_i32_b64 s47, s[50:51]\n"
    "s_sub_u32 s46, s46, s47\n"
    "s_waitcnt lgkmcnt(7)\n"
    "v_mfma_f32_16x16x32_bf16 v[20:23], a[104:107], v[80:83], v[20:23]\n"
    "buffer_load_dwordx4 v1, s[36:39], s74 offen offset:1024 nt lds\n"
    "s_add_u32 s58, s46, 5\n"
    "s_waitcnt lgkmcnt(6)\n"
    "v_mfma_f32_16x16x32_bf16 v[20:23], a[108:111], v[104:107], v[20:23]\n"
    "v_mov_b32 v45, s58\n"
    "v_mbcnt_lo_u32_b32 v47, s48, 5\n"
    "v_mbcnt_lo_u32_b32 v45, s50, v45\n"
    "s_waitcnt lgkmcnt(5)\n"
    "v_mfma_f32_16x16x32_bf16 v[20:23], a[112:115], v[128:131], v[20:23]\n"
    "buffer_load_dwordx4 v2, s[36:39], s74 offen offset:2048 nt lds\n"
    "v_mbcnt_hi_u32_b32 v47, s49, v47\n"
    "s_waitcnt lgkmcnt(4)\n"
    "v_mfma_f32_16x16x32_bf16 v[20:23], a[116:119], v[152:155], v[20:23]\n"
    "v_mbcnt_hi_u32_b32 v45, s51, v45\n"
    "v_cndmask_b32 v47, v19, v47, s[48:49]\n"
    "s_sub_i32 s59, s47, 4\n"
    "s_waitcnt lgkmcnt(3)\n"
    "v_mfma_f32_16x16x32_bf16 v[20:23], a[120:123], v[176:179], v[20:23]\n"
    "v_cndmask_b32 v47, v47, v45, s[50:51]\n"
    "s_max_i32 s59, s59, 0\n"
    "v_lshlrev_b32 v47, 2, v47\n"
    "s_waitcnt lgkmcnt(2)\n"
    "v_mfma_f32_16x16x32_bf16 v[20:23], a[124:127], v[200:203], v[20:23]\n"
    "s_mul_i32 s59, s59, 9\n"
    "s_min_u32 s59, s59, 256\n"
    "ds_permute_b32 v3, v47, v3\n"
    "s_waitcnt lgkmcnt(2)\n"
    "v_mfma_f32_16x16x32_bf16 v[20:23], a[128:131], v[224:227], v[20:23]\n"
    "s_bcnt1_i32_b64 s73, s[24:25]\n"
    "s_mul_i32 s62, s73, 16\n"
    "s_lshr_b32 s62, s62, 5\n"
    "s_waitcnt lgkmcnt(1)\n"
    "v_mfma_f32_16x16x32_bf16 v[20:23], a[132:135], v[248:251], v[20:23]\n"
    "s_add_u32 s62, s62, 32\n"
    "s_sub_u32 s63, s62, 30\n"
    "v_bfe_i32 v11, v11, 0, 16\n"
    "v_mfma_f32_16x16x32_bf16 v[24:27], a[132:135], v[252:255], 0\n"
    "s_nop 0\n"
    "v_readlane_b32 s7, v11, 0\n"
    "v_readlane_b32 s8, v11, 1\n"
    "v_mfma_f32_16x16x32_bf16 v[24:27], a[128:131], v[228:231], v[24:27]\n"
    "v_readlane_b32 s60, v11, 2\n"
    "v_readlane_b32 s61, v11, 3\n"
    "s_max_i32 s7, s7, s8\n"
    "v_mfma_f32_16x16x32_bf16 v[24:27], a[124:127], v[204:207], v[24:27]\n"
    "s_max_i32 s8, s60, s61\n"
    "s_max_i32 s7, s7, s8\n"
    "s_mul_i32 s7, s7, 3\n"
    "v_mfma_f32_16x16x32_bf16 v[24:27], a[120:123], v[180:183], v[24:27]\n"
    "s_add_i32 s7, s7, 32\n"
    "s_ashr_i32 s7, s7, 6\n"
    "s_max_i32 s7, s7, 0\n"
    "v_mfma_f32_16x16x32_bf16 v[24:27], a[116:119], v[156:159], v[24:27]\n"
    "s_min_i32 s72, s7, 4\n"
    "v_mul_i32_i24 v40, s59, v43\n"
    "s_mul_i32 s7, s2, 9\n"
    "v_mfma_f32_16x16x32_bf16 v[24:27], a[112:115], v[132:135], v[24:27]\n"
    "s_and_b32 s7, s7, 0xff\n"
    "v_add_u32 v40, s7, v40\n"
    "v_subrev_u32 v42, s46, v43\n"
    "v_mfma_f32_16x16x32_bf16 v[24:27], a[108:111], v[108:111], v[24:27]\n"
    "v_add_u32 v41, s59, v40\n"
    "v_ashrrev_i32 v40, 8, v40\n"
    "v_ashrrev_i32 v41, 8, v41\n"
    "v_mfma_f32_16x16x32_bf16 v[24:27], a[104:107], v[84:87], v[24:27]\n"
    "v_min_i32 v40, s47, v40\n"
    "v_min_i32 v41, s47, v41\n"
    "v_max_i32 v40, v40, v42\n"
    "v_mfma_f32_16x16x32_bf16 v[24:27], a[100:103], v[60:63], v[24:27]\n"
    "v_add_u32 v42, 1, v42\n"
    "v_max_i32 v41, v41, v42\n"
    "v_cmp_gt_i32 s[60:61], v41, v40\n"
    "v_mfma_f32_16x16x32_bf16 v[24:27], a[96:99], a[68:71], v[24:27]\n"
    "s_waitcnt vmcnt(3)\n"
    "v_add_u32 v41, s58, v40\n"
    "v_sub_u32 v40, v19, v40\n"
    "v_mfma_f32_16x16x32_bf16 v[24:27], a[92:95], a[44:47], v[24:27]\n"
    "ds_write_b32 v13, v14\n"
    "v_cndmask_b32 v40, v40, v41, s[60:61]\n"
    "v_mfma_f32_16x16x32_bf16 v[24:27], a[88:91], a[20:23], v[24:27]\n"
    "s_add_u32 m0, s57, 36864\n"
    "v_cndmask_b32 v40, v19, v40, s[44:45]\n"
    "v_lshlrev_b32 v40, 2, v40\n"
    "v_mfma_f32_16x16x32_bf16 v[28:31], a[88:91], a[0:3], 0\n"
    "buffer_load_dwordx4 v0, s[36:39], s75 offen nt lds\n"
    "v_mfma_f32_16x16x32_bf16 v[28:31], a[92:95], a[24:27], v[28:31]\n"
    "s_waitcnt lgkmcnt(1)\n"
    "ds_bpermute_b32 v3, v40, v3\n"
    "s_mul_i32 s94, s73, 8\n"
    "v_mfma_f32_16x16x32_bf16 v[28:31], a[96:99], a[48:51], v[28:31]\n"
    "buffer_load_dwordx4 v1, s[36:39], s75 offen offset:1024 nt lds\n"
    "s_mul_i32 s31, s73, 6\n"
    "v_mfma_f32_16x16x32_bf16 v[28:31], a[100:103], a[72:75], v[28:31]\n"
    "v_cvt_pk_bf16_f32 v52, v20, v24\n"
    "s_lshr_b32 s94, s94, 5\n"
    "s_lshr_b32 s31, s31, 5\n"
    "v_mfma_f32_16x16x32_bf16 v[28:31], a[104:107], v[64:67], v[28:31]\n"
    "ds_read_b32 v15, v12\n"
    "s_mul_i32 s7, s95, -2\n"
    "s_add_u32 s7, s7, 67\n"
    "v_mfma_f32_16x16x32_bf16 v[28:31], a[108:111], v[88:91], v[28:31]\n"
    "v_cvt_pk_bf16_f32 v53, v21, v25\n"
    "s_add_u32 s94, s94, s7\n"
    "s_add_u32 s31, s31, s7\n"
    "v_mfma_f32_16x16x32_bf16 v[28:31], a[112:115], v[112:115], v[28:31]\n"
    "v_cvt_pk_bf16_f32 v54, v22, v26\n"
    "s_sub_u32 s7, 6, s72\n"
    "s_add_u32 s31, s31, s7\n"
    "v_mfma_f32_16x16x32_bf16 v[28:31], a[116:119], v[136:139], v[28:31]\n"
    "v_cvt_pk_bf16_f32 v55, v23, v27\n"
    "s_waitcnt lgkmcnt(0)\n"
    "v_cmp_lt_u32 vcc, v15, v14\n"
    "v_mfma_f32_16x16x32_bf16 v[28:31], a[120:123], v[160:163], v[28:31]\n"
    "s_add_u32 s76, s52, 4\n"
    "v_subrev_u32 v18, 5, v19\n"
    "s_mov_b32 s30, s94\n"
    "v_mfma_f32_16x16x32_bf16 v[28:31], a[124:127], v[184:187], v[28:31]\n"
    "s_cbranch_vccnz 81b\n"
    "82:\n"
    "s_add_u32 s94, s94, 12\n"
    "v_mfma_f32_16x16x32_bf16 v[28:31], a[128:131], v[208:211], v[28:31]\n"
    "ds_write2st64_b32 v38, v52, v53 offset0:0 offset1:2\n"
    "s_sub_u32 s7, s52, 5\n"
    "v_mfma_f32_16x16x32_bf16 v[28:31], a[132:135], v[232:235], v[28:31]\n"
    "ds_read_b128 a[136:139], v4 offset:12288\n"
    "s_mov_b64 vcc, s[12:13]\n"
    "s_andn2_b32 s7, s7, 1\n"
    "v_mfma_f32_16x16x32_bf16 v[32:35], a[132:135], v[236:239], 0\n"
    "ds_write2st64_b32 v38, v54, v55 offset0:4 offset1:6\n"
    "v_mfma_f32_16x16x32_bf16 v[32:35], a[128:131], v[212:215], v[32:35]\n"
    "ds_read_b128 a[140:143], v5 offset:12288\n"
    "v_add_u32 v14, 1, v14\n"
    "v_mfma_f32_16x16x32_bf16 v[32:35], a[124:127], v[188:191], v[32:35]\n"
    "buffer_load_dwordx4 v2, s[36:39], s75 offen offset:2048 nt lds\n"
    "v_mfma_f32_16x16x32_bf16 v[32:35], a[120:123], v[164:167], v[32:35]\n"
    "ds_read_b128 a[144:147], v6 offset:12288\n"
    "buffer_load_dword a212, off, s[36:39], s33\n"
    "v_mfma_f32_16x16x32_bf16 v[32:35], a[116:119], v[140:143], v[32:35]\n"
    "buffer_load_dword a212, off, s[36:39], s33\n"
    "buffer_load_dword a212, off, s[36:39], s33\n"
    "v_mfma_f32_16x16x32_bf16 v[32:35], a[112:115], v[116:119], v[32:35]\n"
    "s_cmp_lt_i32 s7, 2\n"
    "s_cselect_b32 s7, 0x1000, s7\n"
    "v_mfma_f32_16x16x32_bf16 v[32:35], a[108:111], v[92:95], v[32:35]\n"
    "ds_read_b128 a[148:151], v7 offset:12288\n"
    "s_cmp_lt_u32 s47, 3\n"
    "s_cselect_b32 s7, 0x1000, s7\n"
    "v_mfma_f32_16x16x32_bf16 v[32:35], a[104:107], v[68:71], v[32:35]\n"
    "s_add_u32 s70, s7, -1\n"
    "v_subrev_u32 v40, s70, v18\n"
    "v_cmp_gt_u32 s[60:61], 2, v40\n"
    "v_mfma_f32_16x16x32_bf16 v[32:35], a[100:103], a[76:79], v[32:35]\n"
    "ds_read_b128 a[152:155], v4 offset:12544\n"
    "s_add_u32 s64, s7, 5\n"
    "s_add_u32 s65, s7, 6\n"
    "v_mfma_f32_16x16x32_bf16 v[32:35], a[96:99], a[52:55], v[32:35]\n"
    "v_cndmask_b32 v18, v18, 63, s[60:61]\n"
    "s_add_u32 s71, s7, 5\n"
    "v_lshlrev_b32 v18, 2, v18\n"
    "v_mfma_f32_16x16x32_bf16 v[32:35], a[92:95], a[28:31], v[32:35]\n"
    "ds_read_b128 a[156:159], v5 offset:12544\n"
    "ds_bpermute_b32 v18, v18, v3\n"
    "v_mfma_f32_16x16x32_bf16 v[32:35], a[88:91], a[4:7], v[32:35]\n"
    "v_readlane_b32 s84, v3, s86\n"
    "v_mfma_f32_16x16x32_bf16 v[20:23], a[88:91], a[8:11], 0\n"
    "ds_read_b128 a[160:163], v6 offset:12544\n"
    "v_mfma_f32_16x16x32_bf16 v[20:23], a[92:95], a[32:35], v[20:23]\n"
    "v_mfma_f32_16x16x32_bf16 v[20:23], a[96:99], a[56:59], v[20:23]\n"
    "ds_read_b128 a[164:167], v7 offset:12544\n"
    "v_mfma_f32_16x16x32_bf16 v[20:23], a[100:103], a[80:83], v[20:23]\n"
    "v_cvt_pk_bf16_f32 v36, v28, v32\n"
    "v_mfma_f32_16x16x32_bf16 v[20:23], a[104:107], v[72:75], v[20:23]\n"
    "ds_read_b128 a[168:171], v4 offset:12800\n"
    "v_mfma_f32_16x16x32_bf16 v[20:23], a[108:111], v[96:99], v[20:23]\n"
    "v_cvt_pk_bf16_f32 v46, v29, v33\n"
    "v_mfma_f32_16x16x32_bf16 v[20:23], a[112:115], v[120:123], v[20:23]\n"
    "ds_read_b128 a[172:175], v5 offset:12800\n"
    "s_add_u32 m0, s57, 49152\n"
    "v_mfma_f32_16x16x32_bf16 v[20:23], a[116:119], v[144:147], v[20:23]\n"
    "buffer_load_dwordx4 v0, s[36:39], s81 offen nt lds\n"
    "v_mfma_f32_16x16x32_bf16 v[20:23], a[120:123], v[168:171], v[20:23]\n"
    "v_cvt_pk_bf16_f32 v37, v30, v34\n"
    "v_mfma_f32_16x16x32_bf16 v[20:23], a[124:127], v[192:195], v[20:23]\n"
    "ds_read_b128 a[176:179], v6 offset:12800\n"
    "v_mfma_f32_16x16x32_bf16 v[20:23], a[128:131], v[216:219], v[20:23]\n"
    "v_cvt_pk_bf16_f32 v50, v31, v35\n"
    "v_mfma_f32_16x16x32_bf16 v[20:23], a[132:135], v[240:243], v[20:23]\n"
    "ds_read_b128 a[180:183], v7 offset:12800\n"
    "v_mfma_f32_16x16x32_bf16 v[24:27], a[132:135], v[244:247], 0\n"
    "buffer_load_dwordx4 v1, s[36:39], s81 offen offset:1024 nt lds\n"
    "v_mfma_f32_16x16x32_bf16 v[24:27], a[128:131], v[220:223], v[24:27]\n"
    "v_cndmask_b32_dpp v44, v46, v36, vcc row_ror:8 row_mask:0xf bank_mask:0xf\n"
    "v_mfma_f32_16x16x32_bf16 v[24:27], a[124:127], v[196:199], v[24:27]\n"
    "buffer_load_dwordx4 v2, s[36:39], s81 offen offset:2048 nt lds\n"
    "v_mfma_f32_16x16x32_bf16 v[24:27], a[120:123], v[172:175], v[24:27]\n"
    "s_memrealtime s[92:93]\n"
    "buffer_load_dword a212, off, s[36:39], s33\n"
    "v_mfma_f32_16x16x32_bf16 v[24:27], a[116:119], v[148:151], v[24:27]\n"
    "v_cndmask_b32_dpp v48, v50, v37, vcc row_ror:8 row_mask:0xf bank_mask:0xf\n"
    "v_mfma_f32_16x16x32_bf16 v[24:27], a[112:115], v[124:127], v[24:27]\n"
    "v_mov_b32_dpp v46, v36 row_ror:8 row_mask:0xf bank_mask:0x3\n"
    "v_mfma_f32_16x16x32_bf16 v[24:27], a[108:111], v[100:103], v[24:27]\n"
    "v_mov_b32_dpp v50, v37 row_ror:8 row_mask:0xf bank_mask:0x3\n"
    "v_mfma_f32_16x16x32_bf16 v[24:27], a[104:107], v[76:79], v[24:27]\n"
    "v_mfma_f32_16x16x32_bf16 v[24:27], a[100:103], a[84:87], v[24:27]\n"
    "s_waitcnt vmcnt(10) lgkmcnt(0)\n"
    "s_sub_u32 s85, s92, s89\n"
    "v_mfma_f32_16x16x32_bf16 v[24:27], a[96:99], a[60:63], v[24:27]\n"
    "s_add_i32 s63, s63, s85\n"
    "s_max_i32 s63, s63, s62\n"
    "s_add_u32 s89, s89, s63\n"
    "v_mfma_f32_16x16x32_bf16 v[24:27], a[92:95], a[36:39], v[24:27]\n"
    "s_mov_b32 s81, s84\n"
    "s_add_u32 m0, s57, 61440\n"
    "v_mfma_f32_16x16x32_bf16 v[24:27], a[88:91], a[12:15], v[24:27]\n"
    "s_branch 52f\n"
    ".p2align 6\n"
    "51:\n"
    "v_mfma_f32_16x16x32_bf16 v[20:23], a[88:91], a[16:19], 0\n"
    "s_add_u32 s86, s86, 1\n"
    "v_mfma_f32_16x16x32_bf16 v[20:23], a[92:95], a[40:43], v[20:23]\n"
    "s_waitcnt vmcnt(13)\n"
    "v_mfma_f32_16x16x32_bf16 v[20:23], a[96:99], a[64:67], v[20:23]\n"
    "ds_write_b32 v13, v14\n"
    "v_mfma_f32_16x16x32_bf16 v[20:23], a[100:103], v[56:59], v[20:23]\n"
    "v_cvt_pk_bf16_f32 v36, v28, v32\n"
    "v_mfma_f32_16x16x32_bf16 v[20:23], a[104:107], v[80:83], v[20:23]\n"
    "v_cvt_pk_bf16_f32 v47, v29, v33\n"
    "v_mfma_f32_16x16x32_bf16 v[20:23], a[108:111], v[104:107], v[20:23]\n"
    "v_cvt_pk_bf16_f32 v37, v30, v34\n"
    "v_mfma_f32_16x16x32_bf16 v[20:23], a[112:115], v[128:131], v[20:23]\n"
    "v_cvt_pk_bf16_f32 v51, v31, v35\n"
    "v_mfma_f32_16x16x32_bf16 v[20:23], a[116:119], v[152:155], v[20:23]\n"
    "v_cndmask_b32_dpp v45, v47, v36, vcc row_ror:8 row_mask:0xf bank_mask:0xf\n"
    "v_mfma_f32_16x16x32_bf16 v[20:23], a[120:123], v[176:179], v[20:23]\n"
    "v_cndmask_b32_dpp v49, v51, v37, vcc row_ror:8 row_mask:0xf bank_mask:0xf\n"
    "v_mfma_f32_16x16x32_bf16 v[20:23], a[124:127], v[200:203], v[20:23]\n"
    "v_mov_b32_dpp v47, v36 row_ror:8 row_mask:0xf bank_mask:0x3\n"
    "v_mfma_f32_16x16x32_bf16 v[20:23], a[128:131], v[224:227], v[20:23]\n"
    "v_mov_b32_dpp v51, v37 row_ror:8 row_mask:0xf bank_mask:0x3\n"
    "v_mfma_f32_16x16x32_bf16 v[20:23], a[132:135], v[248:251], v[20:23]\n"
    "ds_read_b32 v15, v12\n"
    "v_mfma_f32_16x16x32_bf16 v[24:27], a[132:135], v[252:255], 0\n"
    "s_add_u32 s89, s89, s94\n"
    "v_mfma_f32_16x16x32_bf16 v[24:27], a[128:131], v[228:231], v[24:27]\n"
    "s_sub_u32 s94, s94, 6\n"
    "v_mfma_f32_16x16x32_bf16 v[24:27], a[124:127], v[204:207], v[24:27]\n"
    "buffer_store_dwordx4 v[44:47], v8, s[40:43], s82 offen sc1\n"
    "v_mfma_f32_16x16x32_bf16 v[24:27], a[120:123], v[180:183], v[24:27]\n"
    "s_max_u32 s94, s94, s30\n"
    "v_mfma_f32_16x16x32_bf16 v[24:27], a[116:119], v[156:159], v[24:27]\n"
    "s_waitcnt lgkmcnt(0)\n"
    "v_cmp_lt_u32 vcc, v15, v14\n"
    "v_mfma_f32_16x16x32_bf16 v[24:27], a[112:115], v[132:135], v[24:27]\n"
    "v_mfma_f32_16x16x32_bf16 v[24:27], a[108:111], v[108:111], v[24:27]\n"
    "s_cbranch_vccnz 41b\n"
    "42:\n"
    "v_mfma_f32_16x16x32_bf16 v[24:27], a[104:107], v[84:87], v[24:27]\n"
    "ds_read_b128 a[184:187], v39 offset:4096\n"
    "v_mfma_f32_16x16x32_bf16 v[24:27], a[100:103], v[60:63], v[24:27]\n"
    "s_cmp_eq_u32 s86, s64\n"
    "s_cbranch_scc1 45b\n"
    "v_mfma_f32_16x16x32_bf16 v[24:27], a[96:99], a[68:71], v[24:27]\n"
    "ds_read_b128 a[136:139], v4 offset:12288\n"
    "v_mfma_f32_16x16x32_bf16 v[24:27], a[92:95], a[44:47], v[24:27]\n"
    "v_add_u32 v14, 1, v14\n"
    "v_mfma_f32_16x16x32_bf16 v[24:27], a[88:91], a[20:23], v[24:27]\n"
    "s_mov_b64 vcc, s[12:13]\n"
    "v_mfma_f32_16x16x32_bf16 v[28:31], a[88:91], a[0:3], 0\n"
    "buffer_load_dwordx4 v0, s[36:39], s81 offen nt lds\n"
    "v_mfma_f32_16x16x32_bf16 v[28:31], a[92:95], a[24:27], v[28:31]\n"
    "ds_read_b128 a[140:143], v5 offset:12288\n"
    "v_mfma_f32_16x16x32_bf16 v[28:31], a[96:99], a[48:51], v[28:31]\n"
    "ds_read_b128 a[144:147], v6 offset:12288\n"
    "v_mfma_f32_16x16x32_bf16 v[28:31], a[100:103], a[72:75], v[28:31]\n"
    "v_cvt_pk_bf16_f32 v52, v20, v24\n"
    "v_mfma_f32_16x16x32_bf16 v[28:31], a[104:107], v[64:67], v[28:31]\n"
    "v_cvt_pk_bf16_f32 v53, v21, v25\n"
    "v_mfma_f32_16x16x32_bf16 v[28:31], a[108:111], v[88:91], v[28:31]\n"
    "v_cvt_pk_bf16_f32 v54, v22, v26\n"
    "v_mfma_f32_16x16x32_bf16 v[28:31], a[112:115], v[112:115], v[28:31]\n"
    "v_cvt_pk_bf16_f32 v55, v23, v27\n"
    "v_mfma_f32_16x16x32_bf16 v[28:31], a[116:119], v[136:139], v[28:31]\n"
    "ds_read_b128 a[148:151], v7 offset:12288\n"
    "v_mfma_f32_16x16x32_bf16 v[28:31], a[120:123], v[160:163], v[28:31]\n"
    "v_mfma_f32_16x16x32_bf16 v[28:31], a[124:127], v[184:187], v[28:31]\n"
    "buffer_load_dwordx4 v1, s[36:39], s81 offen offset:1024 nt lds\n"
    "v_mfma_f32_16x16x32_bf16 v[28:31], a[128:131], v[208:211], v[28:31]\n"
    "v_mfma_f32_16x16x32_bf16 v[28:31], a[132:135], v[232:235], v[28:31]\n"
    "ds_read_b128 a[152:155], v4 offset:12544\n"
    "v_mfma_f32_16x16x32_bf16 v[32:35], a[132:135], v[236:239], 0\n"
    "v_mfma_f32_16x16x32_bf16 v[32:35], a[128:131], v[212:215], v[32:35]\n"
    "ds_write2st64_b32 v38, v52, v53 offset0:0 offset1:2\n"
    "v_mfma_f32_16x16x32_bf16 v[32:35], a[124:127], v[188:191], v[32:35]\n"
    "46:\n"
    "v_mfma_f32_16x16x32_bf16 v[32:35], a[120:123], v[164:167], v[32:35]\n"
    "ds_read_b128 a[156:159], v5 offset:12544\n"
    "v_mfma_f32_16x16x32_bf16 v[32:35], a[116:119], v[140:143], v[32:35]\n"
    "v_mfma_f32_16x16x32_bf16 v[32:35], a[112:115], v[116:119], v[32:35]\n"
    "v_mfma_f32_16x16x32_bf16 v[32:35], a[108:111], v[92:95], v[32:35]\n"
    "buffer_store_dwordx4 v[48:51], v9, s[40:43], s82 offen sc1\n"
    "v_mfma_f32_16x16x32_bf16 v[32:35], a[104:107], v[68:71], v[32:35]\n"
    "v_mfma_f32_16x16x32_bf16 v[32:35], a[100:103], a[76:79], v[32:35]\n"
    "ds_read_b128 a[160:163], v6 offset:12544\n"
    "v_mfma_f32_16x16x32_bf16 v[32:35], a[96:99], a[52:55], v[32:35]\n"
    "ds_read_b128 a[164:167], v7 offset:12544\n"
    "v_mfma_f32_16x16x32_bf16 v[32:35], a[92:95], a[28:31], v[32:35]\n"
    "ds_read_b128 a[168:171], v4 offset:12800\n"
    "v_mfma_f32_16x16x32_bf16 v[32:35], a[88:91], a[4:7], v[32:35]\n"
    "v_mfma_f32_16x16x32_bf16 v[20:23], a[88:91], a[8:11], 0\n"
    "ds_write2st64_b32 v38, v54, v55 offset0:4 offset1:6\n"
    "v_mfma_f32_16x16x32_bf16 v[20:23], a[92:95], a[32:35], v[20:23]\n"
    "v_mfma_f32_16x16x32_bf16 v[20:23], a[96:99], a[56:59], v[20:23]\n"
    "ds_read_b128 a[172:175], v5 offset:12800\n"
    "v_mfma_f32_16x16x32_bf16 v[20:23], a[100:103], a[80:83], v[20:23]\n"
    "v_cvt_pk_bf16_f32 v36, v28, v32\n"
    "v_mfma_f32_16x16x32_bf16 v[20:23], a[104:107], v[72:75], v[20:23]\n"
    "v_cvt_pk_bf16_f32 v46, v29, v33\n"
    "v_mfma_f32_16x16x32_bf16 v[20:23], a[108:111], v[96:99], v[20:23]\n"
    "ds_read_b128 a[176:179], v6 offset:12800\n"
    "v_mfma_f32_16x16x32_bf16 v[20:23], a[112:115], v[120:123], v[20:23]\n"
    "v_cvt_pk_bf16_f32 v37, v30, v34\n"
    "v_mfma_f32_16x16x32_bf16 v[20:23], a[116:119], v[144:147], v[20:23]\n"
    "v_cvt_pk_bf16_f32 v50, v31, v35\n"
    "v_mfma_f32_16x16x32_bf16 v[20:23], a[120:123], v[168:171], v[20:23]\n"
    "buffer_load_dwordx4 v2, s[36:39], s81 offen offset:2048 nt lds\n"
    "v_mfma_f32_16x16x32_bf16 v[20:23], a[124:127], v[192:195], v[20:23]\n"
    "v_cndmask_b32_dpp v44, v46, v36, vcc row_ror:8 row_mask:0xf bank_mask:0xf\n"
    "v_mfma_f32_16x16x32_bf16 v[20:23], a[128:131], v[216:219], v[20:23]\n"
    "ds_read_b128 a[180:183], v7 offset:12800\n"
    "v_mfma_f32_16x16x32_bf16 v[20:23], a[132:135], v[240:243], v[20:23]\n"
    "v_cndmask_b32_dpp v48, v50, v37, vcc row_ror:8 row_mask:0xf bank_mask:0xf\n"
    "v_mfma_f32_16x16x32_bf16 v[24:27], a[132:135], v[244:247], 0\n"
    "s_memrealtime s[92:93]\n"
    "v_mfma_f32_16x16x32_bf16 v[24:27], a[128:131], v[220:223], v[24:27]\n"
    "v_mov_b32_dpp v46, v36 row_ror:8 row_mask:0xf bank_mask:0x3\n"
    "v_mfma_f32_16x16x32_bf16 v[24:27], a[124:127], v[196:199], v[24:27]\n"
    "v_mov_b32_dpp v50, v37 row_ror:8 row_mask:0xf bank_mask:0x3\n"
    "v_mfma_f32_16x16x32_bf16 v[24:27], a[120:123], v[172:175], v[24:27]\n"
    "v_readlane_b32 s81, v3, s86\n"
    "v_mfma_f32_16x16x32_bf16 v[24:27], a[116:119], v[148:151], v[24:27]\n"
    "s_waitcnt lgkmcnt(14)\n"
    "buffer_store_dwordx4 a[184:187], v10, s[40:43], s82 offen sc1\n"
    "v_mfma_f32_16x16x32_bf16 v[24:27], a[112:115], v[124:127], v[24:27]\n"
    "s_addk_i32 m0, 12288\n"
    "v_mfma_f32_16x16x32_bf16 v[24:27], a[108:111], v[100:103], v[24:27]\n"
    "v_readlane_b32 s82, v18, s86\n"
    "v_mfma_f32_16x16x32_bf16 v[24:27], a[104:107], v[76:79], v[24:27]\n"
    "s_waitcnt lgkmcnt(0)\n"
    "s_sub_u32 s85, s92, s89\n"
    "v_mfma_f32_16x16x32_bf16 v[24:27], a[100:103], a[84:87], v[24:27]\n"
    "s_cbranch_scc1 43b\n"
    "44:\n"
    "v_mfma_f32_16x16x32_bf16 v[24:27], a[96:99], a[60:63], v[24:27]\n"
    "s_cmp_eq_u32 m0, s77\n"
    "s_cselect_b32 s78, s87, s88\n"
    "v_mfma_f32_16x16x32_bf16 v[24:27], a[92:95], a[36:39], v[24:27]\n"
    "s_cmp_lt_u32 s86, s76\n"
    "v_mfma_f32_16x16x32_bf16 v[24:27], a[88:91], a[12:15], v[24:27]\n"
    "s_cbranch_scc0 55f\n"
    "52:\n"
    "v_mfma_f32_16x16x32_bf16 v[28:31], a[136:139], a[16:19], 0\n"
    "s_add_u32 s86, s86, 1\n"
    "v_mfma_f32_16x16x32_bf16 v[28:31], a[140:143], a[40:43], v[28:31]\n"
    "s_waitcnt vmcnt(13)\n"
    "v_mfma_f32_16x16x32_bf16 v[28:31], a[144:147], a[64:67], v[28:31]\n"
    "ds_write_b32 v13, v14\n"
    "v_mfma_f32_16x16x32_bf16 v[28:31], a[148:151], v[56:59], v[28:31]\n"
    "v_cvt_pk_bf16_f32 v36, v20, v24\n"
    "v_mfma_f32_16x16x32_bf16 v[28:31], a[152:155], v[80:83], v[28:31]\n"
    "v_cvt_pk_bf16_f32 v47, v21, v25\n"
    "v_mfma_f32_16x16x32_bf16 v[28:31], a[156:159], v[104:107], v[28:31]\n"
    "v_cvt_pk_bf16_f32 v37, v22, v26\n"
    "v_mfma_f32_16x16x32_bf16 v[28:31], a[160:163], v[128:131], v[28:31]\n"
    "v_cvt_pk_bf16_f32 v51, v23, v27\n"
    "v_mfma_f32_16x16x32_bf16 v[28:31], a[164:167], v[152:155], v[28:31]\n"
    "v_cndmask_b32_dpp v45, v47, v36, vcc row_ror:8 row_mask:0xf bank_mask:0xf\n"
    "v_mfma_f32_16x16x32_bf16 v[28:31], a[168:171], v[176:179], v[28:31]\n"
    "v_cndmask_b32_dpp v49, v51, v37, vcc row_ror:8 row_mask:0xf bank_mask:0xf\n"
    "v_mfma_f32_16x16x32_bf16 v[28:31], a[172:175], v[200:203], v[28:31]\n"
    "v_mov_b32_dpp v47, v36 row_ror:8 row_mask:0xf bank_mask:0x3\n"
    "v_mfma_f32_16x16x32_bf16 v[28:31], a[176:179], v[224:227], v[28:31]\n"
    "v_mov_b32_dpp v51, v37 row_ror:8 row_mask:0xf bank_mask:0x3\n"
    "v_mfma_f32_16x16x32_bf16 v[28:31], a[180:183], v[248:251], v[28:31]\n"
    "ds_read_b32 v15, v12\n"
    "v_mfma_f32_16x16x32_bf16 v[32:35], a[180:183], v[252:255], 0\n"
    "s_add_u32 s89, s89, s94\n"
    "v_mfma_f32_16x16x32_bf16 v[32:35], a[176:179], v[228:231], v[32:35]\n"
    "v_add_u32 v4, s78, v4\n"
    "v_mfma_f32_16x16x32_bf16 v[32:35], a[172:175], v[204:207], v[32:35]\n"
    "buffer_store_dwordx4 v[44:47], v8, s[40:43], s82 offen sc1\n"
    "v_mfma_f32_16x16x32_bf16 v[32:35], a[168:171], v[180:183], v[32:35]\n"
    "v_add_u32 v5, s78, v5\n"
    "v_mfma_f32_16x16x32_bf16 v[32:35], a[164:167], v[156:159], v[32:35]\n"
    "s_waitcnt lgkmcnt(0)\n"
    "v_cmp_lt_u32 vcc, v15, v14\n"
    "v_mfma_f32_16x16x32_bf16 v[32:35], a[160:163], v[132:135], v[32:35]\n"
    "v_add_u32 v6, s78, v6\n"
    "v_mfma_f32_16x16x32_bf16 v[32:35], a[156:159], v[108:111], v[32:35]\n"
    "s_cbranch_vccnz 61b\n"
    "62:\n"
    "v_mfma_f32_16x16x32_bf16 v[32:35], a[152:155], v[84:87], v[32:35]\n"
    "ds_read_b128 a[184:187], v39\n"
    "v_mfma_f32_16x16x32_bf16 v[32:35], a[148:151], v[60:63], v[32:35]\n"
    "s_cmp_eq_u32 s86, s65\n"
    "s_cbranch_scc1 65b\n"
    "v_mfma_f32_16x16x32_bf16 v[32:35], a[144:147], a[68:71], v[32:35]\n"
    "ds_read_b128 a[88:91], v4\n"
    "v_mfma_f32_16x16x32_bf16 v[32:35], a[140:143], a[44:47], v[32:35]\n"
    "v_add_u32 v7, s78, v7\n"
    "v_mfma_f32_16x16x32_bf16 v[32:35], a[136:139], a[20:23], v[32:35]\n"
    "v_add_u32 v14, 1, v14\n"
    "v_mfma_f32_16x16x32_bf16 v[20:23], a[136:139], a[0:3], 0\n"
    "buffer_load_dwordx4 v0, s[36:39], s81 offen nt lds\n"
    "v_mfma_f32_16x16x32_bf16 v[20:23], a[140:143], a[24:27], v[20:23]\n"
    "ds_read_b128 a[92:95], v5\n"
    "v_mfma_f32_16x16x32_bf16 v[20:23], a[144:147], a[48:51], v[20:23]\n"
    "ds_read_b128 a[96:99], v6\n"
    "v_mfma_f32_16x16x32_bf16 v[20:23], a[148:151], a[72:75], v[20:23]\n"
    "v_cvt_pk_bf16_f32 v52, v28, v32\n"
    "v_mfma_f32_16x16x32_bf16 v[20:23], a[152:155], v[64:67], v[20:23]\n"
    "v_cvt_pk_bf16_f32 v53, v29, v33\n"
    "v_mfma_f32_16x16x32_bf16 v[20:23], a[156:159], v[88:91], v[20:23]\n"
    "v_cvt_pk_bf16_f32 v54, v30, v34\n"
    "v_mfma_f32_16x16x32_bf16 v[20:23], a[160:163], v[112:115], v[20:23]\n"
    "v_cvt_pk_bf16_f32 v55, v31, v35\n"
    "v_mfma_f32_16x16x32_bf16 v[20:23], a[164:167], v[136:139], v[20:23]\n"
    "ds_read_b128 a[100:103], v7\n"
    "v_mfma_f32_16x16x32_bf16 v[20:23], a[168:171], v[160:163], v[20:23]\n"
    "s_mov_b64 vcc, s[12:13]\n"
    "v_mfma_f32_16x16x32_bf16 v[20:23], a[172:175], v[184:187], v[20:23]\n"
    "buffer_load_dwordx4 v1, s[36:39], s81 offen offset:1024 nt lds\n"
    "v_mfma_f32_16x16x32_bf16 v[20:23], a[176:179], v[208:211], v[20:23]\n"
    "v_mfma_f32_16x16x32_bf16 v[20:23], a[180:183], v[232:235], v[20:23]\n"
    "ds_read_b128 a[104:107], v4 offset:256\n"
    "v_mfma_f32_16x16x32_bf16 v[24:27], a[180:183], v[236:239], 0\n"
    "v_mfma_f32_16x16x32_bf16 v[24:27], a[176:179], v[212:215], v[24:27]\n"
    "ds_write2st64_b32 v38, v52, v53 offset0:16 offset1:18\n"
    "v_mfma_f32_16x16x32_bf16 v[24:27], a[172:175], v[188:191], v[24:27]\n"
    "66:\n"
    "v_mfma_f32_16x16x32_bf16 v[24:27], a[168:171], v[164:167], v[24:27]\n"
    "ds_read_b128 a[108:111], v5 offset:256\n"
    "v_mfma_f32_16x16x32_bf16 v[24:27], a[164:167], v[140:143], v[24:27]\n"
    "s_cmp_eq_u32 s86, 20\n"
    "v_mfma_f32_16x16x32_bf16 v[24:27], a[160:163], v[116:119], v[24:27]\n"
    "s_cselect_b32 s30, s31, s30\n"
    "v_mfma_f32_16x16x32_bf16 v[24:27], a[156:159], v[92:95], v[24:27]\n"
    "buffer_store_dwordx4 v[48:51], v9, s[40:43], s82 offen sc1\n"
    "v_mfma_f32_16x16x32_bf16 v[24:27], a[152:155], v[68:71], v[24:27]\n"
    "v_mfma_f32_16x16x32_bf16 v[24:27], a[148:151], a[76:79], v[24:27]\n"
    "ds_read_b128 a[112:115], v6 offset:256\n"
    "v_mfma_f32_16x16x32_bf16 v[24:27], a[144:147], a[52:55], v[24:27]\n"
    "ds_read_b128 a[116:119], v7 offset:256\n"
    "v_mfma_f32_16x16x32_bf16 v[24:27], a[140:143], a[28:31], v[24:27]\n"
    "ds_read_b128 a[120:123], v4 offset:512\n"
    "v_mfma_f32_16x16x32_bf16 v[24:27], a[136:139], a[4:7], v[24:27]\n"
    "v_mfma_f32_16x16x32_bf16 v[28:31], a[136:139], a[8:11], 0\n"
    "ds_write2st64_b32 v38, v54, v55 offset0:20 offset1:22\n"
    "v_mfma_f32_16x16x32_bf16 v[28:31], a[140:143], a[32:35], v[28:31]\n"
    "v_mfma_f32_16x16x32_bf16 v[28:31], a[144:147], a[56:59], v[28:31]\n"
    "ds_read_b128 a[124:127], v5 offset:512\n"
    "v_mfma_f32_16x16x32_bf16 v[28:31], a[148:151], a[80:83], v[28:31]\n"
    "v_cvt_pk_bf16_f32 v36, v20, v24\n"
    "v_mfma_f32_16x16x32_bf16 v[28:31], a[152:155], v[72:75], v[28:31]\n"
    "v_cvt_pk_bf16_f32 v46, v21, v25\n"
    "v_mfma_f32_16x16x32_bf16 v[28:31], a[156:159], v[96:99], v[28:31]\n"
    "ds_read_b128 a[128:131], v6 offset:512\n"
    "v_mfma_f32_16x16x32_bf16 v[28:31], a[160:163], v[120:123], v[28:31]\n"
    "v_cvt_pk_bf16_f32 v37, v22, v26\n"
    "v_mfma_f32_16x16x32_bf16 v[28:31], a[164:167], v[144:147], v[28:31]\n"
    "v_cvt_pk_bf16_f32 v50, v23, v27\n"
    "v_mfma_f32_16x16x32_bf16 v[28:31], a[168:171], v[168:171], v[28:31]\n"
    "buffer_load_dwordx4 v2, s[36:39], s81 offen offset:2048 nt lds\n"
    "v_mfma_f32_16x16x32_bf16 v[28:31], a[172:175], v[192:195], v[28:31]\n"
    "v_cndmask_b32_dpp v44, v46, v36, vcc row_ror:8 row_mask:0xf bank_mask:0xf\n"
    "v_mfma_f32_16x16x32_bf16 v[28:31], a[176:179], v[216:219], v[28:31]\n"
    "ds_read_b128 a[132:135], v7 offset:512\n"
    "v_mfma_f32_16x16x32_bf16 v[28:31], a[180:183], v[240:243], v[28:31]\n"
    "v_cndmask_b32_dpp v48, v50, v37, vcc row_ror:8 row_mask:0xf bank_mask:0xf\n"
    "v_mfma_f32_16x16x32_bf16 v[32:35], a[180:183], v[244:247], 0\n"
    "s_memrealtime s[92:93]\n"
    "v_mfma_f32_16x16x32_bf16 v[32:35], a[176:179], v[220:223], v[32:35]\n"
    "v_mov_b32_dpp v46, v36 row_ror:8 row_mask:0xf bank_mask:0x3\n"
    "v_mfma_f32_16x16x32_bf16 v[32:35], a[172:175], v[196:199], v[32:35]\n"
    "v_mov_b32_dpp v50, v37 row_ror:8 row_mask:0xf bank_mask:0x3\n"
    "v_mfma_f32_16x16x32_bf16 v[32:35], a[168:171], v[172:175], v[32:35]\n"
    "v_readlane_b32 s81, v3, s86\n"
    "v_mfma_f32_16x16x32_bf16 v[32:35], a[164:167], v[148:151], v[32:35]\n"
    "s_waitcnt lgkmcnt(14)\n"
    "buffer_store_dwordx4 a[184:187], v10, s[40:43], s82 offen sc1\n"
    "v_mfma_f32_16x16x32_bf16 v[32:35], a[160:163], v[124:127], v[32:35]\n"
    "s_addk_i32 m0, 12288\n"
    "v_mfma_f32_16x16x32_bf16 v[32:35], a[156:159], v[100:103], v[32:35]\n"
    "v_readlane_b32 s82, v18, s86\n"
    "v_mfma_f32_16x16x32_bf16 v[32:35], a[152:155], v[76:79], v[32:35]\n"
    "s_waitcnt lgkmcnt(0)\n"
    "s_sub_u32 s85, s92, s89\n"
    "v_mfma_f32_16x16x32_bf16 v[32:35], a[148:151], a[84:87], v[32:35]\n"
    "s_cbranch_scc1 63b\n"
    "64:\n"
    "v_mfma_f32_16x16x32_bf16 v[32:35], a[144:147], a[60:63], v[32:35]\n"
    "s_cmp_eq_u32 m0, s80\n"
    "s_cselect_b32 m0, s57, m0\n"
    "v_mfma_f32_16x16x32_bf16 v[32:35], a[140:143], a[36:39], v[32:35]\n"
    "s_cmp_lt_u32 s86, s76\n"
    "v_mfma_f32_16x16x32_bf16 v[32:35], a[136:139], a[12:15], v[32:35]\n"
    "s_cbranch_scc1 51b\n"
    "s_barrier\n"
    "ds_read_b128 a[184:187], v39 offset:4096\n"
    "s_nop 4\n"
    "v_cvt_pk_bf16_f32 v36, v28, v32\n"
    "v_cvt_pk_bf16_f32 v47, v29, v33\n"
    "v_cvt_pk_bf16_f32 v37, v30, v34\n"
    "v_cvt_pk_bf16_f32 v51, v31, v35\n"
    "s_branch 56f\n"
    "55:\n"
    "s_barrier\n"
    "ds_read_b128 a[184:187], v39\n"
    "s_nop 4\n"
    "v_cvt_pk_bf16_f32 v36, v20, v24\n"
    "v_cvt_pk_bf16_f32 v47, v21, v25\n"
    "v_cvt_pk_bf16_f32 v37, v22, v26\n"
    "v_cvt_pk_bf16_f32 v51, v23, v27\n"
    "56:\n"
    "v_cndmask_b32_dpp v45, v47, v36, vcc row_ror:8 row_mask:0xf bank_mask:0xf\n"
    "v_mov_b32_dpp v47, v36 row_ror:8 row_mask:0xf bank_mask:0x3\n"
    "v_cndmask_b32_dpp v49, v51, v37, vcc row_ror:8 row_mask:0xf bank_mask:0xf\n"
    "buffer_store_dwordx4 v[44:47], v8, s[40:43], s82 offen sc1\n"
    "v_mov_b32_dpp v51, v37 row_ror:8 row_mask:0xf bank_mask:0x3\n"
    "s_waitcnt lgkmcnt(0)\n"
    "buffer_store_dwordx4 a[184:187], v10, s[40:43], s82 offen sc1\n"
    "buffer_store_dwordx4 v[48:51], v9, s[40:43], s82 offen sc1\n"
    "s_waitcnt vmcnt(0) lgkmcnt(0)\n"
    "s_endpgm\n"
    "75:\n"
    "s_sleep 2\n"
    "global_load_dwordx4 v[236:239], v16, s[54:55] sc1\n"
    "s_waitcnt vmcnt(0)\n"
    "v_sub_u32 v11, s0, v236\n"
    "v_or_b32 v236, 0xffff, v236\n"
    "v_or_b32 v238, 0xffff, v238\n"
    "v_and_b32 v242, 15, v237\n"
    "v_and_b32 v243, 1, v239\n"
    "v_or_b32 v237, 15, v237\n"
    "v_or_b32 v239, 15, v239\n"
    "v_cmp_ne_u64 vcc, s[18:19], v[236:237]\n"
    "v_cmp_ne_u64 s[48:49], s[18:19], v[238:239]\n"
    "v_readlane_b32 s9, v242, 0\n"
    "v_and_b32 v236, 1, v242\n"
    "s_or_b64 s[48:49], s[48:49], vcc\n"
    "s_cbranch_scc1 75b\n"
    "s_branch 77b\n"
    "79:\n"
    "s_nop 0\n"
    : "+{s[0:1]}"(dp), "+{s[2:3]}"(ka), "+{s[4:5]}"(did), "+{s6}"(bid), "+{v0}"(tid)
    : [lds] "{s98}"(lds0)
    : "v1","v2","v3","v4","v5","v6","v7","v8","v9","v10","v11","v12","v13","v14","v15","v16","v17","v18","v19","v20","v21","v22","v23","v24","v25","v26","v27","v28","v29","v30","v31","v32","v33","v34","v35","v36","v37","v38","v39","v40","v41","v42","v43","v44","v45","v46","v47","v48","v49","v50","v51","v52","v53","v54","v55","v56","v57","v58","v59","v60","v61","v62","v63","v64","v65","v66","v67","v68","v69","v70","v71","v72","v73","v74","v75","v76","v77","v78","v79","v80","v81","v82","v83","v84","v85","v86","v87","v88","v89","v90","v91","v92","v93","v94","v95","v96","v97","v98","v99","v100","v101","v102","v103","v104","v105","v106","v107","v108","v109","v110","v111","v112","v113","v114","v115","v116","v117","v118","v119","v120","v121","v122","v123","v124","v125","v126","v127","v128","v129","v130","v131","v132","v133","v134","v135","v136","v137","v138","v139","v140","v141","v142","v143","v144","v145","v146","v147","v148","v149","v150","v151","v152","v153","v154","v155","v156","v157","v158","v159","v160","v161","v162","v163","v164","v165","v166","v167","v168","v169","v170","v171","v172","v173","v174","v175","v176","v177","v178","v179","v180","v181","v182","v183","v184","v185","v186","v187","v188","v189","v190","v191","v192","v193","v194","v195","v196","v197","v198","v199","v200","v201","v202","v203","v204","v205","v206","v207","v208","v209","v210","v211","v212","v213","v214","v215","v216","v217","v218","v219","v220","v221","v222","v223","v224","v225","v226","v227","v228","v229","v230","v231","v232","v233","v234","v235","v236","v237","v238","v239","v240","v241","v242","v243","v244","v245","v246","v247","v248","v249","v250","v251","v252","v253","v254","v255","a0","a1","a2","a3","a4","a5","a6","a7","a8","a9","a10","a11","a12","a13","a14","a15","a16","a17","a18","a19","a20","a21","a22","a23","a24","a25","a26","a27","a28","a29","a30","a31","a32","a33","a34","a35","a36","a37","a38","a39","a40","a41","a42","a43","a44","a45","a46","a47","a48","a49","a50","a51","a52","a53","a54","a55","a56","a57","a58","a59","a60","a61","a62","a63","a64","a65","a66","a67","a68","a69","a70","a71","a72","a73","a74","a75","a76","a77","a78","a79","a80","a81","a82","a83","a84","a85","a86","a87","a88","a89","a90","a91","a92","a93","a94","a95","a96","a97","a98","a99","a100","a101","a102","a103","a104","a105","a106","a107","a108","a109","a110","a111","a112","a113","a114","a115","a116","a117","a118","a119","a120","a121","a122","a123","a124","a125","a126","a127","a128","a129","a130","a131","a132","a133","a134","a135","a136","a137","a138","a139","a140","a141","a142","a143","a144","a145","a146","a147","a148","a149","a150","a151","a152","a153","a154","a155","a156","a157","a158","a159","a160","a161","a162","a163","a164","a165","a166","a167","a168","a169","a170","a171","a172","a173","a174","a175","a176","a177","a178","a179","a180","a181","a182","a183","a184","a185","a186","a187","a188","a189","a190","a191","a192","a193","a194","a195","a196","a197","a198","a199","a200","a201","a202","a203","a204","a205","a206","a207","a208","a209","a210","a211","a212","s7","s8","s9","s10","s11","s12","s13","s14","s15","s16","s17","s18","s19","s20","s21","s22","s23","s24","s25","s26","s27","s28","s29","s30","s31","s33","s34","s35","s36","s37","s38","s39","s40","s41","s42","s43","s44","s45","s46","s47","s48","s49","s50","s51","s52","s53","s54","s55","s56","s57","s58","s59","s60","s61","s62","s63","s64","s65","s66","s67","s68","s69","s70","s71","s72","s73","s74","s75","s76","s77","s78","s79","s80","s81","s82","s83","s84","s85","s86","s87","s88","s89","s90","s91","s92","s93","s94","s95","s96","s97","vcc","scc","memory");
}

extern "C" void kernel_launch(void* const* d_in, const int* in_sizes, int n_in,
                              void* d_out, int out_size, void* d_ws, size_t ws_size,
                              hipStream_t stream) {
  Shapes sh;
  for (int j = 0; j < 16; ++j)
    for (int w = 0; w < 4; ++w) {
      const int ty = j >= 11, per = ty ? 10 : 11, n = 16 * (j - 11 * ty);
      auto row = [&](int k) { return ty ? 64 * (k / 10) + 11 + k % 10 : 64 * (k / 22) + 21 * (k / 11 % 2) + k % 11; };
      const int n0 = n + 4 * w, bp = per - n0 % per;
      uint32_t* e = sh.v + 16 * j + 4 * w;
      e[0] = row(n0) * 768 - 512;
      e[1] = (row(n0 + bp) - bp) * 768 - 512;
      e[2] = bp * 768;
      e[3] = (row(n + 8) + 16) * 768 + ty;
      for (int i = 0; i < 4; ++i) sh.rows[16 * j + 4 * w + i] = row(n0 + i) * 768;
    }
  const int grid = 256, maxr = 40;
  const size_t M = (size_t)in_sizes[0] / CH;
  const uint32_t fl = 0x00020000u;
  for (size_t r0 = 0; r0 < M; r0 += (size_t)maxr * grid * TM) {
    const size_t rows = M - r0 < (size_t)maxr * grid * TM ? M - r0 : (size_t)maxr * grid * TM;
    const int nr = (int)((rows + grid * TM - 1) / (grid * TM)), nit = nr < 2 ? 2 : nr;
    const uint32_t nbytes = (uint32_t)(rows * CH * 2);
    const uint32_t np = (nbytes >> 21) & ~1u;
    const uint64_t pages = (np >= 24 ? (np - 14) & ~7u : 64u) | (uint64_t)(np >= 2 ? (np - 2) << 21 : 0u) << 32;
    dwsep_kernel<<<grid, 256, 0, stream>>>((const char*)d_in[0] + r0 * CH * 2, nbytes, fl, (char*)d_out + r0 * CH * 2, nbytes, fl,
                                           (const char*)d_in[2], CH * CH * 2, fl, (const char*)d_in[1], CH * 2, fl,
                                           (uint32_t)nit, (uint32_t)grid * TILE_BYTES, d_ws, pages, sh);
  }
}
```

```cpp
#include <hip/hip_runtime.h>
#include <stdint.h>

#define CH 384
#define TM 16
#define TILE_BYTES (TM * CH * 2)
#define LDS_BYTES (13 * TILE_BYTES + 1024 + 64)

struct Shapes { uint32_t v[256], rows[256]; };
extern "C" __device__ uint64_t dispatch_id_() __asm("llvm.amdgcn.dispatch.id");

__global__ __launch_bounds__(256, 1)
void dwsep_kernel(const char* x, uint32_t xbytes, uint32_t f0, char* out, uint32_t obytes, uint32_t f1,
                  const char* pw, uint32_t pwbytes, uint32_t f2, const char* dw, uint32_t dwbytes, uint32_t f3,
                  uint32_t nit, uint32_t tstride, void* ws, uint64_t pages, Shapes shapes) {
  __shared__ __attribute__((aligned(1024))) char lds[LDS_BYTES];
  uint32_t lds0 = (uint32_t)(uintptr_t)(__attribute__((address_space(3))) char*)lds;
  uint64_t ka = (uint64_t)(uintptr_t)__builtin_amdgcn_kernarg_segment_ptr();
  uint64_t did = dispatch_id_(), dp = (uint64_t)(uintptr_t)__builtin_amdgcn_dispatch_ptr();
  uint32_t bid = blockIdx.x, tid = threadIdx.x;
  asm volatile(
    "s_load_dwordx16 s[36:51], s[2:3], 0x0\n"
    "v_readfirstlane_b32 s8, v0\n"
    "s_load_dwordx4 s[52:55], s[2:3], 0x40\n"
    "s_bfe_u32 s7, s6, 0x40003\n"
    "s_lshr_b32 s9, s8, 2\n"
    "s_lshl_b32 s7, s7, 6\n"
    "s_add_u32 s9, s7, s9\n"
    "s_load_dwordx4 s[20:23], s[2:3], s9 offset:0x58\n"
    "s_load_dwordx2 s[96:97], s[2:3], 0x50\n"
    "s_getpc_b64 s[14:15]\n"
    "70:\n"
    "v_lshlrev_b32 v48, 6, v0\n"
    "s_mov_b32 s13, 79f-70b\n"
    "v_min_u32 v48, s13, v48\n"
    "global_load_dword v49, v48, s[14:15]\n"
    "v_and_b32 v18, 48, v0\n"
    "v_bfe_u32 v55, v0, 3, 1\n"
    "v_bfe_u32 v54, v0, 6, 1\n"
    "v_lshl_add_u32 v53, v55, 2, v18\n"
    "s_mov_b32 s16, s4\n"
    "v_add_u32 v53, s7, v53\n"
    "v_lshl_add_u32 v54, v54, 3, v53\n"
    "global_load_dword v8, v53, s[2:3] offset:1112\n"
    "global_load_dword v9, v53, s[2:3] offset:1120\n"
    "global_load_dword v10, v54, s[2:3] offset:1112\n"
    "s_mov_b32 s2, s6\n"
    "s_mov_b32 s3, %[lds]\n"
    "s_lshr_b32 s4, s8, 6\n"
    "v_and_b32 v53, 24, v0\n"
    "v_bfe_u32 v54, v0, 5, 1\n"
    "s_bfe_u32 s5, s2, 0x20003\n"
    "v_add_u32 v53, v53, v54\n"
    "s_bfe_u32 s6, s2, 0x20005\n"
    "v_mul_u32_u24 v53, 0x300, v53\n"
    "s_cmp_eq_u32 s6, 3\n"
    "v_and_b32 v55, 7, v0\n"
    "s_cselect_b32 s6, 0, s6\n"
    "v_lshl_add_u32 v53, v55, 4, v53\n"
    "s_lshl_b32 s7, s4, 7\n"
    "v_and_b32 v16, 63, v0\n"
    "s_mul_i32 s58, s5, 24576\n"
    "v_add_u32 v244, s7, v53\n"
    "s_mul_i32 s62, s6, 98304\n"
    "v_lshlrev_b32 v16, 4, v16\n"
    "s_lshl_b32 s7, s5, 6\n"
    "s_lshl_b32 s8, s6, 8\n"
    "v_xor_b32 v53, s7, v16\n"
    "s_add_u32 s12, s3, 159744\n"
    "s_mul_i32 s83, s4, 30720\n"
    "v_add_u32 v53, s8, v53\n"
    "s_mov_b32 m0, s12\n"
    "v_subrev_u32 v55, 0x300, v53\n"
    "s_add_u32 s83, s83, s3\n"
    "v_min_u32 v250, v53, v55\n"
    "v_add_u32 v245, 0x200, v244\n"
    "v_add_u32 v246, 0x400, v244\n"
    "v_add_u32 v247, 0x600, v244\n"
    "v_and_b32 v235, 12, v0\n"
    "s_mul_i32 s57, s4, 3072\n"
    "v_bfe_u32 v52, v0, 4, 2\n"
    "s_lshl_b32 s7, s4, 6\n"
    "v_lshl_add_u32 v53, v235, 1, v52\n"
    "v_mul_u32_u24 v53, 0x300, v53\n"
    "s_add_u32 s57, s57, s3\n"
    "v_and_b32 v54, 3, v0\n"
    "s_add_u32 s7, s7, 512\n"
    "v_lshl_add_u32 v53, v54, 4, v53\n"
    "v_lshlrev_b32 v12, 2, v54\n"
    "v_add_u32 v248, s7, v53\n"
    "v_add_u32 v249, 0x800, v248\n"
    "s_lshr_b64 s[18:19], s[0:1], 12\n"
    "s_lshl4_add_u32 s18, s16, s18\n"
    "s_andn2_b32 s17, s18, 15\n"
    "s_bitset1_b32 s17, 31\n"
    "s_mov_b32 s72, s17\n"
    "s_pack_ll_b32_b16 s18, -1, s16\n"
    "s_or_b32 s19, s17, 15\n"
    "s_lshl_b32 s34, s2, 20\n"
    "s_and_b32 s34, s34, 0x7e00000\n"
    "s_lshl_b32 s7, s16, 8\n"
    "s_add_u32 s7, s7, s2\n"
    "s_mul_i32 s7, s7, 0x9e3779b1\n"
    "s_lshr_b32 s7, s7, 9\n"
    "s_and_b32 s7, s7, 2088832\n"
    "s_or_b32 s34, s34, s7\n"
    "s_and_b32 s35, s2, 7\n"
    "s_lshl_b32 s7, s4, 1\n"
    "s_sub_u32 s35, s35, s7\n"
    "v_and_b32 v19, 15, v0\n"
    "v_add_u32 v18, s12, v18\n"
    "v_and_b32 v54, 14, v0\n"
    "v_lshrrev_b32 v53, 1, v235\n"
    "v_add_u32 v235, v18, v54\n"
    "v_add_u32 v18, v18, v53\n"
    "s_mov_b32 s9, 0x51403020\n"
    "s_mov_b32 s10, 0x52423121\n"
    "s_mov_b32 s11, 0x53433322\n"
    "s_cmp_eq_u32 s6, 1\n"
    "s_cselect_b32 s9, 0x61204030, s9\n"
    "s_cselect_b32 s10, 0x62521131, s10\n"
    "s_cselect_b32 s11, 0x33534302, s11\n"
    "s_cmp_eq_u32 s6, 2\n"
    "s_cselect_b32 s9, 0x71302040, s9\n"
    "s_cselect_b32 s10, 0x42622111, s10\n"
    "s_cselect_b32 s11, 0x43335312, s11\n"
    "s_xor_b32 s7, s4, s5\n"
    "v_lshlrev_b32 v54, 3, v52\n"
    "s_lshl_b32 s7, s7, 2\n"
    "v_xor_b32 v55, s7, v19\n"
    "v_bfe_u32 v53, s11, v54, 8\n"
    "v_xor_b32 v2, v53, v55\n"
    "v_bfe_u32 v53, s10, v54, 8\n"
    "v_xor_b32 v1, v53, v55\n"
    "v_bfe_u32 v53, s9, v54, 8\n"
    "v_xor_b32 v0, v53, v55\n"
    "s_and_b32 s28, s2, 3\n"
    "s_waitcnt lgkmcnt(0)\n"
    "s_memrealtime s[0:1]\n"
    "s_barrier\n"
    "buffer_load_dwordx4 v250, s[48:51], 0 offen lds\n"
    "s_cmp_eq_u32 s35, 0\n"
    "s_cbranch_scc0 72f\n"
    "s_bitcmp1_b32 s2, 7\n"
    "s_cselect_b64 s[68:69], s[40:41], s[36:37]\n"
    "s_mov_b64 s[70:71], s[38:39]\n"
    "s_and_b32 s85, s34, 0x3fffff\n"
    "s_add_u32 s85, s85, s97\n"
    "s_min_u32 s34, s34, s85\n"
    "s_buffer_load_dword s73, s[68:71], s34\n"
    "s_buffer_load_dword s72, s[68:71], s34 offset:0x2000\n"
    "72:\n"
    "s_add_u32 m0, s83, 36864\n"
    "s_add_u32 s84, s58, s62\n"
    "s_lshr_b32 s8, s2, 7\n"
    "s_lshl2_add_u32 s7, s8, s28\n"
    "s_mul_i32 s56, s7, 393216\n"
    "buffer_load_dwordx4 v244, s[44:47], s84 offen lds\n"
    "s_lshr_b32 s29, s2, 2\n"
    "s_mov_b32 s30, s57\n"
    "s_xor_b32 s7, s5, 1\n"
    "buffer_load_dwordx4 v245, s[44:47], s84 offen offset:1024 lds\n"
    "s_mul_i32 s59, s7, 24576\n"
    "s_xor_b32 s7, s5, 2\n"
    "s_mul_i32 s60, s7, 24576\n"
    "buffer_load_dwordx4 v246, s[44:47], s84 offen offset:2048 lds\n"
    "s_xor_b32 s7, s5, 3\n"
    "s_mul_i32 s61, s7, 24576\n"
    "v_mov_b32 v53, s20\n"
    "buffer_load_dwordx4 v247, s[44:47], s84 offen offset:3072 lds\n"
    "s_add_u32 m0, s83, 40960\n"
    "v_mov_b32 v54, s21\n"
    "v_cmp_le_i32 vcc, s22, v16\n"
    "s_sub_u32 s22, s22, 0x400\n"
    "s_mov_b32 s33, 0x90000000\n"
    "v_cndmask_b32 v55, v53, v54, vcc\n"
    "v_lshl_add_u32 v0, v0, 4, v55\n"
    "buffer_load_dwordx4 v248, s[44:47], s84 offen lds\n"
    "v_cmp_le_i32 vcc, s22, v16\n"
    "s_sub_u32 s22, s22, 0x400\n"
    "s_nop 0\n"
    "v_cndmask_b32 v55, v53, v54, vcc\n"
    "v_lshl_add_u32 v1, v1, 4, v55\n"
    "v_cmp_le_i32 vcc, s22, v16\n"
    "s_sub_u32 s22, s22, 0x400\n"
    "s_nop 0\n"
    "v_cndmask_b32 v55, v53, v54, vcc\n"
    "buffer_load_dwordx4 v249, s[44:47], s84 offen offset:1024 lds\n"
    "s_add_u32 m0, s83, 43008\n"
    "s_add_u32 s84, s59, s62\n"
    "v_lshl_add_u32 v2, v2, 4, v55\n"
    "s_add_u32 s7, s83, 36864\n"
    "v_and_b32 v240, 0x70, v16\n"
    "buffer_load_dwordx4 v244, s[44:47], s84 offen lds\n"
    "v_and_b32 v53, 8, v19\n"
    "v_lshl_add_u32 v53, v52, 7, v53\n"
    "v_lshrrev_b32 v54, 2, v16\n"
    "buffer_load_dwordx4 v245, s[44:47], s84 offen offset:1024 lds\n"
    "s_cmp_eq_u32 s35, 0\n"
    "s_cbranch_scc0 73f\n"
    "s_getreg_b32 s88, hwreg(HW_REG_XCC_ID, 0, 3)\n"
    "s_lshl_b32 s89, s2, 3\n"
    "s_and_b32 s89, s89, 0x3f0\n"
    "s_lshr_b32 s82, s2, 7\n"
    "s_lshl3_add_u32 s89, s82, s89\n"
    "v_mov_b32 v20, s89\n"
    "s_lshr_b32 s89, s88, 2\n"
    "s_lshl1_add_u32 s88, s88, s89\n"
    "s_or_b32 s88, s88, s17\n"
    "s_bfe_u32 s89, s2, 0x10001\n"
    "s_xor_b32 s88, s88, s89\n"
    "s_waitcnt lgkmcnt(1)\n"
    "s_nop 3\n"
    "s_cmp_lg_u32 s72, s17\n"
    "s_cselect_b32 s85, 1, 0\n"
    "s_xor_b32 s88, s88, s85\n"
    "v_mov_b32 v23, s88\n"
    "73:\n"
    "v_add3_u32 v15, s7, v53, v240\n"
    "v_add_u32 v38, s7, v54\n"
    "s_lshr_b32 s8, s4, 1\n"
    "buffer_load_dwordx4 v246, s[44:47], s84 offen offset:2048 lds\n"
    "s_lshl_b32 s8, s8, 11\n"
    "s_add_u32 s8, s8, s3\n"
    "s_add_u32 s8, s8, 147456\n"
    "buffer_load_dwordx4 v247, s[44:47], s84 offen offset:3072 lds\n"
    "s_and_b32 s13, s4, 1\n"
    "s_lshl_b32 s14, s13, 8\n"
    "s_sub_u32 s7, s14, s7\n"
    "s_add_u32 s11, s7, s8\n"
    "s_lshl2_add_u32 s8, s14, s8\n"
    "v_and_b32 v53, 0x30, v16\n"
    "v_lshrrev_b32 v54, 2, v19\n"
    "v_lshl_add_u32 v53, v54, 8, v53\n"
    "v_lshl_add_u32 v53, v52, 6, v53\n"
    "v_add_u32 v39, s8, v53\n"
    "v_mov_b32 v251, 0\n"
    "v_mov_b32 v253, 0\n"
    "v_mov_b32 v255, 0\n"
    "s_add_u32 s63, s62, 98304\n"
    "s_cmp_eq_u32 s63, 294912\n"
    "s_cselect_b32 s63, 0, s63\n"
    "s_add_u32 s64, s63, 98304\n"
    "s_cmp_eq_u32 s64, 294912\n"
    "s_cselect_b32 s64, 0, s64\n"
    "s_add_u32 s7, s3, 160768\n"
    "v_mov_b32 v14, 0\n"
    "v_add_u32 v12, s7, v12\n"
    "s_lshl2_add_u32 s7, s4, s7\n"
    "v_mov_b32 v13, s7\n"
    "v_xor_b32 v53, v52, v19\n"
    "v_mul_u32_u24 v54, 0x300, v19\n"
    "v_lshl_add_u32 v54, v53, 4, v54\n"
    "v_add_u32 v4, s3, v54\n"
    "v_xor_b32 v5, 64, v4\n"
    "v_xor_b32 v6, 128, v4\n"
    "v_xor_b32 v7, 192, v4\n"
    "s_lshl_b32 s7, s4, 7\n"
    "s_lshr_b32 s8, s4, 1\n"
    "s_lshl_b32 s8, s8, 7\n"
    "s_add_u32 s8, s8, 512\n"
    "v_add_u32 v241, s8, v240\n"
    "v_add_u32 v240, s7, v240\n"
    "v_lshrrev_b32 v19, 4, v16\n"
    "s_mul_i32 s7, s52, 5\n"
    "s_lshr_b32 s7, s7, 3\n"
    "s_sub_u32 s8, s52, 1\n"
    "s_bitcmp1_b32 s2, 7\n"
    "s_cselect_b32 s7, s7, 0\n"
    "v_add_u32 v53, s7, v19\n"
    "v_cmp_gt_u32 vcc, s8, v19\n"
    "v_subrev_u32 v54, s8, v53\n"
    "v_min_u32 v53, v53, v54\n"
    "v_cndmask_b32 v53, v19, v53, vcc\n"
    "v_mul_lo_u32 v3, v53, s53\n"
    "v_add_u32 v3, s56, v3\n"
    "s_xor_b32 s29, s29, s23\n"
    "s_mul_i32 s7, s52, s53\n"
    "s_add_u32 s7, s7, s56\n"
    "s_mov_b32 s8, -2752512\n"
    "s_bitcmp1_b32 s2, 0\n"
    "s_cselect_b32 s8, -3538944, s8\n"
    "s_add_u32 s7, s7, s8\n"
    "v_cmp_eq_u32 vcc, s52, v19\n"
    "v_mov_b32 v53, s7\n"
    "s_nop 0\n"
    "v_cndmask_b32 v3, v3, v53, vcc\n"
    "v_add_u32 v53, s23, v3\n"
    "v_lshrrev_b32 v53, 21, v53\n"
    "v_and_b32 v232, 56, v53\n"
    "v_and_b32 v233, 4, v53\n"
    "v_xor_b32 v234, s29, v53\n"
    "s_cmp_eq_u32 s35, 0\n"
    "s_cbranch_scc0 71f\n"
    "s_pack_ll_b32_b16 s88, s0, s16\n"
    "s_mov_b64 exec, 1\n"
    "v_mov_b32 v22, s88\n"
    "global_store_dwordx2 v20, v[22:23], s[54:55] sc1\n"
    "s_mov_b64 exec, -1\n"
    "s_waitcnt lgkmcnt(0)\n"
    "71:\n"
    "s_mov_b64 exec, 0\n"
    "s_cmp_eq_u32 s4, 1\n"
    "s_cbranch_scc1 200f\n"
    "s_cmp_eq_u32 s4, 2\n"
    "s_cbranch_scc1 201f\n"
    "199:\n"
    "s_mov_b64 exec, -1\n"
    "s_branch 301f\n"
    "81:\n"
    "ds_read_b32 v15, v12\n"
    "s_waitcnt lgkmcnt(0)\n"
    "v_cmp_lt_u32 vcc, v15, v14\n"
    "s_nop 0\n"
    "s_cbranch_vccnz 81b\n"
    "s_branch 82f\n"
    "301:\n"
    "s_branch 302f\n"
    "41:\n"
    "ds_read_b32 v15, v12\n"
    "s_waitcnt lgkmcnt(0)\n"
    "v_cmp_lt_u32 vcc, v15, v14\n"
    "s_nop 0\n"
    "s_cbranch_vccnz 41b\n"
    "s_branch 42f\n"
    "43:\n"
    "s_memrealtime s[92:93]\n"
    "s_waitcnt lgkmcnt(0)\n"
    "s_sub_u32 s84, s92, s89\n"
    "s_cmp_lt_i32 s84, 0\n"
    "s_cbranch_scc0 44f\n"
    "s_sleep 1\n"
    "s_branch 43b\n"
    "302:\n"
    "s_branch 303f\n"
    "61:\n"
    "ds_read_b32 v15, v12\n"
    "s_waitcnt lgkmcnt(0)\n"
    "v_cmp_lt_u32 vcc, v15, v14\n"
    "s_nop 0\n"
    "s_cbranch_vccnz 61b\n"
    "s_branch 62f\n"
    "63:\n"
    "s_memrealtime s[92:93]\n"
    "s_waitcnt lgkmcnt(0)\n"
    "s_sub_u32 s84, s92, s89\n"
    "s_cmp_lt_i32 s84, 0\n"
    "s_cbranch_scc0 64f\n"
    "s_sleep 1\n"
    "s_branch 63b\n"
    "303:\n"
    "s_branch 304f\n"
    "45:\n"
    "s_sub_u32 s7, s86, s71\n"
    "s_cmp_eq_u32 s7, 2\n"
    "s_cbranch_scc1 402f\n"
    "s_cmp_eq_u32 s7, 4\n"
    "s_cbranch_scc1 404f\n"
    "v_mfma_f32_16x16x32_bf16 v[24:27], a[96:99], a[68:71], v[24:27]\n"
    "s_add_u32 s7, s70, 0\n"
    "s_add_u32 s64, s64, 2\n"
    "ds_read_b128 a[136:139], v4 offset:12288\n"
    "v_mfma_f32_16x16x32_bf16 v[24:27], a[92:95], a[44:47], v[24:27]\n"
    "buffer_load_dwordx4 v0, s[36:39], s81 offen nt lds\n"
    "v_mfma_f32_16x16x32_bf16 v[24:27], a[88:91], a[20:23], v[24:27]\n"
    "v_add_u32 v14, 1, v14\n"
    "v_accvgpr_write_b32 a188, v44\n"
    "v_mfma_f32_16x16x32_bf16 v[28:31], a[88:91], a[0:3], 0\n"
    "s_mov_b64 vcc, s[12:13]\n"
    "v_accvgpr_write_b32 a189, v45\n"
    "v_mfma_f32_16x16x32_bf16 v[28:31], a[92:95], a[24:27], v[28:31]\n"
    "s_waitcnt lgkmcnt(1)\n"
    "ds_read_b128 a[140:143], v5 offset:12288\n"
    "v_mfma_f32_16x16x32_bf16 v[28:31], a[96:99], a[48:51], v[28:31]\n"
    "ds_read_b128 a[144:147], v6 offset:12288\n"
    "v_accvgpr_mov_b32 a197, a185\n"
    "v_mfma_f32_16x16x32_bf16 v[28:31], a[100:103], a[72:75], v[28:31]\n"
    "buffer_load_dwordx4 v1, s[36:39], s81 offen offset:1024 nt lds\n"
    "v_mfma_f32_16x16x32_bf16 v[28:31], a[104:107], v[64:67], v[28:31]\n"
    "v_cvt_pk_bf16_f32 v52, v20, v24\n"
    "v_accvgpr_mov_b32 a198, a186\n"
    "v_mfma_f32_16x16x32_bf16 v[28:31], a[108:111], v[88:91], v[28:31]\n"
    "v_cvt_pk_bf16_f32 v53, v21, v25\n"
    "v_accvgpr_mov_b32 a199, a187\n"
    "v_mfma_f32_16x16x32_bf16 v[28:31], a[112:115], v[112:115], v[28:31]\n"
    "v_cvt_pk_bf16_f32 v54, v22, v26\n"
    "v_accvgpr_write_b32 a190, v46\n"
    "v_mfma_f32_16x16x32_bf16 v[28:31], a[116:119], v[136:139], v[28:31]\n"
    "ds_read_b128 a[148:151], v7 offset:12288\n"
    "v_accvgpr_write_b32 a191, v47\n"
    "v_mfma_f32_16x16x32_bf16 v[28:31], a[120:123], v[160:163], v[28:31]\n"
    "v_cvt_pk_bf16_f32 v55, v23, v27\n"
    "v_accvgpr_write_b32 a192, v48\n"
    "v_mfma_f32_16x16x32_bf16 v[28:31], a[124:127], v[184:187], v[28:31]\n"
    "buffer_load_dwordx4 v2, s[36:39], s81 offen offset:2048 nt lds\n"
    "v_mfma_f32_16x16x32_bf16 v[28:31], a[128:131], v[208:211], v[28:31]\n"
    "v_readlane_b32 s81, v3, s86\n"
    "v_accvgpr_write_b32 a193, v49\n"
    "v_mfma_f32_16x16x32_bf16 v[28:31], a[132:135], v[232:235], v[28:31]\n"
    "ds_read_b128 a[152:155], v4 offset:12544\n"
    "v_accvgpr_write_b32 a194, v50\n"
    "v_mfma_f32_16x16x32_bf16 v[32:35], a[132:135], v[236:239], 0\n"
    "s_addk_i32 m0, 12288\n"
    "v_accvgpr_write_b32 a195, v51\n"
    "v_mfma_f32_16x16x32_bf16 v[32:35], a[128:131], v[212:215], v[32:35]\n"
    "ds_write2st64_b32 v38, v52, v53 offset0:0 offset1:2\n"
    "v_mfma_f32_16x16x32_bf16 v[32:35], a[124:127], v[188:191], v[32:35]\n"
    "v_accvgpr_mov_b32 a196, a184\n"
    "v_readlane_b32 s9, v3, s7\n"
    "s_branch 46f\n"
    "304:\n"
    "s_branch 305f\n"
    "402:\n"
    "v_mfma_f32_16x16x32_bf16 v[24:27], a[96:99], a[68:71], v[24:27]\n"
    "s_add_u32 s64, s64, 2\n"
    "ds_read_b128 a[136:139], v4 offset:12288\n"
    "v_mfma_f32_16x16x32_bf16 v[24:27], a[92:95], a[44:47], v[24:27]\n"
    "buffer_store_dwordx4 a[188:191], v8, s[40:43], s9 offen sc1\n"
    "v_mfma_f32_16x16x32_bf16 v[24:27], a[88:91], a[20:23], v[24:27]\n"
    "v_add_u32 v14, 1, v14\n"
    "v_mfma_f32_16x16x32_bf16 v[28:31], a[88:91], a[0:3], 0\n"
    "s_mov_b64 vcc, s[12:13]\n"
    "v_mfma_f32_16x16x32_bf16 v[28:31], a[92:95], a[24:27], v[28:31]\n"
    "ds_read_b128 a[140:143], v5 offset:12288\n"
    "v_mfma_f32_16x16x32_bf16 v[28:31], a[96:99], a[48:51], v[28:31]\n"
    "ds_read_b128 a[144:147], v6 offset:12288\n"
    "v_mfma_f32_16x16x32_bf16 v[28:31], a[100:103], a[72:75], v[28:31]\n"
    "buffer_store_dwordx4 a[192:195], v9, s[40:43], s9 offen sc1\n"
    "v_mfma_f32_16x16x32_bf16 v[28:31], a[104:107], v[64:67], v[28:31]\n"
    "v_cvt_pk_bf16_f32 v52, v20, v24\n"
    "v_mfma_f32_16x16x32_bf16 v[28:31], a[108:111], v[88:91], v[28:31]\n"
    "v_cvt_pk_bf16_f32 v53, v21, v25\n"
    "v_mfma_f32_16x16x32_bf16 v[28:31], a[112:115], v[112:115], v[28:31]\n"
    "v_cvt_pk_bf16_f32 v54, v22, v26\n"
    "v_mfma_f32_16x16x32_bf16 v[28:31], a[116:119], v[136:139], v[28:31]\n"
    "ds_read_b128 a[148:151], v7 offset:12288\n"
    "v_mfma_f32_16x16x32_bf16 v[28:31], a[120:123], v[160:163], v[28:31]\n"
    "v_cvt_pk_bf16_f32 v55, v23, v27\n"
    "v_mfma_f32_16x16x32_bf16 v[28:31], a[124:127], v[184:187], v[28:31]\n"
    "buffer_store_dwordx4 a[196:199], v10, s[40:43], s9 offen sc1\n"
    "v_mfma_f32_16x16x32_bf16 v[28:31], a[128:131], v[208:211], v[28:31]\n"
    "v_readlane_b32 s81, v3, s86\n"
    "v_mfma_f32_16x16x32_bf16 v[28:31], a[132:135], v[232:235], v[28:31]\n"
    "ds_read_b128 a[152:155], v4 offset:12544\n"
    "v_mfma_f32_16x16x32_bf16 v[32:35], a[132:135], v[236:239], 0\n"
    "s_addk_i32 m0, 12288\n"
    "v_mfma_f32_16x16x32_bf16 v[32:35], a[128:131], v[212:215], v[32:35]\n"
    "ds_write2st64_b32 v38, v52, v53 offset0:0 offset1:2\n"
    "v_mfma_f32_16x16x32_bf16 v[32:35], a[124:127], v[188:191], v[32:35]\n"
    "s_branch 46f\n"
    "305:\n"
    "s_branch 306f\n"
    "404:\n"
    "v_mfma_f32_16x16x32_bf16 v[24:27], a[96:99], a[68:71], v[24:27]\n"
    "s_sub_u32 s89, s89, 36\n"
    "ds_read_b128 a[136:139], v4 offset:12288\n"
    "v_mfma_f32_16x16x32_bf16 v[24:27], a[92:95], a[44:47], v[24:27]\n"
    "buffer_store_dwordx4 a[208:211], v10, s[40:43], s10 offen sc1\n"
    "v_mfma_f32_16x16x32_bf16 v[24:27], a[88:91], a[20:23], v[24:27]\n"
    "v_add_u32 v14, 1, v14\n"
    "v_mfma_f32_16x16x32_bf16 v[28:31], a[88:91], a[0:3], 0\n"
    "s_mov_b64 vcc, s[12:13]\n"
    "v_mfma_f32_16x16x32_bf16 v[28:31], a[92:95], a[24:27], v[28:31]\n"
    "ds_read_b128 a[140:143], v5 offset:12288\n"
    "v_mfma_f32_16x16x32_bf16 v[28:31], a[96:99], a[48:51], v[28:31]\n"
    "ds_read_b128 a[144:147], v6 offset:12288\n"
    "v_mfma_f32_16x16x32_bf16 v[28:31], a[100:103], a[72:75], v[28:31]\n"
    "buffer_load_dwordx4 v1, s[36:39], s81 offen offset:1024 nt lds\n"
    "v_mfma_f32_16x16x32_bf16 v[28:31], a[104:107], v[64:67], v[28:31]\n"
    "v_cvt_pk_bf16_f32 v52, v20, v24\n"
    "v_mfma_f32_16x16x32_bf16 v[28:31], a[108:111], v[88:91], v[28:31]\n"
    "v_cvt_pk_bf16_f32 v53, v21, v25\n"
    "v_mfma_f32_16x16x32_bf16 v[28:31], a[112:115], v[112:115], v[28:31]\n"
    "v_cvt_pk_bf16_f32 v54, v22, v26\n"
    "v_mfma_f32_16x16x32_bf16 v[28:31], a[116:119], v[136:139], v[28:31]\n"
    "ds_read_b128 a[148:151], v7 offset:12288\n"
    "v_mfma_f32_16x16x32_bf16 v[28:31], a[120:123], v[160:163], v[28:31]\n"
    "v_cvt_pk_bf16_f32 v55, v23, v27\n"
    "v_mfma_f32_16x16x32_bf16 v[28:31], a[124:127], v[184:187], v[28:31]\n"
    "buffer_load_dwordx4 v2, s[36:39], s81 offen offset:2048 nt lds\n"
    "v_mfma_f32_16x16x32_bf16 v[28:31], a[128:131], v[208:211], v[28:31]\n"
    "v_readlane_b32 s81, v3, s86\n"
    "v_mfma_f32_16x16x32_bf16 v[28:31], a[132:135], v[232:235], v[28:31]\n"
    "ds_read_b128 a[152:155], v4 offset:12544\n"
    "v_mfma_f32_16x16x32_bf16 v[32:35], a[132:135], v[236:239], 0\n"
    "s_addk_i32 m0, 12288\n"
    "v_mfma_f32_16x16x32_bf16 v[32:35], a[128:131], v[212:215], v[32:35]\n"
    "ds_write2st64_b32 v38, v52, v53 offset0:0 offset1:2\n"
    "v_mfma_f32_16x16x32_bf16 v[32:35], a[124:127], v[188:191], v[32:35]\n"
    "s_branch 46f\n"
    "306:\n"
    "s_branch 307f\n"
    "65:\n"
    "s_sub_u32 s7, s86, s71\n"
    "s_cmp_eq_u32 s7, 3\n"
    "s_cbranch_scc1 403f\n"
    "v_mfma_f32_16x16x32_bf16 v[32:35], a[144:147], a[68:71], v[32:35]\n"
    "s_add_u32 s7, s70, 1\n"
    "s_add_u32 s65, s65, 2\n"
    "ds_read_b128 a[88:91], v4\n"
    "v_mfma_f32_16x16x32_bf16 v[32:35], a[140:143], a[44:47], v[32:35]\n"
    "buffer_load_dwordx4 v0, s[36:39], s81 offen nt lds\n"
    "v_readlane_b32 s10, v3, s7\n"
    "v_mfma_f32_16x16x32_bf16 v[32:35], a[136:139], a[20:23], v[32:35]\n"
    "v_add_u32 v7, s78, v7\n"
    "v_accvgpr_write_b32 a200, v44\n"
    "v_mfma_f32_16x16x32_bf16 v[20:23], a[136:139], a[0:3], 0\n"
    "v_add_u32 v14, 1, v14\n"
    "v_accvgpr_write_b32 a201, v45\n"
    "v_mfma_f32_16x16x32_bf16 v[20:23], a[140:143], a[24:27], v[20:23]\n"
    "s_waitcnt lgkmcnt(1)\n"
    "ds_read_b128 a[92:95], v5\n"
    "v_mfma_f32_16x16x32_bf16 v[20:23], a[144:147], a[48:51], v[20:23]\n"
    "ds_read_b128 a[96:99], v6\n"
    "v_accvgpr_mov_b32 a208, a184\n"
    "v_mfma_f32_16x16x32_bf16 v[20:23], a[148:151], a[72:75], v[20:23]\n"
    "buffer_load_dwordx4 v1, s[36:39], s81 offen offset:1024 nt lds\n"
    "v_mfma_f32_16x16x32_bf16 v[20:23], a[152:155], v[64:67], v[20:23]\n"
    "v_cvt_pk_bf16_f32 v52, v28, v32\n"
    "v_accvgpr_mov_b32 a209, a185\n"
    "v_mfma_f32_16x16x32_bf16 v[20:23], a[156:159], v[88:91], v[20:23]\n"
    "v_cvt_pk_bf16_f32 v53, v29, v33\n"
    "v_accvgpr_mov_b32 a210, a186\n"
    "v_mfma_f32_16x16x32_bf16 v[20:23], a[160:163], v[112:115], v[20:23]\n"
    "v_cvt_pk_bf16_f32 v54, v30, v34\n"
    "v_accvgpr_mov_b32 a211, a187\n"
    "v_mfma_f32_16x16x32_bf16 v[20:23], a[164:167], v[136:139], v[20:23]\n"
    "ds_read_b128 a[100:103], v7\n"
    "v_accvgpr_write_b32 a202, v46\n"
    "v_mfma_f32_16x16x32_bf16 v[20:23], a[168:171], v[160:163], v[20:23]\n"
    "v_cvt_pk_bf16_f32 v55, v31, v35\n"
    "v_accvgpr_write_b32 a203, v47\n"
    "v_mfma_f32_16x16x32_bf16 v[20:23], a[172:175], v[184:187], v[20:23]\n"
    "buffer_load_dwordx4 v2, s[36:39], s81 offen offset:2048 nt lds\n"
    "v_mfma_f32_16x16x32_bf16 v[20:23], a[176:179], v[208:211], v[20:23]\n"
    "s_mov_b64 vcc, s[12:13]\n"
    "v_accvgpr_write_b32 a204, v48\n"
    "v_mfma_f32_16x16x32_bf16 v[20:23], a[180:183], v[232:235], v[20:23]\n"
    "ds_read_b128 a[104:107], v4 offset:256\n"
    "v_accvgpr_write_b32 a205, v49\n"
    "v_mfma_f32_16x16x32_bf16 v[24:27], a[180:183], v[236:239], 0\n"
    "v_readlane_b32 s81, v3, s86\n"
    "v_accvgpr_write_b32 a206, v50\n"
    "v_mfma_f32_16x16x32_bf16 v[24:27], a[176:179], v[212:215], v[24:27]\n"
    "ds_write2st64_b32 v38, v52, v53 offset0:16 offset1:18\n"
    "v_mfma_f32_16x16x32_bf16 v[24:27], a[172:175], v[188:191], v[24:27]\n"
    "s_addk_i32 m0, 12288\n"
    "v_accvgpr_write_b32 a207, v51\n"
    "s_branch 66f\n"
    "307:\n"
    "s_branch 308f\n"
    "403:\n"
    "v_mfma_f32_16x16x32_bf16 v[32:35], a[144:147], a[68:71], v[32:35]\n"
    "s_sub_u32 s89, s89, 36\n"
    "ds_read_b128 a[88:91], v4\n"
    "v_mfma_f32_16x16x32_bf16 v[32:35], a[140:143], a[44:47], v[32:35]\n"
    "buffer_store_dwordx4 a[200:203], v8, s[40:43], s10 offen sc1\n"
    "v_mfma_f32_16x16x32_bf16 v[32:35], a[136:139], a[20:23], v[32:35]\n"
    "v_add_u32 v7, s78, v7\n"
    "v_mfma_f32_16x16x32_bf16 v[20:23], a[136:139], a[0:3], 0\n"
    "v_add_u32 v14, 1, v14\n"
    "v_mfma_f32_16x16x32_bf16 v[20:23], a[140:143], a[24:27], v[20:23]\n"
    "ds_read_b128 a[92:95], v5\n"
    "v_mfma_f32_16x16x32_bf16 v[20:23], a[144:147], a[48:51], v[20:23]\n"
    "ds_read_b128 a[96:99], v6\n"
    "v_mfma_f32_16x16x32_bf16 v[20:23], a[148:151], a[72:75], v[20:23]\n"
    "buffer_store_dwordx4 a[204:207], v9, s[40:43], s10 offen sc1\n"
    "v_mfma_f32_16x16x32_bf16 v[20:23], a[152:155], v[64:67], v[20:23]\n"
    "v_cvt_pk_bf16_f32 v52, v28, v32\n"
    "v_mfma_f32_16x16x32_bf16 v[20:23], a[156:159], v[88:91], v[20:23]\n"
    "v_cvt_pk_bf16_f32 v53, v29, v33\n"
    "v_mfma_f32_16x16x32_bf16 v[20:23], a[160:163], v[112:115], v[20:23]\n"
    "v_cvt_pk_bf16_f32 v54, v30, v34\n"
    "v_mfma_f32_16x16x32_bf16 v[20:23], a[164:167], v[136:139], v[20:23]\n"
    "ds_read_b128 a[100:103], v7\n"
    "v_mfma_f32_16x16x32_bf16 v[20:23], a[168:171], v[160:163], v[20:23]\n"
    "v_cvt_pk_bf16_f32 v55, v31, v35\n"
    "v_mfma_f32_16x16x32_bf16 v[20:23], a[172:175], v[184:187], v[20:23]\n"
    "buffer_load_dwordx4 v2, s[36:39], s81 offen offset:2048 nt lds\n"
    "v_mfma_f32_16x16x32_bf16 v[20:23], a[176:179], v[208:211], v[20:23]\n"
    "s_mov_b64 vcc, s[12:13]\n"
    "v_mfma_f32_16x16x32_bf16 v[20:23], a[180:183], v[232:235], v[20:23]\n"
    "ds_read_b128 a[104:107], v4 offset:256\n"
    "v_mfma_f32_16x16x32_bf16 v[24:27], a[180:183], v[236:239], 0\n"
    "v_readlane_b32 s81, v3, s86\n"
    "v_mfma_f32_16x16x32_bf16 v[24:27], a[176:179], v[212:215], v[24:27]\n"
    "ds_write2st64_b32 v38, v52, v53 offset0:16 offset1:18\n"
    "v_mfma_f32_16x16x32_bf16 v[24:27], a[172:175], v[188:191], v[24:27]\n"
    "s_addk_i32 m0, 12288\n"
    "s_branch 66f\n"
    "308:\n"
    "s_waitcnt vmcnt(10)\n"
    "ds_write_b32 v13, v14\n"
    "ds_read_u16 v250, v18\n"
    "ds_read_u16 v252, v18 offset:8\n"
    "ds_read_u16 v254, v235\n"
    "s_waitcnt vmcnt(9)\n"
    "ds_read2st64_b64 a[188:191], v15 offset0:0 offset1:1\n"
    "s_waitcnt vmcnt(8)\n"
    "ds_read2st64_b64 a[192:195], v15 offset0:2 offset1:3\n"
    "s_waitcnt vmcnt(7)\n"
    "ds_read2st64_b64 a[196:199], v15 offset0:4 offset1:5\n"
    "s_waitcnt lgkmcnt(3)\n"
    "v_lshlrev_b64 v[40:41], v16, v[250:251]\n"
    "v_lshlrev_b64 v[42:43], v16, v[252:253]\n"
    "v_lshlrev_b64 v[44:45], v16, v[254:255]\n"
    "s_waitcnt vmcnt(6) lgkmcnt(2)\n"
    "ds_read2st64_b64 a[200:203], v15 offset0:6 offset1:7\n"
    "s_nop 0\n"
    "v_mfma_f32_4x4x4_16b_bf16 v[20:23], v[40:41], a[188:189], 0 cbsz:2 abid:0\n"
    "ds_read_u16 v250, v18 offset:64\n"
    "s_add_u32 m0, s83, 47104\n"
    "v_mfma_f32_4x4x4_16b_bf16 v[24:27], v[40:41], a[190:191], 0 cbsz:2 abid:1\n"
    "buffer_load_dwordx4 v248, s[44:47], s84 offen lds\n"
    "v_add_u32 v8, v8, v240\n"
    "s_waitcnt vmcnt(6) lgkmcnt(3)\n"
    "ds_read2st64_b32 a[204:205], v38 offset0:16 offset1:17\n"
    "v_mfma_f32_4x4x4_16b_bf16 v[28:31], v[40:41], a[192:193], 0 cbsz:2 abid:2\n"
    "ds_read2st64_b32 a[206:207], v38 offset0:18 offset1:19\n"
    "ds_read_u16 v252, v18 offset:72\n"
    "v_cvt_pk_bf16_f32 v52, v20, v24\n"
    "v_cvt_pk_bf16_f32 v53, v21, v25\n"
    "v_accvgpr_write_b32 a0, v52\n"
    "v_accvgpr_write_b32 a4, v53\n"
    "v_mfma_f32_4x4x4_16b_bf16 v[32:35], v[40:41], a[194:195], 0 cbsz:2 abid:3\n"
    "buffer_load_dwordx4 v249, s[44:47], s84 offen offset:1024 lds\n"
    "v_cvt_pk_bf16_f32 v54, v22, v26\n"
    "v_cvt_pk_bf16_f32 v55, v23, v27\n"
    "v_accvgpr_write_b32 a8, v54\n"
    "v_accvgpr_write_b32 a12, v55\n"
    "v_add_u32 v9, v9, v240\n"
    "s_waitcnt vmcnt(6) lgkmcnt(5)\n"
    "ds_read2st64_b32 a[208:209], v38 offset0:20 offset1:21\n"
    "v_mfma_f32_4x4x4_16b_bf16 v[20:23], v[42:43], a[196:197], 0 cbsz:2 abid:0\n"
    "ds_read2st64_b32 a[210:211], v38 offset0:22 offset1:23\n"
    "ds_read_u16 v254, v235 offset:64\n"
    "s_add_u32 m0, s83, 49152\n"
    "s_add_u32 s84, s60, s62\n"
    "v_cvt_pk_bf16_f32 v52, v28, v32\n"
    "v_cvt_pk_bf16_f32 v53, v29, v33\n"
    "v_accvgpr_write_b32 a1, v52\n"
    "v_accvgpr_write_b32 a5, v53\n"
    "v_mfma_f32_4x4x4_16b_bf16 v[24:27], v[42:43], a[198:199], 0 cbsz:2 abid:1\n"
    "buffer_load_dwordx4 v244, s[44:47], s84 offen lds\n"
    "v_cvt_pk_bf16_f32 v54, v30, v34\n"
    "v_cvt_pk_bf16_f32 v55, v31, v35\n"
    "v_accvgpr_write_b32 a9, v54\n"
    "v_accvgpr_write_b32 a13, v55\n"
    "v_add_u32 v10, v10, v241\n"
    "s_waitcnt vmcnt(6) lgkmcnt(6)\n"
    "ds_read2st64_b64 a[188:191], v15 offset0:12 offset1:13\n"
    "v_mfma_f32_4x4x4_16b_bf16 v[28:31], v[42:43], a[200:201], 0 cbsz:2 abid:2\n"
    "v_cvt_pk_bf16_f32 v52, v20, v24\n"
    "v_cvt_pk_bf16_f32 v53, v21, v25\n"
    "v_accvgpr_write_b32 a2, v52\n"
    "v_accvgpr_write_b32 a6, v53\n"
    "v_mfma_f32_4x4x4_16b_bf16 v[32:35], v[42:43], a[202:203], 0 cbsz:2 abid:3\n"
    "buffer_load_dwordx4 v245, s[44:47], s84 offen offset:1024 lds\n"
    "v_cvt_pk_bf16_f32 v54, v22, v26\n"
    "v_cvt_pk_bf16_f32 v55, v23, v27\n"
    "v_accvgpr_write_b32 a10, v54\n"
    "v_accvgpr_write_b32 a14, v55\n"
    "v_lshlrev_b64 v[46:47], v16, v[250:251]\n"
    "s_mov_b32 s12, 0xff00ff\n"
    "s_waitcnt vmcnt(6) lgkmcnt(4)\n"
    "ds_read2st64_b64 a[192:195], v15 offset0:14 offset1:15\n"
    "v_mfma_f32_4x4x4_16b_bf16 v[20:23], v[44:45], a[204:205], 0 cbsz:2 abid:0\n"
    "v_cvt_pk_bf16_f32 v52, v28, v32\n"
    "v_cvt_pk_bf16_f32 v53, v29, v33\n"
    "v_accvgpr_write_b32 a3, v52\n"
    "v_accvgpr_write_b32 a7, v53\n"
    "v_mfma_f32_4x4x4_16b_bf16 v[24:27], v[44:45], a[206:207], 0 cbsz:2 abid:1\n"
    "buffer_load_dwordx4 v246, s[44:47], s84 offen offset:2048 lds\n"
    "v_cvt_pk_bf16_f32 v54, v30, v34\n"
    "v_cvt_pk_bf16_f32 v55, v31, v35\n"
    "v_accvgpr_write_b32 a11, v54\n"
    "v_accvgpr_write_b32 a15, v55\n"
    "buffer_load_dwordx4 v247, s[44:47], s84 offen offset:3072 lds\n"
    "v_lshlrev_b64 v[48:49], v16, v[252:253]\n"
    "s_mov_b32 s13, 0xff00ff\n"
    "s_waitcnt vmcnt(7) lgkmcnt(2)\n"
    "ds_read2st64_b64 a[196:199], v15 offset0:16 offset1:17\n"
    "v_mfma_f32_4x4x4_16b_bf16 v[28:31], v[44:45], a[208:209], 0 cbsz:2 abid:2\n"
    "s_add_u32 m0, s83, 53248\n"
    "v_cvt_pk_bf16_f32 v52, v20, v22\n"
    "v_cvt_pk_bf16_f32 v53, v21, v23\n"
    "v_accvgpr_write_b32 a16, v52\n"
    "v_accvgpr_write_b32 a20, v53\n"
    "v_mfma_f32_4x4x4_16b_bf16 v[32:35], v[44:45], a[210:211], 0 cbsz:2 abid:3\n"
    "buffer_load_dwordx4 v248, s[44:47], s84 offen lds\n"
    "v_cvt_pk_bf16_f32 v54, v24, v26\n"
    "v_cvt_pk_bf16_f32 v55, v25, v27\n"
    "v_accvgpr_write_b32 a17, v54\n"
    "v_accvgpr_write_b32 a21, v55\n"
    "v_lshlrev_b64 v[50:51], v16, v[254:255]\n"
    "200:\n"
    "s_cbranch_execz 199b\n"
    "s_add_u32 s77, s57, 36864\n"
    "s_waitcnt vmcnt(7) lgkmcnt(2)\n"
    "ds_read2st64_b64 a[200:203], v15 offset0:18 offset1:19\n"
    "v_mfma_f32_4x4x4_16b_bf16 v[20:23], v[46:47], a[188:189], 0 cbsz:2 abid:0\n"
    "ds_read_u16 v250, v18 offset:128\n"
    "v_cvt_pk_bf16_f32 v52, v28, v30\n"
    "v_cvt_pk_bf16_f32 v53, v29, v31\n"
    "v_accvgpr_write_b32 a18, v52\n"
    "v_accvgpr_write_b32 a22, v53\n"
    "v_mfma_f32_4x4x4_16b_bf16 v[24:27], v[46:47], a[190:191], 0 cbsz:2 abid:1\n"
    "buffer_load_dwordx4 v249, s[44:47], s84 offen offset:1024 lds\n"
    "v_cvt_pk_bf16_f32 v54, v32, v34\n"
    "v_cvt_pk_bf16_f32 v55, v33, v35\n"
    "v_accvgpr_write_b32 a19, v54\n"
    "v_accvgpr_write_b32 a23, v55\n"
    "s_movk_i32 s88, 24576\n"
    "s_waitcnt vmcnt(7) lgkmcnt(3)\n"
    "ds_read2st64_b32 a[204:205], v38 offset0:40 offset1:41\n"
    "v_mfma_f32_4x4x4_16b_bf16 v[28:31], v[46:47], a[192:193], 0 cbsz:2 abid:2\n"
    "ds_read2st64_b32 a[206:207], v38 offset0:42 offset1:43\n"
    "ds_read_u16 v252, v18 offset:136\n"
    "s_add_u32 m0, s83, 55296\n"
    "s_add_u32 s84, s61, s62\n"
    "v_cvt_pk_bf16_f32 v52, v20, v24\n"
    "v_cvt_pk_bf16_f32 v53, v21, v25\n"
    "v_accvgpr_write_b32 a24, v52\n"
    "v_accvgpr_write_b32 a28, v53\n"
    "v_mfma_f32_4x4x4_16b_bf16 v[32:35], v[46:47], a[194:195], 0 cbsz:2 abid:3\n"
    "buffer_load_dwordx4 v244, s[44:47], s84 offen lds\n"
    "v_cvt_pk_bf16_f32 v54, v22, v26\n"
    "v_cvt_pk_bf16_f32 v55, v23, v27\n"
    "v_accvgpr_write_b32 a32, v54\n"
    "v_accvgpr_write_b32 a36, v55\n"
    "buffer_load_dwordx4 v245, s[44:47], s84 offen offset:1024 lds\n"
    "s_movk_i32 s78, 24576\n"
    "s_waitcnt vmcnt(8) lgkmcnt(5)\n"
    "ds_read2st64_b32 a[208:209], v38 offset0:44 offset1:45\n"
    "v_mfma_f32_4x4x4_16b_bf16 v[20:23], v[48:49], a[196:197], 0 cbsz:2 abid:0\n"
    "ds_read2st64_b32 a[210:211], v38 offset0:46 offset1:47\n"
    "ds_read_u16 v254, v235 offset:128\n"
    "v_cvt_pk_bf16_f32 v52, v28, v32\n"
    "v_cvt_pk_bf16_f32 v53, v29, v33\n"
    "v_accvgpr_write_b32 a25, v52\n"
    "v_accvgpr_write_b32 a29, v53\n"
    "v_mfma_f32_4x4x4_16b_bf16 v[24:27], v[48:49], a[198:199], 0 cbsz:2 abid:1\n"
    "buffer_load_dwordx4 v246, s[44:47], s84 offen offset:2048 lds\n"
    "v_cvt_pk_bf16_f32 v54, v30, v34\n"
    "v_cvt_pk_bf16_f32 v55, v31, v35\n"
    "v_accvgpr_write_b32 a33, v54\n"
    "v_accvgpr_write_b32 a37, v55\n"
    "s_mov_b32 s87, -122880\n"
    "s_waitcnt vmcnt(8) lgkmcnt(6)\n"
    "ds_read2st64_b64 a[188:191], v15 offset0:24 offset1:25\n"
    "v_mfma_f32_4x4x4_16b_bf16 v[28:31], v[48:49], a[200:201], 0 cbsz:2 abid:2\n"
    "v_cvt_pk_bf16_f32 v52, v20, v24\n"
    "v_cvt_pk_bf16_f32 v53, v21, v25\n"
    "v_accvgpr_write_b32 a26, v52\n"
    "v_accvgpr_write_b32 a30, v53\n"
    "v_mfma_f32_4x4x4_16b_bf16 v[32:35], v[48:49], a[202:203], 0 cbsz:2 abid:3\n"
    "buffer_load_dwordx4 v247, s[44:47], s84 offen offset:3072 lds\n"
    "v_cvt_pk_bf16_f32 v54, v22, v26\n"
    "v_cvt_pk_bf16_f32 v55, v23, v27\n"
    "v_accvgpr_write_b32 a34, v54\n"
    "v_accvgpr_write_b32 a38, v55\n"
    "v_lshlrev_b64 v[40:41], v16, v[250:251]\n"
    "s_add_u32 s80, s57, 147456\n"
    "s_waitcnt vmcnt(8) lgkmcnt(4)\n"
    "ds_read2st64_b64 a[192:195], v15 offset0:26 offset1:27\n"
    "v_mfma_f32_4x4x4_16b_bf16 v[20:23], v[50:51], a[204:205], 0 cbsz:2 abid:0\n"
    "s_add_u32 m0, s83, 59392\n"
    "v_cvt_pk_bf16_f32 v52, v28, v32\n"
    "v_cvt_pk_bf16_f32 v53, v29, v33\n"
    "v_accvgpr_write_b32 a27, v52\n"
    "v_accvgpr_write_b32 a31, v53\n"
    "v_mfma_f32_4x4x4_16b_bf16 v[24:27], v[50:51], a[206:207], 0 cbsz:2 abid:1\n"
    "buffer_load_dwordx4 v248, s[44:47], s84 offen lds\n"
    "v_cvt_pk_bf16_f32 v54, v30, v34\n"
    "v_cvt_pk_bf16_f32 v55, v31, v35\n"
    "v_accvgpr_write_b32 a35, v54\n"
    "v_accvgpr_write_b32 a39, v55\n"
    "v_lshlrev_b64 v[42:43], v16, v[252:253]\n"
    "s_mov_b32 s86, 5\n"
    "s_waitcnt vmcnt(8) lgkmcnt(2)\n"
    "ds_read2st64_b64 a[196:199], v15 offset0:28 offset1:29\n"
    "v_mfma_f32_4x4x4_16b_bf16 v[28:31], v[50:51], a[208:209], 0 cbsz:2 abid:2\n"
    "v_cvt_pk_bf16_f32 v52, v20, v22\n"
    "v_cvt_pk_bf16_f32 v53, v21, v23\n"
    "v_accvgpr_write_b32 a40, v52\n"
    "v_accvgpr_write_b32 a44, v53\n"
    "v_mfma_f32_4x4x4_16b_bf16 v[32:35], v[50:51], a[210:211], 0 cbsz:2 abid:3\n"
    "buffer_load_dwordx4 v249, s[44:47], s84 offen offset:1024 lds\n"
    "s_add_u32 m0, s83, 61440\n"
    "s_add_u32 s84, s58, s63\n"
    "v_cvt_pk_bf16_f32 v54, v24, v26\n"
    "v_cvt_pk_bf16_f32 v55, v25, v27\n"
    "v_accvgpr_write_b32 a41, v54\n"
    "v_accvgpr_write_b32 a45, v55\n"
    "buffer_load_dwordx4 v244, s[44:47], s84 offen lds\n"
    "v_lshlrev_b64 v[44:45], v16, v[254:255]\n"
    "s_lshr_b32 s7, s2, 1\n"
    "s_waitcnt vmcnt(9) lgkmcnt(2)\n"
    "s_barrier\n"
    "ds_read2st64_b64 a[200:203], v15 offset0:30 offset1:31\n"
    "v_mfma_f32_4x4x4_16b_bf16 v[20:23], v[40:41], a[188:189], 0 cbsz:2 abid:0\n"
    "ds_read_u16 v250, v18 offset:192\n"
    "v_cvt_pk_bf16_f32 v52, v28, v30\n"
    "v_cvt_pk_bf16_f32 v53, v29, v31\n"
    "v_accvgpr_write_b32 a42, v52\n"
    "v_accvgpr_write_b32 a46, v53\n"
    "v_mfma_f32_4x4x4_16b_bf16 v[24:27], v[40:41], a[190:191], 0 cbsz:2 abid:1\n"
    "buffer_load_dwordx4 v245, s[44:47], s84 offen offset:1024 lds\n"
    "v_cvt_pk_bf16_f32 v54, v32, v34\n"
    "v_cvt_pk_bf16_f32 v55, v33, v35\n"
    "v_accvgpr_write_b32 a43, v54\n"
    "v_accvgpr_write_b32 a47, v55\n"
    "s_and_b32 s7, s7, 0x7c\n"
    "s_waitcnt vmcnt(9) lgkmcnt(3)\n"
    "ds_read2st64_b32 a[204:205], v38 offset0:64 offset1:65\n"
    "v_mfma_f32_4x4x4_16b_bf16 v[28:31], v[40:41], a[192:193], 0 cbsz:2 abid:2\n"
    "ds_read2st64_b32 a[206:207], v38 offset0:66 offset1:67\n"
    "ds_read_u16 v252, v18 offset:200\n"
    "v_cvt_pk_bf16_f32 v52, v20, v24\n"
    "v_cvt_pk_bf16_f32 v53, v21, v25\n"
    "v_accvgpr_write_b32 a48, v52\n"
    "v_accvgpr_write_b32 a52, v53\n"
    "v_mfma_f32_4x4x4_16b_bf16 v[32:35], v[40:41], a[194:195], 0 cbsz:2 abid:3\n"
    "buffer_load_dwordx4 v246, s[44:47], s84 offen offset:2048 lds\n"
    "v_cvt_pk_bf16_f32 v54, v22, v26\n"
    "v_cvt_pk_bf16_f32 v55, v23, v27\n"
    "v_accvgpr_write_b32 a56, v54\n"
    "v_accvgpr_write_b32 a60, v55\n"
    "s_or_b32 s7, s7, s28\n"
    "s_waitcnt vmcnt(9) lgkmcnt(5)\n"
    "ds_read2st64_b32 a[208:209], v38 offset0:68 offset1:69\n"
    "v_mfma_f32_4x4x4_16b_bf16 v[20:23], v[42:43], a[196:197], 0 cbsz:2 abid:0\n"
    "ds_read2st64_b32 a[210:211], v38 offset0:70 offset1:71\n"
    "ds_read_u16 v254, v235 offset:192\n"
    "v_cvt_pk_bf16_f32 v52, v28, v32\n"
    "v_cvt_pk_bf16_f32 v53, v29, v33\n"
    "v_accvgpr_write_b32 a49, v52\n"
    "v_accvgpr_write_b32 a53, v53\n"
    "v_mfma_f32_4x4x4_16b_bf16 v[24:27], v[42:43], a[198:199], 0 cbsz:2 abid:1\n"
    "buffer_load_dwordx4 v247, s[44:47], s84 offen offset:3072 lds\n"
    "v_cvt_pk_bf16_f32 v54, v30, v34\n"
    "v_cvt_pk_bf16_f32 v55, v31, v35\n"
    "v_accvgpr_write_b32 a57, v54\n"
    "v_accvgpr_write_b32 a61, v55\n"
    "s_mul_i32 s7, s7, 79\n"
    "s_waitcnt vmcnt(9) lgkmcnt(6)\n"
    "ds_read2st64_b64 a[188:191], v15 offset0:36 offset1:37\n"
    "v_mfma_f32_4x4x4_16b_bf16 v[28:31], v[42:43], a[200:201], 0 cbsz:2 abid:2\n"
    "s_add_u32 m0, s83, 65536\n"
    "v_cvt_pk_bf16_f32 v52, v20, v24\n"
    "v_cvt_pk_bf16_f32 v53, v21, v25\n"
    "v_accvgpr_write_b32 a50, v52\n"
    "v_accvgpr_write_b32 a54, v53\n"
    "v_mfma_f32_4x4x4_16b_bf16 v[32:35], v[42:43], a[202:203], 0 cbsz:2 abid:3\n"
    "buffer_load_dwordx4 v248, s[44:47], s84 offen lds\n"
    "v_cvt_pk_bf16_f32 v54, v22, v26\n"
    "v_cvt_pk_bf16_f32 v55, v23, v27\n"
    "v_accvgpr_write_b32 a58, v54\n"
    "v_accvgpr_write_b32 a62, v55\n"
    "buffer_load_dwordx4 v249, s[44:47], s84 offen offset:1024 lds\n"
    "v_lshlrev_b64 v[46:47], v16, v[250:251]\n"
    "s_and_b32 s7, s7, 0x7f\n"
    "s_waitcnt vmcnt(10) lgkmcnt(4)\n"
    "ds_read2st64_b64 a[192:195], v15 offset0:38 offset1:39\n"
    "v_mfma_f32_4x4x4_16b_bf16 v[20:23], v[44:45], a[204:205], 0 cbsz:2 abid:0\n"
    "s_add_u32 m0, s83, 36864\n"
    "s_add_u32 s84, s59, s63\n"
    "v_cvt_pk_bf16_f32 v52, v28, v32\n"
    "v_cvt_pk_bf16_f32 v53, v29, v33\n"
    "v_accvgpr_write_b32 a51, v52\n"
    "v_accvgpr_write_b32 a55, v53\n"
    "v_mfma_f32_4x4x4_16b_bf16 v[24:27], v[44:45], a[206:207], 0 cbsz:2 abid:1\n"
    "buffer_load_dwordx4 v244, s[44:47], s84 offen lds\n"
    "v_cvt_pk_bf16_f32 v54, v30, v34\n"
    "v_cvt_pk_bf16_f32 v55, v31, v35\n"
    "v_accvgpr_write_b32 a59, v54\n"
    "v_accvgpr_write_b32 a63, v55\n"
    "v_lshlrev_b64 v[48:49], v16, v[252:253]\n"
    "s_mul_i32 s7, s7, 50\n"
    "s_waitcnt vmcnt(10) lgkmcnt(2)\n"
    "ds_read2st64_b64 a[196:199], v15 offset0:40 offset1:41\n"
    "v_mfma_f32_4x4x4_16b_bf16 v[28:31], v[44:45], a[208:209], 0 cbsz:2 abid:2\n"
    "v_cvt_pk_bf16_f32 v52, v20, v22\n"
    "v_cvt_pk_bf16_f32 v53, v21, v23\n"
    "v_accvgpr_write_b32 a64, v52\n"
    "v_accvgpr_write_b32 a68, v53\n"
    "v_mfma_f32_4x4x4_16b_bf16 v[32:35], v[44:45], a[210:211], 0 cbsz:2 abid:3\n"
    "buffer_load_dwordx4 v245, s[44:47], s84 offen offset:1024 lds\n"
    "v_cvt_pk_bf16_f32 v54, v24, v26\n"
    "v_cvt_pk_bf16_f32 v55, v25, v27\n"
    "v_accvgpr_write_b32 a65, v54\n"
    "v_accvgpr_write_b32 a69, v55\n"
    "v_lshlrev_b64 v[50:51], v16, v[254:255]\n"
    "s_lshr_b32 s7, s7, 6\n"
    "s_waitcnt vmcnt(10) lgkmcnt(2)\n"
    "ds_read2st64_b64 a[200:203], v15 offset0:42 offset1:43\n"
    "v_mfma_f32_4x4x4_16b_bf16 v[20:23], v[46:47], a[188:189], 0 cbsz:2 abid:0\n"
    "ds_read_u16 v250, v18 offset:256\n"
    "v_cvt_pk_bf16_f32 v52, v28, v30\n"
    "v_cvt_pk_bf16_f32 v53, v29, v31\n"
    "v_accvgpr_write_b32 a66, v52\n"
    "v_accvgpr_write_b32 a70, v53\n"
    "v_mfma_f32_4x4x4_16b_bf16 v[24:27], v[46:47], a[190:191], 0 cbsz:2 abid:1\n"
    "buffer_load_dwordx4 v246, s[44:47], s84 offen offset:2048 lds\n"
    "v_cvt_pk_bf16_f32 v54, v32, v34\n"
    "v_cvt_pk_bf16_f32 v55, v33, v35\n"
    "v_accvgpr_write_b32 a67, v54\n"
    "v_accvgpr_write_b32 a71, v55\n"
    "s_sub_i32 s7, s7, 82\n"
    "s_waitcnt vmcnt(10) lgkmcnt(3)\n"
    "ds_read2st64_b32 a[204:205], v38 offset0:88 offset1:89\n"
    "v_mfma_f32_4x4x4_16b_bf16 v[28:31], v[46:47], a[192:193], 0 cbsz:2 abid:2\n"
    "ds_read2st64_b32 a[206:207], v38 offset0:90 offset1:91\n"
    "ds_read_u16 v252, v18 offset:264\n"
    "v_cvt_pk_bf16_f32 v52, v20, v24\n"
    "v_cvt_pk_bf16_f32 v53, v21, v25\n"
    "v_accvgpr_write_b32 a72, v52\n"
    "v_accvgpr_write_b32 a76, v53\n"
    "v_mfma_f32_4x4x4_16b_bf16 v[32:35], v[46:47], a[194:195], 0 cbsz:2 abid:3\n"
    "buffer_load_dwordx4 v247, s[44:47], s84 offen offset:3072 lds\n"
    "s_add_u32 m0, s83, 40960\n"
    "v_cvt_pk_bf16_f32 v54, v22, v26\n"
    "v_cvt_pk_bf16_f32 v55, v23, v27\n"
    "v_accvgpr_write_b32 a80, v54\n"
    "v_accvgpr_write_b32 a84, v55\n"
    "buffer_load_dwordx4 v248, s[44:47], s84 offen lds\n"
    "s_max_i32 s7, s7, 0\n"
    "s_waitcnt vmcnt(11) lgkmcnt(5)\n"
    "ds_read2st64_b32 a[208:209], v38 offset0:92 offset1:93\n"
    "v_mfma_f32_4x4x4_16b_bf16 v[20:23], v[48:49], a[196:197], 0 cbsz:2 abid:0\n"
    "ds_read2st64_b32 a[210:211], v38 offset0:94 offset1:95\n"
    "ds_read_u16 v254, v235 offset:256\n"
    "v_cvt_pk_bf16_f32 v52, v28, v32\n"
    "v_cvt_pk_bf16_f32 v53, v29, v33\n"
    "v_accvgpr_write_b32 a73, v52\n"
    "v_accvgpr_write_b32 a77, v53\n"
    "v_mfma_f32_4x4x4_16b_bf16 v[24:27], v[48:49], a[198:199], 0 cbsz:2 abid:1\n"
    "buffer_load_dwordx4 v249, s[44:47], s84 offen offset:1024 lds\n"
    "v_cvt_pk_bf16_f32 v54, v30, v34\n"
    "v_cvt_pk_bf16_f32 v55, v31, v35\n"
    "v_accvgpr_write_b32 a81, v54\n"
    "v_accvgpr_write_b32 a85, v55\n"
    "s_min_i32 s7, s7, 50\n"
    "s_waitcnt vmcnt(11) lgkmcnt(6)\n"
    "ds_read2st64_b64 a[188:191], v15 offset0:48 offset1:49\n"
    "v_mfma_f32_4x4x4_16b_bf16 v[28:31], v[48:49], a[200:201], 0 cbsz:2 abid:2\n"
    "s_add_u32 m0, s83, 43008\n"
    "s_add_u32 s84, s60, s63\n"
    "v_cvt_pk_bf16_f32 v52, v20, v24\n"
    "v_cvt_pk_bf16_f32 v53, v21, v25\n"
    "v_accvgpr_write_b32 a74, v52\n"
    "v_accvgpr_write_b32 a78, v53\n"
    "v_mfma_f32_4x4x4_16b_bf16 v[32:35], v[48:49], a[202:203], 0 cbsz:2 abid:3\n"
    "buffer_load_dwordx4 v244, s[44:47], s84 offen lds\n"
    "v_cvt_pk_bf16_f32 v54, v22, v26\n"
    "v_cvt_pk_bf16_f32 v55, v23, v27\n"
    "v_accvgpr_write_b32 a82, v54\n"
    "v_accvgpr_write_b32 a86, v55\n"
    "v_lshlrev_b64 v[40:41], v16, v[250:251]\n"
    "s_bfm_b64 s[34:35], s7, 2\n"
    "s_waitcnt vmcnt(11) lgkmcnt(4)\n"
    "ds_read2st64_b64 a[192:195], v15 offset0:50 offset1:51\n"
    "v_mfma_f32_4x4x4_16b_bf16 v[20:23], v[50:51], a[204:205], 0 cbsz:2 abid:0\n"
    "v_cvt_pk_bf16_f32 v52, v28, v32\n"
    "v_cvt_pk_bf16_f32 v53, v29, v33\n"
    "v_accvgpr_write_b32 a75, v52\n"
    "v_accvgpr_write_b32 a79, v53\n"
    "v_mfma_f32_4x4x4_16b_bf16 v[24:27], v[50:51], a[206:207], 0 cbsz:2 abid:1\n"
    "buffer_load_dwordx4 v245, s[44:47], s84 offen offset:1024 lds\n"
    "v_cvt_pk_bf16_f32 v54, v30, v34\n"
    "v_cvt_pk_bf16_f32 v55, v31, v35\n"
    "v_accvgpr_write_b32 a83, v54\n"
    "v_accvgpr_write_b32 a87, v55\n"
    "v_lshlrev_b64 v[42:43], v16, v[252:253]\n"
    "s_waitcnt vmcnt(11) lgkmcnt(2)\n"
    "ds_read2st64_b64 a[196:199], v15 offset0:52 offset1:53\n"
    "v_mfma_f32_4x4x4_16b_bf16 v[28:31], v[50:51], a[208:209], 0 cbsz:2 abid:2\n"
    "v_cvt_pk_bf16_f32 v56, v20, v22\n"
    "v_cvt_pk_bf16_f32 v60, v21, v23\n"
    "v_mfma_f32_4x4x4_16b_bf16 v[32:35], v[50:51], a[210:211], 0 cbsz:2 abid:3\n"
    "buffer_load_dwordx4 v246, s[44:47], s84 offen offset:2048 lds\n"
    "v_cvt_pk_bf16_f32 v57, v24, v26\n"
    "v_cvt_pk_bf16_f32 v61, v25, v27\n"
    "buffer_load_dwordx4 v247, s[44:47], s84 offen offset:3072 lds\n"
    "v_lshlrev_b64 v[44:45], v16, v[254:255]\n"
    "s_waitcnt vmcnt(12) lgkmcnt(2)\n"
    "ds_read2st64_b64 a[200:203], v15 offset0:54 offset1:55\n"
    "v_mfma_f32_4x4x4_16b_bf16 v[20:23], v[40:41], a[188:189], 0 cbsz:2 abid:0\n"
    "ds_read_u16 v250, v18 offset:320\n"
    "s_add_u32 m0, s83, 47104\n"
    "v_cvt_pk_bf16_f32 v58, v28, v30\n"
    "v_cvt_pk_bf16_f32 v62, v29, v31\n"
    "v_mfma_f32_4x4x4_16b_bf16 v[24:27], v[40:41], a[190:191], 0 cbsz:2 abid:1\n"
    "buffer_load_dwordx4 v248, s[44:47], s84 offen lds\n"
    "v_cvt_pk_bf16_f32 v59, v32, v34\n"
    "v_cvt_pk_bf16_f32 v63, v33, v35\n"
    "s_waitcnt vmcnt(12) lgkmcnt(3)\n"
    "ds_read2st64_b32 a[204:205], v38 offset0:112 offset1:113\n"
    "v_mfma_f32_4x4x4_16b_bf16 v[28:31], v[40:41], a[192:193], 0 cbsz:2 abid:2\n"
    "ds_read2st64_b32 a[206:207], v38 offset0:114 offset1:115\n"
    "ds_read_u16 v252, v18 offset:328\n"
    "v_cvt_pk_bf16_f32 v64, v20, v24\n"
    "v_cvt_pk_bf16_f32 v68, v21, v25\n"
    "v_mfma_f32_4x4x4_16b_bf16 v[32:35], v[40:41], a[194:195], 0 cbsz:2 abid:3\n"
    "buffer_load_dwordx4 v249, s[44:47], s84 offen offset:1024 lds\n"
    "v_cvt_pk_bf16_f32 v72, v22, v26\n"
    "v_cvt_pk_bf16_f32 v76, v23, v27\n"
    "s_waitcnt vmcnt(12) lgkmcnt(5)\n"
    "ds_read2st64_b32 a[208:209], v38 offset0:116 offset1:117\n"
    "v_mfma_f32_4x4x4_16b_bf16 v[20:23], v[42:43], a[196:197], 0 cbsz:2 abid:0\n"
    "ds_read2st64_b32 a[210:211], v38 offset0:118 offset1:119\n"
    "ds_read_u16 v254, v235 offset:320\n"
    "s_add_u32 m0, s83, 49152\n"
    "s_add_u32 s84, s61, s63\n"
    "v_cvt_pk_bf16_f32 v65, v28, v32\n"
    "v_cvt_pk_bf16_f32 v69, v29, v33\n"
    "v_mfma_f32_4x4x4_16b_bf16 v[24:27], v[42:43], a[198:199], 0 cbsz:2 abid:1\n"
    "buffer_load_dwordx4 v244, s[44:47], s84 offen lds\n"
    "v_cvt_pk_bf16_f32 v73, v30, v34\n"
    "v_cvt_pk_bf16_f32 v77, v31, v35\n"
    "buffer_load_dwordx4 v245, s[44:47], s84 offen offset:1024 lds\n"
    "s_waitcnt vmcnt(13) lgkmcnt(6)\n"
    "ds_read2st64_b64 a[188:191], v15 offset0:0 offset1:1\n"
    "v_mfma_f32_4x4x4_16b_bf16 v[28:31], v[42:43], a[200:201], 0 cbsz:2 abid:2\n"
    "v_cvt_pk_bf16_f32 v66, v20, v24\n"
    "v_cvt_pk_bf16_f32 v70, v21, v25\n"
    "v_mfma_f32_4x4x4_16b_bf16 v[32:35], v[42:43], a[202:203], 0 cbsz:2 abid:3\n"
    "buffer_load_dwordx4 v246, s[44:47], s84 offen offset:2048 lds\n"
    "v_cvt_pk_bf16_f32 v74, v22, v26\n"
    "v_cvt_pk_bf16_f32 v78, v23, v27\n"
    "v_lshlrev_b64 v[46:47], v16, v[250:251]\n"
    "s_waitcnt vmcnt(13) lgkmcnt(4)\n"
    "ds_read2st64_b64 a[192:195], v15 offset0:2 offset1:3\n"
    "v_mfma_f32_4x4x4_16b_bf16 v[20:23], v[44:45], a[204:205], 0 cbsz:2 abid:0\n"
    "v_cvt_pk_bf16_f32 v67, v28, v32\n"
    "v_cvt_pk_bf16_f32 v71, v29, v33\n"
    "v_mfma_f32_4x4x4_16b_bf16 v[24:27], v[44:45], a[206:207], 0 cbsz:2 abid:1\n"
    "buffer_load_dwordx4 v247, s[44:47], s84 offen offset:3072 lds\n"
    "v_cvt_pk_bf16_f32 v75, v30, v34\n"
    "v_cvt_pk_bf16_f32 v79, v31, v35\n"
    "v_lshlrev_b64 v[48:49], v16, v[252:253]\n"
    "s_waitcnt vmcnt(13) lgkmcnt(2)\n"
    "ds_read2st64_b64 a[196:199], v15 offset0:4 offset1:5\n"
    "v_mfma_f32_4x4x4_16b_bf16 v[28:31], v[44:45], a[208:209], 0 cbsz:2 abid:2\n"
    "s_add_u32 m0, s83, 53248\n"
    "v_cvt_pk_bf16_f32 v80, v20, v22\n"
    "v_cvt_pk_bf16_f32 v84, v21, v23\n"
    "v_mfma_f32_4x4x4_16b_bf16 v[32:35], v[44:45], a[210:211], 0 cbsz:2 abid:3\n"
    "buffer_load_dwordx4 v248, s[44:47], s84 offen lds\n"
    "v_cvt_pk_bf16_f32 v81, v24, v26\n"
    "v_cvt_pk_bf16_f32 v85, v25, v27\n"
    "v_lshlrev_b64 v[50:51], v16, v[254:255]\n"
    "s_waitcnt vmcnt(13) lgkmcnt(2)\n"
    "ds_read2st64_b64 a[200:203], v15 offset0:6 offset1:7\n"
    "v_mfma_f32_4x4x4_16b_bf16 v[20:23], v[46:47], a[188:189], 0 cbsz:2 abid:0\n"
    "ds_read_u16 v250, v18 offset:384\n"
    "s_memrealtime s[92:93]\n"
    "v_cvt_pk_bf16_f32 v82, v28, v30\n"
    "v_cvt_pk_bf16_f32 v86, v29, v31\n"
    "v_mfma_f32_4x4x4_16b_bf16 v[24:27], v[46:47], a[190:191], 0 cbsz:2 abid:1\n"
    "buffer_load_dwordx4 v249, s[44:47], s84 offen offset:1024 lds\n"
    "s_add_u32 m0, s83, 55296\n"
    "s_add_u32 s84, s58, s64\n"
    "v_cvt_pk_bf16_f32 v83, v32, v34\n"
    "v_cvt_pk_bf16_f32 v87, v33, v35\n"
    "buffer_load_dwordx4 v244, s[44:47], s84 offen lds\n"
    "s_waitcnt vmcnt(14) lgkmcnt(3)\n"
    "ds_read2st64_b32 a[204:205], v38 offset0:16 offset1:17\n"
    "v_mfma_f32_4x4x4_16b_bf16 v[28:31], v[46:47], a[192:193], 0 cbsz:2 abid:2\n"
    "ds_read2st64_b32 a[206:207], v38 offset0:18 offset1:19\n"
    "ds_read_u16 v252, v18 offset:392\n"
    "v_cvt_pk_bf16_f32 v88, v20, v24\n"
    "v_cvt_pk_bf16_f32 v92, v21, v25\n"
    "v_mfma_f32_4x4x4_16b_bf16 v[32:35], v[46:47], a[194:195], 0 cbsz:2 abid:3\n"
    "buffer_load_dwordx4 v245, s[44:47], s84 offen offset:1024 lds\n"
    "v_cvt_pk_bf16_f32 v96, v22, v26\n"
    "v_cvt_pk_bf16_f32 v100, v23, v27\n"
    "s_waitcnt vmcnt(14) lgkmcnt(5)\n"
    "ds_read2st64_b32 a[208:209], v38 offset0:20 offset1:21\n"
    "v_mfma_f32_4x4x4_16b_bf16 v[20:23], v[48:49], a[196:197], 0 cbsz:2 abid:0\n"
    "ds_read2st64_b32 a[210:211], v38 offset0:22 offset1:23\n"
    "ds_read_u16 v254, v235 offset:384\n"
    "global_load_dwordx4 v[236:239], v16, s[54:55]\n"
    "v_cvt_pk_bf16_f32 v89, v28, v32\n"
    "v_cvt_pk_bf16_f32 v93, v29, v33\n"
    "v_mfma_f32_4x4x4_16b_bf16 v[24:27], v[48:49], a[198:199], 0 cbsz:2 abid:1\n"
    "buffer_load_dwordx4 v246, s[44:47], s84 offen offset:2048 lds\n"
    "v_cvt_pk_bf16_f32 v97, v30, v34\n"
    "v_cvt_pk_bf16_f32 v101, v31, v35\n"
    "s_waitcnt vmcnt(15) lgkmcnt(6)\n"
    "ds_read2st64_b64 a[188:191], v15 offset0:12 offset1:13\n"
    "v_mfma_f32_4x4x4_16b_bf16 v[28:31], v[48:49], a[200:201], 0 cbsz:2 abid:2\n"
    "v_cvt_pk_bf16_f32 v90, v20, v24\n"
    "v_cvt_pk_bf16_f32 v94, v21, v25\n"
    "v_mfma_f32_4x4x4_16b_bf16 v[32:35], v[48:49], a[202:203], 0 cbsz:2 abid:3\n"
    "buffer_load_dwordx4 v247, s[44:47], s84 offen offset:3072 lds\n"
    "v_cvt_pk_bf16_f32 v98, v22, v26\n"
    "v_cvt_pk_bf16_f32 v102, v23, v27\n"
    "v_lshlrev_b64 v[40:41], v16, v[250:251]\n"
    "s_waitcnt vmcnt(15) lgkmcnt(4)\n"
    "ds_read2st64_b64 a[192:195], v15 offset0:14 offset1:15\n"
    "v_mfma_f32_4x4x4_16b_bf16 v[20:23], v[50:51], a[204:205], 0 cbsz:2 abid:0\n"
    "s_add_u32 m0, s83, 59392\n"
    "v_cvt_pk_bf16_f32 v91, v28, v32\n"
    "v_cvt_pk_bf16_f32 v95, v29, v33\n"
    "v_mfma_f32_4x4x4_16b_bf16 v[24:27], v[50:51], a[206:207], 0 cbsz:2 abid:1\n"
    "buffer_load_dwordx4 v248, s[44:47], s84 offen lds\n"
    "v_cvt_pk_bf16_f32 v99, v30, v34\n"
    "v_cvt_pk_bf16_f32 v103, v31, v35\n"
    "buffer_load_dwordx4 v249, s[44:47], s84 offen offset:1024 lds\n"
    "v_lshlrev_b64 v[42:43], v16, v[252:253]\n"
    "s_waitcnt vmcnt(16) lgkmcnt(2)\n"
    "ds_read2st64_b64 a[196:199], v15 offset0:16 offset1:17\n"
    "v_mfma_f32_4x4x4_16b_bf16 v[28:31], v[50:51], a[208:209], 0 cbsz:2 abid:2\n"
    "s_add_u32 m0, s83, 61440\n"
    "s_add_u32 s84, s59, s64\n"
    "v_cvt_pk_bf16_f32 v104, v20, v22\n"
    "v_cvt_pk_bf16_f32 v108, v21, v23\n"
    "v_mfma_f32_4x4x4_16b_bf16 v[32:35], v[50:51], a[210:211], 0 cbsz:2 abid:3\n"
    "buffer_load_dwordx4 v244, s[44:47], s84 offen lds\n"
    "v_cvt_pk_bf16_f32 v105, v24, v26\n"
    "v_cvt_pk_bf16_f32 v109, v25, v27\n"
    "v_lshlrev_b64 v[44:45], v16, v[254:255]\n"
    "s_waitcnt vmcnt(16) lgkmcnt(2)\n"
    "ds_read2st64_b64 a[200:203], v15 offset0:18 offset1:19\n"
    "v_mfma_f32_4x4x4_16b_bf16 v[20:23], v[40:41], a[188:189], 0 cbsz:2 abid:0\n"
    "ds_read_u16 v250, v18 offset:448\n"
    "v_cvt_pk_bf16_f32 v106, v28, v30\n"
    "v_cvt_pk_bf16_f32 v110, v29, v31\n"
    "v_mfma_f32_4x4x4_16b_bf16 v[24:27], v[40:41], a[190:191], 0 cbsz:2 abid:1\n"
    "buffer_load_dwordx4 v245, s[44:47], s84 offen offset:1024 lds\n"
    "v_cvt_pk_bf16_f32 v107, v32, v34\n"
    "v_cvt_pk_bf16_f32 v111, v33, v35\n"
    "s_waitcnt vmcnt(16) lgkmcnt(3)\n"
    "ds_read2st64_b32 a[204:205], v38 offset0:40 offset1:41\n"
    "v_mfma_f32_4x4x4_16b_bf16 v[28:31], v[40:41], a[192:193], 0 cbsz:2 abid:2\n"
    "ds_read2st64_b32 a[206:207], v38 offset0:42 offset1:43\n"
    "ds_read_u16 v252, v18 offset:456\n"
    "v_cvt_pk_bf16_f32 v112, v20, v24\n"
    "v_cvt_pk_bf16_f32 v116, v21, v25\n"
    "v_mfma_f32_4x4x4_16b_bf16 v[32:35], v[40:41], a[194:195], 0 cbsz:2 abid:3\n"
    "buffer_load_dwordx4 v246, s[44:47], s84 offen offset:2048 lds\n"
    "v_cvt_pk_bf16_f32 v120, v22, v26\n"
    "v_cvt_pk_bf16_f32 v124, v23, v27\n"
    "s_waitcnt vmcnt(16) lgkmcnt(5)\n"
    "ds_read2st64_b32 a[208:209], v38 offset0:44 offset1:45\n"
    "v_mfma_f32_4x4x4_16b_bf16 v[20:23], v[42:43], a[196:197], 0 cbsz:2 abid:0\n"
    "ds_read2st64_b32 a[210:211], v38 offset0:46 offset1:47\n"
    "ds_read_u16 v254, v235 offset:448\n"
    "v_cvt_pk_bf16_f32 v113, v28, v32\n"
    "v_cvt_pk_bf16_f32 v117, v29, v33\n"
    "v_mfma_f32_4x4x4_16b_bf16 v[24:27], v[42:43], a[198:199], 0 cbsz:2 abid:1\n"
    "buffer_load_dwordx4 v247, s[44:47], s84 offen offset:3072 lds\n"
    "s_add_u32 m0, s83, 65536\n"
    "v_cvt_pk_bf16_f32 v121, v30, v34\n"
    "v_cvt_pk_bf16_f32 v125, v31, v35\n"
    "buffer_load_dwordx4 v248, s[44:47], s84 offen lds\n"
    "s_waitcnt vmcnt(17) lgkmcnt(6)\n"
    "ds_read2st64_b64 a[188:191], v15 offset0:24 offset1:25\n"
    "v_mfma_f32_4x4x4_16b_bf16 v[28:31], v[42:43], a[200:201], 0 cbsz:2 abid:2\n"
    "v_cvt_pk_bf16_f32 v114, v20, v24\n"
    "v_cvt_pk_bf16_f32 v118, v21, v25\n"
    "v_mfma_f32_4x4x4_16b_bf16 v[32:35], v[42:43], a[202:203], 0 cbsz:2 abid:3\n"
    "buffer_load_dwordx4 v249, s[44:47], s84 offen offset:1024 lds\n"
    "v_cvt_pk_bf16_f32 v122, v22, v26\n"
    "v_cvt_pk_bf16_f32 v126, v23, v27\n"
    "v_lshlrev_b64 v[46:47], v16, v[250:251]\n"
    "s_waitcnt vmcnt(17) lgkmcnt(4)\n"
    "ds_read2st64_b64 a[192:195], v15 offset0:26 offset1:27\n"
    "v_mfma_f32_4x4x4_16b_bf16 v[20:23], v[44:45], a[204:205], 0 cbsz:2 abid:0\n"
    "s_add_u32 m0, s83, 36864\n"
    "s_add_u32 s84, s60, s64\n"
    "v_cvt_pk_bf16_f32 v115, v28, v32\n"
    "v_cvt_pk_bf16_f32 v119, v29, v33\n"
    "v_mfma_f32_4x4x4_16b_bf16 v[24:27], v[44:45], a[206:207], 0 cbsz:2 abid:1\n"
    "buffer_load_dwordx4 v244, s[44:47], s84 offen lds\n"
    "v_cvt_pk_bf16_f32 v123, v30, v34\n"
    "v_cvt_pk_bf16_f32 v127, v31, v35\n"
    "v_lshlrev_b64 v[48:49], v16, v[252:253]\n"
    "s_waitcnt vmcnt(17) lgkmcnt(2)\n"
    "ds_read2st64_b64 a[196:199], v15 offset0:28 offset1:29\n"
    "v_mfma_f32_4x4x4_16b_bf16 v[28:31], v[44:45], a[208:209], 0 cbsz:2 abid:2\n"
    "v_cvt_pk_bf16_f32 v128, v20, v22\n"
    "v_cvt_pk_bf16_f32 v132, v21, v23\n"
    "v_mfma_f32_4x4x4_16b_bf16 v[32:35], v[44:45], a[210:211], 0 cbsz:2 abid:3\n"
    "buffer_load_dwordx4 v245, s[44:47], s84 offen offset:1024 lds\n"
    "v_cvt_pk_bf16_f32 v129, v24, v26\n"
    "v_cvt_pk_bf16_f32 v133, v25, v27\n"
    "v_lshlrev_b64 v[50:51], v16, v[254:255]\n"
    "s_waitcnt vmcnt(12)\n"
    "v_sub_u32 v11, s0, v236\n"
    "v_or_b32 v236, 0xffff, v236\n"
    "v_or_b32 v238, 0xffff, v238\n"
    "s_waitcnt vmcnt(17) lgkmcnt(2)\n"
    "ds_read2st64_b64 a[200:203], v15 offset0:30 offset1:31\n"
    "v_mfma_f32_4x4x4_16b_bf16 v[20:23], v[46:47], a[188:189], 0 cbsz:2 abid:0\n"
    "ds_read_u16 v250, v18 offset:512\n"
    "v_cvt_pk_bf16_f32 v130, v28, v30\n"
    "v_cvt_pk_bf16_f32 v134, v29, v31\n"
    "v_mfma_f32_4x4x4_16b_bf16 v[24:27], v[46:47], a[190:191], 0 cbsz:2 abid:1\n"
    "buffer_load_dwordx4 v246, s[44:47], s84 offen offset:2048 lds\n"
    "v_cvt_pk_bf16_f32 v131, v32, v34\n"
    "v_cvt_pk_bf16_f32 v135, v33, v35\n"
    "buffer_load_dwordx4 v247, s[44:47], s84 offen offset:3072 lds\n"
    "v_and_b32 v242, 15, v237\n"
    "v_and_b32 v243, 1, v239\n"
    "v_or_b32 v237, 15, v237\n"
    "v_or_b32 v239, 15, v239\n"
    "s_waitcnt vmcnt(18) lgkmcnt(3)\n"
    "ds_read2st64_b32 a[204:205], v38 offset0:64 offset1:65\n"
    "v_mfma_f32_4x4x4_16b_bf16 v[28:31], v[46:47], a[192:193], 0 cbsz:2 abid:2\n"
    "ds_read2st64_b32 a[206:207], v38 offset0:66 offset1:67\n"
    "ds_read_u16 v252, v18 offset:520\n"
    "s_add_u32 m0, s83, 40960\n"
    "v_cvt_pk_bf16_f32 v136, v20, v24\n"
    "v_cvt_pk_bf16_f32 v140, v21, v25\n"
    "v_mfma_f32_4x4x4_16b_bf16 v[32:35], v[46:47], a[194:195], 0 cbsz:2 abid:3\n"
    "buffer_load_dwordx4 v248, s[44:47], s84 offen lds\n"
    "v_cvt_pk_bf16_f32 v144, v22, v26\n"
    "v_cvt_pk_bf16_f32 v148, v23, v27\n"
    "v_cmp_ne_u64 vcc, s[18:19], v[236:237]\n"
    "v_cmp_ne_u64 s[48:49], s[18:19], v[238:239]\n"
    "v_readlane_b32 s9, v242, 0\n"
    "v_and_b32 v236, 1, v242\n"
    "s_waitcnt vmcnt(18) lgkmcnt(5)\n"
    "ds_read2st64_b32 a[208:209], v38 offset0:68 offset1:69\n"
    "v_mfma_f32_4x4x4_16b_bf16 v[20:23], v[48:49], a[196:197], 0 cbsz:2 abid:0\n"
    "ds_read2st64_b32 a[210:211], v38 offset0:70 offset1:71\n"
    "ds_read_u16 v254, v235 offset:512\n"
    "v_cvt_pk_bf16_f32 v137, v28, v32\n"
    "v_cvt_pk_bf16_f32 v141, v29, v33\n"
    "v_mfma_f32_4x4x4_16b_bf16 v[24:27], v[48:49], a[198:199], 0 cbsz:2 abid:1\n"
    "buffer_load_dwordx4 v249, s[44:47], s84 offen offset:1024 lds\n"
    "v_cvt_pk_bf16_f32 v145, v30, v34\n"
    "v_cvt_pk_bf16_f32 v149, v31, v35\n"
    "s_or_b64 s[48:49], s[48:49], vcc\n"
    "s_cbranch_scc1 75f\n"
    "77:\n"
    "v_cmp_eq_u32 s[24:25], 1, v236\n"
    "s_waitcnt vmcnt(18) lgkmcnt(6)\n"
    "ds_read2st64_b64 a[188:191], v15 offset0:36 offset1:37\n"
    "v_mfma_f32_4x4x4_16b_bf16 v[28:31], v[48:49], a[200:201], 0 cbsz:2 abid:2\n"
    "s_add_u32 m0, s83, 43008\n"
    "s_add_u32 s84, s61, s64\n"
    "v_cvt_pk_bf16_f32 v138, v20, v24\n"
    "201:\n"
    "s_cbranch_execz 199b\n"
    "v_cvt_pk_bf16_f32 v142, v21, v25\n"
    "v_mfma_f32_4x4x4_16b_bf16 v[32:35], v[48:49], a[202:203], 0 cbsz:2 abid:3\n"
    "buffer_load_dwordx4 v244, s[44:47], s84 offen lds\n"
    "v_cvt_pk_bf16_f32 v146, v22, v26\n"
    "v_cvt_pk_bf16_f32 v150, v23, v27\n"
    "buffer_load_dwordx4 v245, s[44:47], s84 offen offset:1024 lds\n"
    "v_lshlrev_b64 v[40:41], v16, v[250:251]\n"
    "s_lshr_b32 s9, s9, 1\n"
    "s_add_u32 s9, s9, s28\n"
    "v_lshrrev_b64 v[240:241], v232, s[24:25]\n"
    "v_and_b32 v241, 0xff, v240\n"
    "s_waitcnt vmcnt(19) lgkmcnt(4)\n"
    "ds_read2st64_b64 a[192:195], v15 offset0:38 offset1:39\n"
    "v_mfma_f32_4x4x4_16b_bf16 v[20:23], v[50:51], a[204:205], 0 cbsz:2 abid:0\n"
    "v_cvt_pk_bf16_f32 v139, v28, v32\n"
    "v_cvt_pk_bf16_f32 v143, v29, v33\n"
    "v_mfma_f32_4x4x4_16b_bf16 v[24:27], v[50:51], a[206:207], 0 cbsz:2 abid:1\n"
    "buffer_load_dwordx4 v246, s[44:47], s84 offen offset:2048 lds\n"
    "v_cvt_pk_bf16_f32 v147, v30, v34\n"
    "v_cvt_pk_bf16_f32 v151, v31, v35\n"
    "v_lshlrev_b64 v[42:43], v16, v[252:253]\n"
    "v_bfe_u32 v240, v240, v233, 4\n"
    "v_bcnt_u32_b32 v241, v241, 8\n"
    "v_bcnt_u32_b32 v240, v240, 0\n"
    "v_lshl_add_u32 v240, v240, 1, v241\n"
    "s_waitcnt vmcnt(18) lgkmcnt(2)\n"
    "ds_read2st64_b64 a[196:199], v15 offset0:40 offset1:41\n"
    "v_mfma_f32_4x4x4_16b_bf16 v[28:31], v[50:51], a[208:209], 0 cbsz:2 abid:2\n"
    "v_cvt_pk_bf16_f32 v152, v20, v22\n"
    "v_cvt_pk_bf16_f32 v156, v21, v23\n"
    "v_mfma_f32_4x4x4_16b_bf16 v[32:35], v[50:51], a[210:211], 0 cbsz:2 abid:3\n"
    "buffer_load_dwordx4 v247, s[44:47], s84 offen offset:3072 lds\n"
    "v_cvt_pk_bf16_f32 v153, v24, v26\n"
    "v_cvt_pk_bf16_f32 v157, v25, v27\n"
    "v_lshlrev_b64 v[44:45], v16, v[254:255]\n"
    "s_lshr_b32 s10, s9, 2\n"
    "v_cmp_lt_u32 vcc, 15, v240\n"
    "v_xor_b32 v234, s10, v234\n"
    "s_mov_b64 s[50:51], vcc\n"
    "s_waitcnt vmcnt(18) lgkmcnt(2)\n"
    "ds_read2st64_b64 a[200:203], v15 offset0:42 offset1:43\n"
    "v_mfma_f32_4x4x4_16b_bf16 v[20:23], v[40:41], a[188:189], 0 cbsz:2 abid:0\n"
    "ds_read_u16 v250, v18 offset:576\n"
    "s_add_u32 m0, s83, 47104\n"
    "v_cvt_pk_bf16_f32 v154, v28, v30\n"
    "v_cvt_pk_bf16_f32 v158, v29, v31\n"
    "v_mfma_f32_4x4x4_16b_bf16 v[24:27], v[40:41], a[190:191], 0 cbsz:2 abid:1\n"
    "buffer_load_dwordx4 v248, s[44:47], s84 offen lds\n"
    "v_cvt_pk_bf16_f32 v155, v32, v34\n"
    "v_cvt_pk_bf16_f32 v159, v33, v35\n"
    "v_addc_co_u32 v234, vcc, 0, v234, vcc\n"
    "v_and_b32 v234, 1, v234\n"
    "v_mad_u32_u24 v236, v234, s88, v3\n"
    "v_cmp_eq_u32 s[26:27], 1, v243\n"
    "s_waitcnt vmcnt(18) lgkmcnt(3)\n"
    "ds_read2st64_b32 a[204:205], v38 offset0:88 offset1:89\n"
    "v_mfma_f32_4x4x4_16b_bf16 v[28:31], v[40:41], a[192:193], 0 cbsz:2 abid:2\n"
    "ds_read2st64_b32 a[206:207], v38 offset0:90 offset1:91\n"
    "ds_read_u16 v252, v18 offset:584\n"
    "v_cvt_pk_bf16_f32 v160, v20, v24\n"
    "v_cvt_pk_bf16_f32 v164, v21, v25\n"
    "v_mfma_f32_4x4x4_16b_bf16 v[32:35], v[40:41], a[194:195], 0 cbsz:2 abid:3\n"
    "buffer_load_dwordx4 v249, s[44:47], s84 offen offset:1024 lds\n"
    "v_cvt_pk_bf16_f32 v168, v22, v26\n"
    "v_cvt_pk_bf16_f32 v172, v23, v27\n"
    "v_readlane_b32 s82, v236, 0\n"
    "v_readlane_b32 s73, v236, 1\n"
    "s_waitcnt vmcnt(18) lgkmcnt(5)\n"
    "ds_read2st64_b32 a[208:209], v38 offset0:92 offset1:93\n"
    "v_mfma_f32_4x4x4_16b_bf16 v[20:23], v[42:43], a[196:197], 0 cbsz:2 abid:0\n"
    "ds_read2st64_b32 a[210:211], v38 offset0:94 offset1:95\n"
    "ds_read_u16 v254, v235 offset:576\n"
    "s_barrier\n"
    "s_mov_b32 m0, s57\n"
    "v_cvt_pk_bf16_f32 v161, v28, v32\n"
    "v_cvt_pk_bf16_f32 v165, v29, v33\n"
    "v_mfma_f32_4x4x4_16b_bf16 v[24:27], v[42:43], a[198:199], 0 cbsz:2 abid:1\n"
    "buffer_load_dwordx4 v0, s[36:39], s82 offen nt lds\n"
    "v_cvt_pk_bf16_f32 v169, v30, v34\n"
    "v_cvt_pk_bf16_f32 v173, v31, v35\n"
    "s_waitcnt vmcnt(18) lgkmcnt(6)\n"
    "ds_read2st64_b64 a[188:191], v15 offset0:48 offset1:49\n"
    "v_mfma_f32_4x4x4_16b_bf16 v[28:31], v[42:43], a[200:201], 0 cbsz:2 abid:2\n"
    "v_cvt_pk_bf16_f32 v162, v20, v24\n"
    "v_cvt_pk_bf16_f32 v166, v21, v25\n"
    "v_mfma_f32_4x4x4_16b_bf16 v[32:35], v[42:43], a[202:203], 0 cbsz:2 abid:3\n"
    "buffer_load_dwordx4 v1, s[36:39], s82 offen offset:1024 nt lds\n"
    "v_cvt_pk_bf16_f32 v170, v22, v26\n"
    "v_cvt_pk_bf16_f32 v174, v23, v27\n"
    "v_lshlrev_b64 v[46:47], v16, v[250:251]\n"
    "v_cmp_gt_u32 s[48:49], s96, v232\n"
    "v_lshrrev_b64 v[240:241], v232, s[26:27]\n"
    "v_and_b32 v241, 0xff, v240\n"
    "v_bfe_u32 v240, v240, v233, 4\n"
    "s_waitcnt vmcnt(18) lgkmcnt(4)\n"
    "ds_read2st64_b64 a[192:195], v15 offset0:50 offset1:51\n"
    "v_mfma_f32_4x4x4_16b_bf16 v[20:23], v[44:45], a[204:205], 0 cbsz:2 abid:0\n"
    "v_cvt_pk_bf16_f32 v163, v28, v32\n"
    "v_cvt_pk_bf16_f32 v167, v29, v33\n"
    "v_mfma_f32_4x4x4_16b_bf16 v[24:27], v[44:45], a[206:207], 0 cbsz:2 abid:1\n"
    "buffer_load_dwordx4 v2, s[36:39], s82 offen offset:2048 nt lds\n"
    "v_cvt_pk_bf16_f32 v171, v30, v34\n"
    "v_cvt_pk_bf16_f32 v175, v31, v35\n"
    "v_lshlrev_b64 v[48:49], v16, v[252:253]\n"
    "v_bcnt_u32_b32 v241, v241, 8\n"
    "v_bcnt_u32_b32 v240, v240, 0\n"
    "v_lshl_add_u32 v240, v240, 1, v241\n"
    "v_cmp_lt_u32 s[26:27], 15, v240\n"
    "s_waitcnt vmcnt(18) lgkmcnt(2)\n"
    "ds_read2st64_b64 a[196:199], v15 offset0:52 offset1:53\n"
    "v_mfma_f32_4x4x4_16b_bf16 v[28:31], v[44:45], a[208:209], 0 cbsz:2 abid:2\n"
    "v_cvt_pk_bf16_f32 v176, v20, v22\n"
    "v_cvt_pk_bf16_f32 v180, v21, v23\n"
    "v_mfma_f32_4x4x4_16b_bf16 v[32:35], v[44:45], a[210:211], 0 cbsz:2 abid:3\n"
    "v_cvt_pk_bf16_f32 v177, v24, v26\n"
    "v_cvt_pk_bf16_f32 v181, v25, v27\n"
    "v_lshlrev_b64 v[50:51], v16, v[254:255]\n"
    "s_bfm_b64 s[24:25], s52, 0\n"
    "s_bitcmp1_b32 s9, 0\n"
    "s_cselect_b32 s95, -1, 1\n"
    "s_waitcnt vmcnt(17) lgkmcnt(2)\n"
    "ds_read2st64_b64 a[200:203], v15 offset0:54 offset1:55\n"
    "v_mfma_f32_4x4x4_16b_bf16 v[20:23], v[46:47], a[188:189], 0 cbsz:2 abid:0\n"
    "ds_read_u16 v250, v18 offset:640\n"
    "v_cvt_pk_bf16_f32 v178, v28, v30\n"
    "v_cvt_pk_bf16_f32 v182, v29, v31\n"
    "v_mfma_f32_4x4x4_16b_bf16 v[24:27], v[46:47], a[190:191], 0 cbsz:2 abid:1\n"
    "v_cvt_pk_bf16_f32 v179, v32, v34\n"
    "v_cvt_pk_bf16_f32 v183, v33, v35\n"
    "s_cmp_lt_u32 s52, 12\n"
    "s_cselect_b32 s95, 0, s95\n"
    "s_add_u32 s52, s52, s95\n"
    "s_xor_b64 s[26:27], s[26:27], s[50:51]\n"
    "s_waitcnt vmcnt(16) lgkmcnt(3)\n"
    "ds_read2st64_b32 a[204:205], v38 offset0:112 offset1:113\n"
    "v_mfma_f32_4x4x4_16b_bf16 v[28:31], v[46:47], a[192:193], 0 cbsz:2 abid:2\n"
    "ds_read2st64_b32 a[206:207], v38 offset0:114 offset1:115\n"
    "ds_read_u16 v252, v18 offset:648\n"
    "v_cvt_pk_bf16_f32 v184, v20, v24\n"
    "v_cvt_pk_bf16_f32 v188, v21, v25\n"
    "v_mfma_f32_4x4x4_16b_bf16 v[32:35], v[46:47], a[194:195], 0 cbsz:2 abid:3\n"
    "v_cvt_pk_bf16_f32 v192, v22, v26\n"
    "v_cvt_pk_bf16_f32 v196, v23, v27\n"
    "s_and_b64 s[48:49], s[48:49], s[24:25]\n"
    "s_and_b64 s[24:25], s[24:25], s[26:27]\n"
    "s_andn2_b64 s[48:49], s[48:49], s[26:27]\n"
    "s_bcnt1_i32_b64 s10, s[48:49]\n"
    "s_waitcnt vmcnt(15) lgkmcnt(5)\n"
    "ds_read2st64_b32 a[208:209], v38 offset0:116 offset1:117\n"
    "v_mfma_f32_4x4x4_16b_bf16 v[20:23], v[48:49], a[196:197], 0 cbsz:2 abid:0\n"
    "ds_read2st64_b32 a[210:211], v38 offset0:118 offset1:119\n"
    "ds_read_u16 v254, v235 offset:640\n"
    "v_cvt_pk_bf16_f32 v185, v28, v32\n"
    "v_cvt_pk_bf16_f32 v189, v29, v33\n"
    "v_mfma_f32_4x4x4_16b_bf16 v[24:27], v[48:49], a[198:199], 0 cbsz:2 abid:1\n"
    "v_cvt_pk_bf16_f32 v193, v30, v34\n"
    "v_cvt_pk_bf16_f32 v197, v31, v35\n"
    "s_cmp_lt_u32 s10, 4\n"
    "s_cselect_b64 s[34:35], s[34:35], 0\n"
    "s_cselect_b32 s96, 1, 0\n"
    "s_and_b64 vcc, s[34:35], s[26:27]\n"
    "s_waitcnt vmcnt(14) lgkmcnt(6)\n"
    "ds_read2st64_b64 a[188:191], v15 offset0:0 offset1:1\n"
    "v_mfma_f32_4x4x4_16b_bf16 v[28:31], v[48:49], a[200:201], 0 cbsz:2 abid:2\n"
    "v_cvt_pk_bf16_f32 v186, v20, v24\n"
    "v_cvt_pk_bf16_f32 v190, v21, v25\n"
    "v_mfma_f32_4x4x4_16b_bf16 v[32:35], v[48:49], a[202:203], 0 cbsz:2 abid:3\n"
    "v_cvt_pk_bf16_f32 v194, v22, v26\n"
    "v_cvt_pk_bf16_f32 v198, v23, v27\n"
    "v_lshlrev_b64 v[40:41], v16, v[250:251]\n"
    "v_addc_co_u32 v234, vcc, 0, v234, vcc\n"
    "v_cmp_le_u32 vcc, s52, v19\n"
    "v_subrev_u32 v240, 15, v240\n"
    "v_cmp_gt_u32 s[48:49], 3, v240\n"
    "s_waitcnt vmcnt(13) lgkmcnt(4)\n"
    "ds_read2st64_b64 a[192:195], v15 offset0:2 offset1:3\n"
    "v_mfma_f32_4x4x4_16b_bf16 v[20:23], v[50:51], a[204:205], 0 cbsz:2 abid:0\n"
    "v_cvt_pk_bf16_f32 v187, v28, v32\n"
    "v_cvt_pk_bf16_f32 v191, v29, v33\n"
    "v_mfma_f32_4x4x4_16b_bf16 v[24:27], v[50:51], a[206:207], 0 cbsz:2 abid:1\n"
    "v_cvt_pk_bf16_f32 v195, v30, v34\n"
    "v_cvt_pk_bf16_f32 v199, v31, v35\n"
    "v_lshlrev_b64 v[42:43], v16, v[252:253]\n"
    "s_or_b64 s[26:27], s[26:27], s[48:49]\n"
    "v_and_b32 v234, 1, v234\n"
    "v_mov_b32 v236, s33\n"
    "v_mad_u32_u24 v3, v234, s88, v3\n"
    "s_waitcnt vmcnt(12) lgkmcnt(2)\n"
    "ds_read2st64_b64 a[196:199], v15 offset0:4 offset1:5\n"
    "v_mfma_f32_4x4x4_16b_bf16 v[28:31], v[50:51], a[208:209], 0 cbsz:2 abid:2\n"
    "v_cvt_pk_bf16_f32 v200, v20, v22\n"
    "v_cvt_pk_bf16_f32 v204, v21, v23\n"
    "v_mfma_f32_4x4x4_16b_bf16 v[32:35], v[50:51], a[210:211], 0 cbsz:2 abid:3\n"
    "v_cvt_pk_bf16_f32 v201, v24, v26\n"
    "v_cvt_pk_bf16_f32 v205, v25, v27\n"
    "v_lshlrev_b64 v[44:45], v16, v[254:255]\n"
    "v_cndmask_b32 v3, v3, v236, vcc\n"
    "s_nop 0\n"
    "v_readlane_b32 s74, v3, 2\n"
    "v_readlane_b32 s75, v3, 3\n"
    "s_waitcnt vmcnt(11) lgkmcnt(2)\n"
    "ds_read2st64_b64 a[200:203], v15 offset0:6 offset1:7\n"
    "v_mfma_f32_4x4x4_16b_bf16 v[20:23], v[40:41], a[188:189], 0 cbsz:2 abid:0\n"
    "ds_read_u16 v250, v18 offset:704\n"
    "v_cvt_pk_bf16_f32 v202, v28, v30\n"
    "v_cvt_pk_bf16_f32 v206, v29, v31\n"
    "v_mfma_f32_4x4x4_16b_bf16 v[24:27], v[40:41], a[190:191], 0 cbsz:2 abid:1\n"
    "v_cvt_pk_bf16_f32 v203, v32, v34\n"
    "v_cvt_pk_bf16_f32 v207, v33, v35\n"
    "v_readlane_b32 s81, v3, 4\n"
    "s_waitcnt vmcnt(10) lgkmcnt(3)\n"
    "ds_read2st64_b32 a[204:205], v38 offset0:16 offset1:17\n"
    "v_mfma_f32_4x4x4_16b_bf16 v[28:31], v[40:41], a[192:193], 0 cbsz:2 abid:2\n"
    "ds_read2st64_b32 a[206:207], v38 offset0:18 offset1:19\n"
    "ds_read_u16 v252, v18 offset:712\n"
    "v_cvt_pk_bf16_f32 v208, v20, v24\n"
    "v_cvt_pk_bf16_f32 v212, v21, v25\n"
    "v_mfma_f32_4x4x4_16b_bf16 v[32:35], v[40:41], a[194:195], 0 cbsz:2 abid:3\n"
    "v_cvt_pk_bf16_f32 v216, v22, v26\n"
    "v_cvt_pk_bf16_f32 v220, v23, v27\n"
    "s_waitcnt vmcnt(9) lgkmcnt(5)\n"
    "ds_read2st64_b32 a[208:209], v38 offset0:20 offset1:21\n"
    "v_mfma_f32_4x4x4_16b_bf16 v[20:23], v[42:43], a[196:197], 0 cbsz:2 abid:0\n"
    "ds_read2st64_b32 a[210:211], v38 offset0:22 offset1:23\n"
    "ds_read_u16 v254, v235 offset:704\n"
    "v_cvt_pk_bf16_f32 v209, v28, v32\n"
    "v_cvt_pk_bf16_f32 v213, v29, v33\n"
    "v_mfma_f32_4x4x4_16b_bf16 v[24:27], v[42:43], a[198:199], 0 cbsz:2 abid:1\n"
    "v_cvt_pk_bf16_f32 v217, v30, v34\n"
    "v_cvt_pk_bf16_f32 v221, v31, v35\n"
    "s_waitcnt vmcnt(8) lgkmcnt(6)\n"
    "ds_read2st64_b64 a[188:191], v15 offset0:12 offset1:13\n"
    "v_mfma_f32_4x4x4_16b_bf16 v[28:31], v[42:43], a[200:201], 0 cbsz:2 abid:2\n"
    "s_nop 0\n"
    "v_cvt_pk_bf16_f32 v210, v20, v24\n"
    "v_cvt_pk_bf16_f32 v214, v21, v25\n"
    "v_mfma_f32_4x4x4_16b_bf16 v[32:35], v[42:43], a[202:203], 0 cbsz:2 abid:3\n"
    "v_cvt_pk_bf16_f32 v218, v22, v26\n"
    "v_cvt_pk_bf16_f32 v222, v23, v27\n"
    "v_lshlrev_b64 v[46:47], v16, v[250:251]\n"
    "s_waitcnt vmcnt(7) lgkmcnt(4)\n"
    "ds_read2st64_b64 a[192:195], v15 offset0:14 offset1:15\n"
    "v_mfma_f32_4x4x4_16b_bf16 v[20:23], v[44:45], a[204:205], 0 cbsz:2 abid:0\n"
    "s_add_u32 m0, s57, 12288\n"
    "v_cvt_pk_bf16_f32 v211, v28, v32\n"
    "v_cvt_pk_bf16_f32 v215, v29, v33\n"
    "v_mfma_f32_4x4x4_16b_bf16 v[24:27], v[44:45], a[206:207], 0 cbsz:2 abid:1\n"
    "buffer_load_dwordx4 v0, s[36:39], s73 offen nt lds\n"
    "v_cvt_pk_bf16_f32 v219, v30, v34\n"
    "v_cvt_pk_bf16_f32 v223, v31, v35\n"
    "v_lshlrev_b64 v[48:49], v16, v[252:253]\n"
    "s_waitcnt vmcnt(7) lgkmcnt(2)\n"
    "ds_read2st64_b64 a[196:199], v15 offset0:16 offset1:17\n"
    "v_mfma_f32_4x4x4_16b_bf16 v[28:31], v[44:45], a[208:209], 0 cbsz:2 abid:2\n"
    "v_cvt_pk_bf16_f32 v224, v20, v22\n"
    "v_cvt_pk_bf16_f32 v228, v21, v23\n"
    "v_mfma_f32_4x4x4_16b_bf16 v[32:35], v[44:45], a[210:211], 0 cbsz:2 abid:3\n"
    "buffer_load_dwordx4 v1, s[36:39], s73 offen offset:1024 nt lds\n"
    "v_cvt_pk_bf16_f32 v225, v24, v26\n"
    "v_cvt_pk_bf16_f32 v229, v25, v27\n"
    "v_lshlrev_b64 v[50:51], v16, v[254:255]\n"
    "s_waitcnt vmcnt(7) lgkmcnt(2)\n"
    "ds_read2st64_b64 a[200:203], v15 offset0:18 offset1:19\n"
    "v_mfma_f32_4x4x4_16b_bf16 v[20:23], v[46:47], a[188:189], 0 cbsz:2 abid:0\n"
    "v_cvt_pk_bf16_f32 v226, v28, v30\n"
    "v_cvt_pk_bf16_f32 v230, v29, v31\n"
    "v_mfma_f32_4x4x4_16b_bf16 v[24:27], v[46:47], a[190:191], 0 cbsz:2 abid:1\n"
    "buffer_load_dwordx4 v2, s[36:39], s73 offen offset:2048 nt lds\n"
    "v_cvt_pk_bf16_f32 v227, v32, v34\n"
    "v_cvt_pk_bf16_f32 v231, v33, v35\n"
    "s_waitcnt vmcnt(7) lgkmcnt(2)\n"
    "ds_read2st64_b32 a[204:205], v38 offset0:40 offset1:41\n"
    "v_mfma_f32_4x4x4_16b_bf16 v[28:31], v[46:47], a[192:193], 0 cbsz:2 abid:2\n"
    "ds_read2st64_b32 a[206:207], v38 offset0:42 offset1:43\n"
    "v_cvt_pk_bf16_f32 v232, v20, v24\n"
    "v_cvt_pk_bf16_f32 v236, v21, v25\n"
    "v_mfma_f32_4x4x4_16b_bf16 v[32:35], v[46:47], a[194:195], 0 cbsz:2 abid:3\n"
    "v_cvt_pk_bf16_f32 v240, v22, v26\n"
    "v_cvt_pk_bf16_f32 v244, v23, v27\n"
    "s_waitcnt vmcnt(6) lgkmcnt(3)\n"
    "ds_read2st64_b32 a[208:209], v38 offset0:44 offset1:45\n"
    "v_mfma_f32_4x4x4_16b_bf16 v[20:23], v[48:49], a[196:197], 0 cbsz:2 abid:0\n"
    "ds_read2st64_b32 a[210:211], v38 offset0:46 offset1:47\n"
    "v_cvt_pk_bf16_f32 v233, v28, v32\n"
    "v_cvt_pk_bf16_f32 v237, v29, v33\n"
    "v_mfma_f32_4x4x4_16b_bf16 v[24:27], v[48:49], a[198:199], 0 cbsz:2 abid:1\n"
    "v_cvt_pk_bf16_f32 v241, v30, v34\n"
    "v_cvt_pk_bf16_f32 v245, v31, v35\n"
    "s_waitcnt lgkmcnt(4)\n"
    "v_mfma_f32_4x4x4_16b_bf16 v[28:31], v[48:49], a[200:201], 0 cbsz:2 abid:2\n"
    "s_nop 1\n"
    "v_cvt_pk_bf16_f32 v234, v20, v24\n"
    "v_cvt_pk_bf16_f32 v238, v21, v25\n"
    "v_mfma_f32_4x4x4_16b_bf16 v[32:35], v[48:49], a[202:203], 0 cbsz:2 abid:3\n"
    "v_cvt_pk_bf16_f32 v242, v22, v26\n"
    "v_cvt_pk_bf16_f32 v246, v23, v27\n"
    "s_waitcnt lgkmcnt(2)\n"
    "v_mfma_f32_4x4x4_16b_bf16 v[20:23], v[50:51], a[204:205], 0 cbsz:2 abid:0\n"
    "s_nop 1\n"
    "v_cvt_pk_bf16_f32 v235, v28, v32\n"
    "v_cvt_pk_bf16_f32 v239, v29, v33\n"
    "v_mfma_f32_4x4x4_16b_bf16 v[24:27], v[50:51], a[206:207], 0 cbsz:2 abid:1\n"
    "v_cvt_pk_bf16_f32 v243, v30, v34\n"
    "v_cvt_pk_bf16_f32 v247, v31, v35\n"
    "s_waitcnt lgkmcnt(0)\n"
    "v_mfma_f32_4x4x4_16b_bf16 v[28:31], v[50:51], a[208:209], 0 cbsz:2 abid:2\n"
    "s_nop 1\n"
    "v_cvt_pk_bf16_f32 v248, v20, v22\n"
    "v_cvt_pk_bf16_f32 v252, v21, v23\n"
    "v_mfma_f32_4x4x4_16b_bf16 v[32:35], v[50:51], a[210:211], 0 cbsz:2 abid:3\n"
    "v_cvt_pk_bf16_f32 v249, v24, v26\n"
    "v_cvt_pk_bf16_f32 v253, v25, v27\n"
    "s_waitcnt lgkmcnt(0)\n"
    "s_add_u32 s89, s92, 250\n"
    "v_add_u32 v38, s11, v38\n"
    "s_nop 0\n"
    "v_cvt_pk_bf16_f32 v250, v28, v30\n"
    "v_cvt_pk_bf16_f32 v254, v29, v31\n"
    "v_cvt_pk_bf16_f32 v251, v32, v34\n"
    "v_cvt_pk_bf16_f32 v255, v33, v35\n"
    "s_waitcnt vmcnt(3)\n"
    "s_barrier\n"
    "ds_read_b128 a[88:91], v4\n"
    "ds_read_b128 a[92:95], v5\n"
    "ds_read_b128 a[96:99], v6\n"
    "ds_read_b128 a[100:103], v7\n"
    "ds_read_b128 a[104:107], v4 offset:256\n"
    "ds_read_b128 a[108:111], v5 offset:256\n"
    "ds_read_b128 a[112:115], v6 offset:256\n"
    "ds_read_b128 a[116:119], v7 offset:256\n"
    "ds_read_b128 a[120:123], v4 offset:512\n"
    "ds_read_b128 a[124:127], v5 offset:512\n"
    "ds_read_b128 a[128:131], v6 offset:512\n"
    "ds_read_b128 a[132:135], v7 offset:512\n"
    "s_waitcnt lgkmcnt(11)\n"
    "v_mfma_f32_16x16x32_bf16 v[20:23], a[88:91], a[16:19], 0\n"
    "v_add_u32 v14, 1, v14\n"
    "s_sub_i32 s46, s52, 5\n"
    "s_max_i32 s46, s46, 0\n"
    "s_waitcnt lgkmcnt(10)\n"
    "v_mfma_f32_16x16x32_bf16 v[20:23], a[92:95], a[40:43], v[20:23]\n"
    "s_add_u32 m0, s57, 24576\n"
    "s_bfm_b64 s[44:45], s46, 5\n"
    "v_subrev_u32 v43, 5, v19\n"
    "s_waitcnt lgkmcnt(9)\n"
    "v_mfma_f32_16x16x32_bf16 v[20:23], a[96:99], a[64:67], v[20:23]\n"
    "buffer_load_dwordx4 v0, s[36:39], s74 offen nt lds\n"
    "s_andn2_b64 s[50:51], s[44:45], s[26:27]\n"
    "s_waitcnt lgkmcnt(8)\n"
    "v_mfma_f32_16x16x32_bf16 v[20:23], a[100:103], v[56:59], v[20:23]\n"
    "s_and_b64 s[48:49], s[44:45], s[26:27]\n"
    "s_bcnt1_i32_b64 s47, s[50:51]\n"
    "s_sub_u32 s46, s46, s47\n"
    "s_waitcnt lgkmcnt(7)\n"
    "v_mfma_f32_16x16x32_bf16 v[20:23], a[104:107], v[80:83], v[20:23]\n"
    "buffer_load_dwordx4 v1, s[36:39], s74 offen offset:1024 nt lds\n"
    "s_add_u32 s58, s46, 5\n"
    "s_waitcnt lgkmcnt(6)\n"
    "v_mfma_f32_16x16x32_bf16 v[20:23], a[108:111], v[104:107], v[20:23]\n"
    "v_mov_b32 v45, s58\n"
    "v_mbcnt_lo_u32_b32 v47, s48, 5\n"
    "v_mbcnt_lo_u32_b32 v45, s50, v45\n"
    "s_waitcnt lgkmcnt(5)\n"
    "v_mfma_f32_16x16x32_bf16 v[20:23], a[112:115], v[128:131], v[20:23]\n"
    "buffer_load_dwordx4 v2, s[36:39], s74 offen offset:2048 nt lds\n"
    "v_mbcnt_hi_u32_b32 v47, s49, v47\n"
    "s_waitcnt lgkmcnt(4)\n"
    "v_mfma_f32_16x16x32_bf16 v[20:23], a[116:119], v[152:155], v[20:23]\n"
    "v_mbcnt_hi_u32_b32 v45, s51, v45\n"
    "v_cndmask_b32 v47, v19, v47, s[48:49]\n"
    "s_sub_i32 s59, s47, 4\n"
    "s_waitcnt lgkmcnt(3)\n"
    "v_mfma_f32_16x16x32_bf16 v[20:23], a[120:123], v[176:179], v[20:23]\n"
    "v_cndmask_b32 v47, v47, v45, s[50:51]\n"
    "s_max_i32 s59, s59, 0\n"
    "v_lshlrev_b32 v47, 2, v47\n"
    "s_waitcnt lgkmcnt(2)\n"
    "v_mfma_f32_16x16x32_bf16 v[20:23], a[124:127], v[200:203], v[20:23]\n"
    "s_mul_i32 s59, s59, 9\n"
    "s_min_u32 s59, s59, 256\n"
    "ds_permute_b32 v3, v47, v3\n"
    "s_waitcnt lgkmcnt(2)\n"
    "v_mfma_f32_16x16x32_bf16 v[20:23], a[128:131], v[224:227], v[20:23]\n"
    "s_bcnt1_i32_b64 s73, s[24:25]\n"
    "s_mul_i32 s62, s73, 12\n"
    "s_lshr_b32 s62, s62, 5\n"
    "s_waitcnt lgkmcnt(1)\n"
    "v_mfma_f32_16x16x32_bf16 v[20:23], a[132:135], v[248:251], v[20:23]\n"
    "s_add_u32 s62, s62, 38\n"
    "s_sub_u32 s63, s62, 25\n"
    "v_bfe_i32 v11, v11, 0, 16\n"
    "v_mfma_f32_16x16x32_bf16 v[24:27], a[132:135], v[252:255], 0\n"
    "s_nop 0\n"
    "v_readlane_b32 s7, v11, 0\n"
    "v_readlane_b32 s8, v11, 1\n"
    "v_mfma_f32_16x16x32_bf16 v[24:27], a[128:131], v[228:231], v[24:27]\n"
    "v_readlane_b32 s60, v11, 2\n"
    "v_readlane_b32 s61, v11, 3\n"
    "s_add_i32 s7, s7, s8\n"
    "v_mfma_f32_16x16x32_bf16 v[24:27], a[124:127], v[204:207], v[24:27]\n"
    "s_add_i32 s8, s60, s61\n"
    "s_add_i32 s7, s7, s8\n"
    "s_ashr_i32 s7, s7, 2\n"
    "v_mfma_f32_16x16x32_bf16 v[24:27], a[120:123], v[180:183], v[24:27]\n"
    "s_sub_u32 s7, s0, s7\n"
    "s_add_u32 s7, s7, s62\n"
    "s_add_u32 s1, s7, 444\n"
    "v_mfma_f32_16x16x32_bf16 v[24:27], a[116:119], v[156:159], v[24:27]\n"
    "s_mov_b32 s72, 0\n"
    "v_mul_i32_i24 v40, s59, v43\n"
    "s_mul_i32 s7, s2, 9\n"
    "v_mfma_f32_16x16x32_bf16 v[24:27], a[112:115], v[132:135], v[24:27]\n"
    "s_and_b32 s7, s7, 0xff\n"
    "v_add_u32 v40, s7, v40\n"
    "v_subrev_u32 v42, s46, v43\n"
    "v_mfma_f32_16x16x32_bf16 v[24:27], a[108:111], v[108:111], v[24:27]\n"
    "v_add_u32 v41, s59, v40\n"
    "v_ashrrev_i32 v40, 8, v40\n"
    "v_ashrrev_i32 v41, 8, v41\n"
    "v_mfma_f32_16x16x32_bf16 v[24:27], a[104:107], v[84:87], v[24:27]\n"
    "v_min_i32 v40, s47, v40\n"
    "v_min_i32 v41, s47, v41\n"
    "v_max_i32 v40, v40, v42\n"
    "v_mfma_f32_16x16x32_bf16 v[24:27], a[100:103], v[60:63], v[24:27]\n"
    "v_add_u32 v42, 1, v42\n"
    "v_max_i32 v41, v41, v42\n"
    "v_cmp_gt_i32 s[60:61], v41, v40\n"
    "v_mfma_f32_16x16x32_bf16 v[24:27], a[96:99], a[68:71], v[24:27]\n"
    "s_waitcnt vmcnt(3)\n"
    "v_add_u32 v41, s58, v40\n"
    "v_sub_u32 v40, v19, v40\n"
    "v_mfma_f32_16x16x32_bf16 v[24:27], a[92:95], a[44:47], v[24:27]\n"
    "ds_write_b32 v13, v14\n"
    "v_cndmask_b32 v40, v40, v41, s[60:61]\n"
    "v_mfma_f32_16x16x32_bf16 v[24:27], a[88:91], a[20:23], v[24:27]\n"
    "s_add_u32 m0, s57, 36864\n"
    "v_cndmask_b32 v40, v19, v40, s[44:45]\n"
    "v_lshlrev_b32 v40, 2, v40\n"
    "v_mfma_f32_16x16x32_bf16 v[28:31], a[88:91], a[0:3], 0\n"
    "buffer_load_dwordx4 v0, s[36:39], s75 offen nt lds\n"
    "v_mfma_f32_16x16x32_bf16 v[28:31], a[92:95], a[24:27], v[28:31]\n"
    "s_waitcnt lgkmcnt(1)\n"
    "ds_bpermute_b32 v3, v40, v3\n"
    "s_mul_i32 s94, s73, 8\n"
    "v_mfma_f32_16x16x32_bf16 v[28:31], a[96:99], a[48:51], v[28:31]\n"
    "buffer_load_dwordx4 v1, s[36:39], s75 offen offset:1024 nt lds\n"
    "s_mul_i32 s31, s73, 6\n"
    "v_mfma_f32_16x16x32_bf16 v[28:31], a[100:103], a[72:75], v[28:31]\n"
    "v_cvt_pk_bf16_f32 v52, v20, v24\n"
    "s_lshr_b32 s94, s94, 5\n"
    "s_lshr_b32 s31, s31, 5\n"
    "v_mfma_f32_16x16x32_bf16 v[28:31], a[104:107], v[64:67], v[28:31]\n"
    "ds_read_b32 v15, v12\n"
    "s_mul_i32 s7, s95, -2\n"
    "s_add_u32 s7, s7, 67\n"
    "v_mfma_f32_16x16x32_bf16 v[28:31], a[108:111], v[88:91], v[28:31]\n"
    "v_cvt_pk_bf16_f32 v53, v21, v25\n"
    "s_add_u32 s94, s94, s7\n"
    "s_add_u32 s31, s31, s7\n"
    "v_mfma_f32_16x16x32_bf16 v[28:31], a[112:115], v[112:115], v[28:31]\n"
    "v_cvt_pk_bf16_f32 v54, v22, v26\n"
    "s_sub_u32 s7, 6, s72\n"
    "s_add_u32 s31, s31, s7\n"
    "v_mfma_f32_16x16x32_bf16 v[28:31], a[116:119], v[136:139], v[28:31]\n"
    "v_cvt_pk_bf16_f32 v55, v23, v27\n"
    "s_waitcnt lgkmcnt(0)\n"
    "v_cmp_lt_u32 vcc, v15, v14\n"
    "v_mfma_f32_16x16x32_bf16 v[28:31], a[120:123], v[160:163], v[28:31]\n"
    "s_add_u32 s76, s52, 4\n"
    "v_subrev_u32 v18, 5, v19\n"
    "s_mov_b32 s30, s94\n"
    "v_mfma_f32_16x16x32_bf16 v[28:31], a[124:127], v[184:187], v[28:31]\n"
    "s_cbranch_vccnz 81b\n"
    "82:\n"
    "s_add_u32 s94, s94, 12\n"
    "v_mfma_f32_16x16x32_bf16 v[28:31], a[128:131], v[208:211], v[28:31]\n"
    "ds_write2st64_b32 v38, v52, v53 offset0:0 offset1:2\n"
    "s_sub_u32 s7, s52, 5\n"
    "v_mfma_f32_16x16x32_bf16 v[28:31], a[132:135], v[232:235], v[28:31]\n"
    "ds_read_b128 a[136:139], v4 offset:12288\n"
    "s_mov_b64 vcc, s[12:13]\n"
    "s_andn2_b32 s7, s7, 1\n"
    "v_mfma_f32_16x16x32_bf16 v[32:35], a[132:135], v[236:239], 0\n"
    "ds_write2st64_b32 v38, v54, v55 offset0:4 offset1:6\n"
    "v_mfma_f32_16x16x32_bf16 v[32:35], a[128:131], v[212:215], v[32:35]\n"
    "ds_read_b128 a[140:143], v5 offset:12288\n"
    "v_add_u32 v14, 1, v14\n"
    "v_mfma_f32_16x16x32_bf16 v[32:35], a[124:127], v[188:191], v[32:35]\n"
    "buffer_load_dwordx4 v2, s[36:39], s75 offen offset:2048 nt lds\n"
    "v_mfma_f32_16x16x32_bf16 v[32:35], a[120:123], v[164:167], v[32:35]\n"
    "ds_read_b128 a[144:147], v6 offset:12288\n"
    "buffer_load_dword a212, off, s[36:39], s33\n"
    "v_mfma_f32_16x16x32_bf16 v[32:35], a[116:119], v[140:143], v[32:35]\n"
    "buffer_load_dword a212, off, s[36:39], s33\n"
    "buffer_load_dword a212, off, s[36:39], s33\n"
    "v_mfma_f32_16x16x32_bf16 v[32:35], a[112:115], v[116:119], v[32:35]\n"
    "s_cmp_lt_i32 s7, 2\n"
    "s_cselect_b32 s7, 0x1000, s7\n"
    "v_mfma_f32_16x16x32_bf16 v[32:35], a[108:111], v[92:95], v[32:35]\n"
    "ds_read_b128 a[148:151], v7 offset:12288\n"
    "s_cmp_lt_u32 s47, 3\n"
    "s_cselect_b32 s7, 0x1000, s7\n"
    "v_mfma_f32_16x16x32_bf16 v[32:35], a[104:107], v[68:71], v[32:35]\n"
    "s_add_u32 s70, s7, -1\n"
    "v_subrev_u32 v40, s70, v18\n"
    "v_cmp_gt_u32 s[60:61], 2, v40\n"
    "v_mfma_f32_16x16x32_bf16 v[32:35], a[100:103], a[76:79], v[32:35]\n"
    "ds_read_b128 a[152:155], v4 offset:12544\n"
    "s_add_u32 s64, s7, 5\n"
    "s_add_u32 s65, s7, 6\n"
    "v_mfma_f32_16x16x32_bf16 v[32:35], a[96:99], a[52:55], v[32:35]\n"
    "v_cndmask_b32 v18, v18, 63, s[60:61]\n"
    "s_add_u32 s71, s7, 5\n"
    "v_lshlrev_b32 v18, 2, v18\n"
    "v_mfma_f32_16x16x32_bf16 v[32:35], a[92:95], a[28:31], v[32:35]\n"
    "ds_read_b128 a[156:159], v5 offset:12544\n"
    "ds_bpermute_b32 v18, v18, v3\n"
    "v_mfma_f32_16x16x32_bf16 v[32:35], a[88:91], a[4:7], v[32:35]\n"
    "v_readlane_b32 s84, v3, s86\n"
    "v_mfma_f32_16x16x32_bf16 v[20:23], a[88:91], a[8:11], 0\n"
    "ds_read_b128 a[160:163], v6 offset:12544\n"
    "v_mfma_f32_16x16x32_bf16 v[20:23], a[92:95], a[32:35], v[20:23]\n"
    "v_mfma_f32_16x16x32_bf16 v[20:23], a[96:99], a[56:59], v[20:23]\n"
    "ds_read_b128 a[164:167], v7 offset:12544\n"
    "v_mfma_f32_16x16x32_bf16 v[20:23], a[100:103], a[80:83], v[20:23]\n"
    "v_cvt_pk_bf16_f32 v36, v28, v32\n"
    "v_mfma_f32_16x16x32_bf16 v[20:23], a[104:107], v[72:75], v[20:23]\n"
    "ds_read_b128 a[168:171], v4 offset:12800\n"
    "v_mfma_f32_16x16x32_bf16 v[20:23], a[108:111], v[96:99], v[20:23]\n"
    "v_cvt_pk_bf16_f32 v46, v29, v33\n"
    "v_mfma_f32_16x16x32_bf16 v[20:23], a[112:115], v[120:123], v[20:23]\n"
    "ds_read_b128 a[172:175], v5 offset:12800\n"
    "s_add_u32 m0, s57, 49152\n"
    "v_mfma_f32_16x16x32_bf16 v[20:23], a[116:119], v[144:147], v[20:23]\n"
    "buffer_load_dwordx4 v0, s[36:39], s81 offen nt lds\n"
    "v_mfma_f32_16x16x32_bf16 v[20:23], a[120:123], v[168:171], v[20:23]\n"
    "v_cvt_pk_bf16_f32 v37, v30, v34\n"
    "v_mfma_f32_16x16x32_bf16 v[20:23], a[124:127], v[192:195], v[20:23]\n"
    "ds_read_b128 a[176:179], v6 offset:12800\n"
    "v_mfma_f32_16x16x32_bf16 v[20:23], a[128:131], v[216:219], v[20:23]\n"
    "v_cvt_pk_bf16_f32 v50, v31, v35\n"
    "v_mfma_f32_16x16x32_bf16 v[20:23], a[132:135], v[240:243], v[20:23]\n"
    "ds_read_b128 a[180:183], v7 offset:12800\n"
    "v_mfma_f32_16x16x32_bf16 v[24:27], a[132:135], v[244:247], 0\n"
    "buffer_load_dwordx4 v1, s[36:39], s81 offen offset:1024 nt lds\n"
    "v_mfma_f32_16x16x32_bf16 v[24:27], a[128:131], v[220:223], v[24:27]\n"
    "v_cndmask_b32_dpp v44, v46, v36, vcc row_ror:8 row_mask:0xf bank_mask:0xf\n"
    "v_mfma_f32_16x16x32_bf16 v[24:27], a[124:127], v[196:199], v[24:27]\n"
    "buffer_load_dwordx4 v2, s[36:39], s81 offen offset:2048 nt lds\n"
    "v_mfma_f32_16x16x32_bf16 v[24:27], a[120:123], v[172:175], v[24:27]\n"
    "s_memrealtime s[92:93]\n"
    "buffer_load_dword a212, off, s[36:39], s33\n"
    "v_mfma_f32_16x16x32_bf16 v[24:27], a[116:119], v[148:151], v[24:27]\n"
    "v_cndmask_b32_dpp v48, v50, v37, vcc row_ror:8 row_mask:0xf bank_mask:0xf\n"
    "buffer_load_dword a212, off, s[36:39], s33\n"
    "v_mfma_f32_16x16x32_bf16 v[24:27], a[112:115], v[124:127], v[24:27]\n"
    "v_mov_b32_dpp v46, v36 row_ror:8 row_mask:0xf bank_mask:0x3\n"
    "v_mfma_f32_16x16x32_bf16 v[24:27], a[108:111], v[100:103], v[24:27]\n"
    "v_mov_b32_dpp v50, v37 row_ror:8 row_mask:0xf bank_mask:0x3\n"
    "v_mfma_f32_16x16x32_bf16 v[24:27], a[104:107], v[76:79], v[24:27]\n"
    "v_mfma_f32_16x16x32_bf16 v[24:27], a[100:103], a[84:87], v[24:27]\n"
    "s_waitcnt vmcnt(11) lgkmcnt(0)\n"
    "s_sub_u32 s85, s92, s89\n"
    "v_mfma_f32_16x16x32_bf16 v[24:27], a[96:99], a[60:63], v[24:27]\n"
    "s_add_i32 s63, s63, s85\n"
    "s_max_i32 s63, s63, s62\n"
    "s_add_u32 s89, s89, s63\n"
    "v_mfma_f32_16x16x32_bf16 v[24:27], a[92:95], a[36:39], v[24:27]\n"
    "s_sub_i32 s7, s89, s1\n"
    "s_mul_i32 s7, s7, 4\n"
    "s_ashr_i32 s7, s7, 6\n"
    "s_mov_b32 s81, s84\n"
    "s_add_u32 m0, s57, 61440\n"
    "v_mfma_f32_16x16x32_bf16 v[24:27], a[88:91], a[12:15], v[24:27]\n"
    "s_max_i32 s7, s7, -2\n"
    "s_min_i32 s7, s7, 4\n"
    "s_sub_u32 s31, s31, s7\n"
    "s_branch 52f\n"
    ".p2align 12\n"
    "51:\n"
    "v_mfma_f32_16x16x32_bf16 v[20:23], a[88:91], a[16:19], 0\n"
    "s_add_u32 s86, s86, 1\n"
    "v_mfma_f32_16x16x32_bf16 v[20:23], a[92:95], a[40:43], v[20:23]\n"
    "s_waitcnt vmcnt(14)\n"
    "v_mfma_f32_16x16x32_bf16 v[20:23], a[96:99], a[64:67], v[20:23]\n"
    "ds_write_b32 v13, v14\n"
    "v_mfma_f32_16x16x32_bf16 v[20:23], a[100:103], v[56:59], v[20:23]\n"
    "v_cvt_pk_bf16_f32 v36, v28, v32\n"
    "v_mfma_f32_16x16x32_bf16 v[20:23], a[104:107], v[80:83], v[20:23]\n"
    "v_cvt_pk_bf16_f32 v47, v29, v33\n"
    "v_mfma_f32_16x16x32_bf16 v[20:23], a[108:111], v[104:107], v[20:23]\n"
    "v_cvt_pk_bf16_f32 v37, v30, v34\n"
    "v_mfma_f32_16x16x32_bf16 v[20:23], a[112:115], v[128:131], v[20:23]\n"
    "v_cvt_pk_bf16_f32 v51, v31, v35\n"
    "v_mfma_f32_16x16x32_bf16 v[20:23], a[116:119], v[152:155], v[20:23]\n"
    "v_cndmask_b32_dpp v45, v47, v36, vcc row_ror:8 row_mask:0xf bank_mask:0xf\n"
    "v_mfma_f32_16x16x32_bf16 v[20:23], a[120:123], v[176:179], v[20:23]\n"
    "v_cndmask_b32_dpp v49, v51, v37, vcc row_ror:8 row_mask:0xf bank_mask:0xf\n"
    "v_mfma_f32_16x16x32_bf16 v[20:23], a[124:127], v[200:203], v[20:23]\n"
    "v_mov_b32_dpp v47, v36 row_ror:8 row_mask:0xf bank_mask:0x3\n"
    "v_mfma_f32_16x16x32_bf16 v[20:23], a[128:131], v[224:227], v[20:23]\n"
    "v_mov_b32_dpp v51, v37 row_ror:8 row_mask:0xf bank_mask:0x3\n"
    "v_mfma_f32_16x16x32_bf16 v[20:23], a[132:135], v[248:251], v[20:23]\n"
    "ds_read_b32 v15, v12\n"
    "v_mfma_f32_16x16x32_bf16 v[24:27], a[132:135], v[252:255], 0\n"
    "s_add_u32 s89, s89, s94\n"
    "v_mfma_f32_16x16x32_bf16 v[24:27], a[128:131], v[228:231], v[24:27]\n"
    "s_sub_u32 s94, s94, 6\n"
    "v_mfma_f32_16x16x32_bf16 v[24:27], a[124:127], v[204:207], v[24:27]\n"
    "buffer_store_dwordx4 v[44:47], v8, s[40:43], s82 offen sc1\n"
    "v_mfma_f32_16x16x32_bf16 v[24:27], a[120:123], v[180:183], v[24:27]\n"
    "s_max_u32 s94, s94, s30\n"
    "v_mfma_f32_16x16x32_bf16 v[24:27], a[116:119], v[156:159], v[24:27]\n"
    "s_waitcnt lgkmcnt(0)\n"
    "v_cmp_lt_u32 vcc, v15, v14\n"
    "v_mfma_f32_16x16x32_bf16 v[24:27], a[112:115], v[132:135], v[24:27]\n"
    "v_mfma_f32_16x16x32_bf16 v[24:27], a[108:111], v[108:111], v[24:27]\n"
    "s_cbranch_vccnz 41b\n"
    "42:\n"
    "v_mfma_f32_16x16x32_bf16 v[24:27], a[104:107], v[84:87], v[24:27]\n"
    "ds_read_b128 a[184:187], v39 offset:4096\n"
    "v_mfma_f32_16x16x32_bf16 v[24:27], a[100:103], v[60:63], v[24:27]\n"
    "s_cmp_eq_u32 s86, s64\n"
    "s_cbranch_scc1 45b\n"
    "v_mfma_f32_16x16x32_bf16 v[24:27], a[96:99], a[68:71], v[24:27]\n"
    "ds_read_b128 a[136:139], v4 offset:12288\n"
    "v_mfma_f32_16x16x32_bf16 v[24:27], a[92:95], a[44:47], v[24:27]\n"
    "buffer_load_dwordx4 v0, s[36:39], s81 offen nt lds\n"
    "v_mfma_f32_16x16x32_bf16 v[24:27], a[88:91], a[20:23], v[24:27]\n"
    "v_add_u32 v14, 1, v14\n"
    "v_mfma_f32_16x16x32_bf16 v[28:31], a[88:91], a[0:3], 0\n"
    "s_mov_b64 vcc, s[12:13]\n"
    "v_mfma_f32_16x16x32_bf16 v[28:31], a[92:95], a[24:27], v[28:31]\n"
    "ds_read_b128 a[140:143], v5 offset:12288\n"
    "v_mfma_f32_16x16x32_bf16 v[28:31], a[96:99], a[48:51], v[28:31]\n"
    "ds_read_b128 a[144:147], v6 offset:12288\n"
    "v_mfma_f32_16x16x32_bf16 v[28:31], a[100:103], a[72:75], v[28:31]\n"
    "buffer_load_dwordx4 v1, s[36:39], s81 offen offset:1024 nt lds\n"
    "v_mfma_f32_16x16x32_bf16 v[28:31], a[104:107], v[64:67], v[28:31]\n"
    "v_cvt_pk_bf16_f32 v52, v20, v24\n"
    "v_mfma_f32_16x16x32_bf16 v[28:31], a[108:111], v[88:91], v[28:31]\n"
    "v_cvt_pk_bf16_f32 v53, v21, v25\n"
    "v_mfma_f32_16x16x32_bf16 v[28:31], a[112:115], v[112:115], v[28:31]\n"
    "v_cvt_pk_bf16_f32 v54, v22, v26\n"
    "v_mfma_f32_16x16x32_bf16 v[28:31], a[116:119], v[136:139], v[28:31]\n"
    "ds_read_b128 a[148:151], v7 offset:12288\n"
    "v_mfma_f32_16x16x32_bf16 v[28:31], a[120:123], v[160:163], v[28:31]\n"
    "v_cvt_pk_bf16_f32 v55, v23, v27\n"
    "v_mfma_f32_16x16x32_bf16 v[28:31], a[124:127], v[184:187], v[28:31]\n"
    "buffer_load_dwordx4 v2, s[36:39], s81 offen offset:2048 nt lds\n"
    "v_mfma_f32_16x16x32_bf16 v[28:31], a[128:131], v[208:211], v[28:31]\n"
    "v_readlane_b32 s81, v3, s86\n"
    "v_mfma_f32_16x16x32_bf16 v[28:31], a[132:135], v[232:235], v[28:31]\n"
    "ds_read_b128 a[152:155], v4 offset:12544\n"
    "v_mfma_f32_16x16x32_bf16 v[32:35], a[132:135], v[236:239], 0\n"
    "s_addk_i32 m0, 12288\n"
    "v_mfma_f32_16x16x32_bf16 v[32:35], a[128:131], v[212:215], v[32:35]\n"
    "ds_write2st64_b32 v38, v52, v53 offset0:0 offset1:2\n"
    "v_mfma_f32_16x16x32_bf16 v[32:35], a[124:127], v[188:191], v[32:35]\n"
    "46:\n"
    "v_mfma_f32_16x16x32_bf16 v[32:35], a[120:123], v[164:167], v[32:35]\n"
    "ds_read_b128 a[156:159], v5 offset:12544\n"
    "v_mfma_f32_16x16x32_bf16 v[32:35], a[116:119], v[140:143], v[32:35]\n"
    "s_cmp_eq_u32 m0, s77\n"
    "v_mfma_f32_16x16x32_bf16 v[32:35], a[112:115], v[116:119], v[32:35]\n"
    "s_cselect_b32 s78, s87, s88\n"
    "v_mfma_f32_16x16x32_bf16 v[32:35], a[108:111], v[92:95], v[32:35]\n"
    "buffer_store_dwordx4 v[48:51], v9, s[40:43], s82 offen sc1\n"
    "v_mfma_f32_16x16x32_bf16 v[32:35], a[104:107], v[68:71], v[32:35]\n"
    "v_mfma_f32_16x16x32_bf16 v[32:35], a[100:103], a[76:79], v[32:35]\n"
    "ds_read_b128 a[160:163], v6 offset:12544\n"
    "v_mfma_f32_16x16x32_bf16 v[32:35], a[96:99], a[52:55], v[32:35]\n"
    "ds_read_b128 a[164:167], v7 offset:12544\n"
    "v_mfma_f32_16x16x32_bf16 v[32:35], a[92:95], a[28:31], v[32:35]\n"
    "ds_read_b128 a[168:171], v4 offset:12800\n"
    "v_mfma_f32_16x16x32_bf16 v[32:35], a[88:91], a[4:7], v[32:35]\n"
    "v_mfma_f32_16x16x32_bf16 v[20:23], a[88:91], a[8:11], 0\n"
    "ds_write2st64_b32 v38, v54, v55 offset0:4 offset1:6\n"
    "v_mfma_f32_16x16x32_bf16 v[20:23], a[92:95], a[32:35], v[20:23]\n"
    "v_mfma_f32_16x16x32_bf16 v[20:23], a[96:99], a[56:59], v[20:23]\n"
    "ds_read_b128 a[172:175], v5 offset:12800\n"
    "v_mfma_f32_16x16x32_bf16 v[20:23], a[100:103], a[80:83], v[20:23]\n"
    "v_cvt_pk_bf16_f32 v36, v28, v32\n"
    "v_mfma_f32_16x16x32_bf16 v[20:23], a[104:107], v[72:75], v[20:23]\n"
    "v_cvt_pk_bf16_f32 v46, v29, v33\n"
    "v_mfma_f32_16x16x32_bf16 v[20:23], a[108:111], v[96:99], v[20:23]\n"
    "ds_read_b128 a[176:179], v6 offset:12800\n"
    "v_mfma_f32_16x16x32_bf16 v[20:23], a[112:115], v[120:123], v[20:23]\n"
    "v_cvt_pk_bf16_f32 v37, v30, v34\n"
    "v_mfma_f32_16x16x32_bf16 v[20:23], a[116:119], v[144:147], v[20:23]\n"
    "v_cvt_pk_bf16_f32 v50, v31, v35\n"
    "v_mfma_f32_16x16x32_bf16 v[20:23], a[120:123], v[168:171], v[20:23]\n"
    "v_mfma_f32_16x16x32_bf16 v[20:23], a[124:127], v[192:195], v[20:23]\n"
    "v_cndmask_b32_dpp v44, v46, v36, vcc row_ror:8 row_mask:0xf bank_mask:0xf\n"
    "v_mfma_f32_16x16x32_bf16 v[20:23], a[128:131], v[216:219], v[20:23]\n"
    "ds_read_b128 a[180:183], v7 offset:12800\n"
    "v_mfma_f32_16x16x32_bf16 v[20:23], a[132:135], v[240:243], v[20:23]\n"
    "v_cndmask_b32_dpp v48, v50, v37, vcc row_ror:8 row_mask:0xf bank_mask:0xf\n"
    "v_mfma_f32_16x16x32_bf16 v[24:27], a[132:135], v[244:247], 0\n"
    "s_memrealtime s[92:93]\n"
    "v_mfma_f32_16x16x32_bf16 v[24:27], a[128:131], v[220:223], v[24:27]\n"
    "v_mov_b32_dpp v46, v36 row_ror:8 row_mask:0xf bank_mask:0x3\n"
    "v_mfma_f32_16x16x32_bf16 v[24:27], a[124:127], v[196:199], v[24:27]\n"
    "v_mov_b32_dpp v50, v37 row_ror:8 row_mask:0xf bank_mask:0x3\n"
    "v_mfma_f32_16x16x32_bf16 v[24:27], a[120:123], v[172:175], v[24:27]\n"
    "s_waitcnt lgkmcnt(14)\n"
    "buffer_store_dwordx4 a[184:187], v10, s[40:43], s82 offen sc1\n"
    "v_mfma_f32_16x16x32_bf16 v[24:27], a[116:119], v[148:151], v[24:27]\n"
    "v_mfma_f32_16x16x32_bf16 v[24:27], a[112:115], v[124:127], v[24:27]\n"
    "v_readlane_b32 s82, v18, s86\n"
    "v_mfma_f32_16x16x32_bf16 v[24:27], a[108:111], v[100:103], v[24:27]\n"
    "v_mfma_f32_16x16x32_bf16 v[24:27], a[104:107], v[76:79], v[24:27]\n"
    "s_waitcnt lgkmcnt(0)\n"
    "s_sub_u32 s85, s92, s89\n"
    "v_mfma_f32_16x16x32_bf16 v[24:27], a[100:103], a[84:87], v[24:27]\n"
    "s_cbranch_scc1 43b\n"
    "44:\n"
    "v_mfma_f32_16x16x32_bf16 v[24:27], a[96:99], a[60:63], v[24:27]\n"
    "v_mfma_f32_16x16x32_bf16 v[24:27], a[92:95], a[36:39], v[24:27]\n"
    "s_cmp_lt_u32 s86, s76\n"
    "v_mfma_f32_16x16x32_bf16 v[24:27], a[88:91], a[12:15], v[24:27]\n"
    "s_cbranch_scc0 55f\n"
    "52:\n"
    "v_mfma_f32_16x16x32_bf16 v[28:31], a[136:139], a[16:19], 0\n"
    "s_add_u32 s86, s86, 1\n"
    "v_mfma_f32_16x16x32_bf16 v[28:31], a[140:143], a[40:43], v[28:31]\n"
    "s_waitcnt vmcnt(14)\n"
    "v_mfma_f32_16x16x32_bf16 v[28:31], a[144:147], a[64:67], v[28:31]\n"
    "ds_write_b32 v13, v14\n"
    "v_mfma_f32_16x16x32_bf16 v[28:31], a[148:151], v[56:59], v[28:31]\n"
    "v_cvt_pk_bf16_f32 v36, v20, v24\n"
    "v_mfma_f32_16x16x32_bf16 v[28:31], a[152:155], v[80:83], v[28:31]\n"
    "v_cvt_pk_bf16_f32 v47, v21, v25\n"
    "v_mfma_f32_16x16x32_bf16 v[28:31], a[156:159], v[104:107], v[28:31]\n"
    "v_cvt_pk_bf16_f32 v37, v22, v26\n"
    "v_mfma_f32_16x16x32_bf16 v[28:31], a[160:163], v[128:131], v[28:31]\n"
    "v_cvt_pk_bf16_f32 v51, v23, v27\n"
    "v_mfma_f32_16x16x32_bf16 v[28:31], a[164:167], v[152:155], v[28:31]\n"
    "v_cndmask_b32_dpp v45, v47, v36, vcc row_ror:8 row_mask:0xf bank_mask:0xf\n"
    "v_mfma_f32_16x16x32_bf16 v[28:31], a[168:171], v[176:179], v[28:31]\n"
    "v_cndmask_b32_dpp v49, v51, v37, vcc row_ror:8 row_mask:0xf bank_mask:0xf\n"
    "v_mfma_f32_16x16x32_bf16 v[28:31], a[172:175], v[200:203], v[28:31]\n"
    "v_mov_b32_dpp v47, v36 row_ror:8 row_mask:0xf bank_mask:0x3\n"
    "v_mfma_f32_16x16x32_bf16 v[28:31], a[176:179], v[224:227], v[28:31]\n"
    "v_mov_b32_dpp v51, v37 row_ror:8 row_mask:0xf bank_mask:0x3\n"
    "v_mfma_f32_16x16x32_bf16 v[28:31], a[180:183], v[248:251], v[28:31]\n"
    "ds_read_b32 v15, v12\n"
    "v_mfma_f32_16x16x32_bf16 v[32:35], a[180:183], v[252:255], 0\n"
    "s_add_u32 s89, s89, s94\n"
    "v_mfma_f32_16x16x32_bf16 v[32:35], a[176:179], v[228:231], v[32:35]\n"
    "v_add_u32 v4, s78, v4\n"
    "v_mfma_f32_16x16x32_bf16 v[32:35], a[172:175], v[204:207], v[32:35]\n"
    "buffer_store_dwordx4 v[44:47], v8, s[40:43], s82 offen sc1\n"
    "v_mfma_f32_16x16x32_bf16 v[32:35], a[168:171], v[180:183], v[32:35]\n"
    "v_add_u32 v5, s78, v5\n"
    "v_mfma_f32_16x16x32_bf16 v[32:35], a[164:167], v[156:159], v[32:35]\n"
    "s_waitcnt lgkmcnt(0)\n"
    "v_cmp_lt_u32 vcc, v15, v14\n"
    "v_mfma_f32_16x16x32_bf16 v[32:35], a[160:163], v[132:135], v[32:35]\n"
    "v_add_u32 v6, s78, v6\n"
    "v_mfma_f32_16x16x32_bf16 v[32:35], a[156:159], v[108:111], v[32:35]\n"
    "s_cbranch_vccnz 61b\n"
    "62:\n"
    "v_mfma_f32_16x16x32_bf16 v[32:35], a[152:155], v[84:87], v[32:35]\n"
    "ds_read_b128 a[184:187], v39\n"
    "v_mfma_f32_16x16x32_bf16 v[32:35], a[148:151], v[60:63], v[32:35]\n"
    "s_cmp_eq_u32 s86, s65\n"
    "s_cbranch_scc1 65b\n"
    "v_mfma_f32_16x16x32_bf16 v[32:35], a[144:147], a[68:71], v[32:35]\n"
    "ds_read_b128 a[88:91], v4\n"
    "v_mfma_f32_16x16x32_bf16 v[32:35], a[140:143], a[44:47], v[32:35]\n"
    "buffer_load_dwordx4 v0, s[36:39], s81 offen nt lds\n"
    "v_mfma_f32_16x16x32_bf16 v[32:35], a[136:139], a[20:23], v[32:35]\n"
    "v_add_u32 v7, s78, v7\n"
    "v_mfma_f32_16x16x32_bf16 v[20:23], a[136:139], a[0:3], 0\n"
    "v_add_u32 v14, 1, v14\n"
    "v_mfma_f32_16x16x32_bf16 v[20:23], a[140:143], a[24:27], v[20:23]\n"
    "ds_read_b128 a[92:95], v5\n"
    "v_mfma_f32_16x16x32_bf16 v[20:23], a[144:147], a[48:51], v[20:23]\n"
    "ds_read_b128 a[96:99], v6\n"
    "v_mfma_f32_16x16x32_bf16 v[20:23], a[148:151], a[72:75], v[20:23]\n"
    "buffer_load_dwordx4 v1, s[36:39], s81 offen offset:1024 nt lds\n"
    "v_mfma_f32_16x16x32_bf16 v[20:23], a[152:155], v[64:67], v[20:23]\n"
    "v_cvt_pk_bf16_f32 v52, v28, v32\n"
    "v_mfma_f32_16x16x32_bf16 v[20:23], a[156:159], v[88:91], v[20:23]\n"
    "v_cvt_pk_bf16_f32 v53, v29, v33\n"
    "v_mfma_f32_16x16x32_bf16 v[20:23], a[160:163], v[112:115], v[20:23]\n"
    "v_cvt_pk_bf16_f32 v54, v30, v34\n"
    "v_mfma_f32_16x16x32_bf16 v[20:23], a[164:167], v[136:139], v[20:23]\n"
    "ds_read_b128 a[100:103], v7\n"
    "v_mfma_f32_16x16x32_bf16 v[20:23], a[168:171], v[160:163], v[20:23]\n"
    "v_cvt_pk_bf16_f32 v55, v31, v35\n"
    "v_mfma_f32_16x16x32_bf16 v[20:23], a[172:175], v[184:187], v[20:23]\n"
    "buffer_load_dwordx4 v2, s[36:39], s81 offen offset:2048 nt lds\n"
    "v_mfma_f32_16x16x32_bf16 v[20:23], a[176:179], v[208:211], v[20:23]\n"
    "s_mov_b64 vcc, s[12:13]\n"
    "v_mfma_f32_16x16x32_bf16 v[20:23], a[180:183], v[232:235], v[20:23]\n"
    "ds_read_b128 a[104:107], v4 offset:256\n"
    "v_mfma_f32_16x16x32_bf16 v[24:27], a[180:183], v[236:239], 0\n"
    "v_readlane_b32 s81, v3, s86\n"
    "v_mfma_f32_16x16x32_bf16 v[24:27], a[176:179], v[212:215], v[24:27]\n"
    "ds_write2st64_b32 v38, v52, v53 offset0:16 offset1:18\n"
    "v_mfma_f32_16x16x32_bf16 v[24:27], a[172:175], v[188:191], v[24:27]\n"
    "s_addk_i32 m0, 12288\n"
    "66:\n"
    "v_mfma_f32_16x16x32_bf16 v[24:27], a[168:171], v[164:167], v[24:27]\n"
    "ds_read_b128 a[108:111], v5 offset:256\n"
    "v_mfma_f32_16x16x32_bf16 v[24:27], a[164:167], v[140:143], v[24:27]\n"
    "s_cmp_eq_u32 m0, s80\n"
    "v_mfma_f32_16x16x32_bf16 v[24:27], a[160:163], v[116:119], v[24:27]\n"
    "s_cselect_b32 m0, s57, m0\n"
    "v_mfma_f32_16x16x32_bf16 v[24:27], a[156:159], v[92:95], v[24:27]\n"
    "buffer_store_dwordx4 v[48:51], v9, s[40:43], s82 offen sc1\n"
    "v_mfma_f32_16x16x32_bf16 v[24:27], a[152:155], v[68:71], v[24:27]\n"
    "s_cmp_eq_u32 s86, 20\n"
    "s_cselect_b32 s30, s31, s30\n"
    "v_mfma_f32_16x16x32_bf16 v[24:27], a[148:151], a[76:79], v[24:27]\n"
    "ds_read_b128 a[112:115], v6 offset:256\n"
    "v_mfma_f32_16x16x32_bf16 v[24:27], a[144:147], a[52:55], v[24:27]\n"
    "ds_read_b128 a[116:119], v7 offset:256\n"
    "v_mfma_f32_16x16x32_bf16 v[24:27], a[140:143], a[28:31], v[24:27]\n"
    "ds_read_b128 a[120:123], v4 offset:512\n"
    "v_mfma_f32_16x16x32_bf16 v[24:27], a[136:139], a[4:7], v[24:27]\n"
    "v_mfma_f32_16x16x32_bf16 v[28:31], a[136:139], a[8:11], 0\n"
    "ds_write2st64_b32 v38, v54, v55 offset0:20 offset1:22\n"
    "v_mfma_f32_16x16x32_bf16 v[28:31], a[140:143], a[32:35], v[28:31]\n"
    "v_mfma_f32_16x16x32_bf16 v[28:31], a[144:147], a[56:59], v[28:31]\n"
    "ds_read_b128 a[124:127], v5 offset:512\n"
    "v_mfma_f32_16x16x32_bf16 v[28:31], a[148:151], a[80:83], v[28:31]\n"
    "v_cvt_pk_bf16_f32 v36, v20, v24\n"
    "v_mfma_f32_16x16x32_bf16 v[28:31], a[152:155], v[72:75], v[28:31]\n"
    "v_cvt_pk_bf16_f32 v46, v21, v25\n"
    "v_mfma_f32_16x16x32_bf16 v[28:31], a[156:159], v[96:99], v[28:31]\n"
    "ds_read_b128 a[128:131], v6 offset:512\n"
    "v_mfma_f32_16x16x32_bf16 v[28:31], a[160:163], v[120:123], v[28:31]\n"
    "v_cvt_pk_bf16_f32 v37, v22, v26\n"
    "v_mfma_f32_16x16x32_bf16 v[28:31], a[164:167], v[144:147], v[28:31]\n"
    "v_cvt_pk_bf16_f32 v50, v23, v27\n"
    "v_mfma_f32_16x16x32_bf16 v[28:31], a[168:171], v[168:171], v[28:31]\n"
    "v_mfma_f32_16x16x32_bf16 v[28:31], a[172:175], v[192:195], v[28:31]\n"
    "v_cndmask_b32_dpp v44, v46, v36, vcc row_ror:8 row_mask:0xf bank_mask:0xf\n"
    "v_mfma_f32_16x16x32_bf16 v[28:31], a[176:179], v[216:219], v[28:31]\n"
    "ds_read_b128 a[132:135], v7 offset:512\n"
    "v_mfma_f32_16x16x32_bf16 v[28:31], a[180:183], v[240:243], v[28:31]\n"
    "v_cndmask_b32_dpp v48, v50, v37, vcc row_ror:8 row_mask:0xf bank_mask:0xf\n"
    "v_mfma_f32_16x16x32_bf16 v[32:35], a[180:183], v[244:247], 0\n"
    "s_memrealtime s[92:93]\n"
    "v_mfma_f32_16x16x32_bf16 v[32:35], a[176:179], v[220:223], v[32:35]\n"
    "v_mov_b32_dpp v46, v36 row_ror:8 row_mask:0xf bank_mask:0x3\n"
    "v_mfma_f32_16x16x32_bf16 v[32:35], a[172:175], v[196:199], v[32:35]\n"
    "v_mov_b32_dpp v50, v37 row_ror:8 row_mask:0xf bank_mask:0x3\n"
    "v_mfma_f32_16x16x32_bf16 v[32:35], a[168:171], v[172:175], v[32:35]\n"
    "s_waitcnt lgkmcnt(14)\n"
    "buffer_store_dwordx4 a[184:187], v10, s[40:43], s82 offen sc1\n"
    "v_mfma_f32_16x16x32_bf16 v[32:35], a[164:167], v[148:151], v[32:35]\n"
    "v_mfma_f32_16x16x32_bf16 v[32:35], a[160:163], v[124:127], v[32:35]\n"
    "v_readlane_b32 s82, v18, s86\n"
    "v_mfma_f32_16x16x32_bf16 v[32:35], a[156:159], v[100:103], v[32:35]\n"
    "v_mfma_f32_16x16x32_bf16 v[32:35], a[152:155], v[76:79], v[32:35]\n"
    "s_waitcnt lgkmcnt(0)\n"
    "s_sub_u32 s85, s92, s89\n"
    "v_mfma_f32_16x16x32_bf16 v[32:35], a[148:151], a[84:87], v[32:35]\n"
    "s_cbranch_scc1 63b\n"
    "64:\n"
    "v_mfma_f32_16x16x32_bf16 v[32:35], a[144:147], a[60:63], v[32:35]\n"
    "v_mfma_f32_16x16x32_bf16 v[32:35], a[140:143], a[36:39], v[32:35]\n"
    "s_cmp_lt_u32 s86, s76\n"
    "v_mfma_f32_16x16x32_bf16 v[32:35], a[136:139], a[12:15], v[32:35]\n"
    "s_cbranch_scc1 51b\n"
    "s_barrier\n"
    "ds_read_b128 a[184:187], v39 offset:4096\n"
    "s_nop 4\n"
    "v_cvt_pk_bf16_f32 v36, v28, v32\n"
    "v_cvt_pk_bf16_f32 v47, v29, v33\n"
    "v_cvt_pk_bf16_f32 v37, v30, v34\n"
    "v_cvt_pk_bf16_f32 v51, v31, v35\n"
    "s_branch 56f\n"
    "55:\n"
    "s_barrier\n"
    "ds_read_b128 a[184:187], v39\n"
    "s_nop 4\n"
    "v_cvt_pk_bf16_f32 v36, v20, v24\n"
    "v_cvt_pk_bf16_f32 v47, v21, v25\n"
    "v_cvt_pk_bf16_f32 v37, v22, v26\n"
    "v_cvt_pk_bf16_f32 v51, v23, v27\n"
    "56:\n"
    "v_cndmask_b32_dpp v45, v47, v36, vcc row_ror:8 row_mask:0xf bank_mask:0xf\n"
    "v_mov_b32_dpp v47, v36 row_ror:8 row_mask:0xf bank_mask:0x3\n"
    "v_cndmask_b32_dpp v49, v51, v37, vcc row_ror:8 row_mask:0xf bank_mask:0xf\n"
    "buffer_store_dwordx4 v[44:47], v8, s[40:43], s82 offen sc1\n"
    "v_mov_b32_dpp v51, v37 row_ror:8 row_mask:0xf bank_mask:0x3\n"
    "s_waitcnt lgkmcnt(0)\n"
    "buffer_store_dwordx4 a[184:187], v10, s[40:43], s82 offen sc1\n"
    "buffer_store_dwordx4 v[48:51], v9, s[40:43], s82 offen sc1\n"
    "s_waitcnt vmcnt(0) lgkmcnt(0)\n"
    "s_endpgm\n"
    "75:\n"
    "s_sleep 2\n"
    "global_load_dwordx4 v[236:239], v16, s[54:55] sc1\n"
    "s_waitcnt vmcnt(0)\n"
    "v_sub_u32 v11, s0, v236\n"
    "v_or_b32 v236, 0xffff, v236\n"
    "v_or_b32 v238, 0xffff, v238\n"
    "v_and_b32 v242, 15, v237\n"
    "v_and_b32 v243, 1, v239\n"
    "v_or_b32 v237, 15, v237\n"
    "v_or_b32 v239, 15, v239\n"
    "v_cmp_ne_u64 vcc, s[18:19], v[236:237]\n"
    "v_cmp_ne_u64 s[48:49], s[18:19], v[238:239]\n"
    "v_readlane_b32 s9, v242, 0\n"
    "v_and_b32 v236, 1, v242\n"
    "s_or_b64 s[48:49], s[48:49], vcc\n"
    "s_cbranch_scc1 75b\n"
    "s_branch 77b\n"
    "79:\n"
    "s_nop 0\n"
    : "+{s[0:1]}"(dp), "+{s[2:3]}"(ka), "+{s[4:5]}"(did), "+{s6}"(bid), "+{v0}"(tid)
    : [lds] "{s98}"(lds0)
    : "v1","v2","v3","v4","v5","v6","v7","v8","v9","v10","v11","v12","v13","v14","v15","v16","v17","v18","v19","v20","v21","v22","v23","v24","v25","v26","v27","v28","v29","v30","v31","v32","v33","v34","v35","v36","v37","v38","v39","v40","v41","v42","v43","v44","v45","v46","v47","v48","v49","v50","v51","v52","v53","v54","v55","v56","v57","v58","v59","v60","v61","v62","v63","v64","v65","v66","v67","v68","v69","v70","v71","v72","v73","v74","v75","v76","v77","v78","v79","v80","v81","v82","v83","v84","v85","v86","v87","v88","v89","v90","v91","v92","v93","v94","v95","v96","v97","v98","v99","v100","v101","v102","v103","v104","v105","v106","v107","v108","v109","v110","v111","v112","v113","v114","v115","v116","v117","v118","v119","v120","v121","v122","v123","v124","v125","v126","v127","v128","v129","v130","v131","v132","v133","v134","v135","v136","v137","v138","v139","v140","v141","v142","v143","v144","v145","v146","v147","v148","v149","v150","v151","v152","v153","v154","v155","v156","v157","v158","v159","v160","v161","v162","v163","v164","v165","v166","v167","v168","v169","v170","v171","v172","v173","v174","v175","v176","v177","v178","v179","v180","v181","v182","v183","v184","v185","v186","v187","v188","v189","v190","v191","v192","v193","v194","v195","v196","v197","v198","v199","v200","v201","v202","v203","v204","v205","v206","v207","v208","v209","v210","v211","v212","v213","v214","v215","v216","v217","v218","v219","v220","v221","v222","v223","v224","v225","v226","v227","v228","v229","v230","v231","v232","v233","v234","v235","v236","v237","v238","v239","v240","v241","v242","v243","v244","v245","v246","v247","v248","v249","v250","v251","v252","v253","v254","v255","a0","a1","a2","a3","a4","a5","a6","a7","a8","a9","a10","a11","a12","a13","a14","a15","a16","a17","a18","a19","a20","a21","a22","a23","a24","a25","a26","a27","a28","a29","a30","a31","a32","a33","a34","a35","a36","a37","a38","a39","a40","a41","a42","a43","a44","a45","a46","a47","a48","a49","a50","a51","a52","a53","a54","a55","a56","a57","a58","a59","a60","a61","a62","a63","a64","a65","a66","a67","a68","a69","a70","a71","a72","a73","a74","a75","a76","a77","a78","a79","a80","a81","a82","a83","a84","a85","a86","a87","a88","a89","a90","a91","a92","a93","a94","a95","a96","a97","a98","a99","a100","a101","a102","a103","a104","a105","a106","a107","a108","a109","a110","a111","a112","a113","a114","a115","a116","a117","a118","a119","a120","a121","a122","a123","a124","a125","a126","a127","a128","a129","a130","a131","a132","a133","a134","a135","a136","a137","a138","a139","a140","a141","a142","a143","a144","a145","a146","a147","a148","a149","a150","a151","a152","a153","a154","a155","a156","a157","a158","a159","a160","a161","a162","a163","a164","a165","a166","a167","a168","a169","a170","a171","a172","a173","a174","a175","a176","a177","a178","a179","a180","a181","a182","a183","a184","a185","a186","a187","a188","a189","a190","a191","a192","a193","a194","a195","a196","a197","a198","a199","a200","a201","a202","a203","a204","a205","a206","a207","a208","a209","a210","a211","a212","s7","s8","s9","s10","s11","s12","s13","s14","s15","s16","s17","s18","s19","s20","s21","s22","s23","s24","s25","s26","s27","s28","s29","s30","s31","s33","s34","s35","s36","s37","s38","s39","s40","s41","s42","s43","s44","s45","s46","s47","s48","s49","s50","s51","s52","s53","s54","s55","s56","s57","s58","s59","s60","s61","s62","s63","s64","s65","s66","s67","s68","s69","s70","s71","s72","s73","s74","s75","s76","s77","s78","s79","s80","s81","s82","s83","s84","s85","s86","s87","s88","s89","s90","s91","s92","s93","s94","s95","s96","s97","vcc","scc","memory");
}

extern "C" void kernel_launch(void* const* d_in, const int* in_sizes, int n_in,
                              void* d_out, int out_size, void* d_ws, size_t ws_size,
                              hipStream_t stream) {
  Shapes sh;
  for (int j = 0; j < 16; ++j)
    for (int w = 0; w < 4; ++w) {
      const int ty = j >= 11, per = ty ? 10 : 11, n = 16 * (j - 11 * ty);
      auto row = [&](int k) { return ty ? 64 * (k / 10) + 11 + k % 10 : 64 * (k / 22) + 21 * (k / 11 % 2) + k % 11; };
      const int n0 = n + 4 * w, bp = per - n0 % per;
      uint32_t* e = sh.v + 16 * j + 4 * w;
      e[0] = row(n0) * 768 - 512;
      e[1] = (row(n0 + bp) - bp) * 768 - 512;
      e[2] = bp * 768;
      e[3] = (row(n + 8) + 16) * 768 + ty;
      for (int i = 0; i < 4; ++i) sh.rows[16 * j + 4 * w + i] = row(n0 + i) * 768;
    }
  const int grid = 256, maxr = 40;
  const size_t M = (size_t)in_sizes[0] / CH;
  const uint32_t fl = 0x00020000u;
  for (size_t r0 = 0; r0 < M; r0 += (size_t)maxr * grid * TM) {
    const size_t rows = M - r0 < (size_t)maxr * grid * TM ? M - r0 : (size_t)maxr * grid * TM;
    const int nr = (int)((rows + grid * TM - 1) / (grid * TM)), nit = nr < 2 ? 2 : nr;
    const uint32_t nbytes = (uint32_t)(rows * CH * 2);
    const uint32_t np = (nbytes >> 21) & ~1u;
    const uint64_t pages = (np >= 24 ? (np - 14) & ~7u : 64u) | (uint64_t)(np >= 2 ? (np - 2) << 21 : 0u) << 32;
    dwsep_kernel<<<grid, 256, 0, stream>>>((const char*)d_in[0] + r0 * CH * 2, nbytes, fl, (char*)d_out + r0 * CH * 2, nbytes, fl,
                                           (const char*)d_in[2], CH * CH * 2, fl, (const char*)d_in[1], CH * 2, fl,
                                           (uint32_t)nit, (uint32_t)grid * TILE_BYTES, d_ws, pages, sh);
  }
}
```

```cpp
#include <hip/hip_runtime.h>
#include <stdint.h>

#define CH 384
#define TM 16
#define TILE_BYTES (TM * CH * 2)
#define LDS_BYTES (13 * TILE_BYTES + 1024 + 64)

struct Shapes { uint32_t v[256], rows[256]; };
extern "C" __device__ uint64_t dispatch_id_() __asm("llvm.amdgcn.dispatch.id");

__global__ __launch_bounds__(256, 1)
void dwsep_kernel(const char* x, uint32_t xbytes, uint32_t f0, char* out, uint32_t obytes, uint32_t f1,
                  const char* pw, uint32_t pwbytes, uint32_t f2, const char* dw, uint32_t dwbytes, uint32_t f3,
                  uint32_t nit, uint32_t tstride, void* ws, uint64_t pages, Shapes shapes) {
  __shared__ __attribute__((aligned(1024))) char lds[LDS_BYTES];
  uint32_t lds0 = (uint32_t)(uintptr_t)(__attribute__((address_space(3))) char*)lds;
  uint64_t ka = (uint64_t)(uintptr_t)__builtin_amdgcn_kernarg_segment_ptr();
  uint64_t did = dispatch_id_(), dp = (uint64_t)(uintptr_t)__builtin_amdgcn_dispatch_ptr();
  uint32_t bid = blockIdx.x, tid = threadIdx.x;
  asm volatile(
    "s_load_dwordx16 s[36:51], s[2:3], 0x0\n"
    "v_readfirstlane_b32 s8, v0\n"
    "s_load_dwordx4 s[52:55], s[2:3], 0x40\n"
    "s_bfe_u32 s7, s6, 0x40003\n"
    "s_lshr_b32 s9, s8, 2\n"
    "s_lshl_b32 s7, s7, 6\n"
    "s_add_u32 s9, s7, s9\n"
    "s_load_dwordx4 s[20:23], s[2:3], s9 offset:0x58\n"
    "s_load_dwordx2 s[96:97], s[2:3], 0x50\n"
    "s_getpc_b64 s[14:15]\n"
    "70:\n"
    "v_lshlrev_b32 v48, 6, v0\n"
    "s_mov_b32 s13, 79f-70b\n"
    "v_min_u32 v48, s13, v48\n"
    "global_load_dword v49, v48, s[14:15]\n"
    "v_and_b32 v18, 48, v0\n"
    "v_bfe_u32 v55, v0, 3, 1\n"
    "v_bfe_u32 v54, v0, 6, 1\n"
    "v_lshl_add_u32 v53, v55, 2, v18\n"
    "s_mov_b32 s16, s4\n"
    "v_add_u32 v53, s7, v53\n"
    "v_lshl_add_u32 v54, v54, 3, v53\n"
    "global_load_dword v8, v53, s[2:3] offset:1112\n"
    "global_load_dword v9, v53, s[2:3] offset:1120\n"
    "global_load_dword v10, v54, s[2:3] offset:1112\n"
    "s_mov_b32 s2, s6\n"
    "s_mov_b32 s3, %[lds]\n"
    "s_lshr_b32 s4, s8, 6\n"
    "v_and_b32 v53, 24, v0\n"
    "v_bfe_u32 v54, v0, 5, 1\n"
    "s_bfe_u32 s5, s2, 0x20003\n"
    "v_add_u32 v53, v53, v54\n"
    "s_bfe_u32 s6, s2, 0x20005\n"
    "v_mul_u32_u24 v53, 0x300, v53\n"
    "s_cmp_eq_u32 s6, 3\n"
    "v_and_b32 v55, 7, v0\n"
    "s_cselect_b32 s6, 0, s6\n"
    "v_lshl_add_u32 v53, v55, 4, v53\n"
    "s_lshl_b32 s7, s4, 7\n"
    "v_and_b32 v16, 63, v0\n"
    "s_mul_i32 s58, s5, 24576\n"
    "v_add_u32 v244, s7, v53\n"
    "s_mul_i32 s62, s6, 98304\n"
    "v_lshlrev_b32 v16, 4, v16\n"
    "s_lshl_b32 s7, s5, 6\n"
    "s_lshl_b32 s8, s6, 8\n"
    "v_xor_b32 v53, s7, v16\n"
    "s_add_u32 s12, s3, 159744\n"
    "s_mul_i32 s83, s4, 30720\n"
    "v_add_u32 v53, s8, v53\n"
    "s_mov_b32 m0, s12\n"
    "v_subrev_u32 v55, 0x300, v53\n"
    "s_add_u32 s83, s83, s3\n"
    "v_min_u32 v250, v53, v55\n"
    "v_add_u32 v245, 0x200, v244\n"
    "v_add_u32 v246, 0x400, v244\n"
    "v_add_u32 v247, 0x600, v244\n"
    "v_and_b32 v235, 12, v0\n"
    "s_mul_i32 s57, s4, 3072\n"
    "v_bfe_u32 v52, v0, 4, 2\n"
    "s_lshl_b32 s7, s4, 6\n"
    "v_lshl_add_u32 v53, v235, 1, v52\n"
    "v_mul_u32_u24 v53, 0x300, v53\n"
    "s_add_u32 s57, s57, s3\n"
    "v_and_b32 v54, 3, v0\n"
    "s_add_u32 s7, s7, 512\n"
    "v_lshl_add_u32 v53, v54, 4, v53\n"
    "v_lshlrev_b32 v12, 2, v54\n"
    "v_add_u32 v248, s7, v53\n"
    "v_add_u32 v249, 0x800, v248\n"
    "s_lshr_b64 s[18:19], s[0:1], 12\n"
    "s_lshl4_add_u32 s18, s16, s18\n"
    "s_andn2_b32 s17, s18, 15\n"
    "s_bitset1_b32 s17, 31\n"
    "s_mov_b32 s72, s17\n"
    "s_pack_ll_b32_b16 s18, -1, s16\n"
    "s_or_b32 s19, s17, 15\n"
    "s_lshl_b32 s34, s2, 20\n"
    "s_and_b32 s34, s34, 0x7e00000\n"
    "s_lshl_b32 s7, s16, 8\n"
    "s_add_u32 s7, s7, s2\n"
    "s_mul_i32 s7, s7, 0x9e3779b1\n"
    "s_lshr_b32 s7, s7, 9\n"
    "s_and_b32 s7, s7, 2088832\n"
    "s_or_b32 s34, s34, s7\n"
    "s_and_b32 s35, s2, 7\n"
    "s_lshl_b32 s7, s4, 1\n"
    "s_sub_u32 s35, s35, s7\n"
    "v_and_b32 v19, 15, v0\n"
    "v_add_u32 v18, s12, v18\n"
    "v_and_b32 v54, 14, v0\n"
    "v_lshrrev_b32 v53, 1, v235\n"
    "v_add_u32 v235, v18, v54\n"
    "v_add_u32 v18, v18, v53\n"
    "s_mov_b32 s9, 0x51403020\n"
    "s_mov_b32 s10, 0x52423121\n"
    "s_mov_b32 s11, 0x53433322\n"
    "s_cmp_eq_u32 s6, 1\n"
    "s_cselect_b32 s9, 0x61204030, s9\n"
    "s_cselect_b32 s10, 0x62521131, s10\n"
    "s_cselect_b32 s11, 0x33534302, s11\n"
    "s_cmp_eq_u32 s6, 2\n"
    "s_cselect_b32 s9, 0x71302040, s9\n"
    "s_cselect_b32 s10, 0x42622111, s10\n"
    "s_cselect_b32 s11, 0x43335312, s11\n"
    "s_xor_b32 s7, s4, s5\n"
    "v_lshlrev_b32 v54, 3, v52\n"
    "s_lshl_b32 s7, s7, 2\n"
    "v_xor_b32 v55, s7, v19\n"
    "v_bfe_u32 v53, s11, v54, 8\n"
    "v_xor_b32 v2, v53, v55\n"
    "v_bfe_u32 v53, s10, v54, 8\n"
    "v_xor_b32 v1, v53, v55\n"
    "v_bfe_u32 v53, s9, v54, 8\n"
    "v_xor_b32 v0, v53, v55\n"
    "s_and_b32 s28, s2, 3\n"
    "s_waitcnt lgkmcnt(0)\n"
    "s_memrealtime s[0:1]\n"
    "s_barrier\n"
    "buffer_load_dwordx4 v250, s[48:51], 0 offen lds\n"
    "s_cmp_eq_u32 s35, 0\n"
    "s_cbranch_scc0 72f\n"
    "s_bitcmp1_b32 s2, 7\n"
    "s_cselect_b64 s[68:69], s[40:41], s[36:37]\n"
    "s_mov_b64 s[70:71], s[38:39]\n"
    "s_and_b32 s85, s34, 0x3fffff\n"
    "s_add_u32 s85, s85, s97\n"
    "s_min_u32 s34, s34, s85\n"
    "s_buffer_load_dword s73, s[68:71], s34\n"
    "s_buffer_load_dword s72, s[68:71], s34 offset:0x2000\n"
    "72:\n"
    "s_add_u32 m0, s83, 36864\n"
    "s_add_u32 s84, s58, s62\n"
    "s_lshr_b32 s8, s2, 7\n"
    "s_lshl2_add_u32 s7, s8, s28\n"
    "s_mul_i32 s56, s7, 393216\n"
    "buffer_load_dwordx4 v244, s[44:47], s84 offen lds\n"
    "s_lshr_b32 s29, s2, 2\n"
    "s_mov_b32 s30, s57\n"
    "s_xor_b32 s7, s5, 1\n"
    "buffer_load_dwordx4 v245, s[44:47], s84 offen offset:1024 lds\n"
    "s_mul_i32 s59, s7, 24576\n"
    "s_xor_b32 s7, s5, 2\n"
    "s_mul_i32 s60, s7, 24576\n"
    "buffer_load_dwordx4 v246, s[44:47], s84 offen offset:2048 lds\n"
    "s_xor_b32 s7, s5, 3\n"
    "s_mul_i32 s61, s7, 24576\n"
    "v_mov_b32 v53, s20\n"
    "buffer_load_dwordx4 v247, s[44:47], s84 offen offset:3072 lds\n"
    "s_add_u32 m0, s83, 40960\n"
    "v_mov_b32 v54, s21\n"
    "v_cmp_le_i32 vcc, s22, v16\n"
    "s_sub_u32 s22, s22, 0x400\n"
    "s_mov_b32 s33, 0x90000000\n"
    "v_cndmask_b32 v55, v53, v54, vcc\n"
    "v_lshl_add_u32 v0, v0, 4, v55\n"
    "buffer_load_dwordx4 v248, s[44:47], s84 offen lds\n"
    "v_cmp_le_i32 vcc, s22, v16\n"
    "s_sub_u32 s22, s22, 0x400\n"
    "s_nop 0\n"
    "v_cndmask_b32 v55, v53, v54, vcc\n"
    "v_lshl_add_u32 v1, v1, 4, v55\n"
    "v_cmp_le_i32 vcc, s22, v16\n"
    "s_sub_u32 s22, s22, 0x400\n"
    "s_nop 0\n"
    "v_cndmask_b32 v55, v53, v54, vcc\n"
    "buffer_load_dwordx4 v249, s[44:47], s84 offen offset:1024 lds\n"
    "s_add_u32 m0, s83, 43008\n"
    "s_add_u32 s84, s59, s62\n"
    "v_lshl_add_u32 v2, v2, 4, v55\n"
    "s_add_u32 s7, s83, 36864\n"
    "v_and_b32 v240, 0x70, v16\n"
    "buffer_load_dwordx4 v244, s[44:47], s84 offen lds\n"
    "v_and_b32 v53, 8, v19\n"
    "v_lshl_add_u32 v53, v52, 7, v53\n"
    "v_lshrrev_b32 v54, 2, v16\n"
    "buffer_load_dwordx4 v245, s[44:47], s84 offen offset:1024 lds\n"
    "s_cmp_eq_u32 s35, 0\n"
    "s_cbranch_scc0 73f\n"
    "s_getreg_b32 s88, hwreg(HW_REG_XCC_ID, 0, 3)\n"
    "s_lshl_b32 s89, s2, 3\n"
    "s_and_b32 s89, s89, 0x3f0\n"
    "s_lshr_b32 s82, s2, 7\n"
    "s_lshl3_add_u32 s89, s82, s89\n"
    "v_mov_b32 v20, s89\n"
    "s_lshr_b32 s89, s88, 2\n"
    "s_lshl1_add_u32 s88, s88, s89\n"
    "s_or_b32 s88, s88, s17\n"
    "s_bfe_u32 s89, s2, 0x10001\n"
    "s_xor_b32 s88, s88, s89\n"
    "s_waitcnt lgkmcnt(1)\n"
    "s_nop 3\n"
    "s_cmp_lg_u32 s72, s17\n"
    "s_cselect_b32 s85, 1, 0\n"
    "s_xor_b32 s88, s88, s85\n"
    "v_mov_b32 v23, s88\n"
    "73:\n"
    "v_add3_u32 v15, s7, v53, v240\n"
    "v_add_u32 v38, s7, v54\n"
    "s_lshr_b32 s8, s4, 1\n"
    "buffer_load_dwordx4 v246, s[44:47], s84 offen offset:2048 lds\n"
    "s_lshl_b32 s8, s8, 11\n"
    "s_add_u32 s8, s8, s3\n"
    "s_add_u32 s8, s8, 147456\n"
    "buffer_load_dwordx4 v247, s[44:47], s84 offen offset:3072 lds\n"
    "s_and_b32 s13, s4, 1\n"
    "s_lshl_b32 s14, s13, 8\n"
    "s_sub_u32 s7, s14, s7\n"
    "s_add_u32 s11, s7, s8\n"
    "s_lshl2_add_u32 s8, s14, s8\n"
    "v_and_b32 v53, 0x30, v16\n"
    "v_lshrrev_b32 v54, 2, v19\n"
    "v_lshl_add_u32 v53, v54, 8, v53\n"
    "v_lshl_add_u32 v53, v52, 6, v53\n"
    "v_add_u32 v39, s8, v53\n"
    "v_mov_b32 v251, 0\n"
    "v_mov_b32 v253, 0\n"
    "v_mov_b32 v255, 0\n"
    "s_add_u32 s63, s62, 98304\n"
    "s_cmp_eq_u32 s63, 294912\n"
    "s_cselect_b32 s63, 0, s63\n"
    "s_add_u32 s64, s63, 98304\n"
    "s_cmp_eq_u32 s64, 294912\n"
    "s_cselect_b32 s64, 0, s64\n"
    "s_add_u32 s7, s3, 160768\n"
    "v_mov_b32 v14, 0\n"
    "v_add_u32 v12, s7, v12\n"
    "s_lshl2_add_u32 s7, s4, s7\n"
    "v_mov_b32 v13, s7\n"
    "v_xor_b32 v53, v52, v19\n"
    "v_mul_u32_u24 v54, 0x300, v19\n"
    "v_lshl_add_u32 v54, v53, 4, v54\n"
    "v_add_u32 v4, s3, v54\n"
    "v_xor_b32 v5, 64, v4\n"
    "v_xor_b32 v6, 128, v4\n"
    "v_xor_b32 v7, 192, v4\n"
    "s_lshl_b32 s7, s4, 7\n"
    "s_lshr_b32 s8, s4, 1\n"
    "s_lshl_b32 s8, s8, 7\n"
    "s_add_u32 s8, s8, 512\n"
    "v_add_u32 v241, s8, v240\n"
    "v_add_u32 v240, s7, v240\n"
    "v_lshrrev_b32 v19, 4, v16\n"
    "s_mul_i32 s7, s52, 5\n"
    "s_lshr_b32 s7, s7, 3\n"
    "s_sub_u32 s8, s52, 1\n"
    "s_bitcmp1_b32 s2, 7\n"
    "s_cselect_b32 s7, s7, 0\n"
    "v_add_u32 v53, s7, v19\n"
    "v_cmp_gt_u32 vcc, s8, v19\n"
    "v_subrev_u32 v54, s8, v53\n"
    "v_min_u32 v53, v53, v54\n"
    "v_cndmask_b32 v53, v19, v53, vcc\n"
    "v_mul_lo_u32 v3, v53, s53\n"
    "v_add_u32 v3, s56, v3\n"
    "s_xor_b32 s29, s29, s23\n"
    "s_mul_i32 s7, s52, s53\n"
    "s_add_u32 s7, s7, s56\n"
    "s_mov_b32 s8, -2752512\n"
    "s_bitcmp1_b32 s2, 0\n"
    "s_cselect_b32 s8, -3538944, s8\n"
    "s_add_u32 s7, s7, s8\n"
    "v_cmp_eq_u32 vcc, s52, v19\n"
    "v_mov_b32 v53, s7\n"
    "s_nop 0\n"
    "v_cndmask_b32 v3, v3, v53, vcc\n"
    "v_add_u32 v53, s23, v3\n"
    "v_lshrrev_b32 v53, 21, v53\n"
    "v_and_b32 v232, 56, v53\n"
    "v_and_b32 v233, 4, v53\n"
    "v_xor_b32 v234, s29, v53\n"
    "s_cmp_eq_u32 s35, 0\n"
    "s_cbranch_scc0 71f\n"
    "s_pack_ll_b32_b16 s88, s0, s16\n"
    "s_mov_b64 exec, 1\n"
    "v_mov_b32 v22, s88\n"
    "global_store_dwordx2 v20, v[22:23], s[54:55] sc1\n"
    "s_mov_b64 exec, -1\n"
    "s_waitcnt lgkmcnt(0)\n"
    "71:\n"
    "s_mov_b64 exec, 0\n"
    "s_cmp_eq_u32 s4, 1\n"
    "s_cbranch_scc1 200f\n"
    "s_cmp_eq_u32 s4, 2\n"
    "s_cbranch_scc1 201f\n"
    "199:\n"
    "s_mov_b64 exec, -1\n"
    "s_branch 301f\n"
    "81:\n"
    "ds_read_b32 v15, v12\n"
    "s_waitcnt lgkmcnt(0)\n"
    "v_cmp_lt_u32 vcc, v15, v14\n"
    "s_nop 0\n"
    "s_cbranch_vccnz 81b\n"
    "s_branch 82f\n"
    "301:\n"
    "s_branch 302f\n"
    "41:\n"
    "ds_read_b32 v15, v12\n"
    "s_waitcnt lgkmcnt(0)\n"
    "v_cmp_lt_u32 vcc, v15, v14\n"
    "s_nop 0\n"
    "s_cbranch_vccnz 41b\n"
    "s_branch 42f\n"
    "43:\n"
    "s_memrealtime s[92:93]\n"
    "s_waitcnt lgkmcnt(0)\n"
    "s_sub_u32 s84, s92, s89\n"
    "s_cmp_lt_i32 s84, 0\n"
    "s_cbranch_scc0 44f\n"
    "s_sleep 1\n"
    "s_branch 43b\n"
    "302:\n"
    "s_branch 303f\n"
    "61:\n"
    "ds_read_b32 v15, v12\n"
    "s_waitcnt lgkmcnt(0)\n"
    "v_cmp_lt_u32 vcc, v15, v14\n"
    "s_nop 0\n"
    "s_cbranch_vccnz 61b\n"
    "s_branch 62f\n"
    "63:\n"
    "s_memrealtime s[92:93]\n"
    "s_waitcnt lgkmcnt(0)\n"
    "s_sub_u32 s84, s92, s89\n"
    "s_cmp_lt_i32 s84, 0\n"
    "s_cbranch_scc0 64f\n"
    "s_sleep 1\n"
    "s_branch 63b\n"
    "303:\n"
    "s_branch 304f\n"
    "45:\n"
    "s_sub_u32 s7, s86, s71\n"
    "s_cmp_eq_u32 s7, 2\n"
    "s_cbranch_scc1 402f\n"
    "s_cmp_eq_u32 s7, 4\n"
    "s_cbranch_scc1 404f\n"
    "v_mfma_f32_16x16x32_bf16 v[24:27], a[96:99], a[68:71], v[24:27]\n"
    "s_add_u32 s7, s70, 0\n"
    "s_add_u32 s64, s64, 2\n"
    "ds_read_b128 a[136:139], v4 offset:12288\n"
    "v_mfma_f32_16x16x32_bf16 v[24:27], a[92:95], a[44:47], v[24:27]\n"
    "buffer_load_dwordx4 v0, s[36:39], s81 offen nt lds\n"
    "v_mfma_f32_16x16x32_bf16 v[24:27], a[88:91], a[20:23], v[24:27]\n"
    "v_add_u32 v14, 1, v14\n"
    "v_accvgpr_write_b32 a188, v44\n"
    "v_mfma_f32_16x16x32_bf16 v[28:31], a[88:91], a[0:3], 0\n"
    "s_mov_b64 vcc, s[12:13]\n"
    "v_accvgpr_write_b32 a189, v45\n"
    "v_mfma_f32_16x16x32_bf16 v[28:31], a[92:95], a[24:27], v[28:31]\n"
    "s_waitcnt lgkmcnt(1)\n"
    "ds_read_b128 a[140:143], v5 offset:12288\n"
    "v_mfma_f32_16x16x32_bf16 v[28:31], a[96:99], a[48:51], v[28:31]\n"
    "ds_read_b128 a[144:147], v6 offset:12288\n"
    "v_accvgpr_mov_b32 a197, a185\n"
    "v_mfma_f32_16x16x32_bf16 v[28:31], a[100:103], a[72:75], v[28:31]\n"
    "buffer_load_dwordx4 v1, s[36:39], s81 offen offset:1024 nt lds\n"
    "v_mfma_f32_16x16x32_bf16 v[28:31], a[104:107], v[64:67], v[28:31]\n"
    "v_cvt_pk_bf16_f32 v52, v20, v24\n"
    "v_accvgpr_mov_b32 a198, a186\n"
    "v_mfma_f32_16x16x32_bf16 v[28:31], a[108:111], v[88:91], v[28:31]\n"
    "v_cvt_pk_bf16_f32 v53, v21, v25\n"
    "v_accvgpr_mov_b32 a199, a187\n"
    "v_mfma_f32_16x16x32_bf16 v[28:31], a[112:115], v[112:115], v[28:31]\n"
    "v_cvt_pk_bf16_f32 v54, v22, v26\n"
    "v_accvgpr_write_b32 a190, v46\n"
    "v_mfma_f32_16x16x32_bf16 v[28:31], a[116:119], v[136:139], v[28:31]\n"
    "ds_read_b128 a[148:151], v7 offset:12288\n"
    "v_accvgpr_write_b32 a191, v47\n"
    "v_mfma_f32_16x16x32_bf16 v[28:31], a[120:123], v[160:163], v[28:31]\n"
    "v_cvt_pk_bf16_f32 v55, v23, v27\n"
    "v_accvgpr_write_b32 a192, v48\n"
    "v_mfma_f32_16x16x32_bf16 v[28:31], a[124:127], v[184:187], v[28:31]\n"
    "buffer_load_dwordx4 v2, s[36:39], s81 offen offset:2048 nt lds\n"
    "v_mfma_f32_16x16x32_bf16 v[28:31], a[128:131], v[208:211], v[28:31]\n"
    "v_readlane_b32 s81, v3, s86\n"
    "v_accvgpr_write_b32 a193, v49\n"
    "v_mfma_f32_16x16x32_bf16 v[28:31], a[132:135], v[232:235], v[28:31]\n"
    "ds_read_b128 a[152:155], v4 offset:12544\n"
    "v_accvgpr_write_b32 a194, v50\n"
    "v_mfma_f32_16x16x32_bf16 v[32:35], a[132:135], v[236:239], 0\n"
    "s_addk_i32 m0, 12288\n"
    "v_accvgpr_write_b32 a195, v51\n"
    "v_mfma_f32_16x16x32_bf16 v[32:35], a[128:131], v[212:215], v[32:35]\n"
    "ds_write2st64_b32 v38, v52, v53 offset0:0 offset1:2\n"
    "v_mfma_f32_16x16x32_bf16 v[32:35], a[124:127], v[188:191], v[32:35]\n"
    "v_accvgpr_mov_b32 a196, a184\n"
    "v_readlane_b32 s9, v3, s7\n"
    "s_branch 46f\n"
    "304:\n"
    "s_branch 305f\n"
    "402:\n"
    "v_mfma_f32_16x16x32_bf16 v[24:27], a[96:99], a[68:71], v[24:27]\n"
    "s_add_u32 s64, s64, 2\n"
    "ds_read_b128 a[136:139], v4 offset:12288\n"
    "v_mfma_f32_16x16x32_bf16 v[24:27], a[92:95], a[44:47], v[24:27]\n"
    "buffer_store_dwordx4 a[188:191], v8, s[40:43], s9 offen sc1\n"
    "v_mfma_f32_16x16x32_bf16 v[24:27], a[88:91], a[20:23], v[24:27]\n"
    "v_add_u32 v14, 1, v14\n"
    "v_mfma_f32_16x16x32_bf16 v[28:31], a[88:91], a[0:3], 0\n"
    "s_mov_b64 vcc, s[12:13]\n"
    "v_mfma_f32_16x16x32_bf16 v[28:31], a[92:95], a[24:27], v[28:31]\n"
    "ds_read_b128 a[140:143], v5 offset:12288\n"
    "v_mfma_f32_16x16x32_bf16 v[28:31], a[96:99], a[48:51], v[28:31]\n"
    "ds_read_b128 a[144:147], v6 offset:12288\n"
    "v_mfma_f32_16x16x32_bf16 v[28:31], a[100:103], a[72:75], v[28:31]\n"
    "buffer_store_dwordx4 a[192:195], v9, s[40:43], s9 offen sc1\n"
    "v_mfma_f32_16x16x32_bf16 v[28:31], a[104:107], v[64:67], v[28:31]\n"
    "v_cvt_pk_bf16_f32 v52, v20, v24\n"
    "v_mfma_f32_16x16x32_bf16 v[28:31], a[108:111], v[88:91], v[28:31]\n"
    "v_cvt_pk_bf16_f32 v53, v21, v25\n"
    "v_mfma_f32_16x16x32_bf16 v[28:31], a[112:115], v[112:115], v[28:31]\n"
    "v_cvt_pk_bf16_f32 v54, v22, v26\n"
    "v_mfma_f32_16x16x32_bf16 v[28:31], a[116:119], v[136:139], v[28:31]\n"
    "ds_read_b128 a[148:151], v7 offset:12288\n"
    "v_mfma_f32_16x16x32_bf16 v[28:31], a[120:123], v[160:163], v[28:31]\n"
    "v_cvt_pk_bf16_f32 v55, v23, v27\n"
    "v_mfma_f32_16x16x32_bf16 v[28:31], a[124:127], v[184:187], v[28:31]\n"
    "buffer_store_dwordx4 a[196:199], v10, s[40:43], s9 offen sc1\n"
    "v_mfma_f32_16x16x32_bf16 v[28:31], a[128:131], v[208:211], v[28:31]\n"
    "v_readlane_b32 s81, v3, s86\n"
    "v_mfma_f32_16x16x32_bf16 v[28:31], a[132:135], v[232:235], v[28:31]\n"
    "ds_read_b128 a[152:155], v4 offset:12544\n"
    "v_mfma_f32_16x16x32_bf16 v[32:35], a[132:135], v[236:239], 0\n"
    "s_addk_i32 m0, 12288\n"
    "v_mfma_f32_16x16x32_bf16 v[32:35], a[128:131], v[212:215], v[32:35]\n"
    "ds_write2st64_b32 v38, v52, v53 offset0:0 offset1:2\n"
    "v_mfma_f32_16x16x32_bf16 v[32:35], a[124:127], v[188:191], v[32:35]\n"
    "s_branch 46f\n"
    "305:\n"
    "s_branch 306f\n"
    "404:\n"
    "v_mfma_f32_16x16x32_bf16 v[24:27], a[96:99], a[68:71], v[24:27]\n"
    "s_sub_u32 s89, s89, 36\n"
    "ds_read_b128 a[136:139], v4 offset:12288\n"
    "v_mfma_f32_16x16x32_bf16 v[24:27], a[92:95], a[44:47], v[24:27]\n"
    "buffer_store_dwordx4 a[208:211], v10, s[40:43], s10 offen sc1\n"
    "v_mfma_f32_16x16x32_bf16 v[24:27], a[88:91], a[20:23], v[24:27]\n"
    "v_add_u32 v14, 1, v14\n"
    "v_mfma_f32_16x16x32_bf16 v[28:31], a[88:91], a[0:3], 0\n"
    "s_mov_b64 vcc, s[12:13]\n"
    "v_mfma_f32_16x16x32_bf16 v[28:31], a[92:95], a[24:27], v[28:31]\n"
    "ds_read_b128 a[140:143], v5 offset:12288\n"
    "v_mfma_f32_16x16x32_bf16 v[28:31], a[96:99], a[48:51], v[28:31]\n"
    "ds_read_b128 a[144:147], v6 offset:12288\n"
    "v_mfma_f32_16x16x32_bf16 v[28:31], a[100:103], a[72:75], v[28:31]\n"
    "buffer_load_dwordx4 v1, s[36:39], s81 offen offset:1024 nt lds\n"
    "v_mfma_f32_16x16x32_bf16 v[28:31], a[104:107], v[64:67], v[28:31]\n"
    "v_cvt_pk_bf16_f32 v52, v20, v24\n"
    "v_mfma_f32_16x16x32_bf16 v[28:31], a[108:111], v[88:91], v[28:31]\n"
    "v_cvt_pk_bf16_f32 v53, v21, v25\n"
    "v_mfma_f32_16x16x32_bf16 v[28:31], a[112:115], v[112:115], v[28:31]\n"
    "v_cvt_pk_bf16_f32 v54, v22, v26\n"
    "v_mfma_f32_16x16x32_bf16 v[28:31], a[116:119], v[136:139], v[28:31]\n"
    "ds_read_b128 a[148:151], v7 offset:12288\n"
    "v_mfma_f32_16x16x32_bf16 v[28:31], a[120:123], v[160:163], v[28:31]\n"
    "v_cvt_pk_bf16_f32 v55, v23, v27\n"
    "v_mfma_f32_16x16x32_bf16 v[28:31], a[124:127], v[184:187], v[28:31]\n"
    "buffer_load_dwordx4 v2, s[36:39], s81 offen offset:2048 nt lds\n"
    "v_mfma_f32_16x16x32_bf16 v[28:31], a[128:131], v[208:211], v[28:31]\n"
    "v_readlane_b32 s81, v3, s86\n"
    "v_mfma_f32_16x16x32_bf16 v[28:31], a[132:135], v[232:235], v[28:31]\n"
    "ds_read_b128 a[152:155], v4 offset:12544\n"
    "v_mfma_f32_16x16x32_bf16 v[32:35], a[132:135], v[236:239], 0\n"
    "s_addk_i32 m0, 12288\n"
    "v_mfma_f32_16x16x32_bf16 v[32:35], a[128:131], v[212:215], v[32:35]\n"
    "ds_write2st64_b32 v38, v52, v53 offset0:0 offset1:2\n"
    "v_mfma_f32_16x16x32_bf16 v[32:35], a[124:127], v[188:191], v[32:35]\n"
    "s_branch 46f\n"
    "306:\n"
    "s_branch 307f\n"
    "65:\n"
    "s_sub_u32 s7, s86, s71\n"
    "s_cmp_eq_u32 s7, 3\n"
    "s_cbranch_scc1 403f\n"
    "v_mfma_f32_16x16x32_bf16 v[32:35], a[144:147], a[68:71], v[32:35]\n"
    "s_add_u32 s7, s70, 1\n"
    "s_add_u32 s65, s65, 2\n"
    "ds_read_b128 a[88:91], v4\n"
    "v_mfma_f32_16x16x32_bf16 v[32:35], a[140:143], a[44:47], v[32:35]\n"
    "buffer_load_dwordx4 v0, s[36:39], s81 offen nt lds\n"
    "v_readlane_b32 s10, v3, s7\n"
    "v_mfma_f32_16x16x32_bf16 v[32:35], a[136:139], a[20:23], v[32:35]\n"
    "v_add_u32 v7, s78, v7\n"
    "v_accvgpr_write_b32 a200, v44\n"
    "v_mfma_f32_16x16x32_bf16 v[20:23], a[136:139], a[0:3], 0\n"
    "v_add_u32 v14, 1, v14\n"
    "v_accvgpr_write_b32 a201, v45\n"
    "v_mfma_f32_16x16x32_bf16 v[20:23], a[140:143], a[24:27], v[20:23]\n"
    "s_waitcnt lgkmcnt(1)\n"
    "ds_read_b128 a[92:95], v5\n"
    "v_mfma_f32_16x16x32_bf16 v[20:23], a[144:147], a[48:51], v[20:23]\n"
    "ds_read_b128 a[96:99], v6\n"
    "v_accvgpr_mov_b32 a208, a184\n"
    "v_mfma_f32_16x16x32_bf16 v[20:23], a[148:151], a[72:75], v[20:23]\n"
    "buffer_load_dwordx4 v1, s[36:39], s81 offen offset:1024 nt lds\n"
    "v_mfma_f32_16x16x32_bf16 v[20:23], a[152:155], v[64:67], v[20:23]\n"
    "v_cvt_pk_bf16_f32 v52, v28, v32\n"
    "v_accvgpr_mov_b32 a209, a185\n"
    "v_mfma_f32_16x16x32_bf16 v[20:23], a[156:159], v[88:91], v[20:23]\n"
    "v_cvt_pk_bf16_f32 v53, v29, v33\n"
    "v_accvgpr_mov_b32 a210, a186\n"
    "v_mfma_f32_16x16x32_bf16 v[20:23], a[160:163], v[112:115], v[20:23]\n"
    "v_cvt_pk_bf16_f32 v54, v30, v34\n"
    "v_accvgpr_mov_b32 a211, a187\n"
    "v_mfma_f32_16x16x32_bf16 v[20:23], a[164:167], v[136:139], v[20:23]\n"
    "ds_read_b128 a[100:103], v7\n"
    "v_accvgpr_write_b32 a202, v46\n"
    "v_mfma_f32_16x16x32_bf16 v[20:23], a[168:171], v[160:163], v[20:23]\n"
    "v_cvt_pk_bf16_f32 v55, v31, v35\n"
    "v_accvgpr_write_b32 a203, v47\n"
    "v_mfma_f32_16x16x32_bf16 v[20:23], a[172:175], v[184:187], v[20:23]\n"
    "buffer_load_dwordx4 v2, s[36:39], s81 offen offset:2048 nt lds\n"
    "v_mfma_f32_16x16x32_bf16 v[20:23], a[176:179], v[208:211], v[20:23]\n"
    "s_mov_b64 vcc, s[12:13]\n"
    "v_accvgpr_write_b32 a204, v48\n"
    "v_mfma_f32_16x16x32_bf16 v[20:23], a[180:183], v[232:235], v[20:23]\n"
    "ds_read_b128 a[104:107], v4 offset:256\n"
    "v_accvgpr_write_b32 a205, v49\n"
    "v_mfma_f32_16x16x32_bf16 v[24:27], a[180:183], v[236:239], 0\n"
    "v_readlane_b32 s81, v3, s86\n"
    "v_accvgpr_write_b32 a206, v50\n"
    "v_mfma_f32_16x16x32_bf16 v[24:27], a[176:179], v[212:215], v[24:27]\n"
    "ds_write2st64_b32 v38, v52, v53 offset0:16 offset1:18\n"
    "v_mfma_f32_16x16x32_bf16 v[24:27], a[172:175], v[188:191], v[24:27]\n"
    "s_addk_i32 m0, 12288\n"
    "v_accvgpr_write_b32 a207, v51\n"
    "s_branch 66f\n"
    "307:\n"
    "s_branch 308f\n"
    "403:\n"
    "v_mfma_f32_16x16x32_bf16 v[32:35], a[144:147], a[68:71], v[32:35]\n"
    "s_sub_u32 s89, s89, 36\n"
    "ds_read_b128 a[88:91], v4\n"
    "v_mfma_f32_16x16x32_bf16 v[32:35], a[140:143], a[44:47], v[32:35]\n"
    "buffer_store_dwordx4 a[200:203], v8, s[40:43], s10 offen sc1\n"
    "v_mfma_f32_16x16x32_bf16 v[32:35], a[136:139], a[20:23], v[32:35]\n"
    "v_add_u32 v7, s78, v7\n"
    "v_mfma_f32_16x16x32_bf16 v[20:23], a[136:139], a[0:3], 0\n"
    "v_add_u32 v14, 1, v14\n"
    "v_mfma_f32_16x16x32_bf16 v[20:23], a[140:143], a[24:27], v[20:23]\n"
    "ds_read_b128 a[92:95], v5\n"
    "v_mfma_f32_16x16x32_bf16 v[20:23], a[144:147], a[48:51], v[20:23]\n"
    "ds_read_b128 a[96:99], v6\n"
    "v_mfma_f32_16x16x32_bf16 v[20:23], a[148:151], a[72:75], v[20:23]\n"
    "buffer_store_dwordx4 a[204:207], v9, s[40:43], s10 offen sc1\n"
    "v_mfma_f32_16x16x32_bf16 v[20:23], a[152:155], v[64:67], v[20:23]\n"
    "v_cvt_pk_bf16_f32 v52, v28, v32\n"
    "v_mfma_f32_16x16x32_bf16 v[20:23], a[156:159], v[88:91], v[20:23]\n"
    "v_cvt_pk_bf16_f32 v53, v29, v33\n"
    "v_mfma_f32_16x16x32_bf16 v[20:23], a[160:163], v[112:115], v[20:23]\n"
    "v_cvt_pk_bf16_f32 v54, v30, v34\n"
    "v_mfma_f32_16x16x32_bf16 v[20:23], a[164:167], v[136:139], v[20:23]\n"
    "ds_read_b128 a[100:103], v7\n"
    "v_mfma_f32_16x16x32_bf16 v[20:23], a[168:171], v[160:163], v[20:23]\n"
    "v_cvt_pk_bf16_f32 v55, v31, v35\n"
    "v_mfma_f32_16x16x32_bf16 v[20:23], a[172:175], v[184:187], v[20:23]\n"
    "buffer_load_dwordx4 v2, s[36:39], s81 offen offset:2048 nt lds\n"
    "v_mfma_f32_16x16x32_bf16 v[20:23], a[176:179], v[208:211], v[20:23]\n"
    "s_mov_b64 vcc, s[12:13]\n"
    "v_mfma_f32_16x16x32_bf16 v[20:23], a[180:183], v[232:235], v[20:23]\n"
    "ds_read_b128 a[104:107], v4 offset:256\n"
    "v_mfma_f32_16x16x32_bf16 v[24:27], a[180:183], v[236:239], 0\n"
    "v_readlane_b32 s81, v3, s86\n"
    "v_mfma_f32_16x16x32_bf16 v[24:27], a[176:179], v[212:215], v[24:27]\n"
    "ds_write2st64_b32 v38, v52, v53 offset0:16 offset1:18\n"
    "v_mfma_f32_16x16x32_bf16 v[24:27], a[172:175], v[188:191], v[24:27]\n"
    "s_addk_i32 m0, 12288\n"
    "s_branch 66f\n"
    "308:\n"
    "s_waitcnt vmcnt(10)\n"
    "ds_write_b32 v13, v14\n"
    "ds_read_u16 v250, v18\n"
    "ds_read_u16 v252, v18 offset:8\n"
    "ds_read_u16 v254, v235\n"
    "s_waitcnt vmcnt(9)\n"
    "ds_read2st64_b64 a[188:191], v15 offset0:0 offset1:1\n"
    "s_waitcnt vmcnt(8)\n"
    "ds_read2st64_b64 a[192:195], v15 offset0:2 offset1:3\n"
    "s_waitcnt vmcnt(7)\n"
    "ds_read2st64_b64 a[196:199], v15 offset0:4 offset1:5\n"
    "s_waitcnt lgkmcnt(3)\n"
    "v_lshlrev_b64 v[40:41], v16, v[250:251]\n"
    "v_lshlrev_b64 v[42:43], v16, v[252:253]\n"
    "v_lshlrev_b64 v[44:45], v16, v[254:255]\n"
    "s_waitcnt vmcnt(6) lgkmcnt(2)\n"
    "ds_read2st64_b64 a[200:203], v15 offset0:6 offset1:7\n"
    "s_nop 0\n"
    "v_mfma_f32_4x4x4_16b_bf16 v[20:23], v[40:41], a[188:189], 0 cbsz:2 abid:0\n"
    "ds_read_u16 v250, v18 offset:64\n"
    "s_add_u32 m0, s83, 47104\n"
    "v_mfma_f32_4x4x4_16b_bf16 v[24:27], v[40:41], a[190:191], 0 cbsz:2 abid:1\n"
    "buffer_load_dwordx4 v248, s[44:47], s84 offen lds\n"
    "v_add_u32 v8, v8, v240\n"
    "s_waitcnt vmcnt(6) lgkmcnt(3)\n"
    "ds_read2st64_b32 a[204:205], v38 offset0:16 offset1:17\n"
    "v_mfma_f32_4x4x4_16b_bf16 v[28:31], v[40:41], a[192:193], 0 cbsz:2 abid:2\n"
    "ds_read2st64_b32 a[206:207], v38 offset0:18 offset1:19\n"
    "ds_read_u16 v252, v18 offset:72\n"
    "v_cvt_pk_bf16_f32 v52, v20, v24\n"
    "v_cvt_pk_bf16_f32 v53, v21, v25\n"
    "v_accvgpr_write_b32 a0, v52\n"
    "v_accvgpr_write_b32 a4, v53\n"
    "v_mfma_f32_4x4x4_16b_bf16 v[32:35], v[40:41], a[194:195], 0 cbsz:2 abid:3\n"
    "buffer_load_dwordx4 v249, s[44:47], s84 offen offset:1024 lds\n"
    "v_cvt_pk_bf16_f32 v54, v22, v26\n"
    "v_cvt_pk_bf16_f32 v55, v23, v27\n"
    "v_accvgpr_write_b32 a8, v54\n"
    "v_accvgpr_write_b32 a12, v55\n"
    "v_add_u32 v9, v9, v240\n"
    "s_waitcnt vmcnt(6) lgkmcnt(5)\n"
    "ds_read2st64_b32 a[208:209], v38 offset0:20 offset1:21\n"
    "v_mfma_f32_4x4x4_16b_bf16 v[20:23], v[42:43], a[196:197], 0 cbsz:2 abid:0\n"
    "ds_read2st64_b32 a[210:211], v38 offset0:22 offset1:23\n"
    "ds_read_u16 v254, v235 offset:64\n"
    "s_add_u32 m0, s83, 49152\n"
    "s_add_u32 s84, s60, s62\n"
    "v_cvt_pk_bf16_f32 v52, v28, v32\n"
    "v_cvt_pk_bf16_f32 v53, v29, v33\n"
    "v_accvgpr_write_b32 a1, v52\n"
    "v_accvgpr_write_b32 a5, v53\n"
    "v_mfma_f32_4x4x4_16b_bf16 v[24:27], v[42:43], a[198:199], 0 cbsz:2 abid:1\n"
    "buffer_load_dwordx4 v244, s[44:47], s84 offen lds\n"
    "v_cvt_pk_bf16_f32 v54, v30, v34\n"
    "v_cvt_pk_bf16_f32 v55, v31, v35\n"
    "v_accvgpr_write_b32 a9, v54\n"
    "v_accvgpr_write_b32 a13, v55\n"
    "v_add_u32 v10, v10, v241\n"
    "s_waitcnt vmcnt(6) lgkmcnt(6)\n"
    "ds_read2st64_b64 a[188:191], v15 offset0:12 offset1:13\n"
    "v_mfma_f32_4x4x4_16b_bf16 v[28:31], v[42:43], a[200:201], 0 cbsz:2 abid:2\n"
    "v_cvt_pk_bf16_f32 v52, v20, v24\n"
    "v_cvt_pk_bf16_f32 v53, v21, v25\n"
    "v_accvgpr_write_b32 a2, v52\n"
    "v_accvgpr_write_b32 a6, v53\n"
    "v_mfma_f32_4x4x4_16b_bf16 v[32:35], v[42:43], a[202:203], 0 cbsz:2 abid:3\n"
    "buffer_load_dwordx4 v245, s[44:47], s84 offen offset:1024 lds\n"
    "v_cvt_pk_bf16_f32 v54, v22, v26\n"
    "v_cvt_pk_bf16_f32 v55, v23, v27\n"
    "v_accvgpr_write_b32 a10, v54\n"
    "v_accvgpr_write_b32 a14, v55\n"
    "v_lshlrev_b64 v[46:47], v16, v[250:251]\n"
    "s_mov_b32 s12, 0xff00ff\n"
    "s_waitcnt vmcnt(6) lgkmcnt(4)\n"
    "ds_read2st64_b64 a[192:195], v15 offset0:14 offset1:15\n"
    "v_mfma_f32_4x4x4_16b_bf16 v[20:23], v[44:45], a[204:205], 0 cbsz:2 abid:0\n"
    "v_cvt_pk_bf16_f32 v52, v28, v32\n"
    "v_cvt_pk_bf16_f32 v53, v29, v33\n"
    "v_accvgpr_write_b32 a3, v52\n"
    "v_accvgpr_write_b32 a7, v53\n"
    "v_mfma_f32_4x4x4_16b_bf16 v[24:27], v[44:45], a[206:207], 0 cbsz:2 abid:1\n"
    "buffer_load_dwordx4 v246, s[44:47], s84 offen offset:2048 lds\n"
    "v_cvt_pk_bf16_f32 v54, v30, v34\n"
    "v_cvt_pk_bf16_f32 v55, v31, v35\n"
    "v_accvgpr_write_b32 a11, v54\n"
    "v_accvgpr_write_b32 a15, v55\n"
    "buffer_load_dwordx4 v247, s[44:47], s84 offen offset:3072 lds\n"
    "v_lshlrev_b64 v[48:49], v16, v[252:253]\n"
    "s_mov_b32 s13, 0xff00ff\n"
    "s_waitcnt vmcnt(7) lgkmcnt(2)\n"
    "ds_read2st64_b64 a[196:199], v15 offset0:16 offset1:17\n"
    "v_mfma_f32_4x4x4_16b_bf16 v[28:31], v[44:45], a[208:209], 0 cbsz:2 abid:2\n"
    "s_add_u32 m0, s83, 53248\n"
    "v_cvt_pk_bf16_f32 v52, v20, v22\n"
    "v_cvt_pk_bf16_f32 v53, v21, v23\n"
    "v_accvgpr_write_b32 a16, v52\n"
    "v_accvgpr_write_b32 a20, v53\n"
    "v_mfma_f32_4x4x4_16b_bf16 v[32:35], v[44:45], a[210:211], 0 cbsz:2 abid:3\n"
    "buffer_load_dwordx4 v248, s[44:47], s84 offen lds\n"
    "v_cvt_pk_bf16_f32 v54, v24, v26\n"
    "v_cvt_pk_bf16_f32 v55, v25, v27\n"
    "v_accvgpr_write_b32 a17, v54\n"
    "v_accvgpr_write_b32 a21, v55\n"
    "v_lshlrev_b64 v[50:51], v16, v[254:255]\n"
    "200:\n"
    "s_cbranch_execz 199b\n"
    "s_add_u32 s77, s57, 36864\n"
    "s_waitcnt vmcnt(7) lgkmcnt(2)\n"
    "ds_read2st64_b64 a[200:203], v15 offset0:18 offset1:19\n"
    "v_mfma_f32_4x4x4_16b_bf16 v[20:23], v[46:47], a[188:189], 0 cbsz:2 abid:0\n"
    "ds_read_u16 v250, v18 offset:128\n"
    "v_cvt_pk_bf16_f32 v52, v28, v30\n"
    "v_cvt_pk_bf16_f32 v53, v29, v31\n"
    "v_accvgpr_write_b32 a18, v52\n"
    "v_accvgpr_write_b32 a22, v53\n"
    "v_mfma_f32_4x4x4_16b_bf16 v[24:27], v[46:47], a[190:191], 0 cbsz:2 abid:1\n"
    "buffer_load_dwordx4 v249, s[44:47], s84 offen offset:1024 lds\n"
    "v_cvt_pk_bf16_f32 v54, v32, v34\n"
    "v_cvt_pk_bf16_f32 v55, v33, v35\n"
    "v_accvgpr_write_b32 a19, v54\n"
    "v_accvgpr_write_b32 a23, v55\n"
    "s_movk_i32 s88, 24576\n"
    "s_waitcnt vmcnt(7) lgkmcnt(3)\n"
    "ds_read2st64_b32 a[204:205], v38 offset0:40 offset1:41\n"
    "v_mfma_f32_4x4x4_16b_bf16 v[28:31], v[46:47], a[192:193], 0 cbsz:2 abid:2\n"
    "ds_read2st64_b32 a[206:207], v38 offset0:42 offset1:43\n"
    "ds_read_u16 v252, v18 offset:136\n"
    "s_add_u32 m0, s83, 55296\n"
    "s_add_u32 s84, s61, s62\n"
    "v_cvt_pk_bf16_f32 v52, v20, v24\n"
    "v_cvt_pk_bf16_f32 v53, v21, v25\n"
    "v_accvgpr_write_b32 a24, v52\n"
    "v_accvgpr_write_b32 a28, v53\n"
    "v_mfma_f32_4x4x4_16b_bf16 v[32:35], v[46:47], a[194:195], 0 cbsz:2 abid:3\n"
    "buffer_load_dwordx4 v244, s[44:47], s84 offen lds\n"
    "v_cvt_pk_bf16_f32 v54, v22, v26\n"
    "v_cvt_pk_bf16_f32 v55, v23, v27\n"
    "v_accvgpr_write_b32 a32, v54\n"
    "v_accvgpr_write_b32 a36, v55\n"
    "buffer_load_dwordx4 v245, s[44:47], s84 offen offset:1024 lds\n"
    "s_movk_i32 s78, 24576\n"
    "s_waitcnt vmcnt(8) lgkmcnt(5)\n"
    "ds_read2st64_b32 a[208:209], v38 offset0:44 offset1:45\n"
    "v_mfma_f32_4x4x4_16b_bf16 v[20:23], v[48:49], a[196:197], 0 cbsz:2 abid:0\n"
    "ds_read2st64_b32 a[210:211], v38 offset0:46 offset1:47\n"
    "ds_read_u16 v254, v235 offset:128\n"
    "v_cvt_pk_bf16_f32 v52, v28, v32\n"
    "v_cvt_pk_bf16_f32 v53, v29, v33\n"
    "v_accvgpr_write_b32 a25, v52\n"
    "v_accvgpr_write_b32 a29, v53\n"
    "v_mfma_f32_4x4x4_16b_bf16 v[24:27], v[48:49], a[198:199], 0 cbsz:2 abid:1\n"
    "buffer_load_dwordx4 v246, s[44:47], s84 offen offset:2048 lds\n"
    "v_cvt_pk_bf16_f32 v54, v30, v34\n"
    "v_cvt_pk_bf16_f32 v55, v31, v35\n"
    "v_accvgpr_write_b32 a33, v54\n"
    "v_accvgpr_write_b32 a37, v55\n"
    "s_mov_b32 s87, -122880\n"
    "s_waitcnt vmcnt(8) lgkmcnt(6)\n"
    "ds_read2st64_b64 a[188:191], v15 offset0:24 offset1:25\n"
    "v_mfma_f32_4x4x4_16b_bf16 v[28:31], v[48:49], a[200:201], 0 cbsz:2 abid:2\n"
    "v_cvt_pk_bf16_f32 v52, v20, v24\n"
    "v_cvt_pk_bf16_f32 v53, v21, v25\n"
    "v_accvgpr_write_b32 a26, v52\n"
    "v_accvgpr_write_b32 a30, v53\n"
    "v_mfma_f32_4x4x4_16b_bf16 v[32:35], v[48:49], a[202:203], 0 cbsz:2 abid:3\n"
    "buffer_load_dwordx4 v247, s[44:47], s84 offen offset:3072 lds\n"
    "v_cvt_pk_bf16_f32 v54, v22, v26\n"
    "v_cvt_pk_bf16_f32 v55, v23, v27\n"
    "v_accvgpr_write_b32 a34, v54\n"
    "v_accvgpr_write_b32 a38, v55\n"
    "v_lshlrev_b64 v[40:41], v16, v[250:251]\n"
    "s_add_u32 s80, s57, 147456\n"
    "s_waitcnt vmcnt(8) lgkmcnt(4)\n"
    "ds_read2st64_b64 a[192:195], v15 offset0:26 offset1:27\n"
    "v_mfma_f32_4x4x4_16b_bf16 v[20:23], v[50:51], a[204:205], 0 cbsz:2 abid:0\n"
    "s_add_u32 m0, s83, 59392\n"
    "v_cvt_pk_bf16_f32 v52, v28, v32\n"
    "v_cvt_pk_bf16_f32 v53, v29, v33\n"
    "v_accvgpr_write_b32 a27, v52\n"
    "v_accvgpr_write_b32 a31, v53\n"
    "v_mfma_f32_4x4x4_16b_bf16 v[24:27], v[50:51], a[206:207], 0 cbsz:2 abid:1\n"
    "buffer_load_dwordx4 v248, s[44:47], s84 offen lds\n"
    "v_cvt_pk_bf16_f32 v54, v30, v34\n"
    "v_cvt_pk_bf16_f32 v55, v31, v35\n"
    "v_accvgpr_write_b32 a35, v54\n"
    "v_accvgpr_write_b32 a39, v55\n"
    "v_lshlrev_b64 v[42:43], v16, v[252:253]\n"
    "s_mov_b32 s86, 5\n"
    "s_waitcnt vmcnt(8) lgkmcnt(2)\n"
    "ds_read2st64_b64 a[196:199], v15 offset0:28 offset1:29\n"
    "v_mfma_f32_4x4x4_16b_bf16 v[28:31], v[50:51], a[208:209], 0 cbsz:2 abid:2\n"
    "v_cvt_pk_bf16_f32 v52, v20, v22\n"
    "v_cvt_pk_bf16_f32 v53, v21, v23\n"
    "v_accvgpr_write_b32 a40, v52\n"
    "v_accvgpr_write_b32 a44, v53\n"
    "v_mfma_f32_4x4x4_16b_bf16 v[32:35], v[50:51], a[210:211], 0 cbsz:2 abid:3\n"
    "buffer_load_dwordx4 v249, s[44:47], s84 offen offset:1024 lds\n"
    "s_add_u32 m0, s83, 61440\n"
    "s_add_u32 s84, s58, s63\n"
    "v_cvt_pk_bf16_f32 v54, v24, v26\n"
    "v_cvt_pk_bf16_f32 v55, v25, v27\n"
    "v_accvgpr_write_b32 a41, v54\n"
    "v_accvgpr_write_b32 a45, v55\n"
    "buffer_load_dwordx4 v244, s[44:47], s84 offen lds\n"
    "v_lshlrev_b64 v[44:45], v16, v[254:255]\n"
    "s_lshr_b32 s7, s2, 1\n"
    "s_waitcnt vmcnt(9) lgkmcnt(2)\n"
    "s_barrier\n"
    "ds_read2st64_b64 a[200:203], v15 offset0:30 offset1:31\n"
    "v_mfma_f32_4x4x4_16b_bf16 v[20:23], v[40:41], a[188:189], 0 cbsz:2 abid:0\n"
    "ds_read_u16 v250, v18 offset:192\n"
    "v_cvt_pk_bf16_f32 v52, v28, v30\n"
    "v_cvt_pk_bf16_f32 v53, v29, v31\n"
    "v_accvgpr_write_b32 a42, v52\n"
    "v_accvgpr_write_b32 a46, v53\n"
    "v_mfma_f32_4x4x4_16b_bf16 v[24:27], v[40:41], a[190:191], 0 cbsz:2 abid:1\n"
    "buffer_load_dwordx4 v245, s[44:47], s84 offen offset:1024 lds\n"
    "v_cvt_pk_bf16_f32 v54, v32, v34\n"
    "v_cvt_pk_bf16_f32 v55, v33, v35\n"
    "v_accvgpr_write_b32 a43, v54\n"
    "v_accvgpr_write_b32 a47, v55\n"
    "s_and_b32 s7, s7, 0x7c\n"
    "s_waitcnt vmcnt(9) lgkmcnt(3)\n"
    "ds_read2st64_b32 a[204:205], v38 offset0:64 offset1:65\n"
    "v_mfma_f32_4x4x4_16b_bf16 v[28:31], v[40:41], a[192:193], 0 cbsz:2 abid:2\n"
    "ds_read2st64_b32 a[206:207], v38 offset0:66 offset1:67\n"
    "ds_read_u16 v252, v18 offset:200\n"
    "v_cvt_pk_bf16_f32 v52, v20, v24\n"
    "v_cvt_pk_bf16_f32 v53, v21, v25\n"
    "v_accvgpr_write_b32 a48, v52\n"
    "v_accvgpr_write_b32 a52, v53\n"
    "v_mfma_f32_4x4x4_16b_bf16 v[32:35], v[40:41], a[194:195], 0 cbsz:2 abid:3\n"
    "buffer_load_dwordx4 v246, s[44:47], s84 offen offset:2048 lds\n"
    "v_cvt_pk_bf16_f32 v54, v22, v26\n"
    "v_cvt_pk_bf16_f32 v55, v23, v27\n"
    "v_accvgpr_write_b32 a56, v54\n"
    "v_accvgpr_write_b32 a60, v55\n"
    "s_or_b32 s7, s7, s28\n"
    "s_waitcnt vmcnt(9) lgkmcnt(5)\n"
    "ds_read2st64_b32 a[208:209], v38 offset0:68 offset1:69\n"
    "v_mfma_f32_4x4x4_16b_bf16 v[20:23], v[42:43], a[196:197], 0 cbsz:2 abid:0\n"
    "ds_read2st64_b32 a[210:211], v38 offset0:70 offset1:71\n"
    "ds_read_u16 v254, v235 offset:192\n"
    "v_cvt_pk_bf16_f32 v52, v28, v32\n"
    "v_cvt_pk_bf16_f32 v53, v29, v33\n"
    "v_accvgpr_write_b32 a49, v52\n"
    "v_accvgpr_write_b32 a53, v53\n"
    "v_mfma_f32_4x4x4_16b_bf16 v[24:27], v[42:43], a[198:199], 0 cbsz:2 abid:1\n"
    "buffer_load_dwordx4 v247, s[44:47], s84 offen offset:3072 lds\n"
    "v_cvt_pk_bf16_f32 v54, v30, v34\n"
    "v_cvt_pk_bf16_f32 v55, v31, v35\n"
    "v_accvgpr_write_b32 a57, v54\n"
    "v_accvgpr_write_b32 a61, v55\n"
    "s_mul_i32 s7, s7, 79\n"
    "s_waitcnt vmcnt(9) lgkmcnt(6)\n"
    "ds_read2st64_b64 a[188:191], v15 offset0:36 offset1:37\n"
    "v_mfma_f32_4x4x4_16b_bf16 v[28:31], v[42:43], a[200:201], 0 cbsz:2 abid:2\n"
    "s_add_u32 m0, s83, 65536\n"
    "v_cvt_pk_bf16_f32 v52, v20, v24\n"
    "v_cvt_pk_bf16_f32 v53, v21, v25\n"
    "v_accvgpr_write_b32 a50, v52\n"
    "v_accvgpr_write_b32 a54, v53\n"
    "v_mfma_f32_4x4x4_16b_bf16 v[32:35], v[42:43], a[202:203], 0 cbsz:2 abid:3\n"
    "buffer_load_dwordx4 v248, s[44:47], s84 offen lds\n"
    "v_cvt_pk_bf16_f32 v54, v22, v26\n"
    "v_cvt_pk_bf16_f32 v55, v23, v27\n"
    "v_accvgpr_write_b32 a58, v54\n"
    "v_accvgpr_write_b32 a62, v55\n"
    "buffer_load_dwordx4 v249, s[44:47], s84 offen offset:1024 lds\n"
    "v_lshlrev_b64 v[46:47], v16, v[250:251]\n"
    "s_and_b32 s7, s7, 0x7f\n"
    "s_waitcnt vmcnt(10) lgkmcnt(4)\n"
    "ds_read2st64_b64 a[192:195], v15 offset0:38 offset1:39\n"
    "v_mfma_f32_4x4x4_16b_bf16 v[20:23], v[44:45], a[204:205], 0 cbsz:2 abid:0\n"
    "s_add_u32 m0, s83, 36864\n"
    "s_add_u32 s84, s59, s63\n"
    "v_cvt_pk_bf16_f32 v52, v28, v32\n"
    "v_cvt_pk_bf16_f32 v53, v29, v33\n"
    "v_accvgpr_write_b32 a51, v52\n"
    "v_accvgpr_write_b32 a55, v53\n"
    "v_mfma_f32_4x4x4_16b_bf16 v[24:27], v[44:45], a[206:207], 0 cbsz:2 abid:1\n"
    "buffer_load_dwordx4 v244, s[44:47], s84 offen lds\n"
    "v_cvt_pk_bf16_f32 v54, v30, v34\n"
    "v_cvt_pk_bf16_f32 v55, v31, v35\n"
    "v_accvgpr_write_b32 a59, v54\n"
    "v_accvgpr_write_b32 a63, v55\n"
    "v_lshlrev_b64 v[48:49], v16, v[252:253]\n"
    "s_mul_i32 s7, s7, 50\n"
    "s_waitcnt vmcnt(10) lgkmcnt(2)\n"
    "ds_read2st64_b64 a[196:199], v15 offset0:40 offset1:41\n"
    "v_mfma_f32_4x4x4_16b_bf16 v[28:31], v[44:45], a[208:209], 0 cbsz:2 abid:2\n"
    "v_cvt_pk_bf16_f32 v52, v20, v22\n"
    "v_cvt_pk_bf16_f32 v53, v21, v23\n"
    "v_accvgpr_write_b32 a64, v52\n"
    "v_accvgpr_write_b32 a68, v53\n"
    "v_mfma_f32_4x4x4_16b_bf16 v[32:35], v[44:45], a[210:211], 0 cbsz:2 abid:3\n"
    "buffer_load_dwordx4 v245, s[44:47], s84 offen offset:1024 lds\n"
    "v_cvt_pk_bf16_f32 v54, v24, v26\n"
    "v_cvt_pk_bf16_f32 v55, v25, v27\n"
    "v_accvgpr_write_b32 a65, v54\n"
    "v_accvgpr_write_b32 a69, v55\n"
    "v_lshlrev_b64 v[50:51], v16, v[254:255]\n"
    "s_lshr_b32 s7, s7, 6\n"
    "s_waitcnt vmcnt(10) lgkmcnt(2)\n"
    "ds_read2st64_b64 a[200:203], v15 offset0:42 offset1:43\n"
    "v_mfma_f32_4x4x4_16b_bf16 v[20:23], v[46:47], a[188:189], 0 cbsz:2 abid:0\n"
    "ds_read_u16 v250, v18 offset:256\n"
    "v_cvt_pk_bf16_f32 v52, v28, v30\n"
    "v_cvt_pk_bf16_f32 v53, v29, v31\n"
    "v_accvgpr_write_b32 a66, v52\n"
    "v_accvgpr_write_b32 a70, v53\n"
    "v_mfma_f32_4x4x4_16b_bf16 v[24:27], v[46:47], a[190:191], 0 cbsz:2 abid:1\n"
    "buffer_load_dwordx4 v246, s[44:47], s84 offen offset:2048 lds\n"
    "v_cvt_pk_bf16_f32 v54, v32, v34\n"
    "v_cvt_pk_bf16_f32 v55, v33, v35\n"
    "v_accvgpr_write_b32 a67, v54\n"
    "v_accvgpr_write_b32 a71, v55\n"
    "s_sub_i32 s7, s7, 82\n"
    "s_waitcnt vmcnt(10) lgkmcnt(3)\n"
    "ds_read2st64_b32 a[204:205], v38 offset0:88 offset1:89\n"
    "v_mfma_f32_4x4x4_16b_bf16 v[28:31], v[46:47], a[192:193], 0 cbsz:2 abid:2\n"
    "ds_read2st64_b32 a[206:207], v38 offset0:90 offset1:91\n"
    "ds_read_u16 v252, v18 offset:264\n"
    "v_cvt_pk_bf16_f32 v52, v20, v24\n"
    "v_cvt_pk_bf16_f32 v53, v21, v25\n"
    "v_accvgpr_write_b32 a72, v52\n"
    "v_accvgpr_write_b32 a76, v53\n"
    "v_mfma_f32_4x4x4_16b_bf16 v[32:35], v[46:47], a[194:195], 0 cbsz:2 abid:3\n"
    "buffer_load_dwordx4 v247, s[44:47], s84 offen offset:3072 lds\n"
    "s_add_u32 m0, s83, 40960\n"
    "v_cvt_pk_bf16_f32 v54, v22, v26\n"
    "v_cvt_pk_bf16_f32 v55, v23, v27\n"
    "v_accvgpr_write_b32 a80, v54\n"
    "v_accvgpr_write_b32 a84, v55\n"
    "buffer_load_dwordx4 v248, s[44:47], s84 offen lds\n"
    "s_max_i32 s7, s7, 0\n"
    "s_waitcnt vmcnt(11) lgkmcnt(5)\n"
    "ds_read2st64_b32 a[208:209], v38 offset0:92 offset1:93\n"
    "v_mfma_f32_4x4x4_16b_bf16 v[20:23], v[48:49], a[196:197], 0 cbsz:2 abid:0\n"
    "ds_read2st64_b32 a[210:211], v38 offset0:94 offset1:95\n"
    "ds_read_u16 v254, v235 offset:256\n"
    "v_cvt_pk_bf16_f32 v52, v28, v32\n"
    "v_cvt_pk_bf16_f32 v53, v29, v33\n"
    "v_accvgpr_write_b32 a73, v52\n"
    "v_accvgpr_write_b32 a77, v53\n"
    "v_mfma_f32_4x4x4_16b_bf16 v[24:27], v[48:49], a[198:199], 0 cbsz:2 abid:1\n"
    "buffer_load_dwordx4 v249, s[44:47], s84 offen offset:1024 lds\n"
    "v_cvt_pk_bf16_f32 v54, v30, v34\n"
    "v_cvt_pk_bf16_f32 v55, v31, v35\n"
    "v_accvgpr_write_b32 a81, v54\n"
    "v_accvgpr_write_b32 a85, v55\n"
    "s_min_i32 s7, s7, 50\n"
    "s_waitcnt vmcnt(11) lgkmcnt(6)\n"
    "ds_read2st64_b64 a[188:191], v15 offset0:48 offset1:49\n"
    "v_mfma_f32_4x4x4_16b_bf16 v[28:31], v[48:49], a[200:201], 0 cbsz:2 abid:2\n"
    "s_add_u32 m0, s83, 43008\n"
    "s_add_u32 s84, s60, s63\n"
    "v_cvt_pk_bf16_f32 v52, v20, v24\n"
    "v_cvt_pk_bf16_f32 v53, v21, v25\n"
    "v_accvgpr_write_b32 a74, v52\n"
    "v_accvgpr_write_b32 a78, v53\n"
    "v_mfma_f32_4x4x4_16b_bf16 v[32:35], v[48:49], a[202:203], 0 cbsz:2 abid:3\n"
    "buffer_load_dwordx4 v244, s[44:47], s84 offen lds\n"
    "v_cvt_pk_bf16_f32 v54, v22, v26\n"
    "v_cvt_pk_bf16_f32 v55, v23, v27\n"
    "v_accvgpr_write_b32 a82, v54\n"
    "v_accvgpr_write_b32 a86, v55\n"
    "v_lshlrev_b64 v[40:41], v16, v[250:251]\n"
    "s_bfm_b64 s[34:35], s7, 2\n"
    "s_waitcnt vmcnt(11) lgkmcnt(4)\n"
    "ds_read2st64_b64 a[192:195], v15 offset0:50 offset1:51\n"
    "v_mfma_f32_4x4x4_16b_bf16 v[20:23], v[50:51], a[204:205], 0 cbsz:2 abid:0\n"
    "v_cvt_pk_bf16_f32 v52, v28, v32\n"
    "v_cvt_pk_bf16_f32 v53, v29, v33\n"
    "v_accvgpr_write_b32 a75, v52\n"
    "v_accvgpr_write_b32 a79, v53\n"
    "v_mfma_f32_4x4x4_16b_bf16 v[24:27], v[50:51], a[206:207], 0 cbsz:2 abid:1\n"
    "buffer_load_dwordx4 v245, s[44:47], s84 offen offset:1024 lds\n"
    "v_cvt_pk_bf16_f32 v54, v30, v34\n"
    "v_cvt_pk_bf16_f32 v55, v31, v35\n"
    "v_accvgpr_write_b32 a83, v54\n"
    "v_accvgpr_write_b32 a87, v55\n"
    "v_lshlrev_b64 v[42:43], v16, v[252:253]\n"
    "s_waitcnt vmcnt(11) lgkmcnt(2)\n"
    "ds_read2st64_b64 a[196:199], v15 offset0:52 offset1:53\n"
    "v_mfma_f32_4x4x4_16b_bf16 v[28:31], v[50:51], a[208:209], 0 cbsz:2 abid:2\n"
    "v_cvt_pk_bf16_f32 v56, v20, v22\n"
    "v_cvt_pk_bf16_f32 v60, v21, v23\n"
    "v_mfma_f32_4x4x4_16b_bf16 v[32:35], v[50:51], a[210:211], 0 cbsz:2 abid:3\n"
    "buffer_load_dwordx4 v246, s[44:47], s84 offen offset:2048 lds\n"
    "v_cvt_pk_bf16_f32 v57, v24, v26\n"
    "v_cvt_pk_bf16_f32 v61, v25, v27\n"
    "buffer_load_dwordx4 v247, s[44:47], s84 offen offset:3072 lds\n"
    "v_lshlrev_b64 v[44:45], v16, v[254:255]\n"
    "s_waitcnt vmcnt(12) lgkmcnt(2)\n"
    "ds_read2st64_b64 a[200:203], v15 offset0:54 offset1:55\n"
    "v_mfma_f32_4x4x4_16b_bf16 v[20:23], v[40:41], a[188:189], 0 cbsz:2 abid:0\n"
    "ds_read_u16 v250, v18 offset:320\n"
    "s_add_u32 m0, s83, 47104\n"
    "v_cvt_pk_bf16_f32 v58, v28, v30\n"
    "v_cvt_pk_bf16_f32 v62, v29, v31\n"
    "v_mfma_f32_4x4x4_16b_bf16 v[24:27], v[40:41], a[190:191], 0 cbsz:2 abid:1\n"
    "buffer_load_dwordx4 v248, s[44:47], s84 offen lds\n"
    "v_cvt_pk_bf16_f32 v59, v32, v34\n"
    "v_cvt_pk_bf16_f32 v63, v33, v35\n"
    "s_waitcnt vmcnt(12) lgkmcnt(3)\n"
    "ds_read2st64_b32 a[204:205], v38 offset0:112 offset1:113\n"
    "v_mfma_f32_4x4x4_16b_bf16 v[28:31], v[40:41], a[192:193], 0 cbsz:2 abid:2\n"
    "ds_read2st64_b32 a[206:207], v38 offset0:114 offset1:115\n"
    "ds_read_u16 v252, v18 offset:328\n"
    "v_cvt_pk_bf16_f32 v64, v20, v24\n"
    "v_cvt_pk_bf16_f32 v68, v21, v25\n"
    "v_mfma_f32_4x4x4_16b_bf16 v[32:35], v[40:41], a[194:195], 0 cbsz:2 abid:3\n"
    "buffer_load_dwordx4 v249, s[44:47], s84 offen offset:1024 lds\n"
    "v_cvt_pk_bf16_f32 v72, v22, v26\n"
    "v_cvt_pk_bf16_f32 v76, v23, v27\n"
    "s_waitcnt vmcnt(12) lgkmcnt(5)\n"
    "ds_read2st64_b32 a[208:209], v38 offset0:116 offset1:117\n"
    "v_mfma_f32_4x4x4_16b_bf16 v[20:23], v[42:43], a[196:197], 0 cbsz:2 abid:0\n"
    "ds_read2st64_b32 a[210:211], v38 offset0:118 offset1:119\n"
    "ds_read_u16 v254, v235 offset:320\n"
    "s_add_u32 m0, s83, 49152\n"
    "s_add_u32 s84, s61, s63\n"
    "v_cvt_pk_bf16_f32 v65, v28, v32\n"
    "v_cvt_pk_bf16_f32 v69, v29, v33\n"
    "v_mfma_f32_4x4x4_16b_bf16 v[24:27], v[42:43], a[198:199], 0 cbsz:2 abid:1\n"
    "buffer_load_dwordx4 v244, s[44:47], s84 offen lds\n"
    "v_cvt_pk_bf16_f32 v73, v30, v34\n"
    "v_cvt_pk_bf16_f32 v77, v31, v35\n"
    "buffer_load_dwordx4 v245, s[44:47], s84 offen offset:1024 lds\n"
    "s_waitcnt vmcnt(13) lgkmcnt(6)\n"
    "ds_read2st64_b64 a[188:191], v15 offset0:0 offset1:1\n"
    "v_mfma_f32_4x4x4_16b_bf16 v[28:31], v[42:43], a[200:201], 0 cbsz:2 abid:2\n"
    "v_cvt_pk_bf16_f32 v66, v20, v24\n"
    "v_cvt_pk_bf16_f32 v70, v21, v25\n"
    "v_mfma_f32_4x4x4_16b_bf16 v[32:35], v[42:43], a[202:203], 0 cbsz:2 abid:3\n"
    "buffer_load_dwordx4 v246, s[44:47], s84 offen offset:2048 lds\n"
    "v_cvt_pk_bf16_f32 v74, v22, v26\n"
    "v_cvt_pk_bf16_f32 v78, v23, v27\n"
    "v_lshlrev_b64 v[46:47], v16, v[250:251]\n"
    "s_waitcnt vmcnt(13) lgkmcnt(4)\n"
    "ds_read2st64_b64 a[192:195], v15 offset0:2 offset1:3\n"
    "v_mfma_f32_4x4x4_16b_bf16 v[20:23], v[44:45], a[204:205], 0 cbsz:2 abid:0\n"
    "v_cvt_pk_bf16_f32 v67, v28, v32\n"
    "v_cvt_pk_bf16_f32 v71, v29, v33\n"
    "v_mfma_f32_4x4x4_16b_bf16 v[24:27], v[44:45], a[206:207], 0 cbsz:2 abid:1\n"
    "buffer_load_dwordx4 v247, s[44:47], s84 offen offset:3072 lds\n"
    "v_cvt_pk_bf16_f32 v75, v30, v34\n"
    "v_cvt_pk_bf16_f32 v79, v31, v35\n"
    "v_lshlrev_b64 v[48:49], v16, v[252:253]\n"
    "s_waitcnt vmcnt(13) lgkmcnt(2)\n"
    "ds_read2st64_b64 a[196:199], v15 offset0:4 offset1:5\n"
    "v_mfma_f32_4x4x4_16b_bf16 v[28:31], v[44:45], a[208:209], 0 cbsz:2 abid:2\n"
    "s_add_u32 m0, s83, 53248\n"
    "v_cvt_pk_bf16_f32 v80, v20, v22\n"
    "v_cvt_pk_bf16_f32 v84, v21, v23\n"
    "v_mfma_f32_4x4x4_16b_bf16 v[32:35], v[44:45], a[210:211], 0 cbsz:2 abid:3\n"
    "buffer_load_dwordx4 v248, s[44:47], s84 offen lds\n"
    "v_cvt_pk_bf16_f32 v81, v24, v26\n"
    "v_cvt_pk_bf16_f32 v85, v25, v27\n"
    "v_lshlrev_b64 v[50:51], v16, v[254:255]\n"
    "s_waitcnt vmcnt(13) lgkmcnt(2)\n"
    "ds_read2st64_b64 a[200:203], v15 offset0:6 offset1:7\n"
    "v_mfma_f32_4x4x4_16b_bf16 v[20:23], v[46:47], a[188:189], 0 cbsz:2 abid:0\n"
    "ds_read_u16 v250, v18 offset:384\n"
    "s_memrealtime s[92:93]\n"
    "v_cvt_pk_bf16_f32 v82, v28, v30\n"
    "v_cvt_pk_bf16_f32 v86, v29, v31\n"
    "v_mfma_f32_4x4x4_16b_bf16 v[24:27], v[46:47], a[190:191], 0 cbsz:2 abid:1\n"
    "buffer_load_dwordx4 v249, s[44:47], s84 offen offset:1024 lds\n"
    "s_add_u32 m0, s83, 55296\n"
    "s_add_u32 s84, s58, s64\n"
    "v_cvt_pk_bf16_f32 v83, v32, v34\n"
    "v_cvt_pk_bf16_f32 v87, v33, v35\n"
    "buffer_load_dwordx4 v244, s[44:47], s84 offen lds\n"
    "s_waitcnt vmcnt(14) lgkmcnt(3)\n"
    "ds_read2st64_b32 a[204:205], v38 offset0:16 offset1:17\n"
    "v_mfma_f32_4x4x4_16b_bf16 v[28:31], v[46:47], a[192:193], 0 cbsz:2 abid:2\n"
    "ds_read2st64_b32 a[206:207], v38 offset0:18 offset1:19\n"
    "ds_read_u16 v252, v18 offset:392\n"
    "v_cvt_pk_bf16_f32 v88, v20, v24\n"
    "v_cvt_pk_bf16_f32 v92, v21, v25\n"
    "v_mfma_f32_4x4x4_16b_bf16 v[32:35], v[46:47], a[194:195], 0 cbsz:2 abid:3\n"
    "buffer_load_dwordx4 v245, s[44:47], s84 offen offset:1024 lds\n"
    "v_cvt_pk_bf16_f32 v96, v22, v26\n"
    "v_cvt_pk_bf16_f32 v100, v23, v27\n"
    "s_waitcnt vmcnt(14) lgkmcnt(5)\n"
    "ds_read2st64_b32 a[208:209], v38 offset0:20 offset1:21\n"
    "v_mfma_f32_4x4x4_16b_bf16 v[20:23], v[48:49], a[196:197], 0 cbsz:2 abid:0\n"
    "ds_read2st64_b32 a[210:211], v38 offset0:22 offset1:23\n"
    "ds_read_u16 v254, v235 offset:384\n"
    "global_load_dwordx4 v[236:239], v16, s[54:55]\n"
    "v_cvt_pk_bf16_f32 v89, v28, v32\n"
    "v_cvt_pk_bf16_f32 v93, v29, v33\n"
    "v_mfma_f32_4x4x4_16b_bf16 v[24:27], v[48:49], a[198:199], 0 cbsz:2 abid:1\n"
    "buffer_load_dwordx4 v246, s[44:47], s84 offen offset:2048 lds\n"
    "v_cvt_pk_bf16_f32 v97, v30, v34\n"
    "v_cvt_pk_bf16_f32 v101, v31, v35\n"
    "s_waitcnt vmcnt(15) lgkmcnt(6)\n"
    "ds_read2st64_b64 a[188:191], v15 offset0:12 offset1:13\n"
    "v_mfma_f32_4x4x4_16b_bf16 v[28:31], v[48:49], a[200:201], 0 cbsz:2 abid:2\n"
    "v_cvt_pk_bf16_f32 v90, v20, v24\n"
    "v_cvt_pk_bf16_f32 v94, v21, v25\n"
    "v_mfma_f32_4x4x4_16b_bf16 v[32:35], v[48:49], a[202:203], 0 cbsz:2 abid:3\n"
    "buffer_load_dwordx4 v247, s[44:47], s84 offen offset:3072 lds\n"
    "v_cvt_pk_bf16_f32 v98, v22, v26\n"
    "v_cvt_pk_bf16_f32 v102, v23, v27\n"
    "v_lshlrev_b64 v[40:41], v16, v[250:251]\n"
    "s_waitcnt vmcnt(15) lgkmcnt(4)\n"
    "ds_read2st64_b64 a[192:195], v15 offset0:14 offset1:15\n"
    "v_mfma_f32_4x4x4_16b_bf16 v[20:23], v[50:51], a[204:205], 0 cbsz:2 abid:0\n"
    "s_add_u32 m0, s83, 59392\n"
    "v_cvt_pk_bf16_f32 v91, v28, v32\n"
    "v_cvt_pk_bf16_f32 v95, v29, v33\n"
    "v_mfma_f32_4x4x4_16b_bf16 v[24:27], v[50:51], a[206:207], 0 cbsz:2 abid:1\n"
    "buffer_load_dwordx4 v248, s[44:47], s84 offen lds\n"
    "v_cvt_pk_bf16_f32 v99, v30, v34\n"
    "v_cvt_pk_bf16_f32 v103, v31, v35\n"
    "buffer_load_dwordx4 v249, s[44:47], s84 offen offset:1024 lds\n"
    "v_lshlrev_b64 v[42:43], v16, v[252:253]\n"
    "s_waitcnt vmcnt(16) lgkmcnt(2)\n"
    "ds_read2st64_b64 a[196:199], v15 offset0:16 offset1:17\n"
    "v_mfma_f32_4x4x4_16b_bf16 v[28:31], v[50:51], a[208:209], 0 cbsz:2 abid:2\n"
    "s_add_u32 m0, s83, 61440\n"
    "s_add_u32 s84, s59, s64\n"
    "v_cvt_pk_bf16_f32 v104, v20, v22\n"
    "v_cvt_pk_bf16_f32 v108, v21, v23\n"
    "v_mfma_f32_4x4x4_16b_bf16 v[32:35], v[50:51], a[210:211], 0 cbsz:2 abid:3\n"
    "buffer_load_dwordx4 v244, s[44:47], s84 offen lds\n"
    "v_cvt_pk_bf16_f32 v105, v24, v26\n"
    "v_cvt_pk_bf16_f32 v109, v25, v27\n"
    "v_lshlrev_b64 v[44:45], v16, v[254:255]\n"
    "s_waitcnt vmcnt(16) lgkmcnt(2)\n"
    "ds_read2st64_b64 a[200:203], v15 offset0:18 offset1:19\n"
    "v_mfma_f32_4x4x4_16b_bf16 v[20:23], v[40:41], a[188:189], 0 cbsz:2 abid:0\n"
    "ds_read_u16 v250, v18 offset:448\n"
    "v_cvt_pk_bf16_f32 v106, v28, v30\n"
    "v_cvt_pk_bf16_f32 v110, v29, v31\n"
    "v_mfma_f32_4x4x4_16b_bf16 v[24:27], v[40:41], a[190:191], 0 cbsz:2 abid:1\n"
    "buffer_load_dwordx4 v245, s[44:47], s84 offen offset:1024 lds\n"
    "v_cvt_pk_bf16_f32 v107, v32, v34\n"
    "v_cvt_pk_bf16_f32 v111, v33, v35\n"
    "s_waitcnt vmcnt(16) lgkmcnt(3)\n"
    "ds_read2st64_b32 a[204:205], v38 offset0:40 offset1:41\n"
    "v_mfma_f32_4x4x4_16b_bf16 v[28:31], v[40:41], a[192:193], 0 cbsz:2 abid:2\n"
    "ds_read2st64_b32 a[206:207], v38 offset0:42 offset1:43\n"
    "ds_read_u16 v252, v18 offset:456\n"
    "v_cvt_pk_bf16_f32 v112, v20, v24\n"
    "v_cvt_pk_bf16_f32 v116, v21, v25\n"
    "v_mfma_f32_4x4x4_16b_bf16 v[32:35], v[40:41], a[194:195], 0 cbsz:2 abid:3\n"
    "buffer_load_dwordx4 v246, s[44:47], s84 offen offset:2048 lds\n"
    "v_cvt_pk_bf16_f32 v120, v22, v26\n"
    "v_cvt_pk_bf16_f32 v124, v23, v27\n"
    "s_waitcnt vmcnt(16) lgkmcnt(5)\n"
    "ds_read2st64_b32 a[208:209], v38 offset0:44 offset1:45\n"
    "v_mfma_f32_4x4x4_16b_bf16 v[20:23], v[42:43], a[196:197], 0 cbsz:2 abid:0\n"
    "ds_read2st64_b32 a[210:211], v38 offset0:46 offset1:47\n"
    "ds_read_u16 v254, v235 offset:448\n"
    "v_cvt_pk_bf16_f32 v113, v28, v32\n"
    "v_cvt_pk_bf16_f32 v117, v29, v33\n"
    "v_mfma_f32_4x4x4_16b_bf16 v[24:27], v[42:43], a[198:199], 0 cbsz:2 abid:1\n"
    "buffer_load_dwordx4 v247, s[44:47], s84 offen offset:3072 lds\n"
    "s_add_u32 m0, s83, 65536\n"
    "v_cvt_pk_bf16_f32 v121, v30, v34\n"
    "v_cvt_pk_bf16_f32 v125, v31, v35\n"
    "buffer_load_dwordx4 v248, s[44:47], s84 offen lds\n"
    "s_waitcnt vmcnt(17) lgkmcnt(6)\n"
    "ds_read2st64_b64 a[188:191], v15 offset0:24 offset1:25\n"
    "v_mfma_f32_4x4x4_16b_bf16 v[28:31], v[42:43], a[200:201], 0 cbsz:2 abid:2\n"
    "v_cvt_pk_bf16_f32 v114, v20, v24\n"
    "v_cvt_pk_bf16_f32 v118, v21, v25\n"
    "v_mfma_f32_4x4x4_16b_bf16 v[32:35], v[42:43], a[202:203], 0 cbsz:2 abid:3\n"
    "buffer_load_dwordx4 v249, s[44:47], s84 offen offset:1024 lds\n"
    "v_cvt_pk_bf16_f32 v122, v22, v26\n"
    "v_cvt_pk_bf16_f32 v126, v23, v27\n"
    "v_lshlrev_b64 v[46:47], v16, v[250:251]\n"
    "s_waitcnt vmcnt(17) lgkmcnt(4)\n"
    "ds_read2st64_b64 a[192:195], v15 offset0:26 offset1:27\n"
    "v_mfma_f32_4x4x4_16b_bf16 v[20:23], v[44:45], a[204:205], 0 cbsz:2 abid:0\n"
    "s_add_u32 m0, s83, 36864\n"
    "s_add_u32 s84, s60, s64\n"
    "v_cvt_pk_bf16_f32 v115, v28, v32\n"
    "v_cvt_pk_bf16_f32 v119, v29, v33\n"
    "v_mfma_f32_4x4x4_16b_bf16 v[24:27], v[44:45], a[206:207], 0 cbsz:2 abid:1\n"
    "buffer_load_dwordx4 v244, s[44:47], s84 offen lds\n"
    "v_cvt_pk_bf16_f32 v123, v30, v34\n"
    "v_cvt_pk_bf16_f32 v127, v31, v35\n"
    "v_lshlrev_b64 v[48:49], v16, v[252:253]\n"
    "s_waitcnt vmcnt(17) lgkmcnt(2)\n"
    "ds_read2st64_b64 a[196:199], v15 offset0:28 offset1:29\n"
    "v_mfma_f32_4x4x4_16b_bf16 v[28:31], v[44:45], a[208:209], 0 cbsz:2 abid:2\n"
    "v_cvt_pk_bf16_f32 v128, v20, v22\n"
    "v_cvt_pk_bf16_f32 v132, v21, v23\n"
    "v_mfma_f32_4x4x4_16b_bf16 v[32:35], v[44:45], a[210:211], 0 cbsz:2 abid:3\n"
    "buffer_load_dwordx4 v245, s[44:47], s84 offen offset:1024 lds\n"
    "v_cvt_pk_bf16_f32 v129, v24, v26\n"
    "v_cvt_pk_bf16_f32 v133, v25, v27\n"
    "v_lshlrev_b64 v[50:51], v16, v[254:255]\n"
    "s_waitcnt vmcnt(12)\n"
    "v_sub_u32 v11, s0, v236\n"
    "v_or_b32 v236, 0xffff, v236\n"
    "v_or_b32 v238, 0xffff, v238\n"
    "s_waitcnt vmcnt(17) lgkmcnt(2)\n"
    "ds_read2st64_b64 a[200:203], v15 offset0:30 offset1:31\n"
    "v_mfma_f32_4x4x4_16b_bf16 v[20:23], v[46:47], a[188:189], 0 cbsz:2 abid:0\n"
    "ds_read_u16 v250, v18 offset:512\n"
    "v_cvt_pk_bf16_f32 v130, v28, v30\n"
    "v_cvt_pk_bf16_f32 v134, v29, v31\n"
    "v_mfma_f32_4x4x4_16b_bf16 v[24:27], v[46:47], a[190:191], 0 cbsz:2 abid:1\n"
    "buffer_load_dwordx4 v246, s[44:47], s84 offen offset:2048 lds\n"
    "v_cvt_pk_bf16_f32 v131, v32, v34\n"
    "v_cvt_pk_bf16_f32 v135, v33, v35\n"
    "buffer_load_dwordx4 v247, s[44:47], s84 offen offset:3072 lds\n"
    "v_and_b32 v242, 15, v237\n"
    "v_and_b32 v243, 1, v239\n"
    "v_or_b32 v237, 15, v237\n"
    "v_or_b32 v239, 15, v239\n"
    "s_waitcnt vmcnt(18) lgkmcnt(3)\n"
    "ds_read2st64_b32 a[204:205], v38 offset0:64 offset1:65\n"
    "v_mfma_f32_4x4x4_16b_bf16 v[28:31], v[46:47], a[192:193], 0 cbsz:2 abid:2\n"
    "ds_read2st64_b32 a[206:207], v38 offset0:66 offset1:67\n"
    "ds_read_u16 v252, v18 offset:520\n"
    "s_add_u32 m0, s83, 40960\n"
    "v_cvt_pk_bf16_f32 v136, v20, v24\n"
    "v_cvt_pk_bf16_f32 v140, v21, v25\n"
    "v_mfma_f32_4x4x4_16b_bf16 v[32:35], v[46:47], a[194:195], 0 cbsz:2 abid:3\n"
    "buffer_load_dwordx4 v248, s[44:47], s84 offen lds\n"
    "v_cvt_pk_bf16_f32 v144, v22, v26\n"
    "v_cvt_pk_bf16_f32 v148, v23, v27\n"
    "v_cmp_ne_u64 vcc, s[18:19], v[236:237]\n"
    "v_cmp_ne_u64 s[48:49], s[18:19], v[238:239]\n"
    "v_readlane_b32 s9, v242, 0\n"
    "v_and_b32 v236, 1, v242\n"
    "s_waitcnt vmcnt(18) lgkmcnt(5)\n"
    "ds_read2st64_b32 a[208:209], v38 offset0:68 offset1:69\n"
    "v_mfma_f32_4x4x4_16b_bf16 v[20:23], v[48:49], a[196:197], 0 cbsz:2 abid:0\n"
    "ds_read2st64_b32 a[210:211], v38 offset0:70 offset1:71\n"
    "ds_read_u16 v254, v235 offset:512\n"
    "v_cvt_pk_bf16_f32 v137, v28, v32\n"
    "v_cvt_pk_bf16_f32 v141, v29, v33\n"
    "v_mfma_f32_4x4x4_16b_bf16 v[24:27], v[48:49], a[198:199], 0 cbsz:2 abid:1\n"
    "buffer_load_dwordx4 v249, s[44:47], s84 offen offset:1024 lds\n"
    "v_cvt_pk_bf16_f32 v145, v30, v34\n"
    "v_cvt_pk_bf16_f32 v149, v31, v35\n"
    "s_or_b64 s[48:49], s[48:49], vcc\n"
    "s_cbranch_scc1 75f\n"
    "77:\n"
    "v_cmp_eq_u32 s[24:25], 1, v236\n"
    "s_waitcnt vmcnt(18) lgkmcnt(6)\n"
    "ds_read2st64_b64 a[188:191], v15 offset0:36 offset1:37\n"
    "v_mfma_f32_4x4x4_16b_bf16 v[28:31], v[48:49], a[200:201], 0 cbsz:2 abid:2\n"
    "s_add_u32 m0, s83, 43008\n"
    "s_add_u32 s84, s61, s64\n"
    "v_cvt_pk_bf16_f32 v138, v20, v24\n"
    "201:\n"
    "s_cbranch_execz 199b\n"
    "v_cvt_pk_bf16_f32 v142, v21, v25\n"
    "v_mfma_f32_4x4x4_16b_bf16 v[32:35], v[48:49], a[202:203], 0 cbsz:2 abid:3\n"
    "buffer_load_dwordx4 v244, s[44:47], s84 offen lds\n"
    "v_cvt_pk_bf16_f32 v146, v22, v26\n"
    "v_cvt_pk_bf16_f32 v150, v23, v27\n"
    "buffer_load_dwordx4 v245, s[44:47], s84 offen offset:1024 lds\n"
    "v_lshlrev_b64 v[40:41], v16, v[250:251]\n"
    "s_lshr_b32 s9, s9, 1\n"
    "s_add_u32 s9, s9, s28\n"
    "v_lshrrev_b64 v[240:241], v232, s[24:25]\n"
    "v_and_b32 v241, 0xff, v240\n"
    "s_waitcnt vmcnt(19) lgkmcnt(4)\n"
    "ds_read2st64_b64 a[192:195], v15 offset0:38 offset1:39\n"
    "v_mfma_f32_4x4x4_16b_bf16 v[20:23], v[50:51], a[204:205], 0 cbsz:2 abid:0\n"
    "v_cvt_pk_bf16_f32 v139, v28, v32\n"
    "v_cvt_pk_bf16_f32 v143, v29, v33\n"
    "v_mfma_f32_4x4x4_16b_bf16 v[24:27], v[50:51], a[206:207], 0 cbsz:2 abid:1\n"
    "buffer_load_dwordx4 v246, s[44:47], s84 offen offset:2048 lds\n"
    "v_cvt_pk_bf16_f32 v147, v30, v34\n"
    "v_cvt_pk_bf16_f32 v151, v31, v35\n"
    "v_lshlrev_b64 v[42:43], v16, v[252:253]\n"
    "v_bfe_u32 v240, v240, v233, 4\n"
    "v_bcnt_u32_b32 v241, v241, 8\n"
    "v_bcnt_u32_b32 v240, v240, 0\n"
    "v_lshl_add_u32 v240, v240, 1, v241\n"
    "s_waitcnt vmcnt(18) lgkmcnt(2)\n"
    "ds_read2st64_b64 a[196:199], v15 offset0:40 offset1:41\n"
    "v_mfma_f32_4x4x4_16b_bf16 v[28:31], v[50:51], a[208:209], 0 cbsz:2 abid:2\n"
    "v_cvt_pk_bf16_f32 v152, v20, v22\n"
    "v_cvt_pk_bf16_f32 v156, v21, v23\n"
    "v_mfma_f32_4x4x4_16b_bf16 v[32:35], v[50:51], a[210:211], 0 cbsz:2 abid:3\n"
    "buffer_load_dwordx4 v247, s[44:47], s84 offen offset:3072 lds\n"
    "v_cvt_pk_bf16_f32 v153, v24, v26\n"
    "v_cvt_pk_bf16_f32 v157, v25, v27\n"
    "v_lshlrev_b64 v[44:45], v16, v[254:255]\n"
    "s_lshr_b32 s10, s9, 2\n"
    "v_cmp_lt_u32 vcc, 15, v240\n"
    "v_xor_b32 v234, s10, v234\n"
    "s_mov_b64 s[50:51], vcc\n"
    "s_waitcnt vmcnt(18) lgkmcnt(2)\n"
    "ds_read2st64_b64 a[200:203], v15 offset0:42 offset1:43\n"
    "v_mfma_f32_4x4x4_16b_bf16 v[20:23], v[40:41], a[188:189], 0 cbsz:2 abid:0\n"
    "ds_read_u16 v250, v18 offset:576\n"
    "s_add_u32 m0, s83, 47104\n"
    "v_cvt_pk_bf16_f32 v154, v28, v30\n"
    "v_cvt_pk_bf16_f32 v158, v29, v31\n"
    "v_mfma_f32_4x4x4_16b_bf16 v[24:27], v[40:41], a[190:191], 0 cbsz:2 abid:1\n"
    "buffer_load_dwordx4 v248, s[44:47], s84 offen lds\n"
    "v_cvt_pk_bf16_f32 v155, v32, v34\n"
    "v_cvt_pk_bf16_f32 v159, v33, v35\n"
    "v_addc_co_u32 v234, vcc, 0, v234, vcc\n"
    "v_and_b32 v234, 1, v234\n"
    "v_mad_u32_u24 v236, v234, s88, v3\n"
    "v_cmp_eq_u32 s[26:27], 1, v243\n"
    "s_waitcnt vmcnt(18) lgkmcnt(3)\n"
    "ds_read2st64_b32 a[204:205], v38 offset0:88 offset1:89\n"
    "v_mfma_f32_4x4x4_16b_bf16 v[28:31], v[40:41], a[192:193], 0 cbsz:2 abid:2\n"
    "ds_read2st64_b32 a[206:207], v38 offset0:90 offset1:91\n"
    "ds_read_u16 v252, v18 offset:584\n"
    "v_cvt_pk_bf16_f32 v160, v20, v24\n"
    "v_cvt_pk_bf16_f32 v164, v21, v25\n"
    "v_mfma_f32_4x4x4_16b_bf16 v[32:35], v[40:41], a[194:195], 0 cbsz:2 abid:3\n"
    "buffer_load_dwordx4 v249, s[44:47], s84 offen offset:1024 lds\n"
    "v_cvt_pk_bf16_f32 v168, v22, v26\n"
    "v_cvt_pk_bf16_f32 v172, v23, v27\n"
    "v_readlane_b32 s82, v236, 0\n"
    "v_readlane_b32 s73, v236, 1\n"
    "s_waitcnt vmcnt(18) lgkmcnt(5)\n"
    "ds_read2st64_b32 a[208:209], v38 offset0:92 offset1:93\n"
    "v_mfma_f32_4x4x4_16b_bf16 v[20:23], v[42:43], a[196:197], 0 cbsz:2 abid:0\n"
    "ds_read2st64_b32 a[210:211], v38 offset0:94 offset1:95\n"
    "ds_read_u16 v254, v235 offset:576\n"
    "s_barrier\n"
    "s_mov_b32 m0, s57\n"
    "v_cvt_pk_bf16_f32 v161, v28, v32\n"
    "v_cvt_pk_bf16_f32 v165, v29, v33\n"
    "v_mfma_f32_4x4x4_16b_bf16 v[24:27], v[42:43], a[198:199], 0 cbsz:2 abid:1\n"
    "buffer_load_dwordx4 v0, s[36:39], s82 offen nt lds\n"
    "v_cvt_pk_bf16_f32 v169, v30, v34\n"
    "v_cvt_pk_bf16_f32 v173, v31, v35\n"
    "s_waitcnt vmcnt(18) lgkmcnt(6)\n"
    "ds_read2st64_b64 a[188:191], v15 offset0:48 offset1:49\n"
    "v_mfma_f32_4x4x4_16b_bf16 v[28:31], v[42:43], a[200:201], 0 cbsz:2 abid:2\n"
    "v_cvt_pk_bf16_f32 v162, v20, v24\n"
    "v_cvt_pk_bf16_f32 v166, v21, v25\n"
    "v_mfma_f32_4x4x4_16b_bf16 v[32:35], v[42:43], a[202:203], 0 cbsz:2 abid:3\n"
    "buffer_load_dwordx4 v1, s[36:39], s82 offen offset:1024 nt lds\n"
    "v_cvt_pk_bf16_f32 v170, v22, v26\n"
    "v_cvt_pk_bf16_f32 v174, v23, v27\n"
    "v_lshlrev_b64 v[46:47], v16, v[250:251]\n"
    "v_cmp_gt_u32 s[48:49], s96, v232\n"
    "v_lshrrev_b64 v[240:241], v232, s[26:27]\n"
    "v_and_b32 v241, 0xff, v240\n"
    "v_bfe_u32 v240, v240, v233, 4\n"
    "s_waitcnt vmcnt(18) lgkmcnt(4)\n"
    "ds_read2st64_b64 a[192:195], v15 offset0:50 offset1:51\n"
    "v_mfma_f32_4x4x4_16b_bf16 v[20:23], v[44:45], a[204:205], 0 cbsz:2 abid:0\n"
    "v_cvt_pk_bf16_f32 v163, v28, v32\n"
    "v_cvt_pk_bf16_f32 v167, v29, v33\n"
    "v_mfma_f32_4x4x4_16b_bf16 v[24:27], v[44:45], a[206:207], 0 cbsz:2 abid:1\n"
    "buffer_load_dwordx4 v2, s[36:39], s82 offen offset:2048 nt lds\n"
    "v_cvt_pk_bf16_f32 v171, v30, v34\n"
    "v_cvt_pk_bf16_f32 v175, v31, v35\n"
    "v_lshlrev_b64 v[48:49], v16, v[252:253]\n"
    "v_bcnt_u32_b32 v241, v241, 8\n"
    "v_bcnt_u32_b32 v240, v240, 0\n"
    "v_lshl_add_u32 v240, v240, 1, v241\n"
    "v_cmp_lt_u32 s[26:27], 15, v240\n"
    "s_waitcnt vmcnt(18) lgkmcnt(2)\n"
    "ds_read2st64_b64 a[196:199], v15 offset0:52 offset1:53\n"
    "v_mfma_f32_4x4x4_16b_bf16 v[28:31], v[44:45], a[208:209], 0 cbsz:2 abid:2\n"
    "v_cvt_pk_bf16_f32 v176, v20, v22\n"
    "v_cvt_pk_bf16_f32 v180, v21, v23\n"
    "v_mfma_f32_4x4x4_16b_bf16 v[32:35], v[44:45], a[210:211], 0 cbsz:2 abid:3\n"
    "v_cvt_pk_bf16_f32 v177, v24, v26\n"
    "v_cvt_pk_bf16_f32 v181, v25, v27\n"
    "v_lshlrev_b64 v[50:51], v16, v[254:255]\n"
    "s_bfm_b64 s[24:25], s52, 0\n"
    "s_bitcmp1_b32 s9, 0\n"
    "s_cselect_b32 s95, -1, 1\n"
    "s_waitcnt vmcnt(17) lgkmcnt(2)\n"
    "ds_read2st64_b64 a[200:203], v15 offset0:54 offset1:55\n"
    "v_mfma_f32_4x4x4_16b_bf16 v[20:23], v[46:47], a[188:189], 0 cbsz:2 abid:0\n"
    "ds_read_u16 v250, v18 offset:640\n"
    "v_cvt_pk_bf16_f32 v178, v28, v30\n"
    "v_cvt_pk_bf16_f32 v182, v29, v31\n"
    "v_mfma_f32_4x4x4_16b_bf16 v[24:27], v[46:47], a[190:191], 0 cbsz:2 abid:1\n"
    "v_cvt_pk_bf16_f32 v179, v32, v34\n"
    "v_cvt_pk_bf16_f32 v183, v33, v35\n"
    "s_cmp_lt_u32 s52, 12\n"
    "s_cselect_b32 s95, 0, s95\n"
    "s_add_u32 s52, s52, s95\n"
    "s_xor_b64 s[26:27], s[26:27], s[50:51]\n"
    "s_waitcnt vmcnt(16) lgkmcnt(3)\n"
    "ds_read2st64_b32 a[204:205], v38 offset0:112 offset1:113\n"
    "v_mfma_f32_4x4x4_16b_bf16 v[28:31], v[46:47], a[192:193], 0 cbsz:2 abid:2\n"
    "ds_read2st64_b32 a[206:207], v38 offset0:114 offset1:115\n"
    "ds_read_u16 v252, v18 offset:648\n"
    "v_cvt_pk_bf16_f32 v184, v20, v24\n"
    "v_cvt_pk_bf16_f32 v188, v21, v25\n"
    "v_mfma_f32_4x4x4_16b_bf16 v[32:35], v[46:47], a[194:195], 0 cbsz:2 abid:3\n"
    "v_cvt_pk_bf16_f32 v192, v22, v26\n"
    "v_cvt_pk_bf16_f32 v196, v23, v27\n"
    "s_and_b64 s[48:49], s[48:49], s[24:25]\n"
    "s_and_b64 s[24:25], s[24:25], s[26:27]\n"
    "s_andn2_b64 s[48:49], s[48:49], s[26:27]\n"
    "s_bcnt1_i32_b64 s10, s[48:49]\n"
    "s_waitcnt vmcnt(15) lgkmcnt(5)\n"
    "ds_read2st64_b32 a[208:209], v38 offset0:116 offset1:117\n"
    "v_mfma_f32_4x4x4_16b_bf16 v[20:23], v[48:49], a[196:197], 0 cbsz:2 abid:0\n"
    "ds_read2st64_b32 a[210:211], v38 offset0:118 offset1:119\n"
    "ds_read_u16 v254, v235 offset:640\n"
    "v_cvt_pk_bf16_f32 v185, v28, v32\n"
    "v_cvt_pk_bf16_f32 v189, v29, v33\n"
    "v_mfma_f32_4x4x4_16b_bf16 v[24:27], v[48:49], a[198:199], 0 cbsz:2 abid:1\n"
    "v_cvt_pk_bf16_f32 v193, v30, v34\n"
    "v_cvt_pk_bf16_f32 v197, v31, v35\n"
    "s_cmp_lt_u32 s10, 4\n"
    "s_cselect_b64 s[34:35], s[34:35], 0\n"
    "s_cselect_b32 s96, 1, 0\n"
    "s_and_b64 vcc, s[34:35], s[26:27]\n"
    "s_waitcnt vmcnt(14) lgkmcnt(6)\n"
    "ds_read2st64_b64 a[188:191], v15 offset0:0 offset1:1\n"
    "v_mfma_f32_4x4x4_16b_bf16 v[28:31], v[48:49], a[200:201], 0 cbsz:2 abid:2\n"
    "v_cvt_pk_bf16_f32 v186, v20, v24\n"
    "v_cvt_pk_bf16_f32 v190, v21, v25\n"
    "v_mfma_f32_4x4x4_16b_bf16 v[32:35], v[48:49], a[202:203], 0 cbsz:2 abid:3\n"
    "v_cvt_pk_bf16_f32 v194, v22, v26\n"
    "v_cvt_pk_bf16_f32 v198, v23, v27\n"
    "v_lshlrev_b64 v[40:41], v16, v[250:251]\n"
    "v_addc_co_u32 v234, vcc, 0, v234, vcc\n"
    "v_cmp_le_u32 vcc, s52, v19\n"
    "v_subrev_u32 v240, 15, v240\n"
    "v_cmp_gt_u32 s[48:49], 3, v240\n"
    "s_waitcnt vmcnt(13) lgkmcnt(4)\n"
    "ds_read2st64_b64 a[192:195], v15 offset0:2 offset1:3\n"
    "v_mfma_f32_4x4x4_16b_bf16 v[20:23], v[50:51], a[204:205], 0 cbsz:2 abid:0\n"
    "v_cvt_pk_bf16_f32 v187, v28, v32\n"
    "v_cvt_pk_bf16_f32 v191, v29, v33\n"
    "v_mfma_f32_4x4x4_16b_bf16 v[24:27], v[50:51], a[206:207], 0 cbsz:2 abid:1\n"
    "v_cvt_pk_bf16_f32 v195, v30, v34\n"
    "v_cvt_pk_bf16_f32 v199, v31, v35\n"
    "v_lshlrev_b64 v[42:43], v16, v[252:253]\n"
    "s_or_b64 s[26:27], s[26:27], s[48:49]\n"
    "v_and_b32 v234, 1, v234\n"
    "v_mov_b32 v236, s33\n"
    "v_mad_u32_u24 v3, v234, s88, v3\n"
    "s_waitcnt vmcnt(12) lgkmcnt(2)\n"
    "ds_read2st64_b64 a[196:199], v15 offset0:4 offset1:5\n"
    "v_mfma_f32_4x4x4_16b_bf16 v[28:31], v[50:51], a[208:209], 0 cbsz:2 abid:2\n"
    "v_cvt_pk_bf16_f32 v200, v20, v22\n"
    "v_cvt_pk_bf16_f32 v204, v21, v23\n"
    "v_mfma_f32_4x4x4_16b_bf16 v[32:35], v[50:51], a[210:211], 0 cbsz:2 abid:3\n"
    "v_cvt_pk_bf16_f32 v201, v24, v26\n"
    "v_cvt_pk_bf16_f32 v205, v25, v27\n"
    "v_lshlrev_b64 v[44:45], v16, v[254:255]\n"
    "v_cndmask_b32 v3, v3, v236, vcc\n"
    "s_nop 0\n"
    "v_readlane_b32 s74, v3, 2\n"
    "v_readlane_b32 s75, v3, 3\n"
    "s_waitcnt vmcnt(11) lgkmcnt(2)\n"
    "ds_read2st64_b64 a[200:203], v15 offset0:6 offset1:7\n"
    "v_mfma_f32_4x4x4_16b_bf16 v[20:23], v[40:41], a[188:189], 0 cbsz:2 abid:0\n"
    "ds_read_u16 v250, v18 offset:704\n"
    "v_cvt_pk_bf16_f32 v202, v28, v30\n"
    "v_cvt_pk_bf16_f32 v206, v29, v31\n"
    "v_mfma_f32_4x4x4_16b_bf16 v[24:27], v[40:41], a[190:191], 0 cbsz:2 abid:1\n"
    "v_cvt_pk_bf16_f32 v203, v32, v34\n"
    "v_cvt_pk_bf16_f32 v207, v33, v35\n"
    "v_readlane_b32 s81, v3, 4\n"
    "s_waitcnt vmcnt(10) lgkmcnt(3)\n"
    "ds_read2st64_b32 a[204:205], v38 offset0:16 offset1:17\n"
    "v_mfma_f32_4x4x4_16b_bf16 v[28:31], v[40:41], a[192:193], 0 cbsz:2 abid:2\n"
    "ds_read2st64_b32 a[206:207], v38 offset0:18 offset1:19\n"
    "ds_read_u16 v252, v18 offset:712\n"
    "v_cvt_pk_bf16_f32 v208, v20, v24\n"
    "v_cvt_pk_bf16_f32 v212, v21, v25\n"
    "v_mfma_f32_4x4x4_16b_bf16 v[32:35], v[40:41], a[194:195], 0 cbsz:2 abid:3\n"
    "v_cvt_pk_bf16_f32 v216, v22, v26\n"
    "v_cvt_pk_bf16_f32 v220, v23, v27\n"
    "s_waitcnt vmcnt(9) lgkmcnt(5)\n"
    "ds_read2st64_b32 a[208:209], v38 offset0:20 offset1:21\n"
    "v_mfma_f32_4x4x4_16b_bf16 v[20:23], v[42:43], a[196:197], 0 cbsz:2 abid:0\n"
    "ds_read2st64_b32 a[210:211], v38 offset0:22 offset1:23\n"
    "ds_read_u16 v254, v235 offset:704\n"
    "v_cvt_pk_bf16_f32 v209, v28, v32\n"
    "v_cvt_pk_bf16_f32 v213, v29, v33\n"
    "v_mfma_f32_4x4x4_16b_bf16 v[24:27], v[42:43], a[198:199], 0 cbsz:2 abid:1\n"
    "v_cvt_pk_bf16_f32 v217, v30, v34\n"
    "v_cvt_pk_bf16_f32 v221, v31, v35\n"
    "s_waitcnt vmcnt(8) lgkmcnt(6)\n"
    "ds_read2st64_b64 a[188:191], v15 offset0:12 offset1:13\n"
    "v_mfma_f32_4x4x4_16b_bf16 v[28:31], v[42:43], a[200:201], 0 cbsz:2 abid:2\n"
    "s_nop 0\n"
    "v_cvt_pk_bf16_f32 v210, v20, v24\n"
    "v_cvt_pk_bf16_f32 v214, v21, v25\n"
    "v_mfma_f32_4x4x4_16b_bf16 v[32:35], v[42:43], a[202:203], 0 cbsz:2 abid:3\n"
    "v_cvt_pk_bf16_f32 v218, v22, v26\n"
    "v_cvt_pk_bf16_f32 v222, v23, v27\n"
    "v_lshlrev_b64 v[46:47], v16, v[250:251]\n"
    "s_waitcnt vmcnt(7) lgkmcnt(4)\n"
    "ds_read2st64_b64 a[192:195], v15 offset0:14 offset1:15\n"
    "v_mfma_f32_4x4x4_16b_bf16 v[20:23], v[44:45], a[204:205], 0 cbsz:2 abid:0\n"
    "s_add_u32 m0, s57, 12288\n"
    "v_cvt_pk_bf16_f32 v211, v28, v32\n"
    "v_cvt_pk_bf16_f32 v215, v29, v33\n"
    "v_mfma_f32_4x4x4_16b_bf16 v[24:27], v[44:45], a[206:207], 0 cbsz:2 abid:1\n"
    "buffer_load_dwordx4 v0, s[36:39], s73 offen nt lds\n"
    "v_cvt_pk_bf16_f32 v219, v30, v34\n"
    "v_cvt_pk_bf16_f32 v223, v31, v35\n"
    "v_lshlrev_b64 v[48:49], v16, v[252:253]\n"
    "s_waitcnt vmcnt(7) lgkmcnt(2)\n"
    "ds_read2st64_b64 a[196:199], v15 offset0:16 offset1:17\n"
    "v_mfma_f32_4x4x4_16b_bf16 v[28:31], v[44:45], a[208:209], 0 cbsz:2 abid:2\n"
    "v_cvt_pk_bf16_f32 v224, v20, v22\n"
    "v_cvt_pk_bf16_f32 v228, v21, v23\n"
    "v_mfma_f32_4x4x4_16b_bf16 v[32:35], v[44:45], a[210:211], 0 cbsz:2 abid:3\n"
    "buffer_load_dwordx4 v1, s[36:39], s73 offen offset:1024 nt lds\n"
    "v_cvt_pk_bf16_f32 v225, v24, v26\n"
    "v_cvt_pk_bf16_f32 v229, v25, v27\n"
    "v_lshlrev_b64 v[50:51], v16, v[254:255]\n"
    "s_waitcnt vmcnt(7) lgkmcnt(2)\n"
    "ds_read2st64_b64 a[200:203], v15 offset0:18 offset1:19\n"
    "v_mfma_f32_4x4x4_16b_bf16 v[20:23], v[46:47], a[188:189], 0 cbsz:2 abid:0\n"
    "v_cvt_pk_bf16_f32 v226, v28, v30\n"
    "v_cvt_pk_bf16_f32 v230, v29, v31\n"
    "v_mfma_f32_4x4x4_16b_bf16 v[24:27], v[46:47], a[190:191], 0 cbsz:2 abid:1\n"
    "buffer_load_dwordx4 v2, s[36:39], s73 offen offset:2048 nt lds\n"
    "v_cvt_pk_bf16_f32 v227, v32, v34\n"
    "v_cvt_pk_bf16_f32 v231, v33, v35\n"
    "s_waitcnt vmcnt(7) lgkmcnt(2)\n"
    "ds_read2st64_b32 a[204:205], v38 offset0:40 offset1:41\n"
    "v_mfma_f32_4x4x4_16b_bf16 v[28:31], v[46:47], a[192:193], 0 cbsz:2 abid:2\n"
    "ds_read2st64_b32 a[206:207], v38 offset0:42 offset1:43\n"
    "v_cvt_pk_bf16_f32 v232, v20, v24\n"
    "v_cvt_pk_bf16_f32 v236, v21, v25\n"
    "v_mfma_f32_4x4x4_16b_bf16 v[32:35], v[46:47], a[194:195], 0 cbsz:2 abid:3\n"
    "v_cvt_pk_bf16_f32 v240, v22, v26\n"
    "v_cvt_pk_bf16_f32 v244, v23, v27\n"
    "s_waitcnt vmcnt(6) lgkmcnt(3)\n"
    "ds_read2st64_b32 a[208:209], v38 offset0:44 offset1:45\n"
    "v_mfma_f32_4x4x4_16b_bf16 v[20:23], v[48:49], a[196:197], 0 cbsz:2 abid:0\n"
    "ds_read2st64_b32 a[210:211], v38 offset0:46 offset1:47\n"
    "v_cvt_pk_bf16_f32 v233, v28, v32\n"
    "v_cvt_pk_bf16_f32 v237, v29, v33\n"
    "v_mfma_f32_4x4x4_16b_bf16 v[24:27], v[48:49], a[198:199], 0 cbsz:2 abid:1\n"
    "v_cvt_pk_bf16_f32 v241, v30, v34\n"
    "v_cvt_pk_bf16_f32 v245, v31, v35\n"
    "s_waitcnt lgkmcnt(4)\n"
    "v_mfma_f32_4x4x4_16b_bf16 v[28:31], v[48:49], a[200:201], 0 cbsz:2 abid:2\n"
    "s_nop 1\n"
    "v_cvt_pk_bf16_f32 v234, v20, v24\n"
    "v_cvt_pk_bf16_f32 v238, v21, v25\n"
    "v_mfma_f32_4x4x4_16b_bf16 v[32:35], v[48:49], a[202:203], 0 cbsz:2 abid:3\n"
    "v_cvt_pk_bf16_f32 v242, v22, v26\n"
    "v_cvt_pk_bf16_f32 v246, v23, v27\n"
    "s_waitcnt lgkmcnt(2)\n"
    "v_mfma_f32_4x4x4_16b_bf16 v[20:23], v[50:51], a[204:205], 0 cbsz:2 abid:0\n"
    "s_nop 1\n"
    "v_cvt_pk_bf16_f32 v235, v28, v32\n"
    "v_cvt_pk_bf16_f32 v239, v29, v33\n"
    "v_mfma_f32_4x4x4_16b_bf16 v[24:27], v[50:51], a[206:207], 0 cbsz:2 abid:1\n"
    "v_cvt_pk_bf16_f32 v243, v30, v34\n"
    "v_cvt_pk_bf16_f32 v247, v31, v35\n"
    "s_waitcnt lgkmcnt(0)\n"
    "v_mfma_f32_4x4x4_16b_bf16 v[28:31], v[50:51], a[208:209], 0 cbsz:2 abid:2\n"
    "s_nop 1\n"
    "v_cvt_pk_bf16_f32 v248, v20, v22\n"
    "v_cvt_pk_bf16_f32 v252, v21, v23\n"
    "v_mfma_f32_4x4x4_16b_bf16 v[32:35], v[50:51], a[210:211], 0 cbsz:2 abid:3\n"
    "v_cvt_pk_bf16_f32 v249, v24, v26\n"
    "v_cvt_pk_bf16_f32 v253, v25, v27\n"
    "s_waitcnt lgkmcnt(0)\n"
    "s_add_u32 s89, s92, 250\n"
    "v_add_u32 v38, s11, v38\n"
    "s_nop 0\n"
    "v_cvt_pk_bf16_f32 v250, v28, v30\n"
    "v_cvt_pk_bf16_f32 v254, v29, v31\n"
    "v_cvt_pk_bf16_f32 v251, v32, v34\n"
    "v_cvt_pk_bf16_f32 v255, v33, v35\n"
    "s_waitcnt vmcnt(3)\n"
    "s_barrier\n"
    "ds_read_b128 a[88:91], v4\n"
    "ds_read_b128 a[92:95], v5\n"
    "ds_read_b128 a[96:99], v6\n"
    "ds_read_b128 a[100:103], v7\n"
    "ds_read_b128 a[104:107], v4 offset:256\n"
    "ds_read_b128 a[108:111], v5 offset:256\n"
    "ds_read_b128 a[112:115], v6 offset:256\n"
    "ds_read_b128 a[116:119], v7 offset:256\n"
    "ds_read_b128 a[120:123], v4 offset:512\n"
    "ds_read_b128 a[124:127], v5 offset:512\n"
    "ds_read_b128 a[128:131], v6 offset:512\n"
    "ds_read_b128 a[132:135], v7 offset:512\n"
    "s_waitcnt lgkmcnt(11)\n"
    "v_mfma_f32_16x16x32_bf16 v[20:23], a[88:91], a[16:19], 0\n"
    "v_add_u32 v14, 1, v14\n"
    "s_sub_i32 s46, s52, 5\n"
    "s_max_i32 s46, s46, 0\n"
    "s_waitcnt lgkmcnt(10)\n"
    "v_mfma_f32_16x16x32_bf16 v[20:23], a[92:95], a[40:43], v[20:23]\n"
    "s_add_u32 m0, s57, 24576\n"
    "s_bfm_b64 s[44:45], s46, 5\n"
    "v_subrev_u32 v43, 5, v19\n"
    "s_waitcnt lgkmcnt(9)\n"
    "v_mfma_f32_16x16x32_bf16 v[20:23], a[96:99], a[64:67], v[20:23]\n"
    "buffer_load_dwordx4 v0, s[36:39], s74 offen nt lds\n"
    "s_andn2_b64 s[50:51], s[44:45], s[26:27]\n"
    "s_waitcnt lgkmcnt(8)\n"
    "v_mfma_f32_16x16x32_bf16 v[20:23], a[100:103], v[56:59], v[20:23]\n"
    "s_and_b64 s[48:49], s[44:45], s[26:27]\n"
    "s_bcnt1_i32_b64 s47, s[50:51]\n"
    "s_sub_u32 s46, s46, s47\n"
    "s_waitcnt lgkmcnt(7)\n"
    "v_mfma_f32_16x16x32_bf16 v[20:23], a[104:107], v[80:83], v[20:23]\n"
    "buffer_load_dwordx4 v1, s[36:39], s74 offen offset:1024 nt lds\n"
    "s_add_u32 s58, s46, 5\n"
    "s_waitcnt lgkmcnt(6)\n"
    "v_mfma_f32_16x16x32_bf16 v[20:23], a[108:111], v[104:107], v[20:23]\n"
    "v_mov_b32 v45, s58\n"
    "v_mbcnt_lo_u32_b32 v47, s48, 5\n"
    "v_mbcnt_lo_u32_b32 v45, s50, v45\n"
    "s_waitcnt lgkmcnt(5)\n"
    "v_mfma_f32_16x16x32_bf16 v[20:23], a[112:115], v[128:131], v[20:23]\n"
    "buffer_load_dwordx4 v2, s[36:39], s74 offen offset:2048 nt lds\n"
    "v_mbcnt_hi_u32_b32 v47, s49, v47\n"
    "s_waitcnt lgkmcnt(4)\n"
    "v_mfma_f32_16x16x32_bf16 v[20:23], a[116:119], v[152:155], v[20:23]\n"
    "v_mbcnt_hi_u32_b32 v45, s51, v45\n"
    "v_cndmask_b32 v47, v19, v47, s[48:49]\n"
    "s_sub_i32 s59, s47, 4\n"
    "s_waitcnt lgkmcnt(3)\n"
    "v_mfma_f32_16x16x32_bf16 v[20:23], a[120:123], v[176:179], v[20:23]\n"
    "v_cndmask_b32 v47, v47, v45, s[50:51]\n"
    "s_max_i32 s59, s59, 0\n"
    "v_lshlrev_b32 v47, 2, v47\n"
    "s_waitcnt lgkmcnt(2)\n"
    "v_mfma_f32_16x16x32_bf16 v[20:23], a[124:127], v[200:203], v[20:23]\n"
    "s_mul_i32 s59, s59, 9\n"
    "s_min_u32 s59, s59, 256\n"
    "ds_permute_b32 v3, v47, v3\n"
    "s_waitcnt lgkmcnt(2)\n"
    "v_mfma_f32_16x16x32_bf16 v[20:23], a[128:131], v[224:227], v[20:23]\n"
    "s_bcnt1_i32_b64 s73, s[24:25]\n"
    "s_mul_i32 s62, s73, 11\n"
    "s_lshr_b32 s62, s62, 5\n"
    "s_waitcnt lgkmcnt(1)\n"
    "v_mfma_f32_16x16x32_bf16 v[20:23], a[132:135], v[248:251], v[20:23]\n"
    "s_add_u32 s62, s62, 42\n"
    "s_sub_u32 s63, s62, 25\n"
    "v_bfe_i32 v11, v11, 0, 16\n"
    "v_mfma_f32_16x16x32_bf16 v[24:27], a[132:135], v[252:255], 0\n"
    "s_nop 0\n"
    "v_readlane_b32 s7, v11, 0\n"
    "v_readlane_b32 s8, v11, 1\n"
    "v_mfma_f32_16x16x32_bf16 v[24:27], a[128:131], v[228:231], v[24:27]\n"
    "v_readlane_b32 s60, v11, 2\n"
    "v_readlane_b32 s61, v11, 3\n"
    "s_add_i32 s7, s7, s8\n"
    "v_mfma_f32_16x16x32_bf16 v[24:27], a[124:127], v[204:207], v[24:27]\n"
    "s_add_i32 s8, s60, s61\n"
    "s_add_i32 s7, s7, s8\n"
    "s_ashr_i32 s7, s7, 2\n"
    "v_mfma_f32_16x16x32_bf16 v[24:27], a[120:123], v[180:183], v[24:27]\n"
    "s_sub_u32 s7, s0, s7\n"
    "s_add_u32 s7, s7, s62\n"
    "s_add_u32 s1, s7, 444\n"
    "v_mfma_f32_16x16x32_bf16 v[24:27], a[116:119], v[156:159], v[24:27]\n"
    "s_mov_b32 s72, 0\n"
    "v_mul_i32_i24 v40, s59, v43\n"
    "s_mul_i32 s7, s2, 9\n"
    "v_mfma_f32_16x16x32_bf16 v[24:27], a[112:115], v[132:135], v[24:27]\n"
    "s_and_b32 s7, s7, 0xff\n"
    "v_add_u32 v40, s7, v40\n"
    "v_subrev_u32 v42, s46, v43\n"
    "v_mfma_f32_16x16x32_bf16 v[24:27], a[108:111], v[108:111], v[24:27]\n"
    "v_add_u32 v41, s59, v40\n"
    "v_ashrrev_i32 v40, 8, v40\n"
    "v_ashrrev_i32 v41, 8, v41\n"
    "v_mfma_f32_16x16x32_bf16 v[24:27], a[104:107], v[84:87], v[24:27]\n"
    "v_min_i32 v40, s47, v40\n"
    "v_min_i32 v41, s47, v41\n"
    "v_max_i32 v40, v40, v42\n"
    "v_mfma_f32_16x16x32_bf16 v[24:27], a[100:103], v[60:63], v[24:27]\n"
    "v_add_u32 v42, 1, v42\n"
    "v_max_i32 v41, v41, v42\n"
    "v_cmp_gt_i32 s[60:61], v41, v40\n"
    "v_mfma_f32_16x16x32_bf16 v[24:27], a[96:99], a[68:71], v[24:27]\n"
    "s_waitcnt vmcnt(3)\n"
    "v_add_u32 v41, s58, v40\n"
    "v_sub_u32 v40, v19, v40\n"
    "v_mfma_f32_16x16x32_bf16 v[24:27], a[92:95], a[44:47], v[24:27]\n"
    "ds_write_b32 v13, v14\n"
    "v_cndmask_b32 v40, v40, v41, s[60:61]\n"
    "v_mfma_f32_16x16x32_bf16 v[24:27], a[88:91], a[20:23], v[24:27]\n"
    "s_add_u32 m0, s57, 36864\n"
    "v_cndmask_b32 v40, v19, v40, s[44:45]\n"
    "v_lshlrev_b32 v40, 2, v40\n"
    "v_mfma_f32_16x16x32_bf16 v[28:31], a[88:91], a[0:3], 0\n"
    "buffer_load_dwordx4 v0, s[36:39], s75 offen nt lds\n"
    "v_mfma_f32_16x16x32_bf16 v[28:31], a[92:95], a[24:27], v[28:31]\n"
    "s_waitcnt lgkmcnt(1)\n"
    "ds_bpermute_b32 v3, v40, v3\n"
    "s_mul_i32 s94, s73, 9\n"
    "v_mfma_f32_16x16x32_bf16 v[28:31], a[96:99], a[48:51], v[28:31]\n"
    "buffer_load_dwordx4 v1, s[36:39], s75 offen offset:1024 nt lds\n"
    "s_mul_i32 s31, s73, 5\n"
    "v_mfma_f32_16x16x32_bf16 v[28:31], a[100:103], a[72:75], v[28:31]\n"
    "v_cvt_pk_bf16_f32 v52, v20, v24\n"
    "s_lshr_b32 s94, s94, 5\n"
    "s_lshr_b32 s31, s31, 5\n"
    "v_mfma_f32_16x16x32_bf16 v[28:31], a[104:107], v[64:67], v[28:31]\n"
    "ds_read_b32 v15, v12\n"
    "s_mul_i32 s7, s95, -2\n"
    "s_add_u32 s7, s7, 67\n"
    "v_mfma_f32_16x16x32_bf16 v[28:31], a[108:111], v[88:91], v[28:31]\n"
    "v_cvt_pk_bf16_f32 v53, v21, v25\n"
    "s_add_u32 s94, s94, s7\n"
    "s_add_u32 s31, s31, s7\n"
    "v_mfma_f32_16x16x32_bf16 v[28:31], a[112:115], v[112:115], v[28:31]\n"
    "v_cvt_pk_bf16_f32 v54, v22, v26\n"
    "s_sub_u32 s7, 7, s72\n"
    "s_add_u32 s31, s31, s7\n"
    "v_mfma_f32_16x16x32_bf16 v[28:31], a[116:119], v[136:139], v[28:31]\n"
    "v_cvt_pk_bf16_f32 v55, v23, v27\n"
    "s_waitcnt lgkmcnt(0)\n"
    "v_cmp_lt_u32 vcc, v15, v14\n"
    "v_mfma_f32_16x16x32_bf16 v[28:31], a[120:123], v[160:163], v[28:31]\n"
    "s_add_u32 s76, s52, 4\n"
    "v_subrev_u32 v18, 5, v19\n"
    "s_mov_b32 s30, s94\n"
    "v_mfma_f32_16x16x32_bf16 v[28:31], a[124:127], v[184:187], v[28:31]\n"
    "s_cbranch_vccnz 81b\n"
    "82:\n"
    "s_add_u32 s94, s94, 12\n"
    "v_mfma_f32_16x16x32_bf16 v[28:31], a[128:131], v[208:211], v[28:31]\n"
    "ds_write2st64_b32 v38, v52, v53 offset0:0 offset1:2\n"
    "s_sub_u32 s7, s52, 5\n"
    "v_mfma_f32_16x16x32_bf16 v[28:31], a[132:135], v[232:235], v[28:31]\n"
    "ds_read_b128 a[136:139], v4 offset:12288\n"
    "s_mov_b64 vcc, s[12:13]\n"
    "s_andn2_b32 s7, s7, 1\n"
    "v_mfma_f32_16x16x32_bf16 v[32:35], a[132:135], v[236:239], 0\n"
    "ds_write2st64_b32 v38, v54, v55 offset0:4 offset1:6\n"
    "v_mfma_f32_16x16x32_bf16 v[32:35], a[128:131], v[212:215], v[32:35]\n"
    "ds_read_b128 a[140:143], v5 offset:12288\n"
    "v_add_u32 v14, 1, v14\n"
    "v_mfma_f32_16x16x32_bf16 v[32:35], a[124:127], v[188:191], v[32:35]\n"
    "buffer_load_dwordx4 v2, s[36:39], s75 offen offset:2048 nt lds\n"
    "v_mfma_f32_16x16x32_bf16 v[32:35], a[120:123], v[164:167], v[32:35]\n"
    "ds_read_b128 a[144:147], v6 offset:12288\n"
    "buffer_load_dword a212, off, s[36:39], s33\n"
    "v_mfma_f32_16x16x32_bf16 v[32:35], a[116:119], v[140:143], v[32:35]\n"
    "buffer_load_dword a212, off, s[36:39], s33\n"
    "buffer_load_dword a212, off, s[36:39], s33\n"
    "v_mfma_f32_16x16x32_bf16 v[32:35], a[112:115], v[116:119], v[32:35]\n"
    "s_cmp_lt_i32 s7, 2\n"
    "s_cselect_b32 s7, 0x1000, s7\n"
    "v_mfma_f32_16x16x32_bf16 v[32:35], a[108:111], v[92:95], v[32:35]\n"
    "ds_read_b128 a[148:151], v7 offset:12288\n"
    "s_cmp_lt_u32 s47, 3\n"
    "s_cselect_b32 s7, 0x1000, s7\n"
    "v_mfma_f32_16x16x32_bf16 v[32:35], a[104:107], v[68:71], v[32:35]\n"
    "s_add_u32 s70, s7, -1\n"
    "v_subrev_u32 v40, s70, v18\n"
    "v_cmp_gt_u32 s[60:61], 2, v40\n"
    "v_mfma_f32_16x16x32_bf16 v[32:35], a[100:103], a[76:79], v[32:35]\n"
    "ds_read_b128 a[152:155], v4 offset:12544\n"
    "s_add_u32 s64, s7, 5\n"
    "s_add_u32 s65, s7, 6\n"
    "v_mfma_f32_16x16x32_bf16 v[32:35], a[96:99], a[52:55], v[32:35]\n"
    "v_cndmask_b32 v18, v18, 63, s[60:61]\n"
    "s_add_u32 s71, s7, 5\n"
    "v_lshlrev_b32 v18, 2, v18\n"
    "v_mfma_f32_16x16x32_bf16 v[32:35], a[92:95], a[28:31], v[32:35]\n"
    "ds_read_b128 a[156:159], v5 offset:12544\n"
    "ds_bpermute_b32 v18, v18, v3\n"
    "v_mfma_f32_16x16x32_bf16 v[32:35], a[88:91], a[4:7], v[32:35]\n"
    "v_readlane_b32 s84, v3, s86\n"
    "v_mfma_f32_16x16x32_bf16 v[20:23], a[88:91], a[8:11], 0\n"
    "ds_read_b128 a[160:163], v6 offset:12544\n"
    "v_mfma_f32_16x16x32_bf16 v[20:23], a[92:95], a[32:35], v[20:23]\n"
    "v_mfma_f32_16x16x32_bf16 v[20:23], a[96:99], a[56:59], v[20:23]\n"
    "ds_read_b128 a[164:167], v7 offset:12544\n"
    "v_mfma_f32_16x16x32_bf16 v[20:23], a[100:103], a[80:83], v[20:23]\n"
    "v_cvt_pk_bf16_f32 v36, v28, v32\n"
    "v_mfma_f32_16x16x32_bf16 v[20:23], a[104:107], v[72:75], v[20:23]\n"
    "ds_read_b128 a[168:171], v4 offset:12800\n"
    "v_mfma_f32_16x16x32_bf16 v[20:23], a[108:111], v[96:99], v[20:23]\n"
    "v_cvt_pk_bf16_f32 v46, v29, v33\n"
    "v_mfma_f32_16x16x32_bf16 v[20:23], a[112:115], v[120:123], v[20:23]\n"
    "ds_read_b128 a[172:175], v5 offset:12800\n"
    "s_add_u32 m0, s57, 49152\n"
    "v_mfma_f32_16x16x32_bf16 v[20:23], a[116:119], v[144:147], v[20:23]\n"
    "buffer_load_dwordx4 v0, s[36:39], s81 offen nt lds\n"
    "v_mfma_f32_16x16x32_bf16 v[20:23], a[120:123], v[168:171], v[20:23]\n"
    "v_cvt_pk_bf16_f32 v37, v30, v34\n"
    "v_mfma_f32_16x16x32_bf16 v[20:23], a[124:127], v[192:195], v[20:23]\n"
    "ds_read_b128 a[176:179], v6 offset:12800\n"
    "v_mfma_f32_16x16x32_bf16 v[20:23], a[128:131], v[216:219], v[20:23]\n"
    "v_cvt_pk_bf16_f32 v50, v31, v35\n"
    "v_mfma_f32_16x16x32_bf16 v[20:23], a[132:135], v[240:243], v[20:23]\n"
    "ds_read_b128 a[180:183], v7 offset:12800\n"
    "v_mfma_f32_16x16x32_bf16 v[24:27], a[132:135], v[244:247], 0\n"
    "buffer_load_dwordx4 v1, s[36:39], s81 offen offset:1024 nt lds\n"
    "v_mfma_f32_16x16x32_bf16 v[24:27], a[128:131], v[220:223], v[24:27]\n"
    "v_cndmask_b32_dpp v44, v46, v36, vcc row_ror:8 row_mask:0xf bank_mask:0xf\n"
    "v_mfma_f32_16x16x32_bf16 v[24:27], a[124:127], v[196:199], v[24:27]\n"
    "buffer_load_dwordx4 v2, s[36:39], s81 offen offset:2048 nt lds\n"
    "v_mfma_f32_16x16x32_bf16 v[24:27], a[120:123], v[172:175], v[24:27]\n"
    "s_memrealtime s[92:93]\n"
    "buffer_load_dword a212, off, s[36:39], s33\n"
    "v_mfma_f32_16x16x32_bf16 v[24:27], a[116:119], v[148:151], v[24:27]\n"
    "v_cndmask_b32_dpp v48, v50, v37, vcc row_ror:8 row_mask:0xf bank_mask:0xf\n"
    "buffer_load_dword a212, off, s[36:39], s33\n"
    "v_mfma_f32_16x16x32_bf16 v[24:27], a[112:115], v[124:127], v[24:27]\n"
    "v_mov_b32_dpp v46, v36 row_ror:8 row_mask:0xf bank_mask:0x3\n"
    "v_mfma_f32_16x16x32_bf16 v[24:27], a[108:111], v[100:103], v[24:27]\n"
    "v_mov_b32_dpp v50, v37 row_ror:8 row_mask:0xf bank_mask:0x3\n"
    "v_mfma_f32_16x16x32_bf16 v[24:27], a[104:107], v[76:79], v[24:27]\n"
    "v_mfma_f32_16x16x32_bf16 v[24:27], a[100:103], a[84:87], v[24:27]\n"
    "s_waitcnt vmcnt(11) lgkmcnt(0)\n"
    "s_sub_u32 s85, s92, s89\n"
    "v_mfma_f32_16x16x32_bf16 v[24:27], a[96:99], a[60:63], v[24:27]\n"
    "s_add_i32 s63, s63, s85\n"
    "s_max_i32 s63, s63, s62\n"
    "s_add_u32 s89, s89, s63\n"
    "v_mfma_f32_16x16x32_bf16 v[24:27], a[92:95], a[36:39], v[24:27]\n"
    "s_sub_i32 s7, s89, s1\n"
    "s_mul_i32 s7, s7, 4\n"
    "s_ashr_i32 s7, s7, 6\n"
    "s_mov_b32 s81, s84\n"
    "s_add_u32 m0, s57, 61440\n"
    "v_mfma_f32_16x16x32_bf16 v[24:27], a[88:91], a[12:15], v[24:27]\n"
    "s_max_i32 s7, s7, -2\n"
    "s_min_i32 s7, s7, 4\n"
    "s_sub_u32 s31, s31, s7\n"
    "s_branch 52f\n"
    ".p2align 12\n"
    "51:\n"
    "v_mfma_f32_16x16x32_bf16 v[20:23], a[88:91], a[16:19], 0\n"
    "s_add_u32 s86, s86, 1\n"
    "v_mfma_f32_16x16x32_bf16 v[20:23], a[92:95], a[40:43], v[20:23]\n"
    "s_waitcnt vmcnt(14)\n"
    "v_mfma_f32_16x16x32_bf16 v[20:23], a[96:99], a[64:67], v[20:23]\n"
    "ds_write_b32 v13, v14\n"
    "v_mfma_f32_16x16x32_bf16 v[20:23], a[100:103], v[56:59], v[20:23]\n"
    "v_cvt_pk_bf16_f32 v36, v28, v32\n"
    "v_mfma_f32_16x16x32_bf16 v[20:23], a[104:107], v[80:83], v[20:23]\n"
    "v_cvt_pk_bf16_f32 v47, v29, v33\n"
    "v_mfma_f32_16x16x32_bf16 v[20:23], a[108:111], v[104:107], v[20:23]\n"
    "v_cvt_pk_bf16_f32 v37, v30, v34\n"
    "v_mfma_f32_16x16x32_bf16 v[20:23], a[112:115], v[128:131], v[20:23]\n"
    "v_cvt_pk_bf16_f32 v51, v31, v35\n"
    "v_mfma_f32_16x16x32_bf16 v[20:23], a[116:119], v[152:155], v[20:23]\n"
    "v_cndmask_b32_dpp v45, v47, v36, vcc row_ror:8 row_mask:0xf bank_mask:0xf\n"
    "v_mfma_f32_16x16x32_bf16 v[20:23], a[120:123], v[176:179], v[20:23]\n"
    "v_cndmask_b32_dpp v49, v51, v37, vcc row_ror:8 row_mask:0xf bank_mask:0xf\n"
    "v_mfma_f32_16x16x32_bf16 v[20:23], a[124:127], v[200:203], v[20:23]\n"
    "v_mov_b32_dpp v47, v36 row_ror:8 row_mask:0xf bank_mask:0x3\n"
    "v_mfma_f32_16x16x32_bf16 v[20:23], a[128:131], v[224:227], v[20:23]\n"
    "v_mov_b32_dpp v51, v37 row_ror:8 row_mask:0xf bank_mask:0x3\n"
    "v_mfma_f32_16x16x32_bf16 v[20:23], a[132:135], v[248:251], v[20:23]\n"
    "ds_read_b32 v15, v12\n"
    "v_mfma_f32_16x16x32_bf16 v[24:27], a[132:135], v[252:255], 0\n"
    "s_add_u32 s89, s89, s94\n"
    "v_mfma_f32_16x16x32_bf16 v[24:27], a[128:131], v[228:231], v[24:27]\n"
    "s_sub_u32 s94, s94, 6\n"
    "v_mfma_f32_16x16x32_bf16 v[24:27], a[124:127], v[204:207], v[24:27]\n"
    "buffer_store_dwordx4 v[44:47], v8, s[40:43], s82 offen sc1\n"
    "v_mfma_f32_16x16x32_bf16 v[24:27], a[120:123], v[180:183], v[24:27]\n"
    "s_max_u32 s94, s94, s30\n"
    "v_mfma_f32_16x16x32_bf16 v[24:27], a[116:119], v[156:159], v[24:27]\n"
    "s_waitcnt lgkmcnt(0)\n"
    "v_cmp_lt_u32 vcc, v15, v14\n"
    "v_mfma_f32_16x16x32_bf16 v[24:27], a[112:115], v[132:135], v[24:27]\n"
    "v_mfma_f32_16x16x32_bf16 v[24:27], a[108:111], v[108:111], v[24:27]\n"
    "s_cbranch_vccnz 41b\n"
    "42:\n"
    "v_mfma_f32_16x16x32_bf16 v[24:27], a[104:107], v[84:87], v[24:27]\n"
    "ds_read_b128 a[184:187], v39 offset:4096\n"
    "v_mfma_f32_16x16x32_bf16 v[24:27], a[100:103], v[60:63], v[24:27]\n"
    "s_cmp_eq_u32 s86, s64\n"
    "s_cbranch_scc1 45b\n"
    "v_mfma_f32_16x16x32_bf16 v[24:27], a[96:99], a[68:71], v[24:27]\n"
    "ds_read_b128 a[136:139], v4 offset:12288\n"
    "v_mfma_f32_16x16x32_bf16 v[24:27], a[92:95], a[44:47], v[24:27]\n"
    "buffer_load_dwordx4 v0, s[36:39], s81 offen nt lds\n"
    "v_mfma_f32_16x16x32_bf16 v[24:27], a[88:91], a[20:23], v[24:27]\n"
    "v_add_u32 v14, 1, v14\n"
    "v_mfma_f32_16x16x32_bf16 v[28:31], a[88:91], a[0:3], 0\n"
    "s_mov_b64 vcc, s[12:13]\n"
    "v_mfma_f32_16x16x32_bf16 v[28:31], a[92:95], a[24:27], v[28:31]\n"
    "ds_read_b128 a[140:143], v5 offset:12288\n"
    "v_mfma_f32_16x16x32_bf16 v[28:31], a[96:99], a[48:51], v[28:31]\n"
    "ds_read_b128 a[144:147], v6 offset:12288\n"
    "v_mfma_f32_16x16x32_bf16 v[28:31], a[100:103], a[72:75], v[28:31]\n"
    "buffer_load_dwordx4 v1, s[36:39], s81 offen offset:1024 nt lds\n"
    "v_mfma_f32_16x16x32_bf16 v[28:31], a[104:107], v[64:67], v[28:31]\n"
    "v_cvt_pk_bf16_f32 v52, v20, v24\n"
    "v_mfma_f32_16x16x32_bf16 v[28:31], a[108:111], v[88:91], v[28:31]\n"
    "v_cvt_pk_bf16_f32 v53, v21, v25\n"
    "v_mfma_f32_16x16x32_bf16 v[28:31], a[112:115], v[112:115], v[28:31]\n"
    "v_cvt_pk_bf16_f32 v54, v22, v26\n"
    "v_mfma_f32_16x16x32_bf16 v[28:31], a[116:119], v[136:139], v[28:31]\n"
    "ds_read_b128 a[148:151], v7 offset:12288\n"
    "v_mfma_f32_16x16x32_bf16 v[28:31], a[120:123], v[160:163], v[28:31]\n"
    "v_cvt_pk_bf16_f32 v55, v23, v27\n"
    "v_mfma_f32_16x16x32_bf16 v[28:31], a[124:127], v[184:187], v[28:31]\n"
    "buffer_load_dwordx4 v2, s[36:39], s81 offen offset:2048 nt lds\n"
    "v_mfma_f32_16x16x32_bf16 v[28:31], a[128:131], v[208:211], v[28:31]\n"
    "v_readlane_b32 s81, v3, s86\n"
    "v_mfma_f32_16x16x32_bf16 v[28:31], a[132:135], v[232:235], v[28:31]\n"
    "ds_read_b128 a[152:155], v4 offset:12544\n"
    "v_mfma_f32_16x16x32_bf16 v[32:35], a[132:135], v[236:239], 0\n"
    "s_addk_i32 m0, 12288\n"
    "v_mfma_f32_16x16x32_bf16 v[32:35], a[128:131], v[212:215], v[32:35]\n"
    "ds_write2st64_b32 v38, v52, v53 offset0:0 offset1:2\n"
    "v_mfma_f32_16x16x32_bf16 v[32:35], a[124:127], v[188:191], v[32:35]\n"
    "46:\n"
    "v_mfma_f32_16x16x32_bf16 v[32:35], a[120:123], v[164:167], v[32:35]\n"
    "ds_read_b128 a[156:159], v5 offset:12544\n"
    "v_mfma_f32_16x16x32_bf16 v[32:35], a[116:119], v[140:143], v[32:35]\n"
    "s_cmp_eq_u32 m0, s77\n"
    "v_mfma_f32_16x16x32_bf16 v[32:35], a[112:115], v[116:119], v[32:35]\n"
    "s_cselect_b32 s78, s87, s88\n"
    "v_mfma_f32_16x16x32_bf16 v[32:35], a[108:111], v[92:95], v[32:35]\n"
    "buffer_store_dwordx4 v[48:51], v9, s[40:43], s82 offen sc1\n"
    "v_mfma_f32_16x16x32_bf16 v[32:35], a[104:107], v[68:71], v[32:35]\n"
    "v_mfma_f32_16x16x32_bf16 v[32:35], a[100:103], a[76:79], v[32:35]\n"
    "ds_read_b128 a[160:163], v6 offset:12544\n"
    "v_mfma_f32_16x16x32_bf16 v[32:35], a[96:99], a[52:55], v[32:35]\n"
    "ds_read_b128 a[164:167], v7 offset:12544\n"
    "v_mfma_f32_16x16x32_bf16 v[32:35], a[92:95], a[28:31], v[32:35]\n"
    "ds_read_b128 a[168:171], v4 offset:12800\n"
    "v_mfma_f32_16x16x32_bf16 v[32:35], a[88:91], a[4:7], v[32:35]\n"
    "v_mfma_f32_16x16x32_bf16 v[20:23], a[88:91], a[8:11], 0\n"
    "ds_write2st64_b32 v38, v54, v55 offset0:4 offset1:6\n"
    "v_mfma_f32_16x16x32_bf16 v[20:23], a[92:95], a[32:35], v[20:23]\n"
    "v_mfma_f32_16x16x32_bf16 v[20:23], a[96:99], a[56:59], v[20:23]\n"
    "ds_read_b128 a[172:175], v5 offset:12800\n"
    "v_mfma_f32_16x16x32_bf16 v[20:23], a[100:103], a[80:83], v[20:23]\n"
    "v_cvt_pk_bf16_f32 v36, v28, v32\n"
    "v_mfma_f32_16x16x32_bf16 v[20:23], a[104:107], v[72:75], v[20:23]\n"
    "v_cvt_pk_bf16_f32 v46, v29, v33\n"
    "v_mfma_f32_16x16x32_bf16 v[20:23], a[108:111], v[96:99], v[20:23]\n"
    "ds_read_b128 a[176:179], v6 offset:12800\n"
    "v_mfma_f32_16x16x32_bf16 v[20:23], a[112:115], v[120:123], v[20:23]\n"
    "v_cvt_pk_bf16_f32 v37, v30, v34\n"
    "v_mfma_f32_16x16x32_bf16 v[20:23], a[116:119], v[144:147], v[20:23]\n"
    "v_cvt_pk_bf16_f32 v50, v31, v35\n"
    "v_mfma_f32_16x16x32_bf16 v[20:23], a[120:123], v[168:171], v[20:23]\n"
    "v_mfma_f32_16x16x32_bf16 v[20:23], a[124:127], v[192:195], v[20:23]\n"
    "v_cndmask_b32_dpp v44, v46, v36, vcc row_ror:8 row_mask:0xf bank_mask:0xf\n"
    "v_mfma_f32_16x16x32_bf16 v[20:23], a[128:131], v[216:219], v[20:23]\n"
    "ds_read_b128 a[180:183], v7 offset:12800\n"
    "v_mfma_f32_16x16x32_bf16 v[20:23], a[132:135], v[240:243], v[20:23]\n"
    "v_cndmask_b32_dpp v48, v50, v37, vcc row_ror:8 row_mask:0xf bank_mask:0xf\n"
    "v_mfma_f32_16x16x32_bf16 v[24:27], a[132:135], v[244:247], 0\n"
    "s_memrealtime s[92:93]\n"
    "v_mfma_f32_16x16x32_bf16 v[24:27], a[128:131], v[220:223], v[24:27]\n"
    "v_mov_b32_dpp v46, v36 row_ror:8 row_mask:0xf bank_mask:0x3\n"
    "v_mfma_f32_16x16x32_bf16 v[24:27], a[124:127], v[196:199], v[24:27]\n"
    "v_mov_b32_dpp v50, v37 row_ror:8 row_mask:0xf bank_mask:0x3\n"
    "v_mfma_f32_16x16x32_bf16 v[24:27], a[120:123], v[172:175], v[24:27]\n"
    "s_waitcnt lgkmcnt(14)\n"
    "buffer_store_dwordx4 a[184:187], v10, s[40:43], s82 offen sc1\n"
    "v_mfma_f32_16x16x32_bf16 v[24:27], a[116:119], v[148:151], v[24:27]\n"
    "v_mfma_f32_16x16x32_bf16 v[24:27], a[112:115], v[124:127], v[24:27]\n"
    "v_readlane_b32 s82, v18, s86\n"
    "v_mfma_f32_16x16x32_bf16 v[24:27], a[108:111], v[100:103], v[24:27]\n"
    "v_mfma_f32_16x16x32_bf16 v[24:27], a[104:107], v[76:79], v[24:27]\n"
    "s_waitcnt lgkmcnt(0)\n"
    "s_sub_u32 s85, s92, s89\n"
    "v_mfma_f32_16x16x32_bf16 v[24:27], a[100:103], a[84:87], v[24:27]\n"
    "s_cbranch_scc1 43b\n"
    "44:\n"
    "v_mfma_f32_16x16x32_bf16 v[24:27], a[96:99], a[60:63], v[24:27]\n"
    "v_mfma_f32_16x16x32_bf16 v[24:27], a[92:95], a[36:39], v[24:27]\n"
    "s_cmp_lt_u32 s86, s76\n"
    "v_mfma_f32_16x16x32_bf16 v[24:27], a[88:91], a[12:15], v[24:27]\n"
    "s_cbranch_scc0 55f\n"
    "52:\n"
    "v_mfma_f32_16x16x32_bf16 v[28:31], a[136:139], a[16:19], 0\n"
    "s_add_u32 s86, s86, 1\n"
    "v_mfma_f32_16x16x32_bf16 v[28:31], a[140:143], a[40:43], v[28:31]\n"
    "s_waitcnt vmcnt(14)\n"
    "v_mfma_f32_16x16x32_bf16 v[28:31], a[144:147], a[64:67], v[28:31]\n"
    "ds_write_b32 v13, v14\n"
    "v_mfma_f32_16x16x32_bf16 v[28:31], a[148:151], v[56:59], v[28:31]\n"
    "v_cvt_pk_bf16_f32 v36, v20, v24\n"
    "v_mfma_f32_16x16x32_bf16 v[28:31], a[152:155], v[80:83], v[28:31]\n"
    "v_cvt_pk_bf16_f32 v47, v21, v25\n"
    "v_mfma_f32_16x16x32_bf16 v[28:31], a[156:159], v[104:107], v[28:31]\n"
    "v_cvt_pk_bf16_f32 v37, v22, v26\n"
    "v_mfma_f32_16x16x32_bf16 v[28:31], a[160:163], v[128:131], v[28:31]\n"
    "v_cvt_pk_bf16_f32 v51, v23, v27\n"
    "v_mfma_f32_16x16x32_bf16 v[28:31], a[164:167], v[152:155], v[28:31]\n"
    "v_cndmask_b32_dpp v45, v47, v36, vcc row_ror:8 row_mask:0xf bank_mask:0xf\n"
    "v_mfma_f32_16x16x32_bf16 v[28:31], a[168:171], v[176:179], v[28:31]\n"
    "v_cndmask_b32_dpp v49, v51, v37, vcc row_ror:8 row_mask:0xf bank_mask:0xf\n"
    "v_mfma_f32_16x16x32_bf16 v[28:31], a[172:175], v[200:203], v[28:31]\n"
    "v_mov_b32_dpp v47, v36 row_ror:8 row_mask:0xf bank_mask:0x3\n"
    "v_mfma_f32_16x16x32_bf16 v[28:31], a[176:179], v[224:227], v[28:31]\n"
    "v_mov_b32_dpp v51, v37 row_ror:8 row_mask:0xf bank_mask:0x3\n"
    "v_mfma_f32_16x16x32_bf16 v[28:31], a[180:183], v[248:251], v[28:31]\n"
    "ds_read_b32 v15, v12\n"
    "v_mfma_f32_16x16x32_bf16 v[32:35], a[180:183], v[252:255], 0\n"
    "s_add_u32 s89, s89, s94\n"
    "v_mfma_f32_16x16x32_bf16 v[32:35], a[176:179], v[228:231], v[32:35]\n"
    "v_add_u32 v4, s78, v4\n"
    "v_mfma_f32_16x16x32_bf16 v[32:35], a[172:175], v[204:207], v[32:35]\n"
    "buffer_store_dwordx4 v[44:47], v8, s[40:43], s82 offen sc1\n"
    "v_mfma_f32_16x16x32_bf16 v[32:35], a[168:171], v[180:183], v[32:35]\n"
    "v_add_u32 v5, s78, v5\n"
    "v_mfma_f32_16x16x32_bf16 v[32:35], a[164:167], v[156:159], v[32:35]\n"
    "s_waitcnt lgkmcnt(0)\n"
    "v_cmp_lt_u32 vcc, v15, v14\n"
    "v_mfma_f32_16x16x32_bf16 v[32:35], a[160:163], v[132:135], v[32:35]\n"
    "v_add_u32 v6, s78, v6\n"
    "v_mfma_f32_16x16x32_bf16 v[32:35], a[156:159], v[108:111], v[32:35]\n"
    "s_cbranch_vccnz 61b\n"
    "62:\n"
    "v_mfma_f32_16x16x32_bf16 v[32:35], a[152:155], v[84:87], v[32:35]\n"
    "ds_read_b128 a[184:187], v39\n"
    "v_mfma_f32_16x16x32_bf16 v[32:35], a[148:151], v[60:63], v[32:35]\n"
    "s_cmp_eq_u32 s86, s65\n"
    "s_cbranch_scc1 65b\n"
    "v_mfma_f32_16x16x32_bf16 v[32:35], a[144:147], a[68:71], v[32:35]\n"
    "ds_read_b128 a[88:91], v4\n"
    "v_mfma_f32_16x16x32_bf16 v[32:35], a[140:143], a[44:47], v[32:35]\n"
    "buffer_load_dwordx4 v0, s[36:39], s81 offen nt lds\n"
    "v_mfma_f32_16x16x32_bf16 v[32:35], a[136:139], a[20:23], v[32:35]\n"
    "v_add_u32 v7, s78, v7\n"
    "v_mfma_f32_16x16x32_bf16 v[20:23], a[136:139], a[0:3], 0\n"
    "v_add_u32 v14, 1, v14\n"
    "v_mfma_f32_16x16x32_bf16 v[20:23], a[140:143], a[24:27], v[20:23]\n"
    "ds_read_b128 a[92:95], v5\n"
    "v_mfma_f32_16x16x32_bf16 v[20:23], a[144:147], a[48:51], v[20:23]\n"
    "ds_read_b128 a[96:99], v6\n"
    "v_mfma_f32_16x16x32_bf16 v[20:23], a[148:151], a[72:75], v[20:23]\n"
    "buffer_load_dwordx4 v1, s[36:39], s81 offen offset:1024 nt lds\n"
    "v_mfma_f32_16x16x32_bf16 v[20:23], a[152:155], v[64:67], v[20:23]\n"
    "v_cvt_pk_bf16_f32 v52, v28, v32\n"
    "v_mfma_f32_16x16x32_bf16 v[20:23], a[156:159], v[88:91], v[20:23]\n"
    "v_cvt_pk_bf16_f32 v53, v29, v33\n"
    "v_mfma_f32_16x16x32_bf16 v[20:23], a[160:163], v[112:115], v[20:23]\n"
    "v_cvt_pk_bf16_f32 v54, v30, v34\n"
    "v_mfma_f32_16x16x32_bf16 v[20:23], a[164:167], v[136:139], v[20:23]\n"
    "ds_read_b128 a[100:103], v7\n"
    "v_mfma_f32_16x16x32_bf16 v[20:23], a[168:171], v[160:163], v[20:23]\n"
    "v_cvt_pk_bf16_f32 v55, v31, v35\n"
    "v_mfma_f32_16x16x32_bf16 v[20:23], a[172:175], v[184:187], v[20:23]\n"
    "buffer_load_dwordx4 v2, s[36:39], s81 offen offset:2048 nt lds\n"
    "v_mfma_f32_16x16x32_bf16 v[20:23], a[176:179], v[208:211], v[20:23]\n"
    "s_mov_b64 vcc, s[12:13]\n"
    "v_mfma_f32_16x16x32_bf16 v[20:23], a[180:183], v[232:235], v[20:23]\n"
    "ds_read_b128 a[104:107], v4 offset:256\n"
    "v_mfma_f32_16x16x32_bf16 v[24:27], a[180:183], v[236:239], 0\n"
    "v_readlane_b32 s81, v3, s86\n"
    "v_mfma_f32_16x16x32_bf16 v[24:27], a[176:179], v[212:215], v[24:27]\n"
    "ds_write2st64_b32 v38, v52, v53 offset0:16 offset1:18\n"
    "v_mfma_f32_16x16x32_bf16 v[24:27], a[172:175], v[188:191], v[24:27]\n"
    "s_addk_i32 m0, 12288\n"
    "66:\n"
    "v_mfma_f32_16x16x32_bf16 v[24:27], a[168:171], v[164:167], v[24:27]\n"
    "ds_read_b128 a[108:111], v5 offset:256\n"
    "v_mfma_f32_16x16x32_bf16 v[24:27], a[164:167], v[140:143], v[24:27]\n"
    "s_cmp_eq_u32 m0, s80\n"
    "v_mfma_f32_16x16x32_bf16 v[24:27], a[160:163], v[116:119], v[24:27]\n"
    "s_cselect_b32 m0, s57, m0\n"
    "v_mfma_f32_16x16x32_bf16 v[24:27], a[156:159], v[92:95], v[24:27]\n"
    "buffer_store_dwordx4 v[48:51], v9, s[40:43], s82 offen sc1\n"
    "v_mfma_f32_16x16x32_bf16 v[24:27], a[152:155], v[68:71], v[24:27]\n"
    "s_cmp_eq_u32 s86, 20\n"
    "s_cselect_b32 s30, s31, s30\n"
    "v_mfma_f32_16x16x32_bf16 v[24:27], a[148:151], a[76:79], v[24:27]\n"
    "ds_read_b128 a[112:115], v6 offset:256\n"
    "v_mfma_f32_16x16x32_bf16 v[24:27], a[144:147], a[52:55], v[24:27]\n"
    "ds_read_b128 a[116:119], v7 offset:256\n"
    "v_mfma_f32_16x16x32_bf16 v[24:27], a[140:143], a[28:31], v[24:27]\n"
    "ds_read_b128 a[120:123], v4 offset:512\n"
    "v_mfma_f32_16x16x32_bf16 v[24:27], a[136:139], a[4:7], v[24:27]\n"
    "v_mfma_f32_16x16x32_bf16 v[28:31], a[136:139], a[8:11], 0\n"
    "ds_write2st64_b32 v38, v54, v55 offset0:20 offset1:22\n"
    "v_mfma_f32_16x16x32_bf16 v[28:31], a[140:143], a[32:35], v[28:31]\n"
    "v_mfma_f32_16x16x32_bf16 v[28:31], a[144:147], a[56:59], v[28:31]\n"
    "ds_read_b128 a[124:127], v5 offset:512\n"
    "v_mfma_f32_16x16x32_bf16 v[28:31], a[148:151], a[80:83], v[28:31]\n"
    "v_cvt_pk_bf16_f32 v36, v20, v24\n"
    "v_mfma_f32_16x16x32_bf16 v[28:31], a[152:155], v[72:75], v[28:31]\n"
    "v_cvt_pk_bf16_f32 v46, v21, v25\n"
    "v_mfma_f32_16x16x32_bf16 v[28:31], a[156:159], v[96:99], v[28:31]\n"
    "ds_read_b128 a[128:131], v6 offset:512\n"
    "v_mfma_f32_16x16x32_bf16 v[28:31], a[160:163], v[120:123], v[28:31]\n"
    "v_cvt_pk_bf16_f32 v37, v22, v26\n"
    "v_mfma_f32_16x16x32_bf16 v[28:31], a[164:167], v[144:147], v[28:31]\n"
    "v_cvt_pk_bf16_f32 v50, v23, v27\n"
    "v_mfma_f32_16x16x32_bf16 v[28:31], a[168:171], v[168:171], v[28:31]\n"
    "v_mfma_f32_16x16x32_bf16 v[28:31], a[172:175], v[192:195], v[28:31]\n"
    "v_cndmask_b32_dpp v44, v46, v36, vcc row_ror:8 row_mask:0xf bank_mask:0xf\n"
    "v_mfma_f32_16x16x32_bf16 v[28:31], a[176:179], v[216:219], v[28:31]\n"
    "ds_read_b128 a[132:135], v7 offset:512\n"
    "v_mfma_f32_16x16x32_bf16 v[28:31], a[180:183], v[240:243], v[28:31]\n"
    "v_cndmask_b32_dpp v48, v50, v37, vcc row_ror:8 row_mask:0xf bank_mask:0xf\n"
    "v_mfma_f32_16x16x32_bf16 v[32:35], a[180:183], v[244:247], 0\n"
    "s_memrealtime s[92:93]\n"
    "v_mfma_f32_16x16x32_bf16 v[32:35], a[176:179], v[220:223], v[32:35]\n"
    "v_mov_b32_dpp v46, v36 row_ror:8 row_mask:0xf bank_mask:0x3\n"
    "v_mfma_f32_16x16x32_bf16 v[32:35], a[172:175], v[196:199], v[32:35]\n"
    "v_mov_b32_dpp v50, v37 row_ror:8 row_mask:0xf bank_mask:0x3\n"
    "v_mfma_f32_16x16x32_bf16 v[32:35], a[168:171], v[172:175], v[32:35]\n"
    "s_waitcnt lgkmcnt(14)\n"
    "buffer_store_dwordx4 a[184:187], v10, s[40:43], s82 offen sc1\n"
    "v_mfma_f32_16x16x32_bf16 v[32:35], a[164:167], v[148:151], v[32:35]\n"
    "v_mfma_f32_16x16x32_bf16 v[32:35], a[160:163], v[124:127], v[32:35]\n"
    "v_readlane_b32 s82, v18, s86\n"
    "v_mfma_f32_16x16x32_bf16 v[32:35], a[156:159], v[100:103], v[32:35]\n"
    "v_mfma_f32_16x16x32_bf16 v[32:35], a[152:155], v[76:79], v[32:35]\n"
    "s_waitcnt lgkmcnt(0)\n"
    "s_sub_u32 s85, s92, s89\n"
    "v_mfma_f32_16x16x32_bf16 v[32:35], a[148:151], a[84:87], v[32:35]\n"
    "s_cbranch_scc1 63b\n"
    "64:\n"
    "v_mfma_f32_16x16x32_bf16 v[32:35], a[144:147], a[60:63], v[32:35]\n"
    "v_mfma_f32_16x16x32_bf16 v[32:35], a[140:143], a[36:39], v[32:35]\n"
    "s_cmp_lt_u32 s86, s76\n"
    "v_mfma_f32_16x16x32_bf16 v[32:35], a[136:139], a[12:15], v[32:35]\n"
    "s_cbranch_scc1 51b\n"
    "s_barrier\n"
    "ds_read_b128 a[184:187], v39 offset:4096\n"
    "s_nop 4\n"
    "v_cvt_pk_bf16_f32 v36, v28, v32\n"
    "v_cvt_pk_bf16_f32 v47, v29, v33\n"
    "v_cvt_pk_bf16_f32 v37, v30, v34\n"
    "v_cvt_pk_bf16_f32 v51, v31, v35\n"
    "s_branch 56f\n"
    "55:\n"
    "s_barrier\n"
    "ds_read_b128 a[184:187], v39\n"
    "s_nop 4\n"
    "v_cvt_pk_bf16_f32 v36, v20, v24\n"
    "v_cvt_pk_bf16_f32 v47, v21, v25\n"
    "v_cvt_pk_bf16_f32 v37, v22, v26\n"
    "v_cvt_pk_bf16_f32 v51, v23, v27\n"
    "56:\n"
    "v_cndmask_b32_dpp v45, v47, v36, vcc row_ror:8 row_mask:0xf bank_mask:0xf\n"
    "v_mov_b32_dpp v47, v36 row_ror:8 row_mask:0xf bank_mask:0x3\n"
    "v_cndmask_b32_dpp v49, v51, v37, vcc row_ror:8 row_mask:0xf bank_mask:0xf\n"
    "buffer_store_dwordx4 v[44:47], v8, s[40:43], s82 offen sc1\n"
    "v_mov_b32_dpp v51, v37 row_ror:8 row_mask:0xf bank_mask:0x3\n"
    "s_waitcnt lgkmcnt(0)\n"
    "buffer_store_dwordx4 a[184:187], v10, s[40:43], s82 offen sc1\n"
    "buffer_store_dwordx4 v[48:51], v9, s[40:43], s82 offen sc1\n"
    "s_waitcnt vmcnt(0) lgkmcnt(0)\n"
    "s_endpgm\n"
    "75:\n"
    "s_sleep 2\n"
    "global_load_dwordx4 v[236:239], v16, s[54:55] sc1\n"
    "s_waitcnt vmcnt(0)\n"
    "v_sub_u32 v11, s0, v236\n"
    "v_or_b32 v236, 0xffff, v236\n"
    "v_or_b32 v238, 0xffff, v238\n"
    "v_and_b32 v242, 15, v237\n"
    "v_and_b32 v243, 1, v239\n"
    "v_or_b32 v237, 15, v237\n"
    "v_or_b32 v239, 15, v239\n"
    "v_cmp_ne_u64 vcc, s[18:19], v[236:237]\n"
    "v_cmp_ne_u64 s[48:49], s[18:19], v[238:239]\n"
    "v_readlane_b32 s9, v242, 0\n"
    "v_and_b32 v236, 1, v242\n"
    "s_or_b64 s[48:49], s[48:49], vcc\n"
    "s_cbranch_scc1 75b\n"
    "s_branch 77b\n"
    "79:\n"
    "s_nop 0\n"
    : "+{s[0:1]}"(dp), "+{s[2:3]}"(ka), "+{s[4:5]}"(did), "+{s6}"(bid), "+{v0}"(tid)
    : [lds] "{s98}"(lds0)
    : "v1","v2","v3","v4","v5","v6","v7","v8","v9","v10","v11","v12","v13","v14","v15","v16","v17","v18","v19","v20","v21","v22","v23","v24","v25","v26","v27","v28","v29","v30","v31","v32","v33","v34","v35","v36","v37","v38","v39","v40","v41","v42","v43","v44","v45","v46","v47","v48","v49","v50","v51","v52","v53","v54","v55","v56","v57","v58","v59","v60","v61","v62","v63","v64","v65","v66","v67","v68","v69","v70","v71","v72","v73","v74","v75","v76","v77","v78","v79","v80","v81","v82","v83","v84","v85","v86","v87","v88","v89","v90","v91","v92","v93","v94","v95","v96","v97","v98","v99","v100","v101","v102","v103","v104","v105","v106","v107","v108","v109","v110","v111","v112","v113","v114","v115","v116","v117","v118","v119","v120","v121","v122","v123","v124","v125","v126","v127","v128","v129","v130","v131","v132","v133","v134","v135","v136","v137","v138","v139","v140","v141","v142","v143","v144","v145","v146","v147","v148","v149","v150","v151","v152","v153","v154","v155","v156","v157","v158","v159","v160","v161","v162","v163","v164","v165","v166","v167","v168","v169","v170","v171","v172","v173","v174","v175","v176","v177","v178","v179","v180","v181","v182","v183","v184","v185","v186","v187","v188","v189","v190","v191","v192","v193","v194","v195","v196","v197","v198","v199","v200","v201","v202","v203","v204","v205","v206","v207","v208","v209","v210","v211","v212","v213","v214","v215","v216","v217","v218","v219","v220","v221","v222","v223","v224","v225","v226","v227","v228","v229","v230","v231","v232","v233","v234","v235","v236","v237","v238","v239","v240","v241","v242","v243","v244","v245","v246","v247","v248","v249","v250","v251","v252","v253","v254","v255","a0","a1","a2","a3","a4","a5","a6","a7","a8","a9","a10","a11","a12","a13","a14","a15","a16","a17","a18","a19","a20","a21","a22","a23","a24","a25","a26","a27","a28","a29","a30","a31","a32","a33","a34","a35","a36","a37","a38","a39","a40","a41","a42","a43","a44","a45","a46","a47","a48","a49","a50","a51","a52","a53","a54","a55","a56","a57","a58","a59","a60","a61","a62","a63","a64","a65","a66","a67","a68","a69","a70","a71","a72","a73","a74","a75","a76","a77","a78","a79","a80","a81","a82","a83","a84","a85","a86","a87","a88","a89","a90","a91","a92","a93","a94","a95","a96","a97","a98","a99","a100","a101","a102","a103","a104","a105","a106","a107","a108","a109","a110","a111","a112","a113","a114","a115","a116","a117","a118","a119","a120","a121","a122","a123","a124","a125","a126","a127","a128","a129","a130","a131","a132","a133","a134","a135","a136","a137","a138","a139","a140","a141","a142","a143","a144","a145","a146","a147","a148","a149","a150","a151","a152","a153","a154","a155","a156","a157","a158","a159","a160","a161","a162","a163","a164","a165","a166","a167","a168","a169","a170","a171","a172","a173","a174","a175","a176","a177","a178","a179","a180","a181","a182","a183","a184","a185","a186","a187","a188","a189","a190","a191","a192","a193","a194","a195","a196","a197","a198","a199","a200","a201","a202","a203","a204","a205","a206","a207","a208","a209","a210","a211","a212","s7","s8","s9","s10","s11","s12","s13","s14","s15","s16","s17","s18","s19","s20","s21","s22","s23","s24","s25","s26","s27","s28","s29","s30","s31","s33","s34","s35","s36","s37","s38","s39","s40","s41","s42","s43","s44","s45","s46","s47","s48","s49","s50","s51","s52","s53","s54","s55","s56","s57","s58","s59","s60","s61","s62","s63","s64","s65","s66","s67","s68","s69","s70","s71","s72","s73","s74","s75","s76","s77","s78","s79","s80","s81","s82","s83","s84","s85","s86","s87","s88","s89","s90","s91","s92","s93","s94","s95","s96","s97","vcc","scc","memory");
}

extern "C" void kernel_launch(void* const* d_in, const int* in_sizes, int n_in,
                              void* d_out, int out_size, void* d_ws, size_t ws_size,
                              hipStream_t stream) {
  Shapes sh;
  for (int j = 0; j < 16; ++j)
    for (int w = 0; w < 4; ++w) {
      const int ty = j >= 11, per = ty ? 10 : 11, n = 16 * (j - 11 * ty);
      auto row = [&](int k) { return ty ? 64 * (k / 10) + 11 + k % 10 : 64 * (k / 22) + 21 * (k / 11 % 2) + k % 11; };
      const int n0 = n + 4 * w, bp = per - n0 % per;
      uint32_t* e = sh.v + 16 * j + 4 * w;
      e[0] = row(n0) * 768 - 512;
      e[1] = (row(n0 + bp) - bp) * 768 - 512;
      e[2] = bp * 768;
      e[3] = (row(n + 8) + 16) * 768 + ty;
      for (int i = 0; i < 4; ++i) sh.rows[16 * j + 4 * w + i] = row(n0 + i) * 768;
    }
  const int grid = 256, maxr = 40;
  const size_t M = (size_t)in_sizes[0] / CH;
  const uint32_t fl = 0x00020000u;
  for (size_t r0 = 0; r0 < M; r0 += (size_t)maxr * grid * TM) {
    const size_t rows = M - r0 < (size_t)maxr * grid * TM ? M - r0 : (size_t)maxr * grid * TM;
    const int nr = (int)((rows + grid * TM - 1) / (grid * TM)), nit = nr < 2 ? 2 : nr;
    const uint32_t nbytes = (uint32_t)(rows * CH * 2);
    const uint32_t np = (nbytes >> 21) & ~1u;
    const uint64_t pages = (np >= 24 ? (np - 14) & ~7u : 64u) | (uint64_t)(np >= 2 ? (np - 2) << 21 : 0u) << 32;
    dwsep_kernel<<<grid, 256, 0, stream>>>((const char*)d_in[0] + r0 * CH * 2, nbytes, fl, (char*)d_out + r0 * CH * 2, nbytes, fl,
                                           (const char*)d_in[2], CH * CH * 2, fl, (const char*)d_in[1], CH * 2, fl,
                                           (uint32_t)nit, (uint32_t)grid * TILE_BYTES, d_ws, pages, sh);
  }
}
```

```cpp
#include <hip/hip_runtime.h>
#include <stdint.h>

#define CH 384
#define TM 16
#define TILE_BYTES (TM * CH * 2)
#define LDS_BYTES (13 * TILE_BYTES + 1024 + 64)

struct Shapes { uint32_t v[256], rows[256]; };
extern "C" __device__ uint64_t dispatch_id_() __asm("llvm.amdgcn.dispatch.id");

__global__ __launch_bounds__(256, 1)
void dwsep_kernel(const char* x, uint32_t xbytes, uint32_t f0, char* out, uint32_t obytes, uint32_t f1,
                  const char* pw, uint32_t pwbytes, uint32_t f2, const char* dw, uint32_t dwbytes, uint32_t f3,
                  uint32_t nit, uint32_t tstride, void* ws, uint64_t pages, Shapes shapes) {
  __shared__ __attribute__((aligned(1024))) char lds[LDS_BYTES];
  uint32_t lds0 = (uint32_t)(uintptr_t)(__attribute__((address_space(3))) char*)lds;
  uint64_t ka = (uint64_t)(uintptr_t)__builtin_amdgcn_kernarg_segment_ptr();
  uint64_t did = dispatch_id_(), dp = (uint64_t)(uintptr_t)__builtin_amdgcn_dispatch_ptr();
  uint32_t bid = blockIdx.x, tid = threadIdx.x;
  asm volatile(
    "s_load_dwordx16 s[36:51], s[2:3], 0x0\n"
    "v_readfirstlane_b32 s8, v0\n"
    "s_load_dwordx4 s[52:55], s[2:3], 0x40\n"
    "s_bfe_u32 s7, s6, 0x40003\n"
    "s_lshr_b32 s9, s8, 2\n"
    "s_lshl_b32 s7, s7, 6\n"
    "s_add_u32 s9, s7, s9\n"
    "s_load_dwordx4 s[20:23], s[2:3], s9 offset:0x58\n"
    "s_load_dwordx2 s[96:97], s[2:3], 0x50\n"
    "s_getpc_b64 s[14:15]\n"
    "70:\n"
    "v_lshlrev_b32 v48, 6, v0\n"
    "s_mov_b32 s13, 79f-70b\n"
    "v_min_u32 v48, s13, v48\n"
    "global_load_dword v49, v48, s[14:15]\n"
    "v_and_b32 v18, 48, v0\n"
    "v_bfe_u32 v55, v0, 3, 1\n"
    "v_bfe_u32 v54, v0, 6, 1\n"
    "v_lshl_add_u32 v53, v55, 2, v18\n"
    "s_mov_b32 s16, s4\n"
    "v_add_u32 v53, s7, v53\n"
    "v_lshl_add_u32 v54, v54, 3, v53\n"
    "global_load_dword v8, v53, s[2:3] offset:1112\n"
    "global_load_dword v9, v53, s[2:3] offset:1120\n"
    "global_load_dword v10, v54, s[2:3] offset:1112\n"
    "s_mov_b32 s2, s6\n"
    "s_mov_b32 s3, %[lds]\n"
    "s_lshr_b32 s4, s8, 6\n"
    "v_and_b32 v53, 24, v0\n"
    "v_bfe_u32 v54, v0, 5, 1\n"
    "s_bfe_u32 s5, s2, 0x20003\n"
    "v_add_u32 v53, v53, v54\n"
    "s_bfe_u32 s6, s2, 0x20005\n"
    "v_mul_u32_u24 v53, 0x300, v53\n"
    "s_cmp_eq_u32 s6, 3\n"
    "v_and_b32 v55, 7, v0\n"
    "s_cselect_b32 s6, 0, s6\n"
    "v_lshl_add_u32 v53, v55, 4, v53\n"
    "s_lshl_b32 s7, s4, 7\n"
    "v_and_b32 v16, 63, v0\n"
    "s_mul_i32 s58, s5, 24576\n"
    "v_add_u32 v244, s7, v53\n"
    "s_mul_i32 s62, s6, 98304\n"
    "v_lshlrev_b32 v16, 4, v16\n"
    "s_lshl_b32 s7, s5, 6\n"
    "s_lshl_b32 s8, s6, 8\n"
    "v_xor_b32 v53, s7, v16\n"
    "s_add_u32 s12, s3, 159744\n"
    "s_mul_i32 s83, s4, 30720\n"
    "v_add_u32 v53, s8, v53\n"
    "s_mov_b32 m0, s12\n"
    "v_subrev_u32 v55, 0x300, v53\n"
    "s_add_u32 s83, s83, s3\n"
    "v_min_u32 v250, v53, v55\n"
    "v_add_u32 v245, 0x200, v244\n"
    "v_add_u32 v246, 0x400, v244\n"
    "v_add_u32 v247, 0x600, v244\n"
    "v_and_b32 v235, 12, v0\n"
    "s_mul_i32 s57, s4, 3072\n"
    "v_bfe_u32 v52, v0, 4, 2\n"
    "s_lshl_b32 s7, s4, 6\n"
    "v_lshl_add_u32 v53, v235, 1, v52\n"
    "v_mul_u32_u24 v53, 0x300, v53\n"
    "s_add_u32 s57, s57, s3\n"
    "v_and_b32 v54, 3, v0\n"
    "s_add_u32 s7, s7, 512\n"
    "v_lshl_add_u32 v53, v54, 4, v53\n"
    "v_lshlrev_b32 v12, 2, v54\n"
    "v_add_u32 v248, s7, v53\n"
    "v_add_u32 v249, 0x800, v248\n"
    "s_lshr_b64 s[18:19], s[0:1], 12\n"
    "s_lshl4_add_u32 s18, s16, s18\n"
    "s_andn2_b32 s17, s18, 15\n"
    "s_bitset1_b32 s17, 31\n"
    "s_mov_b32 s72, s17\n"
    "s_pack_ll_b32_b16 s18, -1, s16\n"
    "s_or_b32 s19, s17, 15\n"
    "s_lshl_b32 s34, s2, 20\n"
    "s_and_b32 s34, s34, 0x7e00000\n"
    "s_lshl_b32 s7, s16, 8\n"
    "s_add_u32 s7, s7, s2\n"
    "s_mul_i32 s7, s7, 0x9e3779b1\n"
    "s_lshr_b32 s7, s7, 9\n"
    "s_and_b32 s7, s7, 2088832\n"
    "s_or_b32 s34, s34, s7\n"
    "s_and_b32 s35, s2, 7\n"
    "s_lshl_b32 s7, s4, 1\n"
    "s_sub_u32 s35, s35, s7\n"
    "v_and_b32 v19, 15, v0\n"
    "v_add_u32 v18, s12, v18\n"
    "v_and_b32 v54, 14, v0\n"
    "v_lshrrev_b32 v53, 1, v235\n"
    "v_add_u32 v235, v18, v54\n"
    "v_add_u32 v18, v18, v53\n"
    "s_mov_b32 s9, 0x51403020\n"
    "s_mov_b32 s10, 0x52423121\n"
    "s_mov_b32 s11, 0x53433322\n"
    "s_cmp_eq_u32 s6, 1\n"
    "s_cselect_b32 s9, 0x61204030, s9\n"
    "s_cselect_b32 s10, 0x62521131, s10\n"
    "s_cselect_b32 s11, 0x33534302, s11\n"
    "s_cmp_eq_u32 s6, 2\n"
    "s_cselect_b32 s9, 0x71302040, s9\n"
    "s_cselect_b32 s10, 0x42622111, s10\n"
    "s_cselect_b32 s11, 0x43335312, s11\n"
    "s_xor_b32 s7, s4, s5\n"
    "v_lshlrev_b32 v54, 3, v52\n"
    "s_lshl_b32 s7, s7, 2\n"
    "v_xor_b32 v55, s7, v19\n"
    "v_bfe_u32 v53, s11, v54, 8\n"
    "v_xor_b32 v2, v53, v55\n"
    "v_bfe_u32 v53, s10, v54, 8\n"
    "v_xor_b32 v1, v53, v55\n"
    "v_bfe_u32 v53, s9, v54, 8\n"
    "v_xor_b32 v0, v53, v55\n"
    "s_and_b32 s28, s2, 3\n"
    "s_waitcnt lgkmcnt(0)\n"
    "s_memrealtime s[0:1]\n"
    "s_barrier\n"
    "buffer_load_dwordx4 v250, s[48:51], 0 offen lds\n"
    "s_cmp_eq_u32 s35, 0\n"
    "s_cbranch_scc0 72f\n"
    "s_bitcmp1_b32 s2, 7\n"
    "s_cselect_b64 s[68:69], s[40:41], s[36:37]\n"
    "s_mov_b64 s[70:71], s[38:39]\n"
    "s_and_b32 s85, s34, 0x3fffff\n"
    "s_add_u32 s85, s85, s97\n"
    "s_min_u32 s34, s34, s85\n"
    "s_buffer_load_dword s73, s[68:71], s34\n"
    "s_buffer_load_dword s72, s[68:71], s34 offset:0x2000\n"
    "72:\n"
    "s_add_u32 m0, s83, 36864\n"
    "s_add_u32 s84, s58, s62\n"
    "s_lshr_b32 s8, s2, 7\n"
    "s_lshl2_add_u32 s7, s8, s28\n"
    "s_mul_i32 s56, s7, 393216\n"
    "buffer_load_dwordx4 v244, s[44:47], s84 offen lds\n"
    "s_lshr_b32 s29, s2, 2\n"
    "s_mov_b32 s30, s57\n"
    "s_xor_b32 s7, s5, 1\n"
    "buffer_load_dwordx4 v245, s[44:47], s84 offen offset:1024 lds\n"
    "s_mul_i32 s59, s7, 24576\n"
    "s_xor_b32 s7, s5, 2\n"
    "s_mul_i32 s60, s7, 24576\n"
    "buffer_load_dwordx4 v246, s[44:47], s84 offen offset:2048 lds\n"
    "s_xor_b32 s7, s5, 3\n"
    "s_mul_i32 s61, s7, 24576\n"
    "v_mov_b32 v53, s20\n"
    "buffer_load_dwordx4 v247, s[44:47], s84 offen offset:3072 lds\n"
    "s_add_u32 m0, s83, 40960\n"
    "v_mov_b32 v54, s21\n"
    "v_cmp_le_i32 vcc, s22, v16\n"
    "s_sub_u32 s22, s22, 0x400\n"
    "s_mov_b32 s33, 0x90000000\n"
    "v_cndmask_b32 v55, v53, v54, vcc\n"
    "v_lshl_add_u32 v0, v0, 4, v55\n"
    "buffer_load_dwordx4 v248, s[44:47], s84 offen lds\n"
    "v_cmp_le_i32 vcc, s22, v16\n"
    "s_sub_u32 s22, s22, 0x400\n"
    "s_nop 0\n"
    "v_cndmask_b32 v55, v53, v54, vcc\n"
    "v_lshl_add_u32 v1, v1, 4, v55\n"
    "v_cmp_le_i32 vcc, s22, v16\n"
    "s_sub_u32 s22, s22, 0x400\n"
    "s_nop 0\n"
    "v_cndmask_b32 v55, v53, v54, vcc\n"
    "buffer_load_dwordx4 v249, s[44:47], s84 offen offset:1024 lds\n"
    "s_add_u32 m0, s83, 43008\n"
    "s_add_u32 s84, s59, s62\n"
    "v_lshl_add_u32 v2, v2, 4, v55\n"
    "s_add_u32 s7, s83, 36864\n"
    "v_and_b32 v240, 0x70, v16\n"
    "buffer_load_dwordx4 v244, s[44:47], s84 offen lds\n"
    "v_and_b32 v53, 8, v19\n"
    "v_lshl_add_u32 v53, v52, 7, v53\n"
    "v_lshrrev_b32 v54, 2, v16\n"
    "buffer_load_dwordx4 v245, s[44:47], s84 offen offset:1024 lds\n"
    "s_cmp_eq_u32 s35, 0\n"
    "s_cbranch_scc0 73f\n"
    "s_getreg_b32 s88, hwreg(HW_REG_XCC_ID, 0, 3)\n"
    "s_lshl_b32 s89, s2, 3\n"
    "s_and_b32 s89, s89, 0x3f0\n"
    "s_lshr_b32 s82, s2, 7\n"
    "s_lshl3_add_u32 s89, s82, s89\n"
    "v_mov_b32 v20, s89\n"
    "s_lshr_b32 s89, s88, 2\n"
    "s_lshl1_add_u32 s88, s88, s89\n"
    "s_or_b32 s88, s88, s17\n"
    "s_bfe_u32 s89, s2, 0x10001\n"
    "s_xor_b32 s88, s88, s89\n"
    "s_waitcnt lgkmcnt(1)\n"
    "s_nop 3\n"
    "s_cmp_lg_u32 s72, s17\n"
    "s_cselect_b32 s85, 1, 0\n"
    "s_xor_b32 s88, s88, s85\n"
    "v_mov_b32 v23, s88\n"
    "73:\n"
    "v_add3_u32 v15, s7, v53, v240\n"
    "v_add_u32 v38, s7, v54\n"
    "s_lshr_b32 s8, s4, 1\n"
    "buffer_load_dwordx4 v246, s[44:47], s84 offen offset:2048 lds\n"
    "s_lshl_b32 s8, s8, 11\n"
    "s_add_u32 s8, s8, s3\n"
    "s_add_u32 s8, s8, 147456\n"
    "buffer_load_dwordx4 v247, s[44:47], s84 offen offset:3072 lds\n"
    "s_and_b32 s13, s4, 1\n"
    "s_lshl_b32 s14, s13, 8\n"
    "s_sub_u32 s7, s14, s7\n"
    "s_add_u32 s11, s7, s8\n"
    "s_lshl2_add_u32 s8, s14, s8\n"
    "v_and_b32 v53, 0x30, v16\n"
    "v_lshrrev_b32 v54, 2, v19\n"
    "v_lshl_add_u32 v53, v54, 8, v53\n"
    "v_lshl_add_u32 v53, v52, 6, v53\n"
    "v_add_u32 v39, s8, v53\n"
    "v_mov_b32 v251, 0\n"
    "v_mov_b32 v253, 0\n"
    "v_mov_b32 v255, 0\n"
    "s_add_u32 s63, s62, 98304\n"
    "s_cmp_eq_u32 s63, 294912\n"
    "s_cselect_b32 s63, 0, s63\n"
    "s_add_u32 s64, s63, 98304\n"
    "s_cmp_eq_u32 s64, 294912\n"
    "s_cselect_b32 s64, 0, s64\n"
    "s_add_u32 s7, s3, 160768\n"
    "v_mov_b32 v14, 0\n"
    "v_add_u32 v12, s7, v12\n"
    "s_lshl2_add_u32 s7, s4, s7\n"
    "v_mov_b32 v13, s7\n"
    "v_xor_b32 v53, v52, v19\n"
    "v_mul_u32_u24 v54, 0x300, v19\n"
    "v_lshl_add_u32 v54, v53, 4, v54\n"
    "v_add_u32 v4, s3, v54\n"
    "v_xor_b32 v5, 64, v4\n"
    "v_xor_b32 v6, 128, v4\n"
    "v_xor_b32 v7, 192, v4\n"
    "s_lshl_b32 s7, s4, 7\n"
    "s_lshr_b32 s8, s4, 1\n"
    "s_lshl_b32 s8, s8, 7\n"
    "s_add_u32 s8, s8, 512\n"
    "v_add_u32 v241, s8, v240\n"
    "v_add_u32 v240, s7, v240\n"
    "v_lshrrev_b32 v19, 4, v16\n"
    "s_mul_i32 s7, s52, 5\n"
    "s_lshr_b32 s7, s7, 3\n"
    "s_sub_u32 s8, s52, 1\n"
    "s_bitcmp1_b32 s2, 7\n"
    "s_cselect_b32 s7, s7, 0\n"
    "v_add_u32 v53, s7, v19\n"
    "v_cmp_gt_u32 vcc, s8, v19\n"
    "v_subrev_u32 v54, s8, v53\n"
    "v_min_u32 v53, v53, v54\n"
    "v_cndmask_b32 v53, v19, v53, vcc\n"
    "v_mul_lo_u32 v3, v53, s53\n"
    "v_add_u32 v3, s56, v3\n"
    "s_xor_b32 s29, s29, s23\n"
    "s_mul_i32 s7, s52, s53\n"
    "s_add_u32 s7, s7, s56\n"
    "s_mov_b32 s8, -2752512\n"
    "s_bitcmp1_b32 s2, 0\n"
    "s_cselect_b32 s8, -3538944, s8\n"
    "s_add_u32 s7, s7, s8\n"
    "v_cmp_eq_u32 vcc, s52, v19\n"
    "v_mov_b32 v53, s7\n"
    "s_nop 0\n"
    "v_cndmask_b32 v3, v3, v53, vcc\n"
    "v_add_u32 v53, s23, v3\n"
    "v_lshrrev_b32 v53, 21, v53\n"
    "v_and_b32 v232, 56, v53\n"
    "v_and_b32 v233, 4, v53\n"
    "v_xor_b32 v234, s29, v53\n"
    "s_cmp_eq_u32 s35, 0\n"
    "s_cbranch_scc0 71f\n"
    "s_pack_ll_b32_b16 s88, s0, s16\n"
    "s_mov_b64 exec, 1\n"
    "v_mov_b32 v22, s88\n"
    "global_store_dwordx2 v20, v[22:23], s[54:55] sc1\n"
    "s_mov_b64 exec, -1\n"
    "s_waitcnt lgkmcnt(0)\n"
    "71:\n"
    "s_mov_b64 exec, 0\n"
    "s_cmp_eq_u32 s4, 1\n"
    "s_cbranch_scc1 200f\n"
    "s_cmp_eq_u32 s4, 2\n"
    "s_cbranch_scc1 201f\n"
    "s_cmp_eq_u32 s4, 0\n"
    "s_cbranch_scc1 330f\n"
    "s_branch 331f\n"
    "199:\n"
    "s_mov_b64 exec, -1\n"
    "s_branch 301f\n"
    "81:\n"
    "ds_read_b32 v15, v12\n"
    "s_waitcnt lgkmcnt(0)\n"
    "v_cmp_lt_u32 vcc, v15, v14\n"
    "s_nop 0\n"
    "s_cbranch_vccnz 81b\n"
    "s_branch 82f\n"
    "301:\n"
    "s_branch 302f\n"
    "41:\n"
    "ds_read_b32 v15, v12\n"
    "s_waitcnt lgkmcnt(0)\n"
    "v_cmp_lt_u32 vcc, v15, v14\n"
    "s_nop 0\n"
    "s_cbranch_vccnz 41b\n"
    "s_branch 42f\n"
    "43:\n"
    "s_memrealtime s[92:93]\n"
    "s_waitcnt lgkmcnt(0)\n"
    "s_sub_u32 s84, s92, s89\n"
    "s_cmp_lt_i32 s84, 0\n"
    "s_cbranch_scc0 44f\n"
    "s_sleep 1\n"
    "s_branch 43b\n"
    "302:\n"
    "s_branch 303f\n"
    "61:\n"
    "ds_read_b32 v15, v12\n"
    "s_waitcnt lgkmcnt(0)\n"
    "v_cmp_lt_u32 vcc, v15, v14\n"
    "s_nop 0\n"
    "s_cbranch_vccnz 61b\n"
    "s_branch 62f\n"
    "63:\n"
    "s_memrealtime s[92:93]\n"
    "s_waitcnt lgkmcnt(0)\n"
    "s_sub_u32 s84, s92, s89\n"
    "s_cmp_lt_i32 s84, 0\n"
    "s_cbranch_scc0 64f\n"
    "s_sleep 1\n"
    "s_branch 63b\n"
    "303:\n"
    "s_waitcnt vmcnt(10)\n"
    "ds_write_b32 v13, v14\n"
    "ds_read_u16 v250, v18\n"
    "ds_read_u16 v252, v18 offset:8\n"
    "ds_read_u16 v254, v235\n"
    "s_waitcnt vmcnt(9)\n"
    "ds_read2st64_b64 a[188:191], v15 offset0:0 offset1:1\n"
    "s_waitcnt vmcnt(8)\n"
    "ds_read2st64_b64 a[192:195], v15 offset0:2 offset1:3\n"
    "s_waitcnt vmcnt(7)\n"
    "ds_read2st64_b64 a[196:199], v15 offset0:4 offset1:5\n"
    "s_waitcnt lgkmcnt(3)\n"
    "v_lshlrev_b64 v[40:41], v16, v[250:251]\n"
    "v_lshlrev_b64 v[42:43], v16, v[252:253]\n"
    "v_lshlrev_b64 v[44:45], v16, v[254:255]\n"
    "s_waitcnt vmcnt(6) lgkmcnt(2)\n"
    "ds_read2st64_b64 a[200:203], v15 offset0:6 offset1:7\n"
    "s_nop 0\n"
    "v_mfma_f32_4x4x4_16b_bf16 v[20:23], v[40:41], a[188:189], 0 cbsz:2 abid:0\n"
    "ds_read_u16 v250, v18 offset:64\n"
    "s_add_u32 m0, s83, 47104\n"
    "v_mfma_f32_4x4x4_16b_bf16 v[24:27], v[40:41], a[190:191], 0 cbsz:2 abid:1\n"
    "buffer_load_dwordx4 v248, s[44:47], s84 offen lds\n"
    "v_add_u32 v8, v8, v240\n"
    "s_waitcnt vmcnt(6) lgkmcnt(3)\n"
    "ds_read2st64_b32 a[204:205], v38 offset0:16 offset1:17\n"
    "v_mfma_f32_4x4x4_16b_bf16 v[28:31], v[40:41], a[192:193], 0 cbsz:2 abid:2\n"
    "ds_read2st64_b32 a[206:207], v38 offset0:18 offset1:19\n"
    "ds_read_u16 v252, v18 offset:72\n"
    "v_cvt_pk_bf16_f32 v52, v20, v24\n"
    "v_cvt_pk_bf16_f32 v53, v21, v25\n"
    "v_accvgpr_write_b32 a0, v52\n"
    "v_accvgpr_write_b32 a4, v53\n"
    "v_mfma_f32_4x4x4_16b_bf16 v[32:35], v[40:41], a[194:195], 0 cbsz:2 abid:3\n"
    "buffer_load_dwordx4 v249, s[44:47], s84 offen offset:1024 lds\n"
    "v_cvt_pk_bf16_f32 v54, v22, v26\n"
    "v_cvt_pk_bf16_f32 v55, v23, v27\n"
    "v_accvgpr_write_b32 a8, v54\n"
    "v_accvgpr_write_b32 a12, v55\n"
    "v_add_u32 v9, v9, v240\n"
    "s_waitcnt vmcnt(6) lgkmcnt(5)\n"
    "ds_read2st64_b32 a[208:209], v38 offset0:20 offset1:21\n"
    "v_mfma_f32_4x4x4_16b_bf16 v[20:23], v[42:43], a[196:197], 0 cbsz:2 abid:0\n"
    "ds_read2st64_b32 a[210:211], v38 offset0:22 offset1:23\n"
    "ds_read_u16 v254, v235 offset:64\n"
    "s_add_u32 m0, s83, 49152\n"
    "s_add_u32 s84, s60, s62\n"
    "v_cvt_pk_bf16_f32 v52, v28, v32\n"
    "v_cvt_pk_bf16_f32 v53, v29, v33\n"
    "v_accvgpr_write_b32 a1, v52\n"
    "v_accvgpr_write_b32 a5, v53\n"
    "v_mfma_f32_4x4x4_16b_bf16 v[24:27], v[42:43], a[198:199], 0 cbsz:2 abid:1\n"
    "buffer_load_dwordx4 v244, s[44:47], s84 offen lds\n"
    "v_cvt_pk_bf16_f32 v54, v30, v34\n"
    "v_cvt_pk_bf16_f32 v55, v31, v35\n"
    "v_accvgpr_write_b32 a9, v54\n"
    "v_accvgpr_write_b32 a13, v55\n"
    "v_add_u32 v10, v10, v241\n"
    "s_waitcnt vmcnt(6) lgkmcnt(6)\n"
    "ds_read2st64_b64 a[188:191], v15 offset0:12 offset1:13\n"
    "v_mfma_f32_4x4x4_16b_bf16 v[28:31], v[42:43], a[200:201], 0 cbsz:2 abid:2\n"
    "v_cvt_pk_bf16_f32 v52, v20, v24\n"
    "v_cvt_pk_bf16_f32 v53, v21, v25\n"
    "v_accvgpr_write_b32 a2, v52\n"
    "v_accvgpr_write_b32 a6, v53\n"
    "v_mfma_f32_4x4x4_16b_bf16 v[32:35], v[42:43], a[202:203], 0 cbsz:2 abid:3\n"
    "buffer_load_dwordx4 v245, s[44:47], s84 offen offset:1024 lds\n"
    "v_cvt_pk_bf16_f32 v54, v22, v26\n"
    "v_cvt_pk_bf16_f32 v55, v23, v27\n"
    "v_accvgpr_write_b32 a10, v54\n"
    "v_accvgpr_write_b32 a14, v55\n"
    "v_lshlrev_b64 v[46:47], v16, v[250:251]\n"
    "s_mov_b32 s12, 0xff00ff\n"
    "s_waitcnt vmcnt(6) lgkmcnt(4)\n"
    "ds_read2st64_b64 a[192:195], v15 offset0:14 offset1:15\n"
    "v_mfma_f32_4x4x4_16b_bf16 v[20:23], v[44:45], a[204:205], 0 cbsz:2 abid:0\n"
    "v_cvt_pk_bf16_f32 v52, v28, v32\n"
    "v_cvt_pk_bf16_f32 v53, v29, v33\n"
    "v_accvgpr_write_b32 a3, v52\n"
    "v_accvgpr_write_b32 a7, v53\n"
    "v_mfma_f32_4x4x4_16b_bf16 v[24:27], v[44:45], a[206:207], 0 cbsz:2 abid:1\n"
    "buffer_load_dwordx4 v246, s[44:47], s84 offen offset:2048 lds\n"
    "v_cvt_pk_bf16_f32 v54, v30, v34\n"
    "v_cvt_pk_bf16_f32 v55, v31, v35\n"
    "v_accvgpr_write_b32 a11, v54\n"
    "v_accvgpr_write_b32 a15, v55\n"
    "buffer_load_dwordx4 v247, s[44:47], s84 offen offset:3072 lds\n"
    "v_lshlrev_b64 v[48:49], v16, v[252:253]\n"
    "s_mov_b32 s13, 0xff00ff\n"
    "s_waitcnt vmcnt(7) lgkmcnt(2)\n"
    "ds_read2st64_b64 a[196:199], v15 offset0:16 offset1:17\n"
    "v_mfma_f32_4x4x4_16b_bf16 v[28:31], v[44:45], a[208:209], 0 cbsz:2 abid:2\n"
    "s_add_u32 m0, s83, 53248\n"
    "v_cvt_pk_bf16_f32 v52, v20, v22\n"
    "v_cvt_pk_bf16_f32 v53, v21, v23\n"
    "v_accvgpr_write_b32 a16, v52\n"
    "v_accvgpr_write_b32 a20, v53\n"
    "v_mfma_f32_4x4x4_16b_bf16 v[32:35], v[44:45], a[210:211], 0 cbsz:2 abid:3\n"
    "buffer_load_dwordx4 v248, s[44:47], s84 offen lds\n"
    "v_cvt_pk_bf16_f32 v54, v24, v26\n"
    "v_cvt_pk_bf16_f32 v55, v25, v27\n"
    "v_accvgpr_write_b32 a17, v54\n"
    "v_accvgpr_write_b32 a21, v55\n"
    "v_lshlrev_b64 v[50:51], v16, v[254:255]\n"
    "s_add_u32 s77, s57, 36864\n"
    "s_waitcnt vmcnt(7) lgkmcnt(2)\n"
    "ds_read2st64_b64 a[200:203], v15 offset0:18 offset1:19\n"
    "v_mfma_f32_4x4x4_16b_bf16 v[20:23], v[46:47], a[188:189], 0 cbsz:2 abid:0\n"
    "ds_read_u16 v250, v18 offset:128\n"
    "v_cvt_pk_bf16_f32 v52, v28, v30\n"
    "v_cvt_pk_bf16_f32 v53, v29, v31\n"
    "v_accvgpr_write_b32 a18, v52\n"
    "v_accvgpr_write_b32 a22, v53\n"
    "v_mfma_f32_4x4x4_16b_bf16 v[24:27], v[46:47], a[190:191], 0 cbsz:2 abid:1\n"
    "buffer_load_dwordx4 v249, s[44:47], s84 offen offset:1024 lds\n"
    "v_cvt_pk_bf16_f32 v54, v32, v34\n"
    "v_cvt_pk_bf16_f32 v55, v33, v35\n"
    "v_accvgpr_write_b32 a19, v54\n"
    "v_accvgpr_write_b32 a23, v55\n"
    "s_movk_i32 s88, 24576\n"
    "s_waitcnt vmcnt(7) lgkmcnt(3)\n"
    "ds_read2st64_b32 a[204:205], v38 offset0:40 offset1:41\n"
    "v_mfma_f32_4x4x4_16b_bf16 v[28:31], v[46:47], a[192:193], 0 cbsz:2 abid:2\n"
    "ds_read2st64_b32 a[206:207], v38 offset0:42 offset1:43\n"
    "ds_read_u16 v252, v18 offset:136\n"
    "s_add_u32 m0, s83, 55296\n"
    "s_add_u32 s84, s61, s62\n"
    "v_cvt_pk_bf16_f32 v52, v20, v24\n"
    "v_cvt_pk_bf16_f32 v53, v21, v25\n"
    "v_accvgpr_write_b32 a24, v52\n"
    "v_accvgpr_write_b32 a28, v53\n"
    "v_mfma_f32_4x4x4_16b_bf16 v[32:35], v[46:47], a[194:195], 0 cbsz:2 abid:3\n"
    "buffer_load_dwordx4 v244, s[44:47], s84 offen lds\n"
    "v_cvt_pk_bf16_f32 v54, v22, v26\n"
    "v_cvt_pk_bf16_f32 v55, v23, v27\n"
    "v_accvgpr_write_b32 a32, v54\n"
    "v_accvgpr_write_b32 a36, v55\n"
    "buffer_load_dwordx4 v245, s[44:47], s84 offen offset:1024 lds\n"
    "s_movk_i32 s78, 24576\n"
    "s_waitcnt vmcnt(8) lgkmcnt(5)\n"
    "ds_read2st64_b32 a[208:209], v38 offset0:44 offset1:45\n"
    "v_mfma_f32_4x4x4_16b_bf16 v[20:23], v[48:49], a[196:197], 0 cbsz:2 abid:0\n"
    "ds_read2st64_b32 a[210:211], v38 offset0:46 offset1:47\n"
    "ds_read_u16 v254, v235 offset:128\n"
    "v_cvt_pk_bf16_f32 v52, v28, v32\n"
    "v_cvt_pk_bf16_f32 v53, v29, v33\n"
    "v_accvgpr_write_b32 a25, v52\n"
    "v_accvgpr_write_b32 a29, v53\n"
    "v_mfma_f32_4x4x4_16b_bf16 v[24:27], v[48:49], a[198:199], 0 cbsz:2 abid:1\n"
    "buffer_load_dwordx4 v246, s[44:47], s84 offen offset:2048 lds\n"
    "v_cvt_pk_bf16_f32 v54, v30, v34\n"
    "v_cvt_pk_bf16_f32 v55, v31, v35\n"
    "v_accvgpr_write_b32 a33, v54\n"
    "v_accvgpr_write_b32 a37, v55\n"
    "s_mov_b32 s87, -122880\n"
    "s_waitcnt vmcnt(8) lgkmcnt(6)\n"
    "ds_read2st64_b64 a[188:191], v15 offset0:24 offset1:25\n"
    "v_mfma_f32_4x4x4_16b_bf16 v[28:31], v[48:49], a[200:201], 0 cbsz:2 abid:2\n"
    "v_cvt_pk_bf16_f32 v52, v20, v24\n"
    "v_cvt_pk_bf16_f32 v53, v21, v25\n"
    "v_accvgpr_write_b32 a26, v52\n"
    "v_accvgpr_write_b32 a30, v53\n"
    "v_mfma_f32_4x4x4_16b_bf16 v[32:35], v[48:49], a[202:203], 0 cbsz:2 abid:3\n"
    "buffer_load_dwordx4 v247, s[44:47], s84 offen offset:3072 lds\n"
    "v_cvt_pk_bf16_f32 v54, v22, v26\n"
    "v_cvt_pk_bf16_f32 v55, v23, v27\n"
    "v_accvgpr_write_b32 a34, v54\n"
    "v_accvgpr_write_b32 a38, v55\n"
    "v_lshlrev_b64 v[40:41], v16, v[250:251]\n"
    "s_add_u32 s80, s57, 147456\n"
    "s_waitcnt vmcnt(8) lgkmcnt(4)\n"
    "ds_read2st64_b64 a[192:195], v15 offset0:26 offset1:27\n"
    "v_mfma_f32_4x4x4_16b_bf16 v[20:23], v[50:51], a[204:205], 0 cbsz:2 abid:0\n"
    "s_add_u32 m0, s83, 59392\n"
    "v_cvt_pk_bf16_f32 v52, v28, v32\n"
    "v_cvt_pk_bf16_f32 v53, v29, v33\n"
    "v_accvgpr_write_b32 a27, v52\n"
    "v_accvgpr_write_b32 a31, v53\n"
    "v_mfma_f32_4x4x4_16b_bf16 v[24:27], v[50:51], a[206:207], 0 cbsz:2 abid:1\n"
    "buffer_load_dwordx4 v248, s[44:47], s84 offen lds\n"
    "v_cvt_pk_bf16_f32 v54, v30, v34\n"
    "v_cvt_pk_bf16_f32 v55, v31, v35\n"
    "v_accvgpr_write_b32 a35, v54\n"
    "v_accvgpr_write_b32 a39, v55\n"
    "v_lshlrev_b64 v[42:43], v16, v[252:253]\n"
    "s_mov_b32 s86, 5\n"
    "s_waitcnt vmcnt(8) lgkmcnt(2)\n"
    "ds_read2st64_b64 a[196:199], v15 offset0:28 offset1:29\n"
    "v_mfma_f32_4x4x4_16b_bf16 v[28:31], v[50:51], a[208:209], 0 cbsz:2 abid:2\n"
    "v_cvt_pk_bf16_f32 v52, v20, v22\n"
    "v_cvt_pk_bf16_f32 v53, v21, v23\n"
    "v_accvgpr_write_b32 a40, v52\n"
    "v_accvgpr_write_b32 a44, v53\n"
    "v_mfma_f32_4x4x4_16b_bf16 v[32:35], v[50:51], a[210:211], 0 cbsz:2 abid:3\n"
    "buffer_load_dwordx4 v249, s[44:47], s84 offen offset:1024 lds\n"
    "s_add_u32 m0, s83, 61440\n"
    "s_add_u32 s84, s58, s63\n"
    "v_cvt_pk_bf16_f32 v54, v24, v26\n"
    "v_cvt_pk_bf16_f32 v55, v25, v27\n"
    "v_accvgpr_write_b32 a41, v54\n"
    "v_accvgpr_write_b32 a45, v55\n"
    "buffer_load_dwordx4 v244, s[44:47], s84 offen lds\n"
    "v_lshlrev_b64 v[44:45], v16, v[254:255]\n"
    "s_lshr_b32 s7, s2, 1\n"
    "s_waitcnt vmcnt(9) lgkmcnt(2)\n"
    "s_barrier\n"
    "ds_read2st64_b64 a[200:203], v15 offset0:30 offset1:31\n"
    "v_mfma_f32_4x4x4_16b_bf16 v[20:23], v[40:41], a[188:189], 0 cbsz:2 abid:0\n"
    "ds_read_u16 v250, v18 offset:192\n"
    "v_cvt_pk_bf16_f32 v52, v28, v30\n"
    "v_cvt_pk_bf16_f32 v53, v29, v31\n"
    "v_accvgpr_write_b32 a42, v52\n"
    "v_accvgpr_write_b32 a46, v53\n"
    "v_mfma_f32_4x4x4_16b_bf16 v[24:27], v[40:41], a[190:191], 0 cbsz:2 abid:1\n"
    "buffer_load_dwordx4 v245, s[44:47], s84 offen offset:1024 lds\n"
    "v_cvt_pk_bf16_f32 v54, v32, v34\n"
    "v_cvt_pk_bf16_f32 v55, v33, v35\n"
    "v_accvgpr_write_b32 a43, v54\n"
    "v_accvgpr_write_b32 a47, v55\n"
    "s_and_b32 s7, s7, 0x7c\n"
    "s_waitcnt vmcnt(9) lgkmcnt(3)\n"
    "ds_read2st64_b32 a[204:205], v38 offset0:64 offset1:65\n"
    "v_mfma_f32_4x4x4_16b_bf16 v[28:31], v[40:41], a[192:193], 0 cbsz:2 abid:2\n"
    "ds_read2st64_b32 a[206:207], v38 offset0:66 offset1:67\n"
    "ds_read_u16 v252, v18 offset:200\n"
    "v_cvt_pk_bf16_f32 v52, v20, v24\n"
    "v_cvt_pk_bf16_f32 v53, v21, v25\n"
    "v_accvgpr_write_b32 a48, v52\n"
    "v_accvgpr_write_b32 a52, v53\n"
    "v_mfma_f32_4x4x4_16b_bf16 v[32:35], v[40:41], a[194:195], 0 cbsz:2 abid:3\n"
    "buffer_load_dwordx4 v246, s[44:47], s84 offen offset:2048 lds\n"
    "v_cvt_pk_bf16_f32 v54, v22, v26\n"
    "v_cvt_pk_bf16_f32 v55, v23, v27\n"
    "v_accvgpr_write_b32 a56, v54\n"
    "v_accvgpr_write_b32 a60, v55\n"
    "s_or_b32 s7, s7, s28\n"
    "s_waitcnt vmcnt(9) lgkmcnt(5)\n"
    "ds_read2st64_b32 a[208:209], v38 offset0:68 offset1:69\n"
    "v_mfma_f32_4x4x4_16b_bf16 v[20:23], v[42:43], a[196:197], 0 cbsz:2 abid:0\n"
    "ds_read2st64_b32 a[210:211], v38 offset0:70 offset1:71\n"
    "ds_read_u16 v254, v235 offset:192\n"
    "v_cvt_pk_bf16_f32 v52, v28, v32\n"
    "v_cvt_pk_bf16_f32 v53, v29, v33\n"
    "v_accvgpr_write_b32 a49, v52\n"
    "v_accvgpr_write_b32 a53, v53\n"
    "v_mfma_f32_4x4x4_16b_bf16 v[24:27], v[42:43], a[198:199], 0 cbsz:2 abid:1\n"
    "buffer_load_dwordx4 v247, s[44:47], s84 offen offset:3072 lds\n"
    "v_cvt_pk_bf16_f32 v54, v30, v34\n"
    "v_cvt_pk_bf16_f32 v55, v31, v35\n"
    "v_accvgpr_write_b32 a57, v54\n"
    "v_accvgpr_write_b32 a61, v55\n"
    "s_mul_i32 s7, s7, 79\n"
    "s_waitcnt vmcnt(9) lgkmcnt(6)\n"
    "ds_read2st64_b64 a[188:191], v15 offset0:36 offset1:37\n"
    "v_mfma_f32_4x4x4_16b_bf16 v[28:31], v[42:43], a[200:201], 0 cbsz:2 abid:2\n"
    "s_add_u32 m0, s83, 65536\n"
    "v_cvt_pk_bf16_f32 v52, v20, v24\n"
    "v_cvt_pk_bf16_f32 v53, v21, v25\n"
    "v_accvgpr_write_b32 a50, v52\n"
    "v_accvgpr_write_b32 a54, v53\n"
    "v_mfma_f32_4x4x4_16b_bf16 v[32:35], v[42:43], a[202:203], 0 cbsz:2 abid:3\n"
    "buffer_load_dwordx4 v248, s[44:47], s84 offen lds\n"
    "v_cvt_pk_bf16_f32 v54, v22, v26\n"
    "v_cvt_pk_bf16_f32 v55, v23, v27\n"
    "v_accvgpr_write_b32 a58, v54\n"
    "v_accvgpr_write_b32 a62, v55\n"
    "buffer_load_dwordx4 v249, s[44:47], s84 offen offset:1024 lds\n"
    "v_lshlrev_b64 v[46:47], v16, v[250:251]\n"
    "s_and_b32 s7, s7, 0x7f\n"
    "s_waitcnt vmcnt(10) lgkmcnt(4)\n"
    "ds_read2st64_b64 a[192:195], v15 offset0:38 offset1:39\n"
    "v_mfma_f32_4x4x4_16b_bf16 v[20:23], v[44:45], a[204:205], 0 cbsz:2 abid:0\n"
    "s_add_u32 m0, s83, 36864\n"
    "s_add_u32 s84, s59, s63\n"
    "v_cvt_pk_bf16_f32 v52, v28, v32\n"
    "v_cvt_pk_bf16_f32 v53, v29, v33\n"
    "v_accvgpr_write_b32 a51, v52\n"
    "v_accvgpr_write_b32 a55, v53\n"
    "v_mfma_f32_4x4x4_16b_bf16 v[24:27], v[44:45], a[206:207], 0 cbsz:2 abid:1\n"
    "buffer_load_dwordx4 v244, s[44:47], s84 offen lds\n"
    "v_cvt_pk_bf16_f32 v54, v30, v34\n"
    "v_cvt_pk_bf16_f32 v55, v31, v35\n"
    "v_accvgpr_write_b32 a59, v54\n"
    "v_accvgpr_write_b32 a63, v55\n"
    "v_lshlrev_b64 v[48:49], v16, v[252:253]\n"
    "s_mul_i32 s7, s7, 50\n"
    "s_waitcnt vmcnt(10) lgkmcnt(2)\n"
    "ds_read2st64_b64 a[196:199], v15 offset0:40 offset1:41\n"
    "v_mfma_f32_4x4x4_16b_bf16 v[28:31], v[44:45], a[208:209], 0 cbsz:2 abid:2\n"
    "v_cvt_pk_bf16_f32 v52, v20, v22\n"
    "v_cvt_pk_bf16_f32 v53, v21, v23\n"
    "v_accvgpr_write_b32 a64, v52\n"
    "v_accvgpr_write_b32 a68, v53\n"
    "v_mfma_f32_4x4x4_16b_bf16 v[32:35], v[44:45], a[210:211], 0 cbsz:2 abid:3\n"
    "buffer_load_dwordx4 v245, s[44:47], s84 offen offset:1024 lds\n"
    "v_cvt_pk_bf16_f32 v54, v24, v26\n"
    "v_cvt_pk_bf16_f32 v55, v25, v27\n"
    "v_accvgpr_write_b32 a65, v54\n"
    "v_accvgpr_write_b32 a69, v55\n"
    "v_lshlrev_b64 v[50:51], v16, v[254:255]\n"
    "s_lshr_b32 s7, s7, 6\n"
    "s_waitcnt vmcnt(10) lgkmcnt(2)\n"
    "ds_read2st64_b64 a[200:203], v15 offset0:42 offset1:43\n"
    "v_mfma_f32_4x4x4_16b_bf16 v[20:23], v[46:47], a[188:189], 0 cbsz:2 abid:0\n"
    "ds_read_u16 v250, v18 offset:256\n"
    "v_cvt_pk_bf16_f32 v52, v28, v30\n"
    "v_cvt_pk_bf16_f32 v53, v29, v31\n"
    "v_accvgpr_write_b32 a66, v52\n"
    "v_accvgpr_write_b32 a70, v53\n"
    "v_mfma_f32_4x4x4_16b_bf16 v[24:27], v[46:47], a[190:191], 0 cbsz:2 abid:1\n"
    "buffer_load_dwordx4 v246, s[44:47], s84 offen offset:2048 lds\n"
    "v_cvt_pk_bf16_f32 v54, v32, v34\n"
    "v_cvt_pk_bf16_f32 v55, v33, v35\n"
    "v_accvgpr_write_b32 a67, v54\n"
    "v_accvgpr_write_b32 a71, v55\n"
    "s_sub_i32 s7, s7, 82\n"
    "s_waitcnt vmcnt(10) lgkmcnt(3)\n"
    "ds_read2st64_b32 a[204:205], v38 offset0:88 offset1:89\n"
    "v_mfma_f32_4x4x4_16b_bf16 v[28:31], v[46:47], a[192:193], 0 cbsz:2 abid:2\n"
    "200:\n"
    "s_cbranch_execz 199b\n"
    "ds_read2st64_b32 a[206:207], v38 offset0:90 offset1:91\n"
    "ds_read_u16 v252, v18 offset:264\n"
    "v_cvt_pk_bf16_f32 v52, v20, v24\n"
    "v_cvt_pk_bf16_f32 v53, v21, v25\n"
    "v_accvgpr_write_b32 a72, v52\n"
    "v_accvgpr_write_b32 a76, v53\n"
    "v_mfma_f32_4x4x4_16b_bf16 v[32:35], v[46:47], a[194:195], 0 cbsz:2 abid:3\n"
    "buffer_load_dwordx4 v247, s[44:47], s84 offen offset:3072 lds\n"
    "s_add_u32 m0, s83, 40960\n"
    "v_cvt_pk_bf16_f32 v54, v22, v26\n"
    "v_cvt_pk_bf16_f32 v55, v23, v27\n"
    "v_accvgpr_write_b32 a80, v54\n"
    "v_accvgpr_write_b32 a84, v55\n"
    "buffer_load_dwordx4 v248, s[44:47], s84 offen lds\n"
    "s_max_i32 s7, s7, 0\n"
    "s_waitcnt vmcnt(11) lgkmcnt(5)\n"
    "ds_read2st64_b32 a[208:209], v38 offset0:92 offset1:93\n"
    "v_mfma_f32_4x4x4_16b_bf16 v[20:23], v[48:49], a[196:197], 0 cbsz:2 abid:0\n"
    "ds_read2st64_b32 a[210:211], v38 offset0:94 offset1:95\n"
    "ds_read_u16 v254, v235 offset:256\n"
    "v_cvt_pk_bf16_f32 v52, v28, v32\n"
    "v_cvt_pk_bf16_f32 v53, v29, v33\n"
    "v_accvgpr_write_b32 a73, v52\n"
    "v_accvgpr_write_b32 a77, v53\n"
    "v_mfma_f32_4x4x4_16b_bf16 v[24:27], v[48:49], a[198:199], 0 cbsz:2 abid:1\n"
    "buffer_load_dwordx4 v249, s[44:47], s84 offen offset:1024 lds\n"
    "v_cvt_pk_bf16_f32 v54, v30, v34\n"
    "v_cvt_pk_bf16_f32 v55, v31, v35\n"
    "v_accvgpr_write_b32 a81, v54\n"
    "v_accvgpr_write_b32 a85, v55\n"
    "s_min_i32 s7, s7, 50\n"
    "s_waitcnt vmcnt(11) lgkmcnt(6)\n"
    "ds_read2st64_b64 a[188:191], v15 offset0:48 offset1:49\n"
    "v_mfma_f32_4x4x4_16b_bf16 v[28:31], v[48:49], a[200:201], 0 cbsz:2 abid:2\n"
    "s_add_u32 m0, s83, 43008\n"
    "s_add_u32 s84, s60, s63\n"
    "v_cvt_pk_bf16_f32 v52, v20, v24\n"
    "v_cvt_pk_bf16_f32 v53, v21, v25\n"
    "v_accvgpr_write_b32 a74, v52\n"
    "v_accvgpr_write_b32 a78, v53\n"
    "v_mfma_f32_4x4x4_16b_bf16 v[32:35], v[48:49], a[202:203], 0 cbsz:2 abid:3\n"
    "buffer_load_dwordx4 v244, s[44:47], s84 offen lds\n"
    "v_cvt_pk_bf16_f32 v54, v22, v26\n"
    "v_cvt_pk_bf16_f32 v55, v23, v27\n"
    "v_accvgpr_write_b32 a82, v54\n"
    "v_accvgpr_write_b32 a86, v55\n"
    "v_lshlrev_b64 v[40:41], v16, v[250:251]\n"
    "s_bfm_b64 s[34:35], s7, 2\n"
    "s_waitcnt vmcnt(11) lgkmcnt(4)\n"
    "ds_read2st64_b64 a[192:195], v15 offset0:50 offset1:51\n"
    "v_mfma_f32_4x4x4_16b_bf16 v[20:23], v[50:51], a[204:205], 0 cbsz:2 abid:0\n"
    "v_cvt_pk_bf16_f32 v52, v28, v32\n"
    "v_cvt_pk_bf16_f32 v53, v29, v33\n"
    "v_accvgpr_write_b32 a75, v52\n"
    "v_accvgpr_write_b32 a79, v53\n"
    "v_mfma_f32_4x4x4_16b_bf16 v[24:27], v[50:51], a[206:207], 0 cbsz:2 abid:1\n"
    "buffer_load_dwordx4 v245, s[44:47], s84 offen offset:1024 lds\n"
    "v_cvt_pk_bf16_f32 v54, v30, v34\n"
    "v_cvt_pk_bf16_f32 v55, v31, v35\n"
    "v_accvgpr_write_b32 a83, v54\n"
    "v_accvgpr_write_b32 a87, v55\n"
    "v_lshlrev_b64 v[42:43], v16, v[252:253]\n"
    "s_waitcnt vmcnt(11) lgkmcnt(2)\n"
    "ds_read2st64_b64 a[196:199], v15 offset0:52 offset1:53\n"
    "v_mfma_f32_4x4x4_16b_bf16 v[28:31], v[50:51], a[208:209], 0 cbsz:2 abid:2\n"
    "v_cvt_pk_bf16_f32 v56, v20, v22\n"
    "v_cvt_pk_bf16_f32 v60, v21, v23\n"
    "v_mfma_f32_4x4x4_16b_bf16 v[32:35], v[50:51], a[210:211], 0 cbsz:2 abid:3\n"
    "buffer_load_dwordx4 v246, s[44:47], s84 offen offset:2048 lds\n"
    "v_cvt_pk_bf16_f32 v57, v24, v26\n"
    "v_cvt_pk_bf16_f32 v61, v25, v27\n"
    "buffer_load_dwordx4 v247, s[44:47], s84 offen offset:3072 lds\n"
    "v_lshlrev_b64 v[44:45], v16, v[254:255]\n"
    "s_waitcnt vmcnt(12) lgkmcnt(2)\n"
    "ds_read2st64_b64 a[200:203], v15 offset0:54 offset1:55\n"
    "v_mfma_f32_4x4x4_16b_bf16 v[20:23], v[40:41], a[188:189], 0 cbsz:2 abid:0\n"
    "ds_read_u16 v250, v18 offset:320\n"
    "s_add_u32 m0, s83, 47104\n"
    "v_cvt_pk_bf16_f32 v58, v28, v30\n"
    "v_cvt_pk_bf16_f32 v62, v29, v31\n"
    "v_mfma_f32_4x4x4_16b_bf16 v[24:27], v[40:41], a[190:191], 0 cbsz:2 abid:1\n"
    "buffer_load_dwordx4 v248, s[44:47], s84 offen lds\n"
    "v_cvt_pk_bf16_f32 v59, v32, v34\n"
    "v_cvt_pk_bf16_f32 v63, v33, v35\n"
    "s_waitcnt vmcnt(12) lgkmcnt(3)\n"
    "ds_read2st64_b32 a[204:205], v38 offset0:112 offset1:113\n"
    "v_mfma_f32_4x4x4_16b_bf16 v[28:31], v[40:41], a[192:193], 0 cbsz:2 abid:2\n"
    "ds_read2st64_b32 a[206:207], v38 offset0:114 offset1:115\n"
    "ds_read_u16 v252, v18 offset:328\n"
    "v_cvt_pk_bf16_f32 v64, v20, v24\n"
    "v_cvt_pk_bf16_f32 v68, v21, v25\n"
    "v_mfma_f32_4x4x4_16b_bf16 v[32:35], v[40:41], a[194:195], 0 cbsz:2 abid:3\n"
    "buffer_load_dwordx4 v249, s[44:47], s84 offen offset:1024 lds\n"
    "v_cvt_pk_bf16_f32 v72, v22, v26\n"
    "v_cvt_pk_bf16_f32 v76, v23, v27\n"
    "s_waitcnt vmcnt(12) lgkmcnt(5)\n"
    "ds_read2st64_b32 a[208:209], v38 offset0:116 offset1:117\n"
    "v_mfma_f32_4x4x4_16b_bf16 v[20:23], v[42:43], a[196:197], 0 cbsz:2 abid:0\n"
    "ds_read2st64_b32 a[210:211], v38 offset0:118 offset1:119\n"
    "ds_read_u16 v254, v235 offset:320\n"
    "s_add_u32 m0, s83, 49152\n"
    "s_add_u32 s84, s61, s63\n"
    "v_cvt_pk_bf16_f32 v65, v28, v32\n"
    "v_cvt_pk_bf16_f32 v69, v29, v33\n"
    "v_mfma_f32_4x4x4_16b_bf16 v[24:27], v[42:43], a[198:199], 0 cbsz:2 abid:1\n"
    "buffer_load_dwordx4 v244, s[44:47], s84 offen lds\n"
    "v_cvt_pk_bf16_f32 v73, v30, v34\n"
    "v_cvt_pk_bf16_f32 v77, v31, v35\n"
    "buffer_load_dwordx4 v245, s[44:47], s84 offen offset:1024 lds\n"
    "s_waitcnt vmcnt(13) lgkmcnt(6)\n"
    "ds_read2st64_b64 a[188:191], v15 offset0:0 offset1:1\n"
    "v_mfma_f32_4x4x4_16b_bf16 v[28:31], v[42:43], a[200:201], 0 cbsz:2 abid:2\n"
    "v_cvt_pk_bf16_f32 v66, v20, v24\n"
    "v_cvt_pk_bf16_f32 v70, v21, v25\n"
    "v_mfma_f32_4x4x4_16b_bf16 v[32:35], v[42:43], a[202:203], 0 cbsz:2 abid:3\n"
    "buffer_load_dwordx4 v246, s[44:47], s84 offen offset:2048 lds\n"
    "v_cvt_pk_bf16_f32 v74, v22, v26\n"
    "v_cvt_pk_bf16_f32 v78, v23, v27\n"
    "v_lshlrev_b64 v[46:47], v16, v[250:251]\n"
    "s_waitcnt vmcnt(13) lgkmcnt(4)\n"
    "ds_read2st64_b64 a[192:195], v15 offset0:2 offset1:3\n"
    "v_mfma_f32_4x4x4_16b_bf16 v[20:23], v[44:45], a[204:205], 0 cbsz:2 abid:0\n"
    "v_cvt_pk_bf16_f32 v67, v28, v32\n"
    "v_cvt_pk_bf16_f32 v71, v29, v33\n"
    "v_mfma_f32_4x4x4_16b_bf16 v[24:27], v[44:45], a[206:207], 0 cbsz:2 abid:1\n"
    "buffer_load_dwordx4 v247, s[44:47], s84 offen offset:3072 lds\n"
    "v_cvt_pk_bf16_f32 v75, v30, v34\n"
    "v_cvt_pk_bf16_f32 v79, v31, v35\n"
    "v_lshlrev_b64 v[48:49], v16, v[252:253]\n"
    "s_waitcnt vmcnt(13) lgkmcnt(2)\n"
    "ds_read2st64_b64 a[196:199], v15 offset0:4 offset1:5\n"
    "v_mfma_f32_4x4x4_16b_bf16 v[28:31], v[44:45], a[208:209], 0 cbsz:2 abid:2\n"
    "s_add_u32 m0, s83, 53248\n"
    "v_cvt_pk_bf16_f32 v80, v20, v22\n"
    "v_cvt_pk_bf16_f32 v84, v21, v23\n"
    "v_mfma_f32_4x4x4_16b_bf16 v[32:35], v[44:45], a[210:211], 0 cbsz:2 abid:3\n"
    "buffer_load_dwordx4 v248, s[44:47], s84 offen lds\n"
    "v_cvt_pk_bf16_f32 v81, v24, v26\n"
    "v_cvt_pk_bf16_f32 v85, v25, v27\n"
    "v_lshlrev_b64 v[50:51], v16, v[254:255]\n"
    "s_waitcnt vmcnt(13) lgkmcnt(2)\n"
    "ds_read2st64_b64 a[200:203], v15 offset0:6 offset1:7\n"
    "v_mfma_f32_4x4x4_16b_bf16 v[20:23], v[46:47], a[188:189], 0 cbsz:2 abid:0\n"
    "ds_read_u16 v250, v18 offset:384\n"
    "s_memrealtime s[92:93]\n"
    "v_cvt_pk_bf16_f32 v82, v28, v30\n"
    "v_cvt_pk_bf16_f32 v86, v29, v31\n"
    "v_mfma_f32_4x4x4_16b_bf16 v[24:27], v[46:47], a[190:191], 0 cbsz:2 abid:1\n"
    "buffer_load_dwordx4 v249, s[44:47], s84 offen offset:1024 lds\n"
    "s_add_u32 m0, s83, 55296\n"
    "s_add_u32 s84, s58, s64\n"
    "v_cvt_pk_bf16_f32 v83, v32, v34\n"
    "v_cvt_pk_bf16_f32 v87, v33, v35\n"
    "buffer_load_dwordx4 v244, s[44:47], s84 offen lds\n"
    "s_waitcnt vmcnt(14) lgkmcnt(3)\n"
    "ds_read2st64_b32 a[204:205], v38 offset0:16 offset1:17\n"
    "v_mfma_f32_4x4x4_16b_bf16 v[28:31], v[46:47], a[192:193], 0 cbsz:2 abid:2\n"
    "ds_read2st64_b32 a[206:207], v38 offset0:18 offset1:19\n"
    "ds_read_u16 v252, v18 offset:392\n"
    "v_cvt_pk_bf16_f32 v88, v20, v24\n"
    "v_cvt_pk_bf16_f32 v92, v21, v25\n"
    "v_mfma_f32_4x4x4_16b_bf16 v[32:35], v[46:47], a[194:195], 0 cbsz:2 abid:3\n"
    "buffer_load_dwordx4 v245, s[44:47], s84 offen offset:1024 lds\n"
    "v_cvt_pk_bf16_f32 v96, v22, v26\n"
    "v_cvt_pk_bf16_f32 v100, v23, v27\n"
    "s_waitcnt vmcnt(14) lgkmcnt(5)\n"
    "ds_read2st64_b32 a[208:209], v38 offset0:20 offset1:21\n"
    "v_mfma_f32_4x4x4_16b_bf16 v[20:23], v[48:49], a[196:197], 0 cbsz:2 abid:0\n"
    "ds_read2st64_b32 a[210:211], v38 offset0:22 offset1:23\n"
    "ds_read_u16 v254, v235 offset:384\n"
    "global_load_dwordx4 v[236:239], v16, s[54:55]\n"
    "v_cvt_pk_bf16_f32 v89, v28, v32\n"
    "v_cvt_pk_bf16_f32 v93, v29, v33\n"
    "v_mfma_f32_4x4x4_16b_bf16 v[24:27], v[48:49], a[198:199], 0 cbsz:2 abid:1\n"
    "buffer_load_dwordx4 v246, s[44:47], s84 offen offset:2048 lds\n"
    "v_cvt_pk_bf16_f32 v97, v30, v34\n"
    "v_cvt_pk_bf16_f32 v101, v31, v35\n"
    "s_waitcnt vmcnt(15) lgkmcnt(6)\n"
    "ds_read2st64_b64 a[188:191], v15 offset0:12 offset1:13\n"
    "v_mfma_f32_4x4x4_16b_bf16 v[28:31], v[48:49], a[200:201], 0 cbsz:2 abid:2\n"
    "v_cvt_pk_bf16_f32 v90, v20, v24\n"
    "v_cvt_pk_bf16_f32 v94, v21, v25\n"
    "v_mfma_f32_4x4x4_16b_bf16 v[32:35], v[48:49], a[202:203], 0 cbsz:2 abid:3\n"
    "buffer_load_dwordx4 v247, s[44:47], s84 offen offset:3072 lds\n"
    "v_cvt_pk_bf16_f32 v98, v22, v26\n"
    "v_cvt_pk_bf16_f32 v102, v23, v27\n"
    "v_lshlrev_b64 v[40:41], v16, v[250:251]\n"
    "s_waitcnt vmcnt(15) lgkmcnt(4)\n"
    "ds_read2st64_b64 a[192:195], v15 offset0:14 offset1:15\n"
    "v_mfma_f32_4x4x4_16b_bf16 v[20:23], v[50:51], a[204:205], 0 cbsz:2 abid:0\n"
    "s_add_u32 m0, s83, 59392\n"
    "v_cvt_pk_bf16_f32 v91, v28, v32\n"
    "v_cvt_pk_bf16_f32 v95, v29, v33\n"
    "v_mfma_f32_4x4x4_16b_bf16 v[24:27], v[50:51], a[206:207], 0 cbsz:2 abid:1\n"
    "buffer_load_dwordx4 v248, s[44:47], s84 offen lds\n"
    "v_cvt_pk_bf16_f32 v99, v30, v34\n"
    "v_cvt_pk_bf16_f32 v103, v31, v35\n"
    "buffer_load_dwordx4 v249, s[44:47], s84 offen offset:1024 lds\n"
    "v_lshlrev_b64 v[42:43], v16, v[252:253]\n"
    "s_waitcnt vmcnt(16) lgkmcnt(2)\n"
    "ds_read2st64_b64 a[196:199], v15 offset0:16 offset1:17\n"
    "v_mfma_f32_4x4x4_16b_bf16 v[28:31], v[50:51], a[208:209], 0 cbsz:2 abid:2\n"
    "s_add_u32 m0, s83, 61440\n"
    "s_add_u32 s84, s59, s64\n"
    "v_cvt_pk_bf16_f32 v104, v20, v22\n"
    "v_cvt_pk_bf16_f32 v108, v21, v23\n"
    "v_mfma_f32_4x4x4_16b_bf16 v[32:35], v[50:51], a[210:211], 0 cbsz:2 abid:3\n"
    "buffer_load_dwordx4 v244, s[44:47], s84 offen lds\n"
    "v_cvt_pk_bf16_f32 v105, v24, v26\n"
    "v_cvt_pk_bf16_f32 v109, v25, v27\n"
    "v_lshlrev_b64 v[44:45], v16, v[254:255]\n"
    "s_waitcnt vmcnt(16) lgkmcnt(2)\n"
    "ds_read2st64_b64 a[200:203], v15 offset0:18 offset1:19\n"
    "v_mfma_f32_4x4x4_16b_bf16 v[20:23], v[40:41], a[188:189], 0 cbsz:2 abid:0\n"
    "ds_read_u16 v250, v18 offset:448\n"
    "v_cvt_pk_bf16_f32 v106, v28, v30\n"
    "v_cvt_pk_bf16_f32 v110, v29, v31\n"
    "v_mfma_f32_4x4x4_16b_bf16 v[24:27], v[40:41], a[190:191], 0 cbsz:2 abid:1\n"
    "buffer_load_dwordx4 v245, s[44:47], s84 offen offset:1024 lds\n"
    "v_cvt_pk_bf16_f32 v107, v32, v34\n"
    "v_cvt_pk_bf16_f32 v111, v33, v35\n"
    "s_waitcnt vmcnt(16) lgkmcnt(3)\n"
    "ds_read2st64_b32 a[204:205], v38 offset0:40 offset1:41\n"
    "v_mfma_f32_4x4x4_16b_bf16 v[28:31], v[40:41], a[192:193], 0 cbsz:2 abid:2\n"
    "ds_read2st64_b32 a[206:207], v38 offset0:42 offset1:43\n"
    "ds_read_u16 v252, v18 offset:456\n"
    "v_cvt_pk_bf16_f32 v112, v20, v24\n"
    "v_cvt_pk_bf16_f32 v116, v21, v25\n"
    "v_mfma_f32_4x4x4_16b_bf16 v[32:35], v[40:41], a[194:195], 0 cbsz:2 abid:3\n"
    "buffer_load_dwordx4 v246, s[44:47], s84 offen offset:2048 lds\n"
    "v_cvt_pk_bf16_f32 v120, v22, v26\n"
    "v_cvt_pk_bf16_f32 v124, v23, v27\n"
    "s_waitcnt vmcnt(16) lgkmcnt(5)\n"
    "ds_read2st64_b32 a[208:209], v38 offset0:44 offset1:45\n"
    "v_mfma_f32_4x4x4_16b_bf16 v[20:23], v[42:43], a[196:197], 0 cbsz:2 abid:0\n"
    "ds_read2st64_b32 a[210:211], v38 offset0:46 offset1:47\n"
    "ds_read_u16 v254, v235 offset:448\n"
    "v_cvt_pk_bf16_f32 v113, v28, v32\n"
    "v_cvt_pk_bf16_f32 v117, v29, v33\n"
    "v_mfma_f32_4x4x4_16b_bf16 v[24:27], v[42:43], a[198:199], 0 cbsz:2 abid:1\n"
    "buffer_load_dwordx4 v247, s[44:47], s84 offen offset:3072 lds\n"
    "s_add_u32 m0, s83, 65536\n"
    "v_cvt_pk_bf16_f32 v121, v30, v34\n"
    "v_cvt_pk_bf16_f32 v125, v31, v35\n"
    "buffer_load_dwordx4 v248, s[44:47], s84 offen lds\n"
    "s_waitcnt vmcnt(17) lgkmcnt(6)\n"
    "ds_read2st64_b64 a[188:191], v15 offset0:24 offset1:25\n"
    "v_mfma_f32_4x4x4_16b_bf16 v[28:31], v[42:43], a[200:201], 0 cbsz:2 abid:2\n"
    "v_cvt_pk_bf16_f32 v114, v20, v24\n"
    "v_cvt_pk_bf16_f32 v118, v21, v25\n"
    "v_mfma_f32_4x4x4_16b_bf16 v[32:35], v[42:43], a[202:203], 0 cbsz:2 abid:3\n"
    "buffer_load_dwordx4 v249, s[44:47], s84 offen offset:1024 lds\n"
    "v_cvt_pk_bf16_f32 v122, v22, v26\n"
    "v_cvt_pk_bf16_f32 v126, v23, v27\n"
    "v_lshlrev_b64 v[46:47], v16, v[250:251]\n"
    "s_waitcnt vmcnt(17) lgkmcnt(4)\n"
    "ds_read2st64_b64 a[192:195], v15 offset0:26 offset1:27\n"
    "v_mfma_f32_4x4x4_16b_bf16 v[20:23], v[44:45], a[204:205], 0 cbsz:2 abid:0\n"
    "s_add_u32 m0, s83, 36864\n"
    "s_add_u32 s84, s60, s64\n"
    "v_cvt_pk_bf16_f32 v115, v28, v32\n"
    "v_cvt_pk_bf16_f32 v119, v29, v33\n"
    "v_mfma_f32_4x4x4_16b_bf16 v[24:27], v[44:45], a[206:207], 0 cbsz:2 abid:1\n"
    "buffer_load_dwordx4 v244, s[44:47], s84 offen lds\n"
    "v_cvt_pk_bf16_f32 v123, v30, v34\n"
    "v_cvt_pk_bf16_f32 v127, v31, v35\n"
    "v_lshlrev_b64 v[48:49], v16, v[252:253]\n"
    "s_waitcnt vmcnt(17) lgkmcnt(2)\n"
    "ds_read2st64_b64 a[196:199], v15 offset0:28 offset1:29\n"
    "v_mfma_f32_4x4x4_16b_bf16 v[28:31], v[44:45], a[208:209], 0 cbsz:2 abid:2\n"
    "v_cvt_pk_bf16_f32 v128, v20, v22\n"
    "v_cvt_pk_bf16_f32 v132, v21, v23\n"
    "v_mfma_f32_4x4x4_16b_bf16 v[32:35], v[44:45], a[210:211], 0 cbsz:2 abid:3\n"
    "buffer_load_dwordx4 v245, s[44:47], s84 offen offset:1024 lds\n"
    "v_cvt_pk_bf16_f32 v129, v24, v26\n"
    "v_cvt_pk_bf16_f32 v133, v25, v27\n"
    "v_lshlrev_b64 v[50:51], v16, v[254:255]\n"
    "s_waitcnt vmcnt(12)\n"
    "v_sub_u32 v11, s0, v236\n"
    "v_or_b32 v236, 0xffff, v236\n"
    "v_or_b32 v238, 0xffff, v238\n"
    "s_waitcnt vmcnt(17) lgkmcnt(2)\n"
    "ds_read2st64_b64 a[200:203], v15 offset0:30 offset1:31\n"
    "v_mfma_f32_4x4x4_16b_bf16 v[20:23], v[46:47], a[188:189], 0 cbsz:2 abid:0\n"
    "ds_read_u16 v250, v18 offset:512\n"
    "v_cvt_pk_bf16_f32 v130, v28, v30\n"
    "v_cvt_pk_bf16_f32 v134, v29, v31\n"
    "v_mfma_f32_4x4x4_16b_bf16 v[24:27], v[46:47], a[190:191], 0 cbsz:2 abid:1\n"
    "buffer_load_dwordx4 v246, s[44:47], s84 offen offset:2048 lds\n"
    "v_cvt_pk_bf16_f32 v131, v32, v34\n"
    "v_cvt_pk_bf16_f32 v135, v33, v35\n"
    "buffer_load_dwordx4 v247, s[44:47], s84 offen offset:3072 lds\n"
    "v_and_b32 v242, 15, v237\n"
    "v_and_b32 v243, 1, v239\n"
    "v_or_b32 v237, 15, v237\n"
    "v_or_b32 v239, 15, v239\n"
    "s_waitcnt vmcnt(18) lgkmcnt(3)\n"
    "ds_read2st64_b32 a[204:205], v38 offset0:64 offset1:65\n"
    "v_mfma_f32_4x4x4_16b_bf16 v[28:31], v[46:47], a[192:193], 0 cbsz:2 abid:2\n"
    "ds_read2st64_b32 a[206:207], v38 offset0:66 offset1:67\n"
    "ds_read_u16 v252, v18 offset:520\n"
    "s_add_u32 m0, s83, 40960\n"
    "v_cvt_pk_bf16_f32 v136, v20, v24\n"
    "v_cvt_pk_bf16_f32 v140, v21, v25\n"
    "v_mfma_f32_4x4x4_16b_bf16 v[32:35], v[46:47], a[194:195], 0 cbsz:2 abid:3\n"
    "buffer_load_dwordx4 v248, s[44:47], s84 offen lds\n"
    "v_cvt_pk_bf16_f32 v144, v22, v26\n"
    "v_cvt_pk_bf16_f32 v148, v23, v27\n"
    "v_cmp_ne_u64 vcc, s[18:19], v[236:237]\n"
    "v_cmp_ne_u64 s[48:49], s[18:19], v[238:239]\n"
    "v_readlane_b32 s9, v242, 0\n"
    "v_and_b32 v236, 1, v242\n"
    "s_waitcnt vmcnt(18) lgkmcnt(5)\n"
    "ds_read2st64_b32 a[208:209], v38 offset0:68 offset1:69\n"
    "v_mfma_f32_4x4x4_16b_bf16 v[20:23], v[48:49], a[196:197], 0 cbsz:2 abid:0\n"
    "ds_read2st64_b32 a[210:211], v38 offset0:70 offset1:71\n"
    "ds_read_u16 v254, v235 offset:512\n"
    "v_cvt_pk_bf16_f32 v137, v28, v32\n"
    "v_cvt_pk_bf16_f32 v141, v29, v33\n"
    "v_mfma_f32_4x4x4_16b_bf16 v[24:27], v[48:49], a[198:199], 0 cbsz:2 abid:1\n"
    "buffer_load_dwordx4 v249, s[44:47], s84 offen offset:1024 lds\n"
    "v_cvt_pk_bf16_f32 v145, v30, v34\n"
    "v_cvt_pk_bf16_f32 v149, v31, v35\n"
    "s_or_b64 s[48:49], s[48:49], vcc\n"
    "s_cbranch_scc1 75f\n"
    "77:\n"
    "v_cmp_eq_u32 s[24:25], 1, v236\n"
    "s_waitcnt vmcnt(18) lgkmcnt(6)\n"
    "ds_read2st64_b64 a[188:191], v15 offset0:36 offset1:37\n"
    "v_mfma_f32_4x4x4_16b_bf16 v[28:31], v[48:49], a[200:201], 0 cbsz:2 abid:2\n"
    "s_add_u32 m0, s83, 43008\n"
    "s_add_u32 s84, s61, s64\n"
    "v_cvt_pk_bf16_f32 v138, v20, v24\n"
    "v_cvt_pk_bf16_f32 v142, v21, v25\n"
    "v_mfma_f32_4x4x4_16b_bf16 v[32:35], v[48:49], a[202:203], 0 cbsz:2 abid:3\n"
    "buffer_load_dwordx4 v244, s[44:47], s84 offen lds\n"
    "v_cvt_pk_bf16_f32 v146, v22, v26\n"
    "v_cvt_pk_bf16_f32 v150, v23, v27\n"
    "buffer_load_dwordx4 v245, s[44:47], s84 offen offset:1024 lds\n"
    "v_lshlrev_b64 v[40:41], v16, v[250:251]\n"
    "s_lshr_b32 s9, s9, 1\n"
    "s_add_u32 s9, s9, s28\n"
    "v_lshrrev_b64 v[240:241], v232, s[24:25]\n"
    "v_and_b32 v241, 0xff, v240\n"
    "s_waitcnt vmcnt(19) lgkmcnt(4)\n"
    "ds_read2st64_b64 a[192:195], v15 offset0:38 offset1:39\n"
    "v_mfma_f32_4x4x4_16b_bf16 v[20:23], v[50:51], a[204:205], 0 cbsz:2 abid:0\n"
    "v_cvt_pk_bf16_f32 v139, v28, v32\n"
    "v_cvt_pk_bf16_f32 v143, v29, v33\n"
    "v_mfma_f32_4x4x4_16b_bf16 v[24:27], v[50:51], a[206:207], 0 cbsz:2 abid:1\n"
    "buffer_load_dwordx4 v246, s[44:47], s84 offen offset:2048 lds\n"
    "v_cvt_pk_bf16_f32 v147, v30, v34\n"
    "v_cvt_pk_bf16_f32 v151, v31, v35\n"
    "v_lshlrev_b64 v[42:43], v16, v[252:253]\n"
    "v_bfe_u32 v240, v240, v233, 4\n"
    "v_bcnt_u32_b32 v241, v241, 8\n"
    "v_bcnt_u32_b32 v240, v240, 0\n"
    "v_lshl_add_u32 v240, v240, 1, v241\n"
    "s_waitcnt vmcnt(18) lgkmcnt(2)\n"
    "ds_read2st64_b64 a[196:199], v15 offset0:40 offset1:41\n"
    "v_mfma_f32_4x4x4_16b_bf16 v[28:31], v[50:51], a[208:209], 0 cbsz:2 abid:2\n"
    "v_cvt_pk_bf16_f32 v152, v20, v22\n"
    "v_cvt_pk_bf16_f32 v156, v21, v23\n"
    "v_mfma_f32_4x4x4_16b_bf16 v[32:35], v[50:51], a[210:211], 0 cbsz:2 abid:3\n"
    "buffer_load_dwordx4 v247, s[44:47], s84 offen offset:3072 lds\n"
    "v_cvt_pk_bf16_f32 v153, v24, v26\n"
    "v_cvt_pk_bf16_f32 v157, v25, v27\n"
    "v_lshlrev_b64 v[44:45], v16, v[254:255]\n"
    "s_lshr_b32 s10, s9, 2\n"
    "v_cmp_lt_u32 vcc, 15, v240\n"
    "v_xor_b32 v234, s10, v234\n"
    "s_mov_b64 s[50:51], vcc\n"
    "s_waitcnt vmcnt(18) lgkmcnt(2)\n"
    "ds_read2st64_b64 a[200:203], v15 offset0:42 offset1:43\n"
    "v_mfma_f32_4x4x4_16b_bf16 v[20:23], v[40:41], a[188:189], 0 cbsz:2 abid:0\n"
    "ds_read_u16 v250, v18 offset:576\n"
    "s_add_u32 m0, s83, 47104\n"
    "v_cvt_pk_bf16_f32 v154, v28, v30\n"
    "v_cvt_pk_bf16_f32 v158, v29, v31\n"
    "v_mfma_f32_4x4x4_16b_bf16 v[24:27], v[40:41], a[190:191], 0 cbsz:2 abid:1\n"
    "buffer_load_dwordx4 v248, s[44:47], s84 offen lds\n"
    "v_cvt_pk_bf16_f32 v155, v32, v34\n"
    "v_cvt_pk_bf16_f32 v159, v33, v35\n"
    "v_addc_co_u32 v234, vcc, 0, v234, vcc\n"
    "v_and_b32 v234, 1, v234\n"
    "v_mad_u32_u24 v236, v234, s88, v3\n"
    "v_cmp_eq_u32 s[26:27], 1, v243\n"
    "s_waitcnt vmcnt(18) lgkmcnt(3)\n"
    "ds_read2st64_b32 a[204:205], v38 offset0:88 offset1:89\n"
    "v_mfma_f32_4x4x4_16b_bf16 v[28:31], v[40:41], a[192:193], 0 cbsz:2 abid:2\n"
    "ds_read2st64_b32 a[206:207], v38 offset0:90 offset1:91\n"
    "ds_read_u16 v252, v18 offset:584\n"
    "v_cvt_pk_bf16_f32 v160, v20, v24\n"
    "v_cvt_pk_bf16_f32 v164, v21, v25\n"
    "v_mfma_f32_4x4x4_16b_bf16 v[32:35], v[40:41], a[194:195], 0 cbsz:2 abid:3\n"
    "buffer_load_dwordx4 v249, s[44:47], s84 offen offset:1024 lds\n"
    "v_cvt_pk_bf16_f32 v168, v22, v26\n"
    "v_cvt_pk_bf16_f32 v172, v23, v27\n"
    "v_readlane_b32 s82, v236, 0\n"
    "v_readlane_b32 s73, v236, 1\n"
    "s_waitcnt vmcnt(18) lgkmcnt(5)\n"
    "ds_read2st64_b32 a[208:209], v38 offset0:92 offset1:93\n"
    "v_mfma_f32_4x4x4_16b_bf16 v[20:23], v[42:43], a[196:197], 0 cbsz:2 abid:0\n"
    "ds_read2st64_b32 a[210:211], v38 offset0:94 offset1:95\n"
    "ds_read_u16 v254, v235 offset:576\n"
    "s_barrier\n"
    "s_mov_b32 m0, s57\n"
    "v_cvt_pk_bf16_f32 v161, v28, v32\n"
    "v_cvt_pk_bf16_f32 v165, v29, v33\n"
    "v_mfma_f32_4x4x4_16b_bf16 v[24:27], v[42:43], a[198:199], 0 cbsz:2 abid:1\n"
    "buffer_load_dwordx4 v0, s[36:39], s82 offen nt lds\n"
    "v_cvt_pk_bf16_f32 v169, v30, v34\n"
    "v_cvt_pk_bf16_f32 v173, v31, v35\n"
    "s_waitcnt vmcnt(18) lgkmcnt(6)\n"
    "ds_read2st64_b64 a[188:191], v15 offset0:48 offset1:49\n"
    "v_mfma_f32_4x4x4_16b_bf16 v[28:31], v[42:43], a[200:201], 0 cbsz:2 abid:2\n"
    "v_cvt_pk_bf16_f32 v162, v20, v24\n"
    "v_cvt_pk_bf16_f32 v166, v21, v25\n"
    "v_mfma_f32_4x4x4_16b_bf16 v[32:35], v[42:43], a[202:203], 0 cbsz:2 abid:3\n"
    "buffer_load_dwordx4 v1, s[36:39], s82 offen offset:1024 nt lds\n"
    "v_cvt_pk_bf16_f32 v170, v22, v26\n"
    "v_cvt_pk_bf16_f32 v174, v23, v27\n"
    "v_lshlrev_b64 v[46:47], v16, v[250:251]\n"
    "v_cmp_gt_u32 s[48:49], s96, v232\n"
    "v_lshrrev_b64 v[240:241], v232, s[26:27]\n"
    "v_and_b32 v241, 0xff, v240\n"
    "v_bfe_u32 v240, v240, v233, 4\n"
    "s_waitcnt vmcnt(18) lgkmcnt(4)\n"
    "ds_read2st64_b64 a[192:195], v15 offset0:50 offset1:51\n"
    "v_mfma_f32_4x4x4_16b_bf16 v[20:23], v[44:45], a[204:205], 0 cbsz:2 abid:0\n"
    "v_cvt_pk_bf16_f32 v163, v28, v32\n"
    "v_cvt_pk_bf16_f32 v167, v29, v33\n"
    "v_mfma_f32_4x4x4_16b_bf16 v[24:27], v[44:45], a[206:207], 0 cbsz:2 abid:1\n"
    "buffer_load_dwordx4 v2, s[36:39], s82 offen offset:2048 nt lds\n"
    "v_cvt_pk_bf16_f32 v171, v30, v34\n"
    "v_cvt_pk_bf16_f32 v175, v31, v35\n"
    "v_lshlrev_b64 v[48:49], v16, v[252:253]\n"
    "v_bcnt_u32_b32 v241, v241, 8\n"
    "v_bcnt_u32_b32 v240, v240, 0\n"
    "v_lshl_add_u32 v240, v240, 1, v241\n"
    "v_cmp_lt_u32 s[26:27], 15, v240\n"
    "s_waitcnt vmcnt(18) lgkmcnt(2)\n"
    "ds_read2st64_b64 a[196:199], v15 offset0:52 offset1:53\n"
    "v_mfma_f32_4x4x4_16b_bf16 v[28:31], v[44:45], a[208:209], 0 cbsz:2 abid:2\n"
    "v_cvt_pk_bf16_f32 v176, v20, v22\n"
    "v_cvt_pk_bf16_f32 v180, v21, v23\n"
    "v_mfma_f32_4x4x4_16b_bf16 v[32:35], v[44:45], a[210:211], 0 cbsz:2 abid:3\n"
    "v_cvt_pk_bf16_f32 v177, v24, v26\n"
    "v_cvt_pk_bf16_f32 v181, v25, v27\n"
    "v_lshlrev_b64 v[50:51], v16, v[254:255]\n"
    "s_bfm_b64 s[24:25], s52, 0\n"
    "s_bitcmp1_b32 s9, 0\n"
    "s_cselect_b32 s95, -1, 1\n"
    "s_waitcnt vmcnt(17) lgkmcnt(2)\n"
    "ds_read2st64_b64 a[200:203], v15 offset0:54 offset1:55\n"
    "v_mfma_f32_4x4x4_16b_bf16 v[20:23], v[46:47], a[188:189], 0 cbsz:2 abid:0\n"
    "ds_read_u16 v250, v18 offset:640\n"
    "v_cvt_pk_bf16_f32 v178, v28, v30\n"
    "v_cvt_pk_bf16_f32 v182, v29, v31\n"
    "v_mfma_f32_4x4x4_16b_bf16 v[24:27], v[46:47], a[190:191], 0 cbsz:2 abid:1\n"
    "v_cvt_pk_bf16_f32 v179, v32, v34\n"
    "v_cvt_pk_bf16_f32 v183, v33, v35\n"
    "s_cmp_lt_u32 s52, 12\n"
    "s_cselect_b32 s95, 0, s95\n"
    "s_add_u32 s52, s52, s95\n"
    "s_xor_b64 s[26:27], s[26:27], s[50:51]\n"
    "s_waitcnt vmcnt(16) lgkmcnt(3)\n"
    "ds_read2st64_b32 a[204:205], v38 offset0:112 offset1:113\n"
    "v_mfma_f32_4x4x4_16b_bf16 v[28:31], v[46:47], a[192:193], 0 cbsz:2 abid:2\n"
    "ds_read2st64_b32 a[206:207], v38 offset0:114 offset1:115\n"
    "ds_read_u16 v252, v18 offset:648\n"
    "v_cvt_pk_bf16_f32 v184, v20, v24\n"
    "v_cvt_pk_bf16_f32 v188, v21, v25\n"
    "v_mfma_f32_4x4x4_16b_bf16 v[32:35], v[46:47], a[194:195], 0 cbsz:2 abid:3\n"
    "v_cvt_pk_bf16_f32 v192, v22, v26\n"
    "v_cvt_pk_bf16_f32 v196, v23, v27\n"
    "s_and_b64 s[48:49], s[48:49], s[24:25]\n"
    "s_and_b64 s[24:25], s[24:25], s[26:27]\n"
    "s_andn2_b64 s[48:49], s[48:49], s[26:27]\n"
    "s_bcnt1_i32_b64 s10, s[48:49]\n"
    "s_waitcnt vmcnt(15) lgkmcnt(5)\n"
    "ds_read2st64_b32 a[208:209], v38 offset0:116 offset1:117\n"
    "v_mfma_f32_4x4x4_16b_bf16 v[20:23], v[48:49], a[196:197], 0 cbsz:2 abid:0\n"
    "ds_read2st64_b32 a[210:211], v38 offset0:118 offset1:119\n"
    "ds_read_u16 v254, v235 offset:640\n"
    "v_cvt_pk_bf16_f32 v185, v28, v32\n"
    "v_cvt_pk_bf16_f32 v189, v29, v33\n"
    "v_mfma_f32_4x4x4_16b_bf16 v[24:27], v[48:49], a[198:199], 0 cbsz:2 abid:1\n"
    "v_cvt_pk_bf16_f32 v193, v30, v34\n"
    "v_cvt_pk_bf16_f32 v197, v31, v35\n"
    "s_cmp_lt_u32 s10, 4\n"
    "s_cselect_b64 s[34:35], s[34:35], 0\n"
    "s_cselect_b32 s96, 1, 0\n"
    "s_and_b64 vcc, s[34:35], s[26:27]\n"
    "s_waitcnt vmcnt(14) lgkmcnt(6)\n"
    "ds_read2st64_b64 a[188:191], v15 offset0:0 offset1:1\n"
    "v_mfma_f32_4x4x4_16b_bf16 v[28:31], v[48:49], a[200:201], 0 cbsz:2 abid:2\n"
    "v_cvt_pk_bf16_f32 v186, v20, v24\n"
    "v_cvt_pk_bf16_f32 v190, v21, v25\n"
    "v_mfma_f32_4x4x4_16b_bf16 v[32:35], v[48:49], a[202:203], 0 cbsz:2 abid:3\n"
    "v_cvt_pk_bf16_f32 v194, v22, v26\n"
    "v_cvt_pk_bf16_f32 v198, v23, v27\n"
    "v_lshlrev_b64 v[40:41], v16, v[250:251]\n"
    "v_addc_co_u32 v234, vcc, 0, v234, vcc\n"
    "v_cmp_le_u32 vcc, s52, v19\n"
    "v_subrev_u32 v240, 15, v240\n"
    "v_cmp_gt_u32 s[48:49], 3, v240\n"
    "s_waitcnt vmcnt(13) lgkmcnt(4)\n"
    "ds_read2st64_b64 a[192:195], v15 offset0:2 offset1:3\n"
    "v_mfma_f32_4x4x4_16b_bf16 v[20:23], v[50:51], a[204:205], 0 cbsz:2 abid:0\n"
    "v_cvt_pk_bf16_f32 v187, v28, v32\n"
    "v_cvt_pk_bf16_f32 v191, v29, v33\n"
    "v_mfma_f32_4x4x4_16b_bf16 v[24:27], v[50:51], a[206:207], 0 cbsz:2 abid:1\n"
    "v_cvt_pk_bf16_f32 v195, v30, v34\n"
    "v_cvt_pk_bf16_f32 v199, v31, v35\n"
    "v_lshlrev_b64 v[42:43], v16, v[252:253]\n"
    "s_or_b64 s[26:27], s[26:27], s[48:49]\n"
    "v_and_b32 v234, 1, v234\n"
    "v_mov_b32 v236, s33\n"
    "v_mad_u32_u24 v3, v234, s88, v3\n"
    "s_waitcnt vmcnt(12) lgkmcnt(2)\n"
    "ds_read2st64_b64 a[196:199], v15 offset0:4 offset1:5\n"
    "v_mfma_f32_4x4x4_16b_bf16 v[28:31], v[50:51], a[208:209], 0 cbsz:2 abid:2\n"
    "v_cvt_pk_bf16_f32 v200, v20, v22\n"
    "v_cvt_pk_bf16_f32 v204, v21, v23\n"
    "v_mfma_f32_4x4x4_16b_bf16 v[32:35], v[50:51], a[210:211], 0 cbsz:2 abid:3\n"
    "v_cvt_pk_bf16_f32 v201, v24, v26\n"
    "v_cvt_pk_bf16_f32 v205, v25, v27\n"
    "v_lshlrev_b64 v[44:45], v16, v[254:255]\n"
    "v_cndmask_b32 v3, v3, v236, vcc\n"
    "s_nop 0\n"
    "v_readlane_b32 s74, v3, 2\n"
    "v_readlane_b32 s75, v3, 3\n"
    "s_waitcnt vmcnt(11) lgkmcnt(2)\n"
    "ds_read2st64_b64 a[200:203], v15 offset0:6 offset1:7\n"
    "v_mfma_f32_4x4x4_16b_bf16 v[20:23], v[40:41], a[188:189], 0 cbsz:2 abid:0\n"
    "ds_read_u16 v250, v18 offset:704\n"
    "v_cvt_pk_bf16_f32 v202, v28, v30\n"
    "v_cvt_pk_bf16_f32 v206, v29, v31\n"
    "v_mfma_f32_4x4x4_16b_bf16 v[24:27], v[40:41], a[190:191], 0 cbsz:2 abid:1\n"
    "v_cvt_pk_bf16_f32 v203, v32, v34\n"
    "v_cvt_pk_bf16_f32 v207, v33, v35\n"
    "v_readlane_b32 s81, v3, 4\n"
    "s_waitcnt vmcnt(10) lgkmcnt(3)\n"
    "ds_read2st64_b32 a[204:205], v38 offset0:16 offset1:17\n"
    "v_mfma_f32_4x4x4_16b_bf16 v[28:31], v[40:41], a[192:193], 0 cbsz:2 abid:2\n"
    "ds_read2st64_b32 a[206:207], v38 offset0:18 offset1:19\n"
    "ds_read_u16 v252, v18 offset:712\n"
    "v_cvt_pk_bf16_f32 v208, v20, v24\n"
    "v_cvt_pk_bf16_f32 v212, v21, v25\n"
    "v_mfma_f32_4x4x4_16b_bf16 v[32:35], v[40:41], a[194:195], 0 cbsz:2 abid:3\n"
    "v_cvt_pk_bf16_f32 v216, v22, v26\n"
    "v_cvt_pk_bf16_f32 v220, v23, v27\n"
    "s_waitcnt vmcnt(9) lgkmcnt(5)\n"
    "ds_read2st64_b32 a[208:209], v38 offset0:20 offset1:21\n"
    "v_mfma_f32_4x4x4_16b_bf16 v[20:23], v[42:43], a[196:197], 0 cbsz:2 abid:0\n"
    "ds_read2st64_b32 a[210:211], v38 offset0:22 offset1:23\n"
    "ds_read_u16 v254, v235 offset:704\n"
    "v_cvt_pk_bf16_f32 v209, v28, v32\n"
    "v_cvt_pk_bf16_f32 v213, v29, v33\n"
    "201:\n"
    "s_cbranch_execz 199b\n"
    "v_mfma_f32_4x4x4_16b_bf16 v[24:27], v[42:43], a[198:199], 0 cbsz:2 abid:1\n"
    "v_cvt_pk_bf16_f32 v217, v30, v34\n"
    "v_cvt_pk_bf16_f32 v221, v31, v35\n"
    "s_waitcnt vmcnt(8) lgkmcnt(6)\n"
    "ds_read2st64_b64 a[188:191], v15 offset0:12 offset1:13\n"
    "v_mfma_f32_4x4x4_16b_bf16 v[28:31], v[42:43], a[200:201], 0 cbsz:2 abid:2\n"
    "s_nop 0\n"
    "v_cvt_pk_bf16_f32 v210, v20, v24\n"
    "v_cvt_pk_bf16_f32 v214, v21, v25\n"
    "v_mfma_f32_4x4x4_16b_bf16 v[32:35], v[42:43], a[202:203], 0 cbsz:2 abid:3\n"
    "v_cvt_pk_bf16_f32 v218, v22, v26\n"
    "v_cvt_pk_bf16_f32 v222, v23, v27\n"
    "v_lshlrev_b64 v[46:47], v16, v[250:251]\n"
    "s_waitcnt vmcnt(7) lgkmcnt(4)\n"
    "ds_read2st64_b64 a[192:195], v15 offset0:14 offset1:15\n"
    "v_mfma_f32_4x4x4_16b_bf16 v[20:23], v[44:45], a[204:205], 0 cbsz:2 abid:0\n"
    "s_add_u32 m0, s57, 12288\n"
    "v_cvt_pk_bf16_f32 v211, v28, v32\n"
    "v_cvt_pk_bf16_f32 v215, v29, v33\n"
    "v_mfma_f32_4x4x4_16b_bf16 v[24:27], v[44:45], a[206:207], 0 cbsz:2 abid:1\n"
    "buffer_load_dwordx4 v0, s[36:39], s73 offen nt lds\n"
    "v_cvt_pk_bf16_f32 v219, v30, v34\n"
    "v_cvt_pk_bf16_f32 v223, v31, v35\n"
    "v_lshlrev_b64 v[48:49], v16, v[252:253]\n"
    "s_waitcnt vmcnt(7) lgkmcnt(2)\n"
    "ds_read2st64_b64 a[196:199], v15 offset0:16 offset1:17\n"
    "v_mfma_f32_4x4x4_16b_bf16 v[28:31], v[44:45], a[208:209], 0 cbsz:2 abid:2\n"
    "v_cvt_pk_bf16_f32 v224, v20, v22\n"
    "v_cvt_pk_bf16_f32 v228, v21, v23\n"
    "v_mfma_f32_4x4x4_16b_bf16 v[32:35], v[44:45], a[210:211], 0 cbsz:2 abid:3\n"
    "buffer_load_dwordx4 v1, s[36:39], s73 offen offset:1024 nt lds\n"
    "v_cvt_pk_bf16_f32 v225, v24, v26\n"
    "v_cvt_pk_bf16_f32 v229, v25, v27\n"
    "v_lshlrev_b64 v[50:51], v16, v[254:255]\n"
    "s_waitcnt vmcnt(7) lgkmcnt(2)\n"
    "ds_read2st64_b64 a[200:203], v15 offset0:18 offset1:19\n"
    "v_mfma_f32_4x4x4_16b_bf16 v[20:23], v[46:47], a[188:189], 0 cbsz:2 abid:0\n"
    "v_cvt_pk_bf16_f32 v226, v28, v30\n"
    "v_cvt_pk_bf16_f32 v230, v29, v31\n"
    "v_mfma_f32_4x4x4_16b_bf16 v[24:27], v[46:47], a[190:191], 0 cbsz:2 abid:1\n"
    "buffer_load_dwordx4 v2, s[36:39], s73 offen offset:2048 nt lds\n"
    "v_cvt_pk_bf16_f32 v227, v32, v34\n"
    "v_cvt_pk_bf16_f32 v231, v33, v35\n"
    "s_waitcnt vmcnt(7) lgkmcnt(2)\n"
    "ds_read2st64_b32 a[204:205], v38 offset0:40 offset1:41\n"
    "v_mfma_f32_4x4x4_16b_bf16 v[28:31], v[46:47], a[192:193], 0 cbsz:2 abid:2\n"
    "ds_read2st64_b32 a[206:207], v38 offset0:42 offset1:43\n"
    "v_cvt_pk_bf16_f32 v232, v20, v24\n"
    "v_cvt_pk_bf16_f32 v236, v21, v25\n"
    "v_mfma_f32_4x4x4_16b_bf16 v[32:35], v[46:47], a[194:195], 0 cbsz:2 abid:3\n"
    "v_cvt_pk_bf16_f32 v240, v22, v26\n"
    "v_cvt_pk_bf16_f32 v244, v23, v27\n"
    "s_waitcnt vmcnt(6) lgkmcnt(3)\n"
    "ds_read2st64_b32 a[208:209], v38 offset0:44 offset1:45\n"
    "v_mfma_f32_4x4x4_16b_bf16 v[20:23], v[48:49], a[196:197], 0 cbsz:2 abid:0\n"
    "ds_read2st64_b32 a[210:211], v38 offset0:46 offset1:47\n"
    "v_cvt_pk_bf16_f32 v233, v28, v32\n"
    "v_cvt_pk_bf16_f32 v237, v29, v33\n"
    "v_mfma_f32_4x4x4_16b_bf16 v[24:27], v[48:49], a[198:199], 0 cbsz:2 abid:1\n"
    "v_cvt_pk_bf16_f32 v241, v30, v34\n"
    "v_cvt_pk_bf16_f32 v245, v31, v35\n"
    "s_waitcnt lgkmcnt(4)\n"
    "v_mfma_f32_4x4x4_16b_bf16 v[28:31], v[48:49], a[200:201], 0 cbsz:2 abid:2\n"
    "s_nop 1\n"
    "v_cvt_pk_bf16_f32 v234, v20, v24\n"
    "v_cvt_pk_bf16_f32 v238, v21, v25\n"
    "v_mfma_f32_4x4x4_16b_bf16 v[32:35], v[48:49], a[202:203], 0 cbsz:2 abid:3\n"
    "v_cvt_pk_bf16_f32 v242, v22, v26\n"
    "v_cvt_pk_bf16_f32 v246, v23, v27\n"
    "s_waitcnt lgkmcnt(2)\n"
    "v_mfma_f32_4x4x4_16b_bf16 v[20:23], v[50:51], a[204:205], 0 cbsz:2 abid:0\n"
    "s_nop 1\n"
    "v_cvt_pk_bf16_f32 v235, v28, v32\n"
    "v_cvt_pk_bf16_f32 v239, v29, v33\n"
    "v_mfma_f32_4x4x4_16b_bf16 v[24:27], v[50:51], a[206:207], 0 cbsz:2 abid:1\n"
    "v_cvt_pk_bf16_f32 v243, v30, v34\n"
    "v_cvt_pk_bf16_f32 v247, v31, v35\n"
    "s_waitcnt lgkmcnt(0)\n"
    "v_mfma_f32_4x4x4_16b_bf16 v[28:31], v[50:51], a[208:209], 0 cbsz:2 abid:2\n"
    "s_nop 1\n"
    "v_cvt_pk_bf16_f32 v248, v20, v22\n"
    "v_cvt_pk_bf16_f32 v252, v21, v23\n"
    "v_mfma_f32_4x4x4_16b_bf16 v[32:35], v[50:51], a[210:211], 0 cbsz:2 abid:3\n"
    "v_cvt_pk_bf16_f32 v249, v24, v26\n"
    "v_cvt_pk_bf16_f32 v253, v25, v27\n"
    "s_waitcnt lgkmcnt(0)\n"
    "s_add_u32 s89, s92, 250\n"
    "v_add_u32 v38, s11, v38\n"
    "s_nop 0\n"
    "v_cvt_pk_bf16_f32 v250, v28, v30\n"
    "v_cvt_pk_bf16_f32 v254, v29, v31\n"
    "v_cvt_pk_bf16_f32 v251, v32, v34\n"
    "v_cvt_pk_bf16_f32 v255, v33, v35\n"
    "s_waitcnt vmcnt(3)\n"
    "s_barrier\n"
    "ds_read_b128 a[88:91], v4\n"
    "ds_read_b128 a[92:95], v5\n"
    "ds_read_b128 a[96:99], v6\n"
    "ds_read_b128 a[100:103], v7\n"
    "ds_read_b128 a[104:107], v4 offset:256\n"
    "ds_read_b128 a[108:111], v5 offset:256\n"
    "ds_read_b128 a[112:115], v6 offset:256\n"
    "ds_read_b128 a[116:119], v7 offset:256\n"
    "ds_read_b128 a[120:123], v4 offset:512\n"
    "ds_read_b128 a[124:127], v5 offset:512\n"
    "ds_read_b128 a[128:131], v6 offset:512\n"
    "ds_read_b128 a[132:135], v7 offset:512\n"
    "s_waitcnt lgkmcnt(11)\n"
    "v_mfma_f32_16x16x32_bf16 v[20:23], a[88:91], a[16:19], 0\n"
    "v_add_u32 v14, 1, v14\n"
    "s_sub_i32 s46, s52, 5\n"
    "s_max_i32 s46, s46, 0\n"
    "s_waitcnt lgkmcnt(10)\n"
    "v_mfma_f32_16x16x32_bf16 v[20:23], a[92:95], a[40:43], v[20:23]\n"
    "s_add_u32 m0, s57, 24576\n"
    "s_bfm_b64 s[44:45], s46, 5\n"
    "v_subrev_u32 v43, 5, v19\n"
    "s_waitcnt lgkmcnt(9)\n"
    "v_mfma_f32_16x16x32_bf16 v[20:23], a[96:99], a[64:67], v[20:23]\n"
    "buffer_load_dwordx4 v0, s[36:39], s74 offen nt lds\n"
    "s_andn2_b64 s[50:51], s[44:45], s[26:27]\n"
    "s_waitcnt lgkmcnt(8)\n"
    "v_mfma_f32_16x16x32_bf16 v[20:23], a[100:103], v[56:59], v[20:23]\n"
    "s_and_b64 s[48:49], s[44:45], s[26:27]\n"
    "s_bcnt1_i32_b64 s47, s[50:51]\n"
    "s_sub_u32 s46, s46, s47\n"
    "s_waitcnt lgkmcnt(7)\n"
    "v_mfma_f32_16x16x32_bf16 v[20:23], a[104:107], v[80:83], v[20:23]\n"
    "buffer_load_dwordx4 v1, s[36:39], s74 offen offset:1024 nt lds\n"
    "s_add_u32 s58, s46, 5\n"
    "s_waitcnt lgkmcnt(6)\n"
    "v_mfma_f32_16x16x32_bf16 v[20:23], a[108:111], v[104:107], v[20:23]\n"
    "v_mov_b32 v45, s58\n"
    "v_mbcnt_lo_u32_b32 v47, s48, 5\n"
    "v_mbcnt_lo_u32_b32 v45, s50, v45\n"
    "s_waitcnt lgkmcnt(5)\n"
    "v_mfma_f32_16x16x32_bf16 v[20:23], a[112:115], v[128:131], v[20:23]\n"
    "buffer_load_dwordx4 v2, s[36:39], s74 offen offset:2048 nt lds\n"
    "v_mbcnt_hi_u32_b32 v47, s49, v47\n"
    "s_waitcnt lgkmcnt(4)\n"
    "v_mfma_f32_16x16x32_bf16 v[20:23], a[116:119], v[152:155], v[20:23]\n"
    "v_mbcnt_hi_u32_b32 v45, s51, v45\n"
    "v_cndmask_b32 v47, v19, v47, s[48:49]\n"
    "s_sub_i32 s59, s47, 4\n"
    "s_waitcnt lgkmcnt(3)\n"
    "v_mfma_f32_16x16x32_bf16 v[20:23], a[120:123], v[176:179], v[20:23]\n"
    "v_cndmask_b32 v47, v47, v45, s[50:51]\n"
    "s_max_i32 s59, s59, 0\n"
    "v_lshlrev_b32 v47, 2, v47\n"
    "s_waitcnt lgkmcnt(2)\n"
    "v_mfma_f32_16x16x32_bf16 v[20:23], a[124:127], v[200:203], v[20:23]\n"
    "s_mul_i32 s59, s59, 9\n"
    "s_min_u32 s59, s59, 256\n"
    "ds_permute_b32 v3, v47, v3\n"
    "s_waitcnt lgkmcnt(2)\n"
    "v_mfma_f32_16x16x32_bf16 v[20:23], a[128:131], v[224:227], v[20:23]\n"
    "s_bcnt1_i32_b64 s73, s[24:25]\n"
    "s_mul_i32 s62, s73, 11\n"
    "s_lshr_b32 s62, s62, 5\n"
    "s_waitcnt lgkmcnt(1)\n"
    "v_mfma_f32_16x16x32_bf16 v[20:23], a[132:135], v[248:251], v[20:23]\n"
    "s_add_u32 s62, s62, 42\n"
    "s_sub_u32 s63, s62, 25\n"
    "v_bfe_i32 v11, v11, 0, 16\n"
    "v_mfma_f32_16x16x32_bf16 v[24:27], a[132:135], v[252:255], 0\n"
    "s_nop 0\n"
    "v_readlane_b32 s7, v11, 0\n"
    "v_readlane_b32 s8, v11, 1\n"
    "v_mfma_f32_16x16x32_bf16 v[24:27], a[128:131], v[228:231], v[24:27]\n"
    "v_readlane_b32 s60, v11, 2\n"
    "v_readlane_b32 s61, v11, 3\n"
    "s_add_i32 s7, s7, s8\n"
    "v_mfma_f32_16x16x32_bf16 v[24:27], a[124:127], v[204:207], v[24:27]\n"
    "s_add_i32 s8, s60, s61\n"
    "s_add_i32 s7, s7, s8\n"
    "s_ashr_i32 s7, s7, 2\n"
    "v_mfma_f32_16x16x32_bf16 v[24:27], a[120:123], v[180:183], v[24:27]\n"
    "s_sub_u32 s7, s0, s7\n"
    "s_add_u32 s7, s7, s62\n"
    "s_add_u32 s1, s7, 444\n"
    "v_mfma_f32_16x16x32_bf16 v[24:27], a[116:119], v[156:159], v[24:27]\n"
    "s_mov_b32 s72, 0\n"
    "v_mul_i32_i24 v40, s59, v43\n"
    "s_mul_i32 s7, s2, 9\n"
    "v_mfma_f32_16x16x32_bf16 v[24:27], a[112:115], v[132:135], v[24:27]\n"
    "s_and_b32 s7, s7, 0xff\n"
    "v_add_u32 v40, s7, v40\n"
    "v_subrev_u32 v42, s46, v43\n"
    "v_mfma_f32_16x16x32_bf16 v[24:27], a[108:111], v[108:111], v[24:27]\n"
    "v_add_u32 v41, s59, v40\n"
    "v_ashrrev_i32 v40, 8, v40\n"
    "v_ashrrev_i32 v41, 8, v41\n"
    "v_mfma_f32_16x16x32_bf16 v[24:27], a[104:107], v[84:87], v[24:27]\n"
    "v_min_i32 v40, s47, v40\n"
    "v_min_i32 v41, s47, v41\n"
    "v_max_i32 v40, v40, v42\n"
    "v_mfma_f32_16x16x32_bf16 v[24:27], a[100:103], v[60:63], v[24:27]\n"
    "v_add_u32 v42, 1, v42\n"
    "v_max_i32 v41, v41, v42\n"
    "v_cmp_gt_i32 s[60:61], v41, v40\n"
    "v_mfma_f32_16x16x32_bf16 v[24:27], a[96:99], a[68:71], v[24:27]\n"
    "s_waitcnt vmcnt(3)\n"
    "v_add_u32 v41, s58, v40\n"
    "v_sub_u32 v40, v19, v40\n"
    "v_mfma_f32_16x16x32_bf16 v[24:27], a[92:95], a[44:47], v[24:27]\n"
    "ds_write_b32 v13, v14\n"
    "v_cndmask_b32 v40, v40, v41, s[60:61]\n"
    "v_mfma_f32_16x16x32_bf16 v[24:27], a[88:91], a[20:23], v[24:27]\n"
    "s_add_u32 m0, s57, 36864\n"
    "v_cndmask_b32 v40, v19, v40, s[44:45]\n"
    "v_lshlrev_b32 v40, 2, v40\n"
    "v_mfma_f32_16x16x32_bf16 v[28:31], a[88:91], a[0:3], 0\n"
    "buffer_load_dwordx4 v0, s[36:39], s75 offen nt lds\n"
    "v_mfma_f32_16x16x32_bf16 v[28:31], a[92:95], a[24:27], v[28:31]\n"
    "s_waitcnt lgkmcnt(1)\n"
    "ds_bpermute_b32 v3, v40, v3\n"
    "s_mul_i32 s94, s73, 9\n"
    "v_mfma_f32_16x16x32_bf16 v[28:31], a[96:99], a[48:51], v[28:31]\n"
    "buffer_load_dwordx4 v1, s[36:39], s75 offen offset:1024 nt lds\n"
    "s_mul_i32 s31, s73, 5\n"
    "v_mfma_f32_16x16x32_bf16 v[28:31], a[100:103], a[72:75], v[28:31]\n"
    "v_cvt_pk_bf16_f32 v52, v20, v24\n"
    "s_lshr_b32 s94, s94, 5\n"
    "s_lshr_b32 s31, s31, 5\n"
    "v_mfma_f32_16x16x32_bf16 v[28:31], a[104:107], v[64:67], v[28:31]\n"
    "ds_read_b32 v15, v12\n"
    "s_mul_i32 s7, s95, -2\n"
    "s_add_u32 s7, s7, 67\n"
    "v_mfma_f32_16x16x32_bf16 v[28:31], a[108:111], v[88:91], v[28:31]\n"
    "v_cvt_pk_bf16_f32 v53, v21, v25\n"
    "s_add_u32 s94, s94, s7\n"
    "s_add_u32 s31, s31, s7\n"
    "v_mfma_f32_16x16x32_bf16 v[28:31], a[112:115], v[112:115], v[28:31]\n"
    "v_cvt_pk_bf16_f32 v54, v22, v26\n"
    "s_sub_u32 s7, 7, s72\n"
    "s_add_u32 s31, s31, s7\n"
    "v_mfma_f32_16x16x32_bf16 v[28:31], a[116:119], v[136:139], v[28:31]\n"
    "v_cvt_pk_bf16_f32 v55, v23, v27\n"
    "s_waitcnt lgkmcnt(0)\n"
    "v_cmp_lt_u32 vcc, v15, v14\n"
    "v_mfma_f32_16x16x32_bf16 v[28:31], a[120:123], v[160:163], v[28:31]\n"
    "s_add_u32 s76, s52, 4\n"
    "v_subrev_u32 v18, 5, v19\n"
    "s_mov_b32 s30, s94\n"
    "v_mfma_f32_16x16x32_bf16 v[28:31], a[124:127], v[184:187], v[28:31]\n"
    "s_cbranch_vccnz 81b\n"
    "82:\n"
    "s_add_u32 s94, s94, 12\n"
    "v_mfma_f32_16x16x32_bf16 v[28:31], a[128:131], v[208:211], v[28:31]\n"
    "ds_write2st64_b32 v38, v52, v53 offset0:0 offset1:2\n"
    "s_sub_u32 s7, s52, 5\n"
    "v_mfma_f32_16x16x32_bf16 v[28:31], a[132:135], v[232:235], v[28:31]\n"
    "ds_read_b128 a[136:139], v4 offset:12288\n"
    "s_mov_b64 vcc, s[12:13]\n"
    "s_andn2_b32 s7, s7, 1\n"
    "v_mfma_f32_16x16x32_bf16 v[32:35], a[132:135], v[236:239], 0\n"
    "ds_write2st64_b32 v38, v54, v55 offset0:4 offset1:6\n"
    "v_mfma_f32_16x16x32_bf16 v[32:35], a[128:131], v[212:215], v[32:35]\n"
    "ds_read_b128 a[140:143], v5 offset:12288\n"
    "v_add_u32 v14, 1, v14\n"
    "v_mfma_f32_16x16x32_bf16 v[32:35], a[124:127], v[188:191], v[32:35]\n"
    "buffer_load_dwordx4 v2, s[36:39], s75 offen offset:2048 nt lds\n"
    "v_mfma_f32_16x16x32_bf16 v[32:35], a[120:123], v[164:167], v[32:35]\n"
    "ds_read_b128 a[144:147], v6 offset:12288\n"
    "buffer_load_dword a212, off, s[36:39], s33\n"
    "v_mfma_f32_16x16x32_bf16 v[32:35], a[116:119], v[140:143], v[32:35]\n"
    "buffer_load_dword a212, off, s[36:39], s33\n"
    "buffer_load_dword a212, off, s[36:39], s33\n"
    "v_mfma_f32_16x16x32_bf16 v[32:35], a[112:115], v[116:119], v[32:35]\n"
    "s_cmp_lt_i32 s7, 2\n"
    "s_cselect_b32 s7, 0x1000, s7\n"
    "v_mfma_f32_16x16x32_bf16 v[32:35], a[108:111], v[92:95], v[32:35]\n"
    "ds_read_b128 a[148:151], v7 offset:12288\n"
    "s_cmp_lt_u32 s47, 3\n"
    "s_cselect_b32 s7, 0x1000, s7\n"
    "v_mfma_f32_16x16x32_bf16 v[32:35], a[104:107], v[68:71], v[32:35]\n"
    "s_add_u32 s70, s7, -1\n"
    "v_subrev_u32 v40, s70, v18\n"
    "v_cmp_gt_u32 s[60:61], 2, v40\n"
    "v_mfma_f32_16x16x32_bf16 v[32:35], a[100:103], a[76:79], v[32:35]\n"
    "ds_read_b128 a[152:155], v4 offset:12544\n"
    "s_add_u32 s64, s7, 5\n"
    "s_add_u32 s65, s7, 6\n"
    "v_mfma_f32_16x16x32_bf16 v[32:35], a[96:99], a[52:55], v[32:35]\n"
    "v_cndmask_b32 v18, v18, 63, s[60:61]\n"
    "s_add_u32 s71, s7, 5\n"
    "v_lshlrev_b32 v18, 2, v18\n"
    "v_mfma_f32_16x16x32_bf16 v[32:35], a[92:95], a[28:31], v[32:35]\n"
    "ds_read_b128 a[156:159], v5 offset:12544\n"
    "ds_bpermute_b32 v18, v18, v3\n"
    "v_mfma_f32_16x16x32_bf16 v[32:35], a[88:91], a[4:7], v[32:35]\n"
    "v_readlane_b32 s84, v3, s86\n"
    "v_mfma_f32_16x16x32_bf16 v[20:23], a[88:91], a[8:11], 0\n"
    "ds_read_b128 a[160:163], v6 offset:12544\n"
    "v_mfma_f32_16x16x32_bf16 v[20:23], a[92:95], a[32:35], v[20:23]\n"
    "v_mfma_f32_16x16x32_bf16 v[20:23], a[96:99], a[56:59], v[20:23]\n"
    "ds_read_b128 a[164:167], v7 offset:12544\n"
    "v_mfma_f32_16x16x32_bf16 v[20:23], a[100:103], a[80:83], v[20:23]\n"
    "v_cvt_pk_bf16_f32 v36, v28, v32\n"
    "v_mfma_f32_16x16x32_bf16 v[20:23], a[104:107], v[72:75], v[20:23]\n"
    "ds_read_b128 a[168:171], v4 offset:12800\n"
    "v_mfma_f32_16x16x32_bf16 v[20:23], a[108:111], v[96:99], v[20:23]\n"
    "v_cvt_pk_bf16_f32 v46, v29, v33\n"
    "v_mfma_f32_16x16x32_bf16 v[20:23], a[112:115], v[120:123], v[20:23]\n"
    "ds_read_b128 a[172:175], v5 offset:12800\n"
    "s_add_u32 m0, s57, 49152\n"
    "v_mfma_f32_16x16x32_bf16 v[20:23], a[116:119], v[144:147], v[20:23]\n"
    "buffer_load_dwordx4 v0, s[36:39], s81 offen nt lds\n"
    "v_mfma_f32_16x16x32_bf16 v[20:23], a[120:123], v[168:171], v[20:23]\n"
    "v_cvt_pk_bf16_f32 v37, v30, v34\n"
    "v_mfma_f32_16x16x32_bf16 v[20:23], a[124:127], v[192:195], v[20:23]\n"
    "ds_read_b128 a[176:179], v6 offset:12800\n"
    "v_mfma_f32_16x16x32_bf16 v[20:23], a[128:131], v[216:219], v[20:23]\n"
    "v_cvt_pk_bf16_f32 v50, v31, v35\n"
    "v_mfma_f32_16x16x32_bf16 v[20:23], a[132:135], v[240:243], v[20:23]\n"
    "ds_read_b128 a[180:183], v7 offset:12800\n"
    "v_mfma_f32_16x16x32_bf16 v[24:27], a[132:135], v[244:247], 0\n"
    "buffer_load_dwordx4 v1, s[36:39], s81 offen offset:1024 nt lds\n"
    "v_mfma_f32_16x16x32_bf16 v[24:27], a[128:131], v[220:223], v[24:27]\n"
    "v_cndmask_b32_dpp v44, v46, v36, vcc row_ror:8 row_mask:0xf bank_mask:0xf\n"
    "v_mfma_f32_16x16x32_bf16 v[24:27], a[124:127], v[196:199], v[24:27]\n"
    "buffer_load_dwordx4 v2, s[36:39], s81 offen offset:2048 nt lds\n"
    "v_mfma_f32_16x16x32_bf16 v[24:27], a[120:123], v[172:175], v[24:27]\n"
    "s_memrealtime s[92:93]\n"
    "buffer_load_dword a212, off, s[36:39], s33\n"
    "v_mfma_f32_16x16x32_bf16 v[24:27], a[116:119], v[148:151], v[24:27]\n"
    "v_cndmask_b32_dpp v48, v50, v37, vcc row_ror:8 row_mask:0xf bank_mask:0xf\n"
    "buffer_load_dword a212, off, s[36:39], s33\n"
    "v_mfma_f32_16x16x32_bf16 v[24:27], a[112:115], v[124:127], v[24:27]\n"
    "v_mov_b32_dpp v46, v36 row_ror:8 row_mask:0xf bank_mask:0x3\n"
    "v_mfma_f32_16x16x32_bf16 v[24:27], a[108:111], v[100:103], v[24:27]\n"
    "v_mov_b32_dpp v50, v37 row_ror:8 row_mask:0xf bank_mask:0x3\n"
    "v_mfma_f32_16x16x32_bf16 v[24:27], a[104:107], v[76:79], v[24:27]\n"
    "v_mfma_f32_16x16x32_bf16 v[24:27], a[100:103], a[84:87], v[24:27]\n"
    "s_waitcnt vmcnt(11) lgkmcnt(0)\n"
    "s_sub_u32 s85, s92, s89\n"
    "v_mfma_f32_16x16x32_bf16 v[24:27], a[96:99], a[60:63], v[24:27]\n"
    "s_add_i32 s63, s63, s85\n"
    "s_max_i32 s63, s63, s62\n"
    "s_add_u32 s89, s89, s63\n"
    "v_mfma_f32_16x16x32_bf16 v[24:27], a[92:95], a[36:39], v[24:27]\n"
    "s_sub_i32 s7, s89, s1\n"
    "s_mul_i32 s7, s7, 4\n"
    "s_ashr_i32 s7, s7, 6\n"
    "s_mov_b32 s81, s84\n"
    "s_add_u32 m0, s57, 61440\n"
    "v_mfma_f32_16x16x32_bf16 v[24:27], a[88:91], a[12:15], v[24:27]\n"
    "s_max_i32 s7, s7, -2\n"
    "s_min_i32 s7, s7, 4\n"
    "s_sub_u32 s31, s31, s7\n"
    "s_branch 52f\n"
    ".p2align 12\n"
    "51:\n"
    "v_mfma_f32_16x16x32_bf16 v[20:23], a[88:91], a[16:19], 0\n"
    "s_add_u32 s86, s86, 1\n"
    "v_mfma_f32_16x16x32_bf16 v[20:23], a[92:95], a[40:43], v[20:23]\n"
    "s_waitcnt vmcnt(14)\n"
    "v_mfma_f32_16x16x32_bf16 v[20:23], a[96:99], a[64:67], v[20:23]\n"
    "ds_write_b32 v13, v14\n"
    "v_mfma_f32_16x16x32_bf16 v[20:23], a[100:103], v[56:59], v[20:23]\n"
    "v_cvt_pk_bf16_f32 v36, v28, v32\n"
    "v_mfma_f32_16x16x32_bf16 v[20:23], a[104:107], v[80:83], v[20:23]\n"
    "v_cvt_pk_bf16_f32 v47, v29, v33\n"
    "v_mfma_f32_16x16x32_bf16 v[20:23], a[108:111], v[104:107], v[20:23]\n"
    "v_cvt_pk_bf16_f32 v37, v30, v34\n"
    "v_mfma_f32_16x16x32_bf16 v[20:23], a[112:115], v[128:131], v[20:23]\n"
    "v_cvt_pk_bf16_f32 v51, v31, v35\n"
    "v_mfma_f32_16x16x32_bf16 v[20:23], a[116:119], v[152:155], v[20:23]\n"
    "v_cndmask_b32_dpp v45, v47, v36, vcc row_ror:8 row_mask:0xf bank_mask:0xf\n"
    "v_mfma_f32_16x16x32_bf16 v[20:23], a[120:123], v[176:179], v[20:23]\n"
    "v_cndmask_b32_dpp v49, v51, v37, vcc row_ror:8 row_mask:0xf bank_mask:0xf\n"
    "v_mfma_f32_16x16x32_bf16 v[20:23], a[124:127], v[200:203], v[20:23]\n"
    "v_mov_b32_dpp v47, v36 row_ror:8 row_mask:0xf bank_mask:0x3\n"
    "v_mfma_f32_16x16x32_bf16 v[20:23], a[128:131], v[224:227], v[20:23]\n"
    "v_mov_b32_dpp v51, v37 row_ror:8 row_mask:0xf bank_mask:0x3\n"
    "v_mfma_f32_16x16x32_bf16 v[20:23], a[132:135], v[248:251], v[20:23]\n"
    "ds_read_b32 v15, v12\n"
    "v_mfma_f32_16x16x32_bf16 v[24:27], a[132:135], v[252:255], 0\n"
    "s_add_u32 s89, s89, s94\n"
    "v_mfma_f32_16x16x32_bf16 v[24:27], a[128:131], v[228:231], v[24:27]\n"
    "s_sub_u32 s94, s94, 6\n"
    "v_mfma_f32_16x16x32_bf16 v[24:27], a[124:127], v[204:207], v[24:27]\n"
    "buffer_store_dwordx4 v[44:47], v8, s[40:43], s82 offen sc1\n"
    "v_mfma_f32_16x16x32_bf16 v[24:27], a[120:123], v[180:183], v[24:27]\n"
    "s_max_u32 s94, s94, s30\n"
    "v_mfma_f32_16x16x32_bf16 v[24:27], a[116:119], v[156:159], v[24:27]\n"
    "s_waitcnt lgkmcnt(0)\n"
    "v_cmp_lt_u32 vcc, v15, v14\n"
    "v_mfma_f32_16x16x32_bf16 v[24:27], a[112:115], v[132:135], v[24:27]\n"
    "v_mfma_f32_16x16x32_bf16 v[24:27], a[108:111], v[108:111], v[24:27]\n"
    "s_cbranch_vccnz 41b\n"
    "42:\n"
    "v_mfma_f32_16x16x32_bf16 v[24:27], a[104:107], v[84:87], v[24:27]\n"
    "ds_read_b128 a[184:187], v39 offset:4096\n"
    "v_mfma_f32_16x16x32_bf16 v[24:27], a[100:103], v[60:63], v[24:27]\n"
    "s_cmp_eq_u32 s86, s64\n"
    "s_cbranch_scc1 45f\n"
    "v_mfma_f32_16x16x32_bf16 v[24:27], a[96:99], a[68:71], v[24:27]\n"
    "ds_read_b128 a[136:139], v4 offset:12288\n"
    "v_mfma_f32_16x16x32_bf16 v[24:27], a[92:95], a[44:47], v[24:27]\n"
    "buffer_load_dwordx4 v0, s[36:39], s81 offen nt lds\n"
    "v_mfma_f32_16x16x32_bf16 v[24:27], a[88:91], a[20:23], v[24:27]\n"
    "v_add_u32 v14, 1, v14\n"
    "v_mfma_f32_16x16x32_bf16 v[28:31], a[88:91], a[0:3], 0\n"
    "s_mov_b64 vcc, s[12:13]\n"
    "v_mfma_f32_16x16x32_bf16 v[28:31], a[92:95], a[24:27], v[28:31]\n"
    "ds_read_b128 a[140:143], v5 offset:12288\n"
    "v_mfma_f32_16x16x32_bf16 v[28:31], a[96:99], a[48:51], v[28:31]\n"
    "ds_read_b128 a[144:147], v6 offset:12288\n"
    "v_mfma_f32_16x16x32_bf16 v[28:31], a[100:103], a[72:75], v[28:31]\n"
    "buffer_load_dwordx4 v1, s[36:39], s81 offen offset:1024 nt lds\n"
    "v_mfma_f32_16x16x32_bf16 v[28:31], a[104:107], v[64:67], v[28:31]\n"
    "v_cvt_pk_bf16_f32 v52, v20, v24\n"
    "v_mfma_f32_16x16x32_bf16 v[28:31], a[108:111], v[88:91], v[28:31]\n"
    "v_cvt_pk_bf16_f32 v53, v21, v25\n"
    "v_mfma_f32_16x16x32_bf16 v[28:31], a[112:115], v[112:115], v[28:31]\n"
    "v_cvt_pk_bf16_f32 v54, v22, v26\n"
    "v_mfma_f32_16x16x32_bf16 v[28:31], a[116:119], v[136:139], v[28:31]\n"
    "ds_read_b128 a[148:151], v7 offset:12288\n"
    "v_mfma_f32_16x16x32_bf16 v[28:31], a[120:123], v[160:163], v[28:31]\n"
    "v_cvt_pk_bf16_f32 v55, v23, v27\n"
    "v_mfma_f32_16x16x32_bf16 v[28:31], a[124:127], v[184:187], v[28:31]\n"
    "buffer_load_dwordx4 v2, s[36:39], s81 offen offset:2048 nt lds\n"
    "v_mfma_f32_16x16x32_bf16 v[28:31], a[128:131], v[208:211], v[28:31]\n"
    "v_readlane_b32 s81, v3, s86\n"
    "v_mfma_f32_16x16x32_bf16 v[28:31], a[132:135], v[232:235], v[28:31]\n"
    "ds_read_b128 a[152:155], v4 offset:12544\n"
    "v_mfma_f32_16x16x32_bf16 v[32:35], a[132:135], v[236:239], 0\n"
    "s_addk_i32 m0, 12288\n"
    "v_mfma_f32_16x16x32_bf16 v[32:35], a[128:131], v[212:215], v[32:35]\n"
    "ds_write2st64_b32 v38, v52, v53 offset0:0 offset1:2\n"
    "v_mfma_f32_16x16x32_bf16 v[32:35], a[124:127], v[188:191], v[32:35]\n"
    "46:\n"
    "v_mfma_f32_16x16x32_bf16 v[32:35], a[120:123], v[164:167], v[32:35]\n"
    "ds_read_b128 a[156:159], v5 offset:12544\n"
    "v_mfma_f32_16x16x32_bf16 v[32:35], a[116:119], v[140:143], v[32:35]\n"
    "s_cmp_eq_u32 m0, s77\n"
    "v_mfma_f32_16x16x32_bf16 v[32:35], a[112:115], v[116:119], v[32:35]\n"
    "s_cselect_b32 s78, s87, s88\n"
    "v_mfma_f32_16x16x32_bf16 v[32:35], a[108:111], v[92:95], v[32:35]\n"
    "buffer_store_dwordx4 v[48:51], v9, s[40:43], s82 offen sc1\n"
    "v_mfma_f32_16x16x32_bf16 v[32:35], a[104:107], v[68:71], v[32:35]\n"
    "v_mfma_f32_16x16x32_bf16 v[32:35], a[100:103], a[76:79], v[32:35]\n"
    "ds_read_b128 a[160:163], v6 offset:12544\n"
    "v_mfma_f32_16x16x32_bf16 v[32:35], a[96:99], a[52:55], v[32:35]\n"
    "ds_read_b128 a[164:167], v7 offset:12544\n"
    "v_mfma_f32_16x16x32_bf16 v[32:35], a[92:95], a[28:31], v[32:35]\n"
    "ds_read_b128 a[168:171], v4 offset:12800\n"
    "v_mfma_f32_16x16x32_bf16 v[32:35], a[88:91], a[4:7], v[32:35]\n"
    "v_mfma_f32_16x16x32_bf16 v[20:23], a[88:91], a[8:11], 0\n"
    "ds_write2st64_b32 v38, v54, v55 offset0:4 offset1:6\n"
    "v_mfma_f32_16x16x32_bf16 v[20:23], a[92:95], a[32:35], v[20:23]\n"
    "v_mfma_f32_16x16x32_bf16 v[20:23], a[96:99], a[56:59], v[20:23]\n"
    "ds_read_b128 a[172:175], v5 offset:12800\n"
    "v_mfma_f32_16x16x32_bf16 v[20:23], a[100:103], a[80:83], v[20:23]\n"
    "v_cvt_pk_bf16_f32 v36, v28, v32\n"
    "v_mfma_f32_16x16x32_bf16 v[20:23], a[104:107], v[72:75], v[20:23]\n"
    "v_cvt_pk_bf16_f32 v46, v29, v33\n"
    "v_mfma_f32_16x16x32_bf16 v[20:23], a[108:111], v[96:99], v[20:23]\n"
    "ds_read_b128 a[176:179], v6 offset:12800\n"
    "v_mfma_f32_16x16x32_bf16 v[20:23], a[112:115], v[120:123], v[20:23]\n"
    "v_cvt_pk_bf16_f32 v37, v30, v34\n"
    "v_mfma_f32_16x16x32_bf16 v[20:23], a[116:119], v[144:147], v[20:23]\n"
    "v_cvt_pk_bf16_f32 v50, v31, v35\n"
    "v_mfma_f32_16x16x32_bf16 v[20:23], a[120:123], v[168:171], v[20:23]\n"
    "v_mfma_f32_16x16x32_bf16 v[20:23], a[124:127], v[192:195], v[20:23]\n"
    "v_cndmask_b32_dpp v44, v46, v36, vcc row_ror:8 row_mask:0xf bank_mask:0xf\n"
    "v_mfma_f32_16x16x32_bf16 v[20:23], a[128:131], v[216:219], v[20:23]\n"
    "ds_read_b128 a[180:183], v7 offset:12800\n"
    "v_mfma_f32_16x16x32_bf16 v[20:23], a[132:135], v[240:243], v[20:23]\n"
    "v_cndmask_b32_dpp v48, v50, v37, vcc row_ror:8 row_mask:0xf bank_mask:0xf\n"
    "v_mfma_f32_16x16x32_bf16 v[24:27], a[132:135], v[244:247], 0\n"
    "s_memrealtime s[92:93]\n"
    "v_mfma_f32_16x16x32_bf16 v[24:27], a[128:131], v[220:223], v[24:27]\n"
    "v_mov_b32_dpp v46, v36 row_ror:8 row_mask:0xf bank_mask:0x3\n"
    "v_mfma_f32_16x16x32_bf16 v[24:27], a[124:127], v[196:199], v[24:27]\n"
    "v_mov_b32_dpp v50, v37 row_ror:8 row_mask:0xf bank_mask:0x3\n"
    "v_mfma_f32_16x16x32_bf16 v[24:27], a[120:123], v[172:175], v[24:27]\n"
    "s_waitcnt lgkmcnt(14)\n"
    "buffer_store_dwordx4 a[184:187], v10, s[40:43], s82 offen sc1\n"
    "v_mfma_f32_16x16x32_bf16 v[24:27], a[116:119], v[148:151], v[24:27]\n"
    "v_mfma_f32_16x16x32_bf16 v[24:27], a[112:115], v[124:127], v[24:27]\n"
    "v_readlane_b32 s82, v18, s86\n"
    "v_mfma_f32_16x16x32_bf16 v[24:27], a[108:111], v[100:103], v[24:27]\n"
    "v_mfma_f32_16x16x32_bf16 v[24:27], a[104:107], v[76:79], v[24:27]\n"
    "s_waitcnt lgkmcnt(0)\n"
    "s_sub_u32 s85, s92, s89\n"
    "v_mfma_f32_16x16x32_bf16 v[24:27], a[100:103], a[84:87], v[24:27]\n"
    "s_cbranch_scc1 43b\n"
    "44:\n"
    "v_mfma_f32_16x16x32_bf16 v[24:27], a[96:99], a[60:63], v[24:27]\n"
    "v_mfma_f32_16x16x32_bf16 v[24:27], a[92:95], a[36:39], v[24:27]\n"
    "s_cmp_lt_u32 s86, s76\n"
    "v_mfma_f32_16x16x32_bf16 v[24:27], a[88:91], a[12:15], v[24:27]\n"
    "s_cbranch_scc0 55f\n"
    "52:\n"
    "v_mfma_f32_16x16x32_bf16 v[28:31], a[136:139], a[16:19], 0\n"
    "s_add_u32 s86, s86, 1\n"
    "v_mfma_f32_16x16x32_bf16 v[28:31], a[140:143], a[40:43], v[28:31]\n"
    "s_waitcnt vmcnt(14)\n"
    "v_mfma_f32_16x16x32_bf16 v[28:31], a[144:147], a[64:67], v[28:31]\n"
    "ds_write_b32 v13, v14\n"
    "v_mfma_f32_16x16x32_bf16 v[28:31], a[148:151], v[56:59], v[28:31]\n"
    "v_cvt_pk_bf16_f32 v36, v20, v24\n"
    "v_mfma_f32_16x16x32_bf16 v[28:31], a[152:155], v[80:83], v[28:31]\n"
    "v_cvt_pk_bf16_f32 v47, v21, v25\n"
    "v_mfma_f32_16x16x32_bf16 v[28:31], a[156:159], v[104:107], v[28:31]\n"
    "v_cvt_pk_bf16_f32 v37, v22, v26\n"
    "v_mfma_f32_16x16x32_bf16 v[28:31], a[160:163], v[128:131], v[28:31]\n"
    "v_cvt_pk_bf16_f32 v51, v23, v27\n"
    "v_mfma_f32_16x16x32_bf16 v[28:31], a[164:167], v[152:155], v[28:31]\n"
    "v_cndmask_b32_dpp v45, v47, v36, vcc row_ror:8 row_mask:0xf bank_mask:0xf\n"
    "v_mfma_f32_16x16x32_bf16 v[28:31], a[168:171], v[176:179], v[28:31]\n"
    "v_cndmask_b32_dpp v49, v51, v37, vcc row_ror:8 row_mask:0xf bank_mask:0xf\n"
    "v_mfma_f32_16x16x32_bf16 v[28:31], a[172:175], v[200:203], v[28:31]\n"
    "v_mov_b32_dpp v47, v36 row_ror:8 row_mask:0xf bank_mask:0x3\n"
    "v_mfma_f32_16x16x32_bf16 v[28:31], a[176:179], v[224:227], v[28:31]\n"
    "v_mov_b32_dpp v51, v37 row_ror:8 row_mask:0xf bank_mask:0x3\n"
    "v_mfma_f32_16x16x32_bf16 v[28:31], a[180:183], v[248:251], v[28:31]\n"
    "ds_read_b32 v15, v12\n"
    "v_mfma_f32_16x16x32_bf16 v[32:35], a[180:183], v[252:255], 0\n"
    "s_add_u32 s89, s89, s94\n"
    "v_mfma_f32_16x16x32_bf16 v[32:35], a[176:179], v[228:231], v[32:35]\n"
    "v_add_u32 v4, s78, v4\n"
    "v_mfma_f32_16x16x32_bf16 v[32:35], a[172:175], v[204:207], v[32:35]\n"
    "buffer_store_dwordx4 v[44:47], v8, s[40:43], s82 offen sc1\n"
    "v_mfma_f32_16x16x32_bf16 v[32:35], a[168:171], v[180:183], v[32:35]\n"
    "v_add_u32 v5, s78, v5\n"
    "v_mfma_f32_16x16x32_bf16 v[32:35], a[164:167], v[156:159], v[32:35]\n"
    "s_waitcnt lgkmcnt(0)\n"
    "v_cmp_lt_u32 vcc, v15, v14\n"
    "v_mfma_f32_16x16x32_bf16 v[32:35], a[160:163], v[132:135], v[32:35]\n"
    "v_add_u32 v6, s78, v6\n"
    "v_mfma_f32_16x16x32_bf16 v[32:35], a[156:159], v[108:111], v[32:35]\n"
    "s_cbranch_vccnz 61b\n"
    "62:\n"
    "v_mfma_f32_16x16x32_bf16 v[32:35], a[152:155], v[84:87], v[32:35]\n"
    "ds_read_b128 a[184:187], v39\n"
    "v_mfma_f32_16x16x32_bf16 v[32:35], a[148:151], v[60:63], v[32:35]\n"
    "s_cmp_eq_u32 s86, s65\n"
    "s_cbranch_scc1 65f\n"
    "v_mfma_f32_16x16x32_bf16 v[32:35], a[144:147], a[68:71], v[32:35]\n"
    "ds_read_b128 a[88:91], v4\n"
    "v_mfma_f32_16x16x32_bf16 v[32:35], a[140:143], a[44:47], v[32:35]\n"
    "buffer_load_dwordx4 v0, s[36:39], s81 offen nt lds\n"
    "v_mfma_f32_16x16x32_bf16 v[32:35], a[136:139], a[20:23], v[32:35]\n"
    "v_add_u32 v7, s78, v7\n"
    "v_mfma_f32_16x16x32_bf16 v[20:23], a[136:139], a[0:3], 0\n"
    "v_add_u32 v14, 1, v14\n"
    "v_mfma_f32_16x16x32_bf16 v[20:23], a[140:143], a[24:27], v[20:23]\n"
    "ds_read_b128 a[92:95], v5\n"
    "v_mfma_f32_16x16x32_bf16 v[20:23], a[144:147], a[48:51], v[20:23]\n"
    "ds_read_b128 a[96:99], v6\n"
    "v_mfma_f32_16x16x32_bf16 v[20:23], a[148:151], a[72:75], v[20:23]\n"
    "buffer_load_dwordx4 v1, s[36:39], s81 offen offset:1024 nt lds\n"
    "v_mfma_f32_16x16x32_bf16 v[20:23], a[152:155], v[64:67], v[20:23]\n"
    "v_cvt_pk_bf16_f32 v52, v28, v32\n"
    "v_mfma_f32_16x16x32_bf16 v[20:23], a[156:159], v[88:91], v[20:23]\n"
    "v_cvt_pk_bf16_f32 v53, v29, v33\n"
    "v_mfma_f32_16x16x32_bf16 v[20:23], a[160:163], v[112:115], v[20:23]\n"
    "v_cvt_pk_bf16_f32 v54, v30, v34\n"
    "v_mfma_f32_16x16x32_bf16 v[20:23], a[164:167], v[136:139], v[20:23]\n"
    "ds_read_b128 a[100:103], v7\n"
    "v_mfma_f32_16x16x32_bf16 v[20:23], a[168:171], v[160:163], v[20:23]\n"
    "v_cvt_pk_bf16_f32 v55, v31, v35\n"
    "v_mfma_f32_16x16x32_bf16 v[20:23], a[172:175], v[184:187], v[20:23]\n"
    "buffer_load_dwordx4 v2, s[36:39], s81 offen offset:2048 nt lds\n"
    "v_mfma_f32_16x16x32_bf16 v[20:23], a[176:179], v[208:211], v[20:23]\n"
    "s_mov_b64 vcc, s[12:13]\n"
    "v_mfma_f32_16x16x32_bf16 v[20:23], a[180:183], v[232:235], v[20:23]\n"
    "ds_read_b128 a[104:107], v4 offset:256\n"
    "v_mfma_f32_16x16x32_bf16 v[24:27], a[180:183], v[236:239], 0\n"
    "v_readlane_b32 s81, v3, s86\n"
    "v_mfma_f32_16x16x32_bf16 v[24:27], a[176:179], v[212:215], v[24:27]\n"
    "ds_write2st64_b32 v38, v52, v53 offset0:16 offset1:18\n"
    "v_mfma_f32_16x16x32_bf16 v[24:27], a[172:175], v[188:191], v[24:27]\n"
    "s_addk_i32 m0, 12288\n"
    "66:\n"
    "v_mfma_f32_16x16x32_bf16 v[24:27], a[168:171], v[164:167], v[24:27]\n"
    "ds_read_b128 a[108:111], v5 offset:256\n"
    "v_mfma_f32_16x16x32_bf16 v[24:27], a[164:167], v[140:143], v[24:27]\n"
    "s_cmp_eq_u32 m0, s80\n"
    "v_mfma_f32_16x16x32_bf16 v[24:27], a[160:163], v[116:119], v[24:27]\n"
    "s_cselect_b32 m0, s57, m0\n"
    "v_mfma_f32_16x16x32_bf16 v[24:27], a[156:159], v[92:95], v[24:27]\n"
    "buffer_store_dwordx4 v[48:51], v9, s[40:43], s82 offen sc1\n"
    "v_mfma_f32_16x16x32_bf16 v[24:27], a[152:155], v[68:71], v[24:27]\n"
    "s_cmp_eq_u32 s86, 20\n"
    "s_cselect_b32 s30, s31, s30\n"
    "v_mfma_f32_16x16x32_bf16 v[24:27], a[148:151], a[76:79], v[24:27]\n"
    "ds_read_b128 a[112:115], v6 offset:256\n"
    "v_mfma_f32_16x16x32_bf16 v[24:27], a[144:147], a[52:55], v[24:27]\n"
    "ds_read_b128 a[116:119], v7 offset:256\n"
    "v_mfma_f32_16x16x32_bf16 v[24:27], a[140:143], a[28:31], v[24:27]\n"
    "ds_read_b128 a[120:123], v4 offset:512\n"
    "v_mfma_f32_16x16x32_bf16 v[24:27], a[136:139], a[4:7], v[24:27]\n"
    "v_mfma_f32_16x16x32_bf16 v[28:31], a[136:139], a[8:11], 0\n"
    "ds_write2st64_b32 v38, v54, v55 offset0:20 offset1:22\n"
    "v_mfma_f32_16x16x32_bf16 v[28:31], a[140:143], a[32:35], v[28:31]\n"
    "v_mfma_f32_16x16x32_bf16 v[28:31], a[144:147], a[56:59], v[28:31]\n"
    "ds_read_b128 a[124:127], v5 offset:512\n"
    "v_mfma_f32_16x16x32_bf16 v[28:31], a[148:151], a[80:83], v[28:31]\n"
    "v_cvt_pk_bf16_f32 v36, v20, v24\n"
    "v_mfma_f32_16x16x32_bf16 v[28:31], a[152:155], v[72:75], v[28:31]\n"
    "v_cvt_pk_bf16_f32 v46, v21, v25\n"
    "v_mfma_f32_16x16x32_bf16 v[28:31], a[156:159], v[96:99], v[28:31]\n"
    "ds_read_b128 a[128:131], v6 offset:512\n"
    "v_mfma_f32_16x16x32_bf16 v[28:31], a[160:163], v[120:123], v[28:31]\n"
    "v_cvt_pk_bf16_f32 v37, v22, v26\n"
    "v_mfma_f32_16x16x32_bf16 v[28:31], a[164:167], v[144:147], v[28:31]\n"
    "v_cvt_pk_bf16_f32 v50, v23, v27\n"
    "v_mfma_f32_16x16x32_bf16 v[28:31], a[168:171], v[168:171], v[28:31]\n"
    "v_mfma_f32_16x16x32_bf16 v[28:31], a[172:175], v[192:195], v[28:31]\n"
    "v_cndmask_b32_dpp v44, v46, v36, vcc row_ror:8 row_mask:0xf bank_mask:0xf\n"
    "v_mfma_f32_16x16x32_bf16 v[28:31], a[176:179], v[216:219], v[28:31]\n"
    "ds_read_b128 a[132:135], v7 offset:512\n"
    "v_mfma_f32_16x16x32_bf16 v[28:31], a[180:183], v[240:243], v[28:31]\n"
    "v_cndmask_b32_dpp v48, v50, v37, vcc row_ror:8 row_mask:0xf bank_mask:0xf\n"
    "v_mfma_f32_16x16x32_bf16 v[32:35], a[180:183], v[244:247], 0\n"
    "s_memrealtime s[92:93]\n"
    "v_mfma_f32_16x16x32_bf16 v[32:35], a[176:179], v[220:223], v[32:35]\n"
    "v_mov_b32_dpp v46, v36 row_ror:8 row_mask:0xf bank_mask:0x3\n"
    "v_mfma_f32_16x16x32_bf16 v[32:35], a[172:175], v[196:199], v[32:35]\n"
    "v_mov_b32_dpp v50, v37 row_ror:8 row_mask:0xf bank_mask:0x3\n"
    "v_mfma_f32_16x16x32_bf16 v[32:35], a[168:171], v[172:175], v[32:35]\n"
    "s_waitcnt lgkmcnt(14)\n"
    "buffer_store_dwordx4 a[184:187], v10, s[40:43], s82 offen sc1\n"
    "v_mfma_f32_16x16x32_bf16 v[32:35], a[164:167], v[148:151], v[32:35]\n"
    "v_mfma_f32_16x16x32_bf16 v[32:35], a[160:163], v[124:127], v[32:35]\n"
    "v_readlane_b32 s82, v18, s86\n"
    "v_mfma_f32_16x16x32_bf16 v[32:35], a[156:159], v[100:103], v[32:35]\n"
    "v_mfma_f32_16x16x32_bf16 v[32:35], a[152:155], v[76:79], v[32:35]\n"
    "s_waitcnt lgkmcnt(0)\n"
    "s_sub_u32 s85, s92, s89\n"
    "v_mfma_f32_16x16x32_bf16 v[32:35], a[148:151], a[84:87], v[32:35]\n"
    "s_cbranch_scc1 63b\n"
    "64:\n"
    "v_mfma_f32_16x16x32_bf16 v[32:35], a[144:147], a[60:63], v[32:35]\n"
    "v_mfma_f32_16x16x32_bf16 v[32:35], a[140:143], a[36:39], v[32:35]\n"
    "s_cmp_lt_u32 s86, s76\n"
    "v_mfma_f32_16x16x32_bf16 v[32:35], a[136:139], a[12:15], v[32:35]\n"
    "s_cbranch_scc1 51b\n"
    "s_barrier\n"
    "ds_read_b128 a[184:187], v39 offset:4096\n"
    "s_nop 4\n"
    "v_cvt_pk_bf16_f32 v36, v28, v32\n"
    "v_cvt_pk_bf16_f32 v47, v29, v33\n"
    "v_cvt_pk_bf16_f32 v37, v30, v34\n"
    "v_cvt_pk_bf16_f32 v51, v31, v35\n"
    "s_branch 56f\n"
    "55:\n"
    "s_barrier\n"
    "ds_read_b128 a[184:187], v39\n"
    "s_nop 4\n"
    "v_cvt_pk_bf16_f32 v36, v20, v24\n"
    "v_cvt_pk_bf16_f32 v47, v21, v25\n"
    "v_cvt_pk_bf16_f32 v37, v22, v26\n"
    "v_cvt_pk_bf16_f32 v51, v23, v27\n"
    "56:\n"
    "v_cndmask_b32_dpp v45, v47, v36, vcc row_ror:8 row_mask:0xf bank_mask:0xf\n"
    "v_mov_b32_dpp v47, v36 row_ror:8 row_mask:0xf bank_mask:0x3\n"
    "v_cndmask_b32_dpp v49, v51, v37, vcc row_ror:8 row_mask:0xf bank_mask:0xf\n"
    "buffer_store_dwordx4 v[44:47], v8, s[40:43], s82 offen sc1\n"
    "v_mov_b32_dpp v51, v37 row_ror:8 row_mask:0xf bank_mask:0x3\n"
    "s_waitcnt lgkmcnt(0)\n"
    "buffer_store_dwordx4 a[184:187], v10, s[40:43], s82 offen sc1\n"
    "buffer_store_dwordx4 v[48:51], v9, s[40:43], s82 offen sc1\n"
    "s_waitcnt vmcnt(0) lgkmcnt(0)\n"
    "s_endpgm\n"
    "75:\n"
    "s_sleep 2\n"
    "global_load_dwordx4 v[236:239], v16, s[54:55] sc1\n"
    "s_waitcnt vmcnt(0)\n"
    "v_sub_u32 v11, s0, v236\n"
    "v_or_b32 v236, 0xffff, v236\n"
    "v_or_b32 v238, 0xffff, v238\n"
    "v_and_b32 v242, 15, v237\n"
    "v_and_b32 v243, 1, v239\n"
    "v_or_b32 v237, 15, v237\n"
    "v_or_b32 v239, 15, v239\n"
    "v_cmp_ne_u64 vcc, s[18:19], v[236:237]\n"
    "v_cmp_ne_u64 s[48:49], s[18:19], v[238:239]\n"
    "v_readlane_b32 s9, v242, 0\n"
    "v_and_b32 v236, 1, v242\n"
    "s_or_b64 s[48:49], s[48:49], vcc\n"
    "s_cbranch_scc1 75b\n"
    "s_branch 77b\n"
    "330:\n"
    "s_branch 332f\n"
    "45:\n"
    "s_sub_u32 s7, s86, s71\n"
    "s_cmp_eq_u32 s7, 2\n"
    "s_cbranch_scc1 402f\n"
    "s_cmp_eq_u32 s7, 4\n"
    "s_cbranch_scc1 404f\n"
    "v_mfma_f32_16x16x32_bf16 v[24:27], a[96:99], a[68:71], v[24:27]\n"
    "s_add_u32 s7, s70, 0\n"
    "s_add_u32 s64, s64, 2\n"
    "ds_read_b128 a[136:139], v4 offset:12288\n"
    "v_mfma_f32_16x16x32_bf16 v[24:27], a[92:95], a[44:47], v[24:27]\n"
    "buffer_load_dwordx4 v0, s[36:39], s81 offen nt lds\n"
    "v_mfma_f32_16x16x32_bf16 v[24:27], a[88:91], a[20:23], v[24:27]\n"
    "v_add_u32 v14, 1, v14\n"
    "v_accvgpr_write_b32 a188, v44\n"
    "v_mfma_f32_16x16x32_bf16 v[28:31], a[88:91], a[0:3], 0\n"
    "s_mov_b64 vcc, s[12:13]\n"
    "v_accvgpr_write_b32 a189, v45\n"
    "v_mfma_f32_16x16x32_bf16 v[28:31], a[92:95], a[24:27], v[28:31]\n"
    "s_waitcnt lgkmcnt(1)\n"
    "ds_read_b128 a[140:143], v5 offset:12288\n"
    "v_mfma_f32_16x16x32_bf16 v[28:31], a[96:99], a[48:51], v[28:31]\n"
    "ds_read_b128 a[144:147], v6 offset:12288\n"
    "v_accvgpr_mov_b32 a197, a185\n"
    "v_mfma_f32_16x16x32_bf16 v[28:31], a[100:103], a[72:75], v[28:31]\n"
    "buffer_load_dwordx4 v1, s[36:39], s81 offen offset:1024 nt lds\n"
    "v_mfma_f32_16x16x32_bf16 v[28:31], a[104:107], v[64:67], v[28:31]\n"
    "v_cvt_pk_bf16_f32 v52, v20, v24\n"
    "v_accvgpr_mov_b32 a198, a186\n"
    "v_mfma_f32_16x16x32_bf16 v[28:31], a[108:111], v[88:91], v[28:31]\n"
    "v_cvt_pk_bf16_f32 v53, v21, v25\n"
    "v_accvgpr_mov_b32 a199, a187\n"
    "v_mfma_f32_16x16x32_bf16 v[28:31], a[112:115], v[112:115], v[28:31]\n"
    "v_cvt_pk_bf16_f32 v54, v22, v26\n"
    "v_accvgpr_write_b32 a190, v46\n"
    "v_mfma_f32_16x16x32_bf16 v[28:31], a[116:119], v[136:139], v[28:31]\n"
    "ds_read_b128 a[148:151], v7 offset:12288\n"
    "v_accvgpr_write_b32 a191, v47\n"
    "v_mfma_f32_16x16x32_bf16 v[28:31], a[120:123], v[160:163], v[28:31]\n"
    "v_cvt_pk_bf16_f32 v55, v23, v27\n"
    "v_accvgpr_write_b32 a192, v48\n"
    "v_mfma_f32_16x16x32_bf16 v[28:31], a[124:127], v[184:187], v[28:31]\n"
    "buffer_load_dwordx4 v2, s[36:39], s81 offen offset:2048 nt lds\n"
    "v_mfma_f32_16x16x32_bf16 v[28:31], a[128:131], v[208:211], v[28:31]\n"
    "v_readlane_b32 s81, v3, s86\n"
    "v_accvgpr_write_b32 a193, v49\n"
    "v_mfma_f32_16x16x32_bf16 v[28:31], a[132:135], v[232:235], v[28:31]\n"
    "ds_read_b128 a[152:155], v4 offset:12544\n"
    "v_accvgpr_write_b32 a194, v50\n"
    "v_mfma_f32_16x16x32_bf16 v[32:35], a[132:135], v[236:239], 0\n"
    "s_addk_i32 m0, 12288\n"
    "v_accvgpr_write_b32 a195, v51\n"
    "v_mfma_f32_16x16x32_bf16 v[32:35], a[128:131], v[212:215], v[32:35]\n"
    "ds_write2st64_b32 v38, v52, v53 offset0:0 offset1:2\n"
    "v_mfma_f32_16x16x32_bf16 v[32:35], a[124:127], v[188:191], v[32:35]\n"
    "v_accvgpr_mov_b32 a196, a184\n"
    "v_readlane_b32 s9, v3, s7\n"
    "s_branch 46b\n"
    "331:\n"
    "s_branch 333f\n"
    "402:\n"
    "v_mfma_f32_16x16x32_bf16 v[24:27], a[96:99], a[68:71], v[24:27]\n"
    "s_add_u32 s64, s64, 2\n"
    "ds_read_b128 a[136:139], v4 offset:12288\n"
    "v_mfma_f32_16x16x32_bf16 v[24:27], a[92:95], a[44:47], v[24:27]\n"
    "buffer_store_dwordx4 a[188:191], v8, s[40:43], s9 offen sc1\n"
    "v_mfma_f32_16x16x32_bf16 v[24:27], a[88:91], a[20:23], v[24:27]\n"
    "v_add_u32 v14, 1, v14\n"
    "v_mfma_f32_16x16x32_bf16 v[28:31], a[88:91], a[0:3], 0\n"
    "s_mov_b64 vcc, s[12:13]\n"
    "v_mfma_f32_16x16x32_bf16 v[28:31], a[92:95], a[24:27], v[28:31]\n"
    "ds_read_b128 a[140:143], v5 offset:12288\n"
    "v_mfma_f32_16x16x32_bf16 v[28:31], a[96:99], a[48:51], v[28:31]\n"
    "ds_read_b128 a[144:147], v6 offset:12288\n"
    "v_mfma_f32_16x16x32_bf16 v[28:31], a[100:103], a[72:75], v[28:31]\n"
    "buffer_store_dwordx4 a[192:195], v9, s[40:43], s9 offen sc1\n"
    "v_mfma_f32_16x16x32_bf16 v[28:31], a[104:107], v[64:67], v[28:31]\n"
    "v_cvt_pk_bf16_f32 v52, v20, v24\n"
    "v_mfma_f32_16x16x32_bf16 v[28:31], a[108:111], v[88:91], v[28:31]\n"
    "v_cvt_pk_bf16_f32 v53, v21, v25\n"
    "v_mfma_f32_16x16x32_bf16 v[28:31], a[112:115], v[112:115], v[28:31]\n"
    "v_cvt_pk_bf16_f32 v54, v22, v26\n"
    "v_mfma_f32_16x16x32_bf16 v[28:31], a[116:119], v[136:139], v[28:31]\n"
    "ds_read_b128 a[148:151], v7 offset:12288\n"
    "v_mfma_f32_16x16x32_bf16 v[28:31], a[120:123], v[160:163], v[28:31]\n"
    "v_cvt_pk_bf16_f32 v55, v23, v27\n"
    "v_mfma_f32_16x16x32_bf16 v[28:31], a[124:127], v[184:187], v[28:31]\n"
    "buffer_store_dwordx4 a[196:199], v10, s[40:43], s9 offen sc1\n"
    "v_mfma_f32_16x16x32_bf16 v[28:31], a[128:131], v[208:211], v[28:31]\n"
    "v_readlane_b32 s81, v3, s86\n"
    "v_mfma_f32_16x16x32_bf16 v[28:31], a[132:135], v[232:235], v[28:31]\n"
    "ds_read_b128 a[152:155], v4 offset:12544\n"
    "v_mfma_f32_16x16x32_bf16 v[32:35], a[132:135], v[236:239], 0\n"
    "s_addk_i32 m0, 12288\n"
    "v_mfma_f32_16x16x32_bf16 v[32:35], a[128:131], v[212:215], v[32:35]\n"
    "ds_write2st64_b32 v38, v52, v53 offset0:0 offset1:2\n"
    "v_mfma_f32_16x16x32_bf16 v[32:35], a[124:127], v[188:191], v[32:35]\n"
    "s_branch 46b\n"
    "332:\n"
    "s_branch 334f\n"
    "404:\n"
    "v_mfma_f32_16x16x32_bf16 v[24:27], a[96:99], a[68:71], v[24:27]\n"
    "s_sub_u32 s89, s89, 36\n"
    "ds_read_b128 a[136:139], v4 offset:12288\n"
    "v_mfma_f32_16x16x32_bf16 v[24:27], a[92:95], a[44:47], v[24:27]\n"
    "buffer_store_dwordx4 a[208:211], v10, s[40:43], s10 offen sc1\n"
    "v_mfma_f32_16x16x32_bf16 v[24:27], a[88:91], a[20:23], v[24:27]\n"
    "v_add_u32 v14, 1, v14\n"
    "v_mfma_f32_16x16x32_bf16 v[28:31], a[88:91], a[0:3], 0\n"
    "s_mov_b64 vcc, s[12:13]\n"
    "v_mfma_f32_16x16x32_bf16 v[28:31], a[92:95], a[24:27], v[28:31]\n"
    "ds_read_b128 a[140:143], v5 offset:12288\n"
    "v_mfma_f32_16x16x32_bf16 v[28:31], a[96:99], a[48:51], v[28:31]\n"
    "ds_read_b128 a[144:147], v6 offset:12288\n"
    "v_mfma_f32_16x16x32_bf16 v[28:31], a[100:103], a[72:75], v[28:31]\n"
    "buffer_load_dwordx4 v1, s[36:39], s81 offen offset:1024 nt lds\n"
    "v_mfma_f32_16x16x32_bf16 v[28:31], a[104:107], v[64:67], v[28:31]\n"
    "v_cvt_pk_bf16_f32 v52, v20, v24\n"
    "v_mfma_f32_16x16x32_bf16 v[28:31], a[108:111], v[88:91], v[28:31]\n"
    "v_cvt_pk_bf16_f32 v53, v21, v25\n"
    "v_mfma_f32_16x16x32_bf16 v[28:31], a[112:115], v[112:115], v[28:31]\n"
    "v_cvt_pk_bf16_f32 v54, v22, v26\n"
    "v_mfma_f32_16x16x32_bf16 v[28:31], a[116:119], v[136:139], v[28:31]\n"
    "ds_read_b128 a[148:151], v7 offset:12288\n"
    "v_mfma_f32_16x16x32_bf16 v[28:31], a[120:123], v[160:163], v[28:31]\n"
    "v_cvt_pk_bf16_f32 v55, v23, v27\n"
    "v_mfma_f32_16x16x32_bf16 v[28:31], a[124:127], v[184:187], v[28:31]\n"
    "buffer_load_dwordx4 v2, s[36:39], s81 offen offset:2048 nt lds\n"
    "v_mfma_f32_16x16x32_bf16 v[28:31], a[128:131], v[208:211], v[28:31]\n"
    "v_readlane_b32 s81, v3, s86\n"
    "v_mfma_f32_16x16x32_bf16 v[28:31], a[132:135], v[232:235], v[28:31]\n"
    "ds_read_b128 a[152:155], v4 offset:12544\n"
    "v_mfma_f32_16x16x32_bf16 v[32:35], a[132:135], v[236:239], 0\n"
    "s_addk_i32 m0, 12288\n"
    "v_mfma_f32_16x16x32_bf16 v[32:35], a[128:131], v[212:215], v[32:35]\n"
    "ds_write2st64_b32 v38, v52, v53 offset0:0 offset1:2\n"
    "v_mfma_f32_16x16x32_bf16 v[32:35], a[124:127], v[188:191], v[32:35]\n"
    "s_branch 46b\n"
    "333:\n"
    "s_branch 199b\n"
    "65:\n"
    "s_sub_u32 s7, s86, s71\n"
    "s_cmp_eq_u32 s7, 3\n"
    "s_cbranch_scc1 403f\n"
    "v_mfma_f32_16x16x32_bf16 v[32:35], a[144:147], a[68:71], v[32:35]\n"
    "s_add_u32 s7, s70, 1\n"
    "s_add_u32 s65, s65, 2\n"
    "ds_read_b128 a[88:91], v4\n"
    "v_mfma_f32_16x16x32_bf16 v[32:35], a[140:143], a[44:47], v[32:35]\n"
    "buffer_load_dwordx4 v0, s[36:39], s81 offen nt lds\n"
    "v_readlane_b32 s10, v3, s7\n"
    "v_mfma_f32_16x16x32_bf16 v[32:35], a[136:139], a[20:23], v[32:35]\n"
    "v_add_u32 v7, s78, v7\n"
    "v_accvgpr_write_b32 a200, v44\n"
    "v_mfma_f32_16x16x32_bf16 v[20:23], a[136:139], a[0:3], 0\n"
    "v_add_u32 v14, 1, v14\n"
    "v_accvgpr_write_b32 a201, v45\n"
    "v_mfma_f32_16x16x32_bf16 v[20:23], a[140:143], a[24:27], v[20:23]\n"
    "s_waitcnt lgkmcnt(1)\n"
    "ds_read_b128 a[92:95], v5\n"
    "v_mfma_f32_16x16x32_bf16 v[20:23], a[144:147], a[48:51], v[20:23]\n"
    "ds_read_b128 a[96:99], v6\n"
    "v_accvgpr_mov_b32 a208, a184\n"
    "v_mfma_f32_16x16x32_bf16 v[20:23], a[148:151], a[72:75], v[20:23]\n"
    "buffer_load_dwordx4 v1, s[36:39], s81 offen offset:1024 nt lds\n"
    "v_mfma_f32_16x16x32_bf16 v[20:23], a[152:155], v[64:67], v[20:23]\n"
    "v_cvt_pk_bf16_f32 v52, v28, v32\n"
    "v_accvgpr_mov_b32 a209, a185\n"
    "v_mfma_f32_16x16x32_bf16 v[20:23], a[156:159], v[88:91], v[20:23]\n"
    "v_cvt_pk_bf16_f32 v53, v29, v33\n"
    "v_accvgpr_mov_b32 a210, a186\n"
    "v_mfma_f32_16x16x32_bf16 v[20:23], a[160:163], v[112:115], v[20:23]\n"
    "v_cvt_pk_bf16_f32 v54, v30, v34\n"
    "v_accvgpr_mov_b32 a211, a187\n"
    "v_mfma_f32_16x16x32_bf16 v[20:23], a[164:167], v[136:139], v[20:23]\n"
    "ds_read_b128 a[100:103], v7\n"
    "v_accvgpr_write_b32 a202, v46\n"
    "v_mfma_f32_16x16x32_bf16 v[20:23], a[168:171], v[160:163], v[20:23]\n"
    "v_cvt_pk_bf16_f32 v55, v31, v35\n"
    "v_accvgpr_write_b32 a203, v47\n"
    "v_mfma_f32_16x16x32_bf16 v[20:23], a[172:175], v[184:187], v[20:23]\n"
    "buffer_load_dwordx4 v2, s[36:39], s81 offen offset:2048 nt lds\n"
    "v_mfma_f32_16x16x32_bf16 v[20:23], a[176:179], v[208:211], v[20:23]\n"
    "s_mov_b64 vcc, s[12:13]\n"
    "v_accvgpr_write_b32 a204, v48\n"
    "v_mfma_f32_16x16x32_bf16 v[20:23], a[180:183], v[232:235], v[20:23]\n"
    "ds_read_b128 a[104:107], v4 offset:256\n"
    "v_accvgpr_write_b32 a205, v49\n"
    "v_mfma_f32_16x16x32_bf16 v[24:27], a[180:183], v[236:239], 0\n"
    "v_readlane_b32 s81, v3, s86\n"
    "v_accvgpr_write_b32 a206, v50\n"
    "v_mfma_f32_16x16x32_bf16 v[24:27], a[176:179], v[212:215], v[24:27]\n"
    "ds_write2st64_b32 v38, v52, v53 offset0:16 offset1:18\n"
    "v_mfma_f32_16x16x32_bf16 v[24:27], a[172:175], v[188:191], v[24:27]\n"
    "s_addk_i32 m0, 12288\n"
    "v_accvgpr_write_b32 a207, v51\n"
    "s_branch 66b\n"
    "334:\n"
    "s_branch 199b\n"
    "403:\n"
    "v_mfma_f32_16x16x32_bf16 v[32:35], a[144:147], a[68:71], v[32:35]\n"
    "s_sub_u32 s89, s89, 36\n"
    "ds_read_b128 a[88:91], v4\n"
    "v_mfma_f32_16x16x32_bf16 v[32:35], a[140:143], a[44:47], v[32:35]\n"
    "buffer_store_dwordx4 a[200:203], v8, s[40:43], s10 offen sc1\n"
    "v_mfma_f32_16x16x32_bf16 v[32:35], a[136:139], a[20:23], v[32:35]\n"
    "v_add_u32 v7, s78, v7\n"
    "v_mfma_f32_16x16x32_bf16 v[20:23], a[136:139], a[0:3], 0\n"
    "v_add_u32 v14, 1, v14\n"
    "v_mfma_f32_16x16x32_bf16 v[20:23], a[140:143], a[24:27], v[20:23]\n"
    "ds_read_b128 a[92:95], v5\n"
    "v_mfma_f32_16x16x32_bf16 v[20:23], a[144:147], a[48:51], v[20:23]\n"
    "ds_read_b128 a[96:99], v6\n"
    "v_mfma_f32_16x16x32_bf16 v[20:23], a[148:151], a[72:75], v[20:23]\n"
    "buffer_store_dwordx4 a[204:207], v9, s[40:43], s10 offen sc1\n"
    "v_mfma_f32_16x16x32_bf16 v[20:23], a[152:155], v[64:67], v[20:23]\n"
    "v_cvt_pk_bf16_f32 v52, v28, v32\n"
    "v_mfma_f32_16x16x32_bf16 v[20:23], a[156:159], v[88:91], v[20:23]\n"
    "v_cvt_pk_bf16_f32 v53, v29, v33\n"
    "v_mfma_f32_16x16x32_bf16 v[20:23], a[160:163], v[112:115], v[20:23]\n"
    "v_cvt_pk_bf16_f32 v54, v30, v34\n"
    "v_mfma_f32_16x16x32_bf16 v[20:23], a[164:167], v[136:139], v[20:23]\n"
    "ds_read_b128 a[100:103], v7\n"
    "v_mfma_f32_16x16x32_bf16 v[20:23], a[168:171], v[160:163], v[20:23]\n"
    "v_cvt_pk_bf16_f32 v55, v31, v35\n"
    "v_mfma_f32_16x16x32_bf16 v[20:23], a[172:175], v[184:187], v[20:23]\n"
    "buffer_load_dwordx4 v2, s[36:39], s81 offen offset:2048 nt lds\n"
    "v_mfma_f32_16x16x32_bf16 v[20:23], a[176:179], v[208:211], v[20:23]\n"
    "s_mov_b64 vcc, s[12:13]\n"
    "v_mfma_f32_16x16x32_bf16 v[20:23], a[180:183], v[232:235], v[20:23]\n"
    "ds_read_b128 a[104:107], v4 offset:256\n"
    "v_mfma_f32_16x16x32_bf16 v[24:27], a[180:183], v[236:239], 0\n"
    "v_readlane_b32 s81, v3, s86\n"
    "v_mfma_f32_16x16x32_bf16 v[24:27], a[176:179], v[212:215], v[24:27]\n"
    "ds_write2st64_b32 v38, v52, v53 offset0:16 offset1:18\n"
    "v_mfma_f32_16x16x32_bf16 v[24:27], a[172:175], v[188:191], v[24:27]\n"
    "s_addk_i32 m0, 12288\n"
    "s_branch 66b\n"
    "79:\n"
    "s_nop 0\n"
    : "+{s[0:1]}"(dp), "+{s[2:3]}"(ka), "+{s[4:5]}"(did), "+{s6}"(bid), "+{v0}"(tid)
    : [lds] "{s98}"(lds0)
    : "v1","v2","v3","v4","v5","v6","v7","v8","v9","v10","v11","v12","v13","v14","v15","v16","v17","v18","v19","v20","v21","v22","v23","v24","v25","v26","v27","v28","v29","v30","v31","v32","v33","v34","v35","v36","v37","v38","v39","v40","v41","v42","v43","v44","v45","v46","v47","v48","v49","v50","v51","v52","v53","v54","v55","v56","v57","v58","v59","v60","v61","v62","v63","v64","v65","v66","v67","v68","v69","v70","v71","v72","v73","v74","v75","v76","v77","v78","v79","v80","v81","v82","v83","v84","v85","v86","v87","v88","v89","v90","v91","v92","v93","v94","v95","v96","v97","v98","v99","v100","v101","v102","v103","v104","v105","v106","v107","v108","v109","v110","v111","v112","v113","v114","v115","v116","v117","v118","v119","v120","v121","v122","v123","v124","v125","v126","v127","v128","v129","v130","v131","v132","v133","v134","v135","v136","v137","v138","v139","v140","v141","v142","v143","v144","v145","v146","v147","v148","v149","v150","v151","v152","v153","v154","v155","v156","v157","v158","v159","v160","v161","v162","v163","v164","v165","v166","v167","v168","v169","v170","v171","v172","v173","v174","v175","v176","v177","v178","v179","v180","v181","v182","v183","v184","v185","v186","v187","v188","v189","v190","v191","v192","v193","v194","v195","v196","v197","v198","v199","v200","v201","v202","v203","v204","v205","v206","v207","v208","v209","v210","v211","v212","v213","v214","v215","v216","v217","v218","v219","v220","v221","v222","v223","v224","v225","v226","v227","v228","v229","v230","v231","v232","v233","v234","v235","v236","v237","v238","v239","v240","v241","v242","v243","v244","v245","v246","v247","v248","v249","v250","v251","v252","v253","v254","v255","a0","a1","a2","a3","a4","a5","a6","a7","a8","a9","a10","a11","a12","a13","a14","a15","a16","a17","a18","a19","a20","a21","a22","a23","a24","a25","a26","a27","a28","a29","a30","a31","a32","a33","a34","a35","a36","a37","a38","a39","a40","a41","a42","a43","a44","a45","a46","a47","a48","a49","a50","a51","a52","a53","a54","a55","a56","a57","a58","a59","a60","a61","a62","a63","a64","a65","a66","a67","a68","a69","a70","a71","a72","a73","a74","a75","a76","a77","a78","a79","a80","a81","a82","a83","a84","a85","a86","a87","a88","a89","a90","a91","a92","a93","a94","a95","a96","a97","a98","a99","a100","a101","a102","a103","a104","a105","a106","a107","a108","a109","a110","a111","a112","a113","a114","a115","a116","a117","a118","a119","a120","a121","a122","a123","a124","a125","a126","a127","a128","a129","a130","a131","a132","a133","a134","a135","a136","a137","a138","a139","a140","a141","a142","a143","a144","a145","a146","a147","a148","a149","a150","a151","a152","a153","a154","a155","a156","a157","a158","a159","a160","a161","a162","a163","a164","a165","a166","a167","a168","a169","a170","a171","a172","a173","a174","a175","a176","a177","a178","a179","a180","a181","a182","a183","a184","a185","a186","a187","a188","a189","a190","a191","a192","a193","a194","a195","a196","a197","a198","a199","a200","a201","a202","a203","a204","a205","a206","a207","a208","a209","a210","a211","a212","s7","s8","s9","s10","s11","s12","s13","s14","s15","s16","s17","s18","s19","s20","s21","s22","s23","s24","s25","s26","s27","s28","s29","s30","s31","s33","s34","s35","s36","s37","s38","s39","s40","s41","s42","s43","s44","s45","s46","s47","s48","s49","s50","s51","s52","s53","s54","s55","s56","s57","s58","s59","s60","s61","s62","s63","s64","s65","s66","s67","s68","s69","s70","s71","s72","s73","s74","s75","s76","s77","s78","s79","s80","s81","s82","s83","s84","s85","s86","s87","s88","s89","s90","s91","s92","s93","s94","s95","s96","s97","vcc","scc","memory");
}

extern "C" void kernel_launch(void* const* d_in, const int* in_sizes, int n_in,
                              void* d_out, int out_size, void* d_ws, size_t ws_size,
                              hipStream_t stream) {
  Shapes sh;
  for (int j = 0; j < 16; ++j)
    for (int w = 0; w < 4; ++w) {
      const int ty = j >= 11, per = ty ? 10 : 11, n = 16 * (j - 11 * ty);
      auto row = [&](int k) { return ty ? 64 * (k / 10) + 11 + k % 10 : 64 * (k / 22) + 21 * (k / 11 % 2) + k % 11; };
      const int n0 = n + 4 * w, bp = per - n0 % per;
      uint32_t* e = sh.v + 16 * j + 4 * w;
      e[0] = row(n0) * 768 - 512;
      e[1] = (row(n0 + bp) - bp) * 768 - 512;
      e[2] = bp * 768;
      e[3] = (row(n + 8) + 16) * 768 + ty;
      for (int i = 0; i < 4; ++i) sh.rows[16 * j + 4 * w + i] = row(n0 + i) * 768;
    }
  const int grid = 256, maxr = 40;
  const size_t M = (size_t)in_sizes[0] / CH;
  const uint32_t fl = 0x00020000u;
  for (size_t r0 = 0; r0 < M; r0 += (size_t)maxr * grid * TM) {
    const size_t rows = M - r0 < (size_t)maxr * grid * TM ? M - r0 : (size_t)maxr * grid * TM;
    const int nr = (int)((rows + grid * TM - 1) / (grid * TM)), nit = nr < 2 ? 2 : nr;
    const uint32_t nbytes = (uint32_t)(rows * CH * 2);
    const uint32_t np = (nbytes >> 21) & ~1u;
    const uint64_t pages = (np >= 24 ? (np - 14) & ~7u : 64u) | (uint64_t)(np >= 2 ? (np - 2) << 21 : 0u) << 32;
    dwsep_kernel<<<grid, 256, 0, stream>>>((const char*)d_in[0] + r0 * CH * 2, nbytes, fl, (char*)d_out + r0 * CH * 2, nbytes, fl,
                                           (const char*)d_in[2], CH * CH * 2, fl, (const char*)d_in[1], CH * 2, fl,
                                           (uint32_t)nit, (uint32_t)grid * TILE_BYTES, d_ws, pages, sh);
  }
}
```
